```python
import torch

def _softmax(x, axis=-1):
    return torch.softmax(x, dim=axis)
def _gelu(x):
    return torch.nn.functional.gelu(x, approximate="tanh")
def _silu(x):
    return torch.nn.functional.silu(x)
def _sigmoid(x):
    return torch.sigmoid(x)
def _layernorm(x, axis=-1, eps=1e-5):
    m = x.mean(dim=axis, keepdim=True)
    v = x.var(dim=axis, keepdim=True, unbiased=False)
    return (x - m) / torch.sqrt(v + eps)
def _scatter_add(tgt, idx, src, axis=0):
    return tgt.clone().index_add_(axis, idx, src)
def _segment_sum(src, seg, n):
    out = torch.zeros((n,) + src.shape[1:], dtype=src.dtype)
    return out.index_add_(0, seg, src)
def _pad(x, pw):
    flat = tuple(v for pair in reversed(pw) for v in pair)
    return torch.nn.functional.pad(x, flat)

kernel_name = 'interleave_layers__sliding_block__geglu_ffn__prompt'

def setup_inputs():

    _w = (lambda a: (lambda t: t.to(torch.bfloat16) if t.is_floating_point() else t)(torch.as_tensor(a)))
    def _A_setup():
        B, S, D = 1, 2048, 512
        return {
            "x": _w((lambda *s: torch.randn(*s, dtype=torch.bfloat16) * 0.05)(B, S, D)),
            "wqkv": _w((lambda *s: torch.randn(*s, dtype=torch.bfloat16) * 0.05)(D, 3 * D)),
            "wo": _w((lambda *s: torch.randn(*s, dtype=torch.bfloat16) * 0.05)(D, D)),
        }
    _A_in = _A_setup()

    def _B_setup():
        B, S, D, F = 1, 2048, 512, 1792
        return {
            "x": _w((lambda *s: torch.randn(*s, dtype=torch.bfloat16) * 0.05)(B, S, D)),
            "w_gate": _w((lambda *s: torch.randn(*s, dtype=torch.bfloat16) * 0.05)(D, F)),
            "w_up": _w((lambda *s: torch.randn(*s, dtype=torch.bfloat16) * 0.05)(D, F)),
            "w_down": _w((lambda *s: torch.randn(*s, dtype=torch.bfloat16) * 0.05)(F, D)),
        }
    _B_in = _B_setup()

    return {
        "A_x": _A_in["x"],
        "A_wqkv": _A_in["wqkv"],
        "A_wo": _A_in["wo"],
        "B_x": _B_in["x"],
        "B_w_gate": _B_in["w_gate"],
        "B_w_up": _B_in["w_up"],
        "B_w_down": _B_in["w_down"],
    }

def reference(A_x, A_wqkv, A_wo, B_x, B_w_gate, B_w_up, B_w_down):
    def _A_ref(x, wqkv, wo):
        B, S, D = x.shape
        H = 32; Dh = D // H; W = 256
        qkv = (lambda x, *d: x.permute(*d))((x @ wqkv).reshape(B, S, 3, H, Dh), 2, 0, 3, 1, 4)
        q, k, v = qkv[0], qkv[1], qkv[2]
        scores = torch.einsum("bhqd,bhkd->bhqk", q, k) * (Dh ** -0.5)
        idx = torch.arange(S)
        mask = (idx[:, None] - idx[None, :] >= 0) & (idx[:, None] - idx[None, :] < W)
        scores = torch.where(mask[None, None], scores, -1e9)
        p = _softmax(scores, axis=-1)
        o = torch.einsum("bhqk,bhkd->bhqd", p, v)
        return (lambda x, *d: x.permute(*d))(o, 0, 2, 1, 3).reshape(B, S, D) @ wo

    def _B_ref(x, w_gate, w_up, w_down):
        return (_gelu(x @ w_gate) * (x @ w_up)) @ w_down

    x = A_x
    x = x + _A_ref(_layernorm(x, axis=-1), A_wqkv, A_wo)
    x = x + _B_ref(_layernorm(x, axis=-1), B_w_gate, B_w_up, B_w_down)
    x = x + _A_ref(_layernorm(x, axis=-1), A_wqkv, A_wo)
    x = x + _B_ref(_layernorm(x, axis=-1), B_w_gate, B_w_up, B_w_down)
    return x
```

```cpp
#include <hip/hip_runtime.h>
#include <hip/hip_bf16.h>

#define LAS __attribute__((address_space(3)))
#define GAS __attribute__((address_space(1)))
typedef unsigned u32;
typedef unsigned short u16;
typedef u32 u32x4 __attribute__((ext_vector_type(4)));
typedef u32 u32x2 __attribute__((ext_vector_type(2)));
typedef float f32x16 __attribute__((ext_vector_type(16)));
typedef float f32x4 __attribute__((ext_vector_type(4)));
typedef float f32x2 __attribute__((ext_vector_type(2)));
typedef __bf16 bf16x8_t __attribute__((ext_vector_type(8)));
typedef __bf16 bf16x2_t __attribute__((ext_vector_type(2)));
typedef short s16x4 __attribute__((ext_vector_type(4)));

namespace {

constexpr int SEQ = 2048, DM = 512, FF = 1792;
constexpr size_t KiB = 1024, MiB = 1024 * 1024;
constexpr size_t WS_FLAGS = 0;
constexpr size_t FLAG_BYTES = 2 * 5 * 256 * 128;
constexpr size_t WS_TMO = 448 * KiB;
constexpr size_t WS_CS = 512 * KiB;
constexpr size_t WS_XF0 = 1 * MiB;
constexpr size_t WS_WQ = 3 * MiB;
constexpr size_t WS_WO = 4 * MiB + 512 * KiB;
constexpr size_t WS_WG = 5 * MiB;
constexpr size_t WS_WD = 9 * MiB;
constexpr size_t WS_L0 = 16 * MiB, WS_LSTRIDE = 32 * MiB;
constexpr size_t L_KF = 0, L_VF = 2 * MiB, L_OF = 4 * MiB, L_X1 = 6 * MiB, L_X2 = 8 * MiB, L_HF = 10 * MiB;
constexpr size_t WS_END = WS_L0 + 2 * WS_LSTRIDE;

constexpr int NSLOT = 6, SLOT = 16384;
constexpr int LDS_SCR = NSLOT * SLOT;
constexpr int SC_HAND = LDS_SCR;
constexpr int VREC = 1040;
constexpr int SC_RK = LDS_SCR;
constexpr int SC_RV = LDS_SCR + 16384;
constexpr int SC_QS = LDS_SCR + 33280;
constexpr int SC_KS = SC_QS + 8192;
constexpr int SC_VS = SC_KS + 8192;
constexpr int SC_RED = LDS_SCR;
constexpr int SC_END1 = SC_VS + 8320;
constexpr int SC_ST = SC_END1;
constexpr int SC_MS = SC_ST + 2048;
constexpr int SC_CS = SC_MS + 1024;
constexpr int SC_MISC = SC_CS + 1280;
constexpr int LDS_BYTES = SC_MISC + 64;
static_assert(LDS_BYTES <= 163840, "LDS");
static_assert(SC_RED + 49152 <= SC_ST, "RED overlay");
static_assert(SC_RV + 16 * VREC <= SC_QS, "RV");
static_assert(SC_HAND + 32768 <= SC_QS, "HAND");

constexpr float LN_EPS = 1e-5f;
constexpr float QSCALE = 0.25f * 1.4426950408889634f;
constexpr u32 SPIN_CAP = 1u << 17;
constexpr u32 BF_ONES = 0x3f803f80u;

struct Args {
    const u16* x; const u16* wqkv; const u16* wo; const u16* wg; const u16* wu; const u16* wd;
    u16* out; unsigned char* ws;
};

__device__ __forceinline__ f32x16 mfma32(u32x4 a, u32x4 b, f32x16 c) {
    return __builtin_amdgcn_mfma_f32_32x32x16_bf16(__builtin_bit_cast(bf16x8_t, a), __builtin_bit_cast(bf16x8_t, b), c, 0, 0, 0);
}
__device__ __forceinline__ u32 pack2(float lo, float hi) {
    f32x2 v = {lo, hi};
    return __builtin_bit_cast(u32, __builtin_convertvector(v, bf16x2_t));
}
__device__ __forceinline__ float bflo(u32 u) { return __builtin_bit_cast(float, u << 16); }
__device__ __forceinline__ float bfhi(u32 u) { return __builtin_bit_cast(float, u & 0xffff0000u); }
__device__ __forceinline__ float dot2(u32 a, u32 b, float c) {
    return __builtin_amdgcn_fdot2_f32_bf16(__builtin_bit_cast(bf16x2_t, a), __builtin_bit_cast(bf16x2_t, b), c, false);
}
__device__ __forceinline__ void swap32(float& a, float& b) {
    asm volatile("s_nop 1\n\tv_permlane32_swap_b32 %0, %1\n\ts_nop 1" : "+v"(a), "+v"(b));
}
__device__ __forceinline__ float xor32_max(float v) { float a = v, b = v; swap32(a, b); return __builtin_fmaxf(a, b); }
__device__ __forceinline__ float xor32_sum(float v) { float a = v, b = v; swap32(a, b); return a + b; }
#define BAR() do { asm volatile("s_waitcnt lgkmcnt(0)" ::: "memory"); __builtin_amdgcn_s_barrier(); asm volatile("" ::: "memory"); } while (0)
#define WAIT_VM(n) asm volatile("s_waitcnt vmcnt(" #n ")" ::: "memory")

template <class T> __device__ __forceinline__ T lds_ld(const LAS unsigned char* base, int off) { return *(const LAS T*)(base + off); }
template <class T> __device__ __forceinline__ void lds_st(LAS unsigned char* base, int off, T v) { *(LAS T*)(base + off) = v; }

__device__ __forceinline__ void glds4k(const unsigned char* src, u32 voff, u32 dst) {
    u32 keep;
    asm volatile(
        "s_mov_b32 %0, m0\n\t"
        "s_mov_b32 m0, %3\n\t"
        "s_nop 0\n\t"
        "global_load_lds_dwordx4 %1, %2\n\t"
        "global_load_lds_dwordx4 %1, %2 offset:1024\n\t"
        "global_load_lds_dwordx4 %1, %2 offset:2048\n\t"
        "global_load_lds_dwordx4 %1, %2 offset:3072\n\t"
        "s_mov_b32 m0, %0"
        : "=&s"(keep) : "v"(voff), "s"(src), "s"(dst) : "memory");
}

constexpr int PREP_XI = 64 * 8;
constexpr int NF_WQ = 16 * 96, NF_WO = 8 * 64, NF_WG = 16 * 224, NF_WD = 8 * 224;
constexpr int PREP_WI = NF_WQ + NF_WO + NF_WG + NF_WD;
constexpr int PREP_WAVES = PREP_XI + PREP_WI;
constexpr int PREP_WG_TILE = PREP_WAVES / 8;
constexpr int PREP_WG_CS = (1536 + 1792 + 1792) / 64;
constexpr int PREP_WG_FLAGS = 8;
constexpr int PREP_GRID = PREP_WG_TILE + PREP_WG_CS + PREP_WG_FLAGS;
constexpr int PREP_LDS_WAVE = 4608;

__device__ __forceinline__ void seq_inv(int q, int NT, int& c, int& hf) {
    if (q == 0) { c = 0; hf = 0; }
    else if (q == 2 * NT - 1) { c = NT - 1; hf = 1; }
    else if (q & 1) { c = (q + 1) >> 1; hf = 0; }
    else { c = (q >> 1) - 1; hf = 1; }
}

__global__ void __launch_bounds__(512) prep_kernel(Args a) {
    __shared__ __attribute__((aligned(16))) unsigned char sm[8 * PREP_LDS_WAVE];
    const int tid = threadIdx.x, lane = tid & 63, w = tid >> 6, bid = blockIdx.x;
    unsigned char* ws = a.ws;
    if (bid < PREP_WG_TILE) {
        const int item = bid * 8 + w;
        LAS unsigned char* t = (LAS unsigned char*)sm + w * PREP_LDS_WAVE;
        if (item < PREP_XI) {
            const int tb = item >> 3, g = item & 7;
            const u16* src = a.x + (size_t)(tb * 32) * DM + g * 64;
            u32x4 v[4];
#pragma unroll
            for (int q = 0; q < 4; ++q) v[q] = *(const u32x4*)(src + (size_t)(8 * q + (lane >> 3)) * DM + (lane & 7) * 8);
#pragma unroll
            for (int q = 0; q < 4; ++q) lds_st<u32x4>(t, (8 * q + (lane >> 3)) * 144 + (lane & 7) * 16, v[q]);
            asm volatile("s_waitcnt lgkmcnt(0)" ::: "memory");
            const int h = lane >> 5, r = lane & 31;
#pragma unroll
            for (int kbl = 0; kbl < 4; ++kbl) {
                const u32x2 lo = lds_ld<u32x2>(t, r * 144 + kbl * 32 + 8 * h);
                const u32x2 hi = lds_ld<u32x2>(t, r * 144 + kbl * 32 + 16 + 8 * h);
                u32x4 o = {lo.x, lo.y, hi.x, hi.y};
                *(u32x4*)(ws + WS_XF0 + (size_t)(tb * 32 + 4 * g + kbl) * 1024 + lane * 16) = o;
            }
        } else {
            int f = item - PREP_XI;
            const u16 *pA, *pB; int ld, ks; size_t dst;
            if (f < NF_WQ) {
                const int j = f / 96, rem = f % 96, q = rem >> 4, ksl = rem & 15; int c, hf; seq_inv(q, 3, c, hf);
                ks = 16 * hf + ksl; pA = a.wqkv + c * 512 + 32 * j; pB = pA + 16; ld = 1536; dst = WS_WQ + (size_t)f * 1024;
            } else if ((f -= NF_WQ) < NF_WO) {
                const int cb = f >> 6, rem = f & 63, c = rem >> 4, r2 = rem & 15, kq = r2 >> 2, ks2 = (r2 >> 1) & 1, nt = r2 & 1;
                ks = 8 * kq + 2 * c + ks2; pA = a.wo + 64 * cb + 32 * nt; pB = pA + 16; ld = 512; dst = WS_WO + (size_t)f * 1024;
            } else if ((f -= NF_WO) < NF_WG) {
                const int j = f / 224, rem = f % 224, q = rem >> 4, ksl = rem & 15; int c, hf; seq_inv(q, 7, c, hf);
                ks = 16 * hf + ksl; pA = a.wg + 112 * j + 16 * c; pB = a.wu + 112 * j + 16 * c; ld = FF; dst = WS_WG + (size_t)f * 1024;
            } else {
                f -= NF_WG;
                const int cb = f / 224, rem = f % 224, c = rem >> 4, r2 = rem & 15, kq = r2 >> 2, ks2 = (r2 >> 1) & 1, nt = r2 & 1;
                ks = 28 * kq + 2 * c + ks2; pA = a.wd + 64 * cb + 32 * nt; pB = pA + 16; ld = 512; dst = WS_WD + (size_t)f * 1024;
            }
            const int k = lane >> 2, n8 = lane & 3;
            const u16* src = (n8 < 2 ? pA + n8 * 8 : pB + (n8 - 2) * 8) + (size_t)(16 * ks + k) * ld;
            const u32x4 v = *(const u32x4*)src;
            lds_st<u32x4>(t, k * 64 + n8 * 16, v);
            asm volatile("s_waitcnt lgkmcnt(0)" ::: "memory");
            const int g4 = lane >> 4, h = g4 >> 1, nh = g4 & 1, li = lane & 15, q = li >> 2, p = li & 3;
            const s16x4 lo = __builtin_amdgcn_ds_read_tr16_b64_v4i16((LAS s16x4*)(t + (4 * h + q) * 64 + nh * 32 + p * 8));
            const s16x4 hi = __builtin_amdgcn_ds_read_tr16_b64_v4i16((LAS s16x4*)(t + (8 + 4 * h + q) * 64 + nh * 32 + p * 8));
            const u32x2 l2 = __builtin_bit_cast(u32x2, lo), h2 = __builtin_bit_cast(u32x2, hi);
            u32x4 o = {l2.x, l2.y, h2.x, h2.y};
            *(u32x4*)(ws + dst + lane * 16) = o;
        }
    } else if (bid < PREP_WG_TILE + PREP_WG_CS) {
        const int g = bid - PREP_WG_TILE;
        const u16* src; int ld;
        if (g < 24) { src = a.wqkv + g * 64; ld = 1536; }
        else if (g < 52) { src = a.wg + (g - 24) * 64; ld = FF; }
        else { src = a.wu + (g - 52) * 64; ld = FF; }
        const int rg = lane >> 3, ch = lane & 7;
        u32x4 v[8];
#pragma unroll
        for (int it = 0; it < 8; ++it) v[it] = *(const u32x4*)(src + (size_t)(64 * w + 8 * it + rg) * ld + ch * 8);
        float s[8];
#pragma unroll
        for (int e = 0; e < 8; ++e) s[e] = 0.f;
#pragma unroll
        for (int it = 0; it < 8; ++it) {
            s[0] += bflo(v[it].x); s[1] += bfhi(v[it].x); s[2] += bflo(v[it].y); s[3] += bfhi(v[it].y);
            s[4] += bflo(v[it].z); s[5] += bfhi(v[it].z); s[6] += bflo(v[it].w); s[7] += bfhi(v[it].w);
        }
#pragma unroll
        for (int e = 0; e < 8; ++e) { s[e] += __shfl_xor(s[e], 8); s[e] += __shfl_xor(s[e], 16); s[e] += __shfl_xor(s[e], 32); }
        float* red = (float*)sm;
        if (lane < 8) {
#pragma unroll
            for (int e = 0; e < 8; ++e) red[w * 64 + lane * 8 + e] = s[e];
        }
        __syncthreads();
        if (tid < 64) {
            float t = 0.f;
#pragma unroll
            for (int ww = 0; ww < 8; ++ww) t += red[ww * 64 + tid];
            ((float*)(ws + WS_CS))[g * 64 + tid] = t;
        }
    } else {
        const int g = bid - PREP_WG_TILE - PREP_WG_CS;
        const u32x4 z = {0u, 0u, 0u, 0u};
        for (size_t o = (size_t)(g * 512 + tid) * 16; o < FLAG_BYTES; o += (size_t)PREP_WG_FLAGS * 512 * 16) *(u32x4*)(ws + WS_FLAGS + o) = z;
        if (g == 0 && tid == 0) *(u32*)(ws + WS_TMO) = 0u;
    }
}

struct Ctx {
    LAS unsigned char* L;
    u32 Laddr;
    unsigned char* ws;
    __amdgpu_buffer_rsrc_t rs;
    int lane, w, i, j;
    u32 voff;
    int rot;
};

__device__ __forceinline__ u32x4 ld_sc1(const Ctx& c, u32 soff) { return __builtin_amdgcn_raw_buffer_load_b128(c.rs, c.voff, soff, 16); }
__device__ __forceinline__ void st_sc1(const Ctx& c, u32 soff, u32x4 v) { __builtin_amdgcn_raw_buffer_store_b128(v, c.rs, c.voff, soff, 16); }
__device__ __forceinline__ Ctx fresh(const Ctx& c0) {
    Ctx c = c0;
    asm volatile("" : "+s"(c.i), "+s"(c.j), "+s"(c.w), "+s"(c.rot));
    asm volatile("" : "+v"(c.lane));
    c.voff = c.lane * 16;
    return c;
}

__device__ __forceinline__ int slot_off(const Ctx& c, int n) {
    int s = (n % NSLOT) + c.rot;
    s = s >= NSLOT ? s - NSLOT : s;
    return s * SLOT;
}
__device__ __forceinline__ void issue_chunk(const Ctx& c, int n) {
    const int m = n % 38;
    size_t src;
    const int cb = c.j & 7;
    if (m < 6) src = WS_WQ + (size_t)(c.j * 6 + m) * SLOT;
    else if (m < 10) src = WS_WO + (size_t)(cb * 4 + (m - 6)) * SLOT;
    else if (m < 24) src = WS_WG + (size_t)(c.j * 14 + (m - 10)) * SLOT;
    else src = WS_WD + (size_t)(cb * 14 + (m - 24)) * SLOT;
    glds4k(c.ws + src + c.w * 4096, c.voff, c.Laddr + slot_off(c, n) + c.w * 4096);
}

__device__ __forceinline__ u32 flag_off(int layer, int seam, int ci, int cj) {
    return (u32)(WS_FLAGS + (size_t)(((layer * 5 + seam) * 256) + ci * 16 + cj) * 128);
}
__device__ __forceinline__ void publish_group(const Ctx& c, int layer, int seam, int pubw) {
    WAIT_VM(0);
    BAR();
    if (c.w == pubw) {
        if (c.lane < 16) __builtin_amdgcn_raw_buffer_store_b32(1u, c.rs, flag_off(layer, seam, c.i, c.lane) + c.j * 4, 0, 16);
    }
}
__device__ __forceinline__ void wait_flags(const Ctx& c, int layer, int seam, u32 mask, int pollw, bool need_w = true) {
    if (c.w == pollw) {
        const u32 off = flag_off(layer, seam, c.i, c.j) + (c.lane & 15) * 4;
        const bool need = (mask >> (c.lane & 15)) & 1u;
        for (u32 spins = 0;; ++spins) {
            asm volatile("" ::: "memory");
            const u32 f = __builtin_amdgcn_raw_buffer_load_b32(c.rs, off, 0, 16);
            if (__all(!need || f == 1u)) break;
            if (spins > SPIN_CAP) { if (c.lane == 0) __builtin_amdgcn_raw_buffer_store_b32(1u, c.rs, (u32)WS_TMO, 0, 16); break; }
            __builtin_amdgcn_s_sleep(1);
        }
    } else if (need_w && c.w < 4) {
        WAIT_VM(0);
    }
    BAR();
}

__device__ __forceinline__ int crow(int r, int h) { return (r & 3) + 8 * (r >> 2) + 4 * h; }

__device__ __forceinline__ f32x2 ln_stats(const Ctx& c, int tb, int r) {
    const f32x2 s0 = lds_ld<f32x2>(c.L, SC_ST + (tb * 32 + r) * 8);
    const f32x2 s1 = lds_ld<f32x2>(c.L, SC_ST + 1024 + (tb * 32 + r) * 8);
    const float mean = (s0.x + s1.x) * (1.0f / DM);
    float var = (s0.y + s1.y) * (1.0f / DM) - mean * mean;
    var = var > 0.f ? var : 0.f;
    f32x2 o; o.x = mean; o.y = __builtin_amdgcn_rsqf(var + LN_EPS);
    return o;
}

__device__ __forceinline__ void x_stats(const Ctx& c, const u32x4 (&xf)[16], int tb, int hf) {
    float s1 = 0.f, s2 = 0.f;
#pragma unroll
    for (int ks = 0; ks < 16; ++ks) {
        s1 = dot2(xf[ks].x, BF_ONES, s1); s2 = dot2(xf[ks].x, xf[ks].x, s2);
        s1 = dot2(xf[ks].y, BF_ONES, s1); s2 = dot2(xf[ks].y, xf[ks].y, s2);
        s1 = dot2(xf[ks].z, BF_ONES, s1); s2 = dot2(xf[ks].z, xf[ks].z, s2);
        s1 = dot2(xf[ks].w, BF_ONES, s1); s2 = dot2(xf[ks].w, xf[ks].w, s2);
    }
    s1 = xor32_sum(s1); s2 = xor32_sum(s2);
    if (c.lane < 32) { f32x2 v = {s1, s2}; lds_st<f32x2>(c.L, SC_ST + hf * 1024 + (tb * 32 + c.lane) * 8, v); }
}

__device__ __forceinline__ void hand_write(const Ctx& c, int buf, int tb, const f32x16& acc) {
#pragma unroll
    for (int q = 0; q < 4; ++q) {
        f32x4 v = {acc[4 * q], acc[4 * q + 1], acc[4 * q + 2], acc[4 * q + 3]};
        lds_st<f32x4>(c.L, SC_HAND + (buf * 4 + tb) * 4096 + q * 1024 + c.voff, v);
    }
}
__device__ __forceinline__ f32x16 hand_read(const Ctx& c, int buf, int tb) {
    f32x16 acc;
#pragma unroll
    for (int q = 0; q < 4; ++q) {
        const f32x4 v = lds_ld<f32x4>(c.L, SC_HAND + (buf * 4 + tb) * 4096 + q * 1024 + c.voff);
        acc[4 * q] = v.x; acc[4 * q + 1] = v.y; acc[4 * q + 2] = v.z; acc[4 * q + 3] = v.w;
    }
    return acc;
}

constexpr int seq_lo(int c) { return c == 0 ? 0 : 2 * c - 1; }
constexpr int seq_hi(int c, int NT) { return c == NT - 1 ? 2 * NT - 1 : 2 * c + 2; }

__device__ __forceinline__ void phase_qkv_att(const Ctx& c0, int layer) {
    const Ctx c = fresh(c0);
    const int lane = c.lane, w = c.w, tb = w & 3, hf = w >> 2, h = lane >> 5, r = lane & 31;
    const u32 lbase = (u32)(WS_L0 + (size_t)layer * WS_LSTRIDE);
    const u32 xsrc = layer == 0 ? (u32)WS_XF0 : (u32)(WS_L0 + L_X2);
    if (layer == 0) { if (w < 4) WAIT_VM(0); BAR(); }
    else wait_flags(c, layer - 1, 0, 0xffffu, 4);
    u32x4 xf[16];
#pragma unroll
    for (int ks = 0; ks < 16; ++ks) xf[ks] = ld_sc1(c, xsrc + (u32)((4 * c.i + tb) * 32 + 16 * hf + ks) * 1024);

#pragma unroll
    for (int s = 0; s <= 3; ++s) {
        if (hf == 0) {
            if (s < 3) {
                const int so = slot_off(c, seq_lo(s));
                f32x16 acc = {};
#pragma unroll
                for (int ks = 0; ks < 16; ++ks) {
                    const u32x4 wf = lds_ld<u32x4>(c.L, so + ks * 1024 + c.voff);
                    acc = (s == 2) ? mfma32(xf[ks], wf, acc) : mfma32(wf, xf[ks], acc);
                }
                hand_write(c, s & 1, tb, acc);
                if (s == 0) x_stats(c, xf, tb, 0);
            }
        } else {
            if (s == 0) x_stats(c, xf, tb, 1);
            else {
                const int t = s - 1;
                const int so = slot_off(c, seq_hi(t, 3));
                f32x16 acc = hand_read(c, t & 1, tb);
#pragma unroll
                for (int ks = 0; ks < 16; ++ks) {
                    const u32x4 wf = lds_ld<u32x4>(c.L, so + ks * 1024 + c.voff);
                    acc = (t == 2) ? mfma32(xf[ks], wf, acc) : mfma32(wf, xf[ks], acc);
                }
                if (t < 2) {
                    const f32x2 ms = ln_stats(c, tb, r);
                    if (t == 0 && lane < 32) lds_st<f32x2>(c.L, SC_MS + (tb * 32 + r) * 8, ms);
                    const float sc = t == 0 ? ms.y * QSCALE : ms.y;
                    float v[16];
#pragma unroll
                    for (int q = 0; q < 4; ++q) {
                        const f32x4 cs = lds_ld<f32x4>(c.L, SC_CS + (t * 32 + 8 * q + 4 * h) * 4);
#pragma unroll
                        for (int e = 0; e < 4; ++e) v[4 * q + e] = (acc[4 * q + e] - ms.x * cs[e]) * sc;
                    }
#pragma unroll
                    for (int hh = 0; hh < 2; ++hh) {
                        u32x4 o = {pack2(v[8 * hh], v[8 * hh + 1]), pack2(v[8 * hh + 2], v[8 * hh + 3]), pack2(v[8 * hh + 4], v[8 * hh + 5]), pack2(v[8 * hh + 6], v[8 * hh + 7])};
                        lds_st<u32x4>(c.L, (t == 0 ? SC_QS : SC_KS) + (tb * 2 + hh) * 1024 + c.voff, o);
                        if (t == 1) st_sc1(c, lbase + (u32)L_KF + (u32)((4 * c.i + tb) * 32 + 2 * c.j + hh) * 1024, o);
                    }
                } else {
                    const float cs = lds_ld<float>(c.L, SC_CS + (64 + r) * 4);
                    float v[16];
#pragma unroll
                    for (int q = 0; q < 4; ++q) {
                        const f32x4 m01 = lds_ld<f32x4>(c.L, SC_MS + (tb * 32 + 8 * q + 4 * h) * 8);
                        const f32x4 m23 = lds_ld<f32x4>(c.L, SC_MS + (tb * 32 + 8 * q + 4 * h + 2) * 8);
                        v[4 * q + 0] = (acc[4 * q + 0] - m01.x * cs) * m01.y;
                        v[4 * q + 1] = (acc[4 * q + 1] - m01.z * cs) * m01.w;
                        v[4 * q + 2] = (acc[4 * q + 2] - m23.x * cs) * m23.y;
                        v[4 * q + 3] = (acc[4 * q + 3] - m23.z * cs) * m23.w;
                    }
#pragma unroll
                    for (int sv = 0; sv < 2; ++sv) {
                        u32x4 o = {pack2(v[8 * sv], v[8 * sv + 1]), pack2(v[8 * sv + 2], v[8 * sv + 3]), pack2(v[8 * sv + 4], v[8 * sv + 5]), pack2(v[8 * sv + 6], v[8 * sv + 7])};
                        lds_st<u32x4>(c.L, SC_VS + (tb * 2 + sv) * VREC + c.voff, o);
                        st_sc1(c, lbase + (u32)L_VF + (u32)(((4 * c.i + tb) * 16 + c.j) * 2 + sv) * 1024, o);
                    }
                    if (lane < 2) { u32x4 ones = {BF_ONES, BF_ONES, BF_ONES, BF_ONES}; lds_st<u32x4>(c.L, SC_VS + (tb * 2 + lane) * VREC + 1024, ones); }
                    WAIT_VM(0);
                }
            }
        }
        BAR();
        if (hf == 0) {
            if (s == 0) issue_chunk(c, 6);
            if (s == 1) { issue_chunk(c, 7); issue_chunk(c, 8); }
            if (s == 2) { issue_chunk(c, 9); issue_chunk(c, 10); }
            if (s == 3) issue_chunk(c, 11);
        }
    }
    if (w == 4) {
        if (lane < 2 && c.i + 1 + lane < 16) __builtin_amdgcn_raw_buffer_store_b32(1u, c.rs, flag_off(layer, 1, c.i + 1 + lane, c.j) + lane * 4, 0, 16);
    }
    const int qb = w < 4 ? w : 7 - w, hh = w >> 2;
    const u32x4 qf = lds_ld<u32x4>(c.L, SC_QS + (qb * 2 + hh) * 1024 + c.voff);
    const int vlane = (((lane >> 4) & 1) == hh) ? lane * 16 : 1024;
    const float NEG = -__builtin_inff();
    f32x16 oacc = {};
    float m;
    {
        float mx = NEG;
        for (int b = 0; b < qb; ++b) {
            const u32x4 kf = lds_ld<u32x4>(c.L, SC_KS + (b * 2 + hh) * 1024 + c.voff);
            const f32x16 sv = mfma32(kf, qf, f32x16{});
#pragma unroll
            for (int e = 0; e < 16; e += 2) mx = __builtin_fmaxf(mx, __builtin_fmaxf(sv[e], sv[e + 1]));
        }
        f32x16 bias;
#pragma unroll
        for (int e = 0; e < 16; ++e) bias[e] = crow(e, h) <= r ? 0.f : NEG;
        const u32x4 kd = lds_ld<u32x4>(c.L, SC_KS + (qb * 2 + hh) * 1024 + c.voff);
        {
            const f32x16 sv = mfma32(kd, qf, bias);
#pragma unroll
            for (int e = 0; e < 16; e += 2) mx = __builtin_fmaxf(mx, __builtin_fmaxf(sv[e], sv[e + 1]));
        }
        m = xor32_max(mx);
        f32x16 negm;
#pragma unroll
        for (int e = 0; e < 16; ++e) negm[e] = -m;
        for (int b = 0; b <= qb; ++b) {
            const u32x4 kf = lds_ld<u32x4>(c.L, SC_KS + (b * 2 + hh) * 1024 + c.voff);
            f32x16 cin = negm;
            if (b == qb) {
#pragma unroll
                for (int e = 0; e < 16; ++e) cin[e] = bias[e] - m;
            }
            const f32x16 sv = mfma32(kf, qf, cin);
            u32 p[8];
#pragma unroll
            for (int e = 0; e < 8; ++e) p[e] = pack2(__builtin_amdgcn_exp2f(sv[2 * e]), __builtin_amdgcn_exp2f(sv[2 * e + 1]));
            const u32x4 v0 = lds_ld<u32x4>(c.L, SC_VS + (b * 2 + 0) * VREC + vlane);
            const u32x4 v1 = lds_ld<u32x4>(c.L, SC_VS + (b * 2 + 1) * VREC + vlane);
            oacc = mfma32(v0, u32x4{p[0], p[1], p[2], p[3]}, oacc);
            oacc = mfma32(v1, u32x4{p[4], p[5], p[6], p[7]}, oacc);
        }
    }
    if (c.i > 0) {
        wait_flags(c, layer, 1, c.i >= 2 ? 3u : 1u, 7, false);
        {
            u32x4 st[4];
            const int idx = w;
            const int g = 4 * c.i - 8 + idx;
            if (g >= 0) {
                st[0] = ld_sc1(c, lbase + (u32)L_KF + (u32)(g * 32 + 2 * c.j + 0) * 1024);
                st[1] = ld_sc1(c, lbase + (u32)L_KF + (u32)(g * 32 + 2 * c.j + 1) * 1024);
                st[2] = ld_sc1(c, lbase + (u32)L_VF + (u32)((g * 16 + c.j) * 2 + 0) * 1024);
                st[3] = ld_sc1(c, lbase + (u32)L_VF + (u32)((g * 16 + c.j) * 2 + 1) * 1024);
                lds_st<u32x4>(c.L, SC_RK + (idx * 2 + 0) * 1024 + c.voff, st[0]);
                lds_st<u32x4>(c.L, SC_RK + (idx * 2 + 1) * 1024 + c.voff, st[1]);
                lds_st<u32x4>(c.L, SC_RV + (idx * 2 + 0) * VREC + c.voff, st[2]);
                lds_st<u32x4>(c.L, SC_RV + (idx * 2 + 1) * VREC + c.voff, st[3]);
                if (lane < 2) { u32x4 ones = {BF_ONES, BF_ONES, BF_ONES, BF_ONES}; lds_st<u32x4>(c.L, SC_RV + (idx * 2 + lane) * VREC + 1024, ones); }
            }
        }
        BAR();
        const bool edge = (4 * c.i - 8 + qb) >= 0;
        const int b0 = edge ? qb : 4;
        float mx = NEG;
        f32x16 bias;
#pragma unroll
        for (int e = 0; e < 16; ++e) bias[e] = (!edge || crow(e, h) > r) ? 0.f : NEG;
        {
            const u32x4 kd = lds_ld<u32x4>(c.L, SC_RK + (b0 * 2 + hh) * 1024 + c.voff);
            const f32x16 sv = mfma32(kd, qf, bias);
#pragma unroll
            for (int e = 0; e < 16; e += 2) mx = __builtin_fmaxf(mx, __builtin_fmaxf(sv[e], sv[e + 1]));
        }
        for (int b = b0 + 1; b < 8; ++b) {
            const u32x4 kf = lds_ld<u32x4>(c.L, SC_RK + (b * 2 + hh) * 1024 + c.voff);
            const f32x16 sv = mfma32(kf, qf, f32x16{});
#pragma unroll
            for (int e = 0; e < 16; e += 2) mx = __builtin_fmaxf(mx, __builtin_fmaxf(sv[e], sv[e + 1]));
        }
        mx = xor32_max(mx);
        const float mn = __builtin_fmaxf(m, mx);
        const float f = __builtin_amdgcn_exp2f(m - mn);
        m = mn;
#pragma unroll
        for (int e = 0; e < 16; ++e) oacc[e] *= f;
        f32x16 negm;
#pragma unroll
        for (int e = 0; e < 16; ++e) negm[e] = -m;
        for (int b = b0; b < 8; ++b) {
            const u32x4 kf = lds_ld<u32x4>(c.L, SC_RK + (b * 2 + hh) * 1024 + c.voff);
            f32x16 cin = negm;
            if (b == b0) {
#pragma unroll
                for (int e = 0; e < 16; ++e) cin[e] = bias[e] - m;
            }
            const f32x16 sv = mfma32(kf, qf, cin);
            u32 p[8];
#pragma unroll
            for (int e = 0; e < 8; ++e) p[e] = pack2(__builtin_amdgcn_exp2f(sv[2 * e]), __builtin_amdgcn_exp2f(sv[2 * e + 1]));
            const u32x4 v0 = lds_ld<u32x4>(c.L, SC_RV + (b * 2 + 0) * VREC + vlane);
            const u32x4 v1 = lds_ld<u32x4>(c.L, SC_RV + (b * 2 + 1) * VREC + vlane);
            oacc = mfma32(v0, u32x4{p[0], p[1], p[2], p[3]}, oacc);
            oacc = mfma32(v1, u32x4{p[4], p[5], p[6], p[7]}, oacc);
        }
    }
    {
        u32x4 o;
        if (hh == 0) {
            const float il = __builtin_amdgcn_rcpf(oacc[8]);
            o = u32x4{pack2(oacc[0] * il, oacc[1] * il), pack2(oacc[2] * il, oacc[3] * il), pack2(oacc[4] * il, oacc[5] * il), pack2(oacc[6] * il, oacc[7] * il)};
        } else {
            const float il = __builtin_amdgcn_rcpf(oacc[0]);
            o = u32x4{pack2(oacc[8] * il, oacc[9] * il), pack2(oacc[10] * il, oacc[11] * il), pack2(oacc[12] * il, oacc[13] * il), pack2(oacc[14] * il, oacc[15] * il)};
        }
        st_sc1(c, lbase + (u32)L_OF + (u32)((4 * c.i + qb) * 32 + 2 * c.j + hh) * 1024, o);
    }
    publish_group(c, layer, 2, 4);
}

template <int NKS, int N0, bool DOWN>
__device__ __forceinline__ void phase_proj(const Ctx& c0, int layer, const Args& a) {
    const Ctx c = fresh(c0);
    const int lane = c.lane, w = c.w, t2 = w & 1, kq = w >> 1, h = lane >> 5, r = lane & 31;
    const int th = c.j >> 3, cb = c.j & 7;
    const int tbk = 4 * c.i + 2 * th + t2;
    const u32 lbase = (u32)(WS_L0 + (size_t)layer * WS_LSTRIDE);
    constexpr int KBN = DOWN ? 112 : 32;
    const u32 xsrc = lbase + (u32)(DOWN ? L_HF : L_OF);
    const int kbo = 4 * cb + kq;
    const u32 rsrc_off = DOWN ? lbase + (u32)L_X1 : (layer == 0 ? (u32)WS_XF0 : (u32)(WS_L0 + L_X2));
    const u32x4 res = ld_sc1(c, rsrc_off + (u32)(tbk * 32 + kbo) * 1024);
    wait_flags(c, layer, DOWN ? 4 : 2, 0xffffu, 4);
    u32x4 xf[NKS];
#pragma unroll
    for (int s = 0; s < NKS; ++s) xf[s] = ld_sc1(c, xsrc + (u32)(tbk * KBN + NKS * kq + s) * 1024);
    f32x16 acc0 = {}, acc1 = {};
    constexpr int NCH = NKS / 2;
#pragma unroll
    for (int p = 0; p < NCH / 2; ++p) {
#pragma unroll
        for (int cc = 0; cc < 2; ++cc) {
            const int ch = 2 * p + cc;
            const int so = slot_off(c, N0 + ch) + kq * 4096 + c.voff;
#pragma unroll
            for (int ks2 = 0; ks2 < 2; ++ks2) {
                const u32x4 w0 = lds_ld<u32x4>(c.L, so + (ks2 * 2 + 0) * 1024);
                const u32x4 w1 = lds_ld<u32x4>(c.L, so + (ks2 * 2 + 1) * 1024);
                acc0 = mfma32(w0, xf[2 * ch + ks2], acc0);
                acc1 = mfma32(w1, xf[2 * ch + ks2], acc1);
            }
        }
        if (DOWN) {
            if (w < 4 && p + 1 < NCH / 2 && p >= 2) {
                if (p + 1 <= 5) { if (p <= 4) WAIT_VM(8); else WAIT_VM(0); } else WAIT_VM(0);
            }
            BAR();
            if (w < 4 && p <= 3) { issue_chunk(c, N0 + 2 * p + 6); issue_chunk(c, N0 + 2 * p + 7); }
        }
    }
    if (!DOWN) BAR();
#pragma unroll
    for (int d = 0; d < 4; ++d) {
        if (d != kq) {
            const int slot = kq < d ? kq : kq - 1;
            const int base = SC_RED + ((t2 * 4 + d) * 3 + slot) * 2048 + c.voff;
            f32x4 lo, hi;
            if (d == 0) { lo = f32x4{acc0[0], acc0[1], acc0[2], acc0[3]}; hi = f32x4{acc0[4], acc0[5], acc0[6], acc0[7]}; }
            else if (d == 1) { lo = f32x4{acc0[8], acc0[9], acc0[10], acc0[11]}; hi = f32x4{acc0[12], acc0[13], acc0[14], acc0[15]}; }
            else if (d == 2) { lo = f32x4{acc1[0], acc1[1], acc1[2], acc1[3]}; hi = f32x4{acc1[4], acc1[5], acc1[6], acc1[7]}; }
            else { lo = f32x4{acc1[8], acc1[9], acc1[10], acc1[11]}; hi = f32x4{acc1[12], acc1[13], acc1[14], acc1[15]}; }
            lds_st<f32x4>(c.L, base, lo);
            lds_st<f32x4>(c.L, base + 1024, hi);
        }
    }
    float v[8];
    if (kq == 0) {
#pragma unroll
        for (int e = 0; e < 8; ++e) v[e] = acc0[e];
    } else if (kq == 1) {
#pragma unroll
        for (int e = 0; e < 8; ++e) v[e] = acc0[8 + e];
    } else if (kq == 2) {
#pragma unroll
        for (int e = 0; e < 8; ++e) v[e] = acc1[e];
    } else {
#pragma unroll
        for (int e = 0; e < 8; ++e) v[e] = acc1[8 + e];
    }
    BAR();
#pragma unroll
    for (int s = 0; s < 3; ++s) {
        const int base = SC_RED + ((t2 * 4 + kq) * 3 + s) * 2048 + c.voff;
        const f32x4 lo = lds_ld<f32x4>(c.L, base), hi = lds_ld<f32x4>(c.L, base + 1024);
        v[0] += lo.x; v[1] += lo.y; v[2] += lo.z; v[3] += lo.w; v[4] += hi.x; v[5] += hi.y; v[6] += hi.z; v[7] += hi.w;
    }
    u32x4 o = {pack2(v[0] + bflo(res.x), v[1] + bfhi(res.x)), pack2(v[2] + bflo(res.y), v[3] + bfhi(res.y)),
               pack2(v[4] + bflo(res.z), v[5] + bfhi(res.z)), pack2(v[6] + bflo(res.w), v[7] + bfhi(res.w))};
    if (DOWN && layer == 1) {
        u16* dst = a.out + (size_t)(tbk * 32 + r) * DM + kbo * 16 + 4 * h;
        *(u32x2*)dst = u32x2{o.x, o.y};
        *(u32x2*)(dst + 8) = u32x2{o.z, o.w};
    } else {
        st_sc1(c, lbase + (u32)(DOWN ? L_X2 : L_X1) + (u32)(tbk * 32 + kbo) * 1024, o);
        publish_group(c, layer, DOWN ? 0 : 3, 4);
        if (w < 4) {
            if (DOWN) {
#pragma unroll
                for (int n = 38; n < 44; ++n) issue_chunk(c, n);
            } else {
#pragma unroll
                for (int n = 12; n < 16; ++n) issue_chunk(c, n);
            }
        }
    }
}

__device__ __forceinline__ void phase_gu(const Ctx& c0, int layer) {
    const Ctx c = fresh(c0);
    const int lane = c.lane, w = c.w, tb = w & 3, hf = w >> 2, h = lane >> 5, r = lane & 31;
    const u32 lbase = (u32)(WS_L0 + (size_t)layer * WS_LSTRIDE);
    wait_flags(c, layer, 3, 0xffffu, 4);
    u32x4 xf[16];
#pragma unroll
    for (int ks = 0; ks < 16; ++ks) xf[ks] = ld_sc1(c, lbase + (u32)L_X1 + (u32)((4 * c.i + tb) * 32 + 16 * hf + ks) * 1024);
    f32x2 ms = {0.f, 0.f};
    constexpr float GC1 = -2.0f * 0.7978845608028654f * 1.4426950408889634f, GC2 = GC1 * 0.044715f;
#pragma unroll
    for (int s = 0; s <= 7; ++s) {
        if (hf == 0) {
            if (s < 7) {
                const int so = slot_off(c, 10 + seq_lo(s));
                f32x16 acc = {};
#pragma unroll
                for (int ks = 0; ks < 16; ++ks) acc = mfma32(lds_ld<u32x4>(c.L, so + ks * 1024 + c.voff), xf[ks], acc);
                hand_write(c, s & 1, tb, acc);
                if (s == 0) x_stats(c, xf, tb, 0);
            }
            if (s >= 2) WAIT_VM(8);
        } else {
            if (s == 0) x_stats(c, xf, tb, 1);
            else {
                const int t = s - 1;
                const int so = slot_off(c, 10 + seq_hi(t, 7));
                f32x16 acc = hand_read(c, t & 1, tb);
#pragma unroll
                for (int ks = 0; ks < 16; ++ks) acc = mfma32(lds_ld<u32x4>(c.L, so + ks * 1024 + c.voff), xf[ks], acc);
                if (t == 0) ms = ln_stats(c, tb, r);
                float hv[8];
#pragma unroll
                for (int q = 0; q < 2; ++q) {
                    const f32x4 cg = lds_ld<f32x4>(c.L, SC_CS + (96 + t * 32 + 8 * q + 4 * h) * 4);
                    const f32x4 cu = lds_ld<f32x4>(c.L, SC_CS + (96 + t * 32 + 16 + 8 * q + 4 * h) * 4);
#pragma unroll
                    for (int e = 0; e < 4; ++e) {
                        const float g = (acc[4 * q + e] - ms.x * cg[e]) * ms.y;
                        const float u = (acc[8 + 4 * q + e] - ms.x * cu[e]) * ms.y;
                        const float z = g * (GC1 + GC2 * g * g);
                        const float ex = __builtin_amdgcn_exp2f(z);
                        hv[4 * q + e] = g * u * __builtin_amdgcn_rcpf(1.0f + ex);
                    }
                }
                u32x4 o = {pack2(hv[0], hv[1]), pack2(hv[2], hv[3]), pack2(hv[4], hv[5]), pack2(hv[6], hv[7])};
                st_sc1(c, lbase + (u32)L_HF + (u32)((4 * c.i + tb) * 112 + 7 * c.j + t) * 1024, o);
                if (s == 7) WAIT_VM(0);
            }
        }
        BAR();
        if (hf == 0) {
            if (s == 0) issue_chunk(c, 10 + 0 + 6);
            else if (s < 7) { issue_chunk(c, 10 + 2 * s - 1 + 6); issue_chunk(c, 10 + 2 * s + 6); }
            else issue_chunk(c, 10 + 13 + 6);
        }
    }
    if (w == 4) {
        if (lane < 16) __builtin_amdgcn_raw_buffer_store_b32(1u, c.rs, flag_off(layer, 4, c.i, lane) + c.j * 4, 0, 16);
    }
}

__global__ void __launch_bounds__(512, 2) mega_kernel(Args a) {
    extern __shared__ __attribute__((aligned(16))) unsigned char lds[];
    Ctx c;
    c.L = (LAS unsigned char*)lds;
    c.Laddr = (u32)(size_t)c.L;
    c.ws = a.ws;
    c.rs = __builtin_amdgcn_make_buffer_rsrc((void*)a.ws, 0, (int)WS_END, 0x00020000);
    const int tid = threadIdx.x;
    c.lane = tid & 63;
    c.w = __builtin_amdgcn_readfirstlane(tid >> 6);
    const int bid = blockIdx.x;
    c.i = 2 * (bid & 7) + (bid >> 7);
    c.j = (bid >> 3) & 15;
    c.voff = c.lane * 16;
    c.rot = 0;
    if (c.w < 4) {
#pragma unroll
        for (int n = 0; n < 6; ++n) issue_chunk(c, n);
    }
    if (tid < 320) {
        const float* cs = (const float*)(a.ws + WS_CS);
        float v;
        if (tid < 96) v = cs[(tid >> 5) * 512 + 32 * c.j + (tid & 31)];
        else { const int e = tid - 96, t = e >> 5, n = e & 31; v = cs[1536 + (n >> 4) * 1792 + 112 * c.j + 16 * t + (n & 15)]; }
        lds_st<float>(c.L, SC_CS + tid * 4, v);
    }
    for (int layer = 0; layer < 2; ++layer) {
        c.rot = 2 * layer;
        phase_qkv_att(c, layer);
        phase_proj<8, 6, false>(c, layer, a);
        phase_gu(c, layer);
        phase_proj<28, 24, true>(c, layer, a);
    }
}

}

extern "C" void kernel_launch(void* const* d_in, const int* in_sizes, int n_in, void* d_out, int out_size, void* d_ws, size_t ws_size,
                              hipStream_t stream) {
    Args a;
    a.x = (const u16*)d_in[0]; a.wqkv = (const u16*)d_in[1]; a.wo = (const u16*)d_in[2];
    a.wg = (const u16*)d_in[4]; a.wu = (const u16*)d_in[5]; a.wd = (const u16*)d_in[6];
    a.out = (u16*)d_out; a.ws = (unsigned char*)d_ws;
    (void)hipFuncSetAttribute((const void*)mega_kernel, hipFuncAttributeMaxDynamicSharedMemorySize, LDS_BYTES);
    prep_kernel<<<PREP_GRID, 512, 0, stream>>>(a);
    mega_kernel<<<256, 512, LDS_BYTES, stream>>>(a);
}
```

```cpp
#include <hip/hip_runtime.h>
#include <hip/hip_bf16.h>

#define LAS __attribute__((address_space(3)))
#define GAS __attribute__((address_space(1)))
typedef unsigned u32;
typedef unsigned short u16;
typedef u32 u32x4 __attribute__((ext_vector_type(4)));
typedef u32 u32x2 __attribute__((ext_vector_type(2)));
typedef float f32x16 __attribute__((ext_vector_type(16)));
typedef float f32x4 __attribute__((ext_vector_type(4)));
typedef float f32x2 __attribute__((ext_vector_type(2)));
typedef __bf16 bf16x8_t __attribute__((ext_vector_type(8)));
typedef __bf16 bf16x2_t __attribute__((ext_vector_type(2)));
typedef short s16x4 __attribute__((ext_vector_type(4)));

namespace {

constexpr int SEQ = 2048, DM = 512, FF = 1792;
constexpr size_t KiB = 1024, MiB = 1024 * 1024;
constexpr size_t WS_FLAGS = 0;
constexpr size_t FLAG_BYTES = 2 * 5 * 256 * 128;
constexpr size_t WS_TMO = 448 * KiB;
constexpr size_t WS_CS = 512 * KiB;
constexpr size_t WS_XF0 = 1 * MiB;
constexpr size_t WS_WQ = 3 * MiB;
constexpr size_t WS_WO = 4 * MiB + 512 * KiB;
constexpr size_t WS_WG = 5 * MiB;
constexpr size_t WS_WD = 9 * MiB;
constexpr size_t WS_L0 = 16 * MiB, WS_LSTRIDE = 32 * MiB;
constexpr size_t L_KF = 0, L_VF = 2 * MiB, L_OF = 4 * MiB, L_X1 = 6 * MiB, L_X2 = 8 * MiB, L_HF = 10 * MiB;
constexpr size_t WS_END = WS_L0 + 2 * WS_LSTRIDE;

constexpr int NSLOT = 7, SLOT = 16384;
constexpr int LDS_SCR = NSLOT * SLOT;
constexpr int SC_HAND = LDS_SCR;
constexpr int VREC = 1040;
constexpr int SC_RV = SC_HAND;
constexpr int SC_VS = SC_HAND + 32768;
constexpr int SC_ST = SC_VS + 8320;
constexpr int SC_MS = SC_ST + 2048;
constexpr int SC_CS = SC_MS + 1024;
constexpr int LDS_BYTES = SC_CS + 1280 + 64;
constexpr int SC_KS = 0 * SLOT;
constexpr int SC_QS = 0 * SLOT + 8192;
constexpr int SC_RK = 5 * SLOT;
constexpr int SC_RED = 1 * SLOT;
static_assert(LDS_BYTES <= 163840, "LDS");

constexpr float LN_EPS = 1e-5f;
constexpr float QSCALE = 0.25f * 1.4426950408889634f;
constexpr u32 SPIN_CAP = 1u << 17;
constexpr u32 BF_ONES = 0x3f803f80u;

struct Args {
    const u16* x; const u16* wqkv; const u16* wo; const u16* wg; const u16* wu; const u16* wd;
    u16* out; unsigned char* ws;
};

__device__ __forceinline__ f32x16 mfma32(u32x4 a, u32x4 b, f32x16 c) {
    return __builtin_amdgcn_mfma_f32_32x32x16_bf16(__builtin_bit_cast(bf16x8_t, a), __builtin_bit_cast(bf16x8_t, b), c, 0, 0, 0);
}
__device__ __forceinline__ u32 pack2(float lo, float hi) {
    f32x2 v = {lo, hi};
    return __builtin_bit_cast(u32, __builtin_convertvector(v, bf16x2_t));
}
__device__ __forceinline__ float bflo(u32 u) { return __builtin_bit_cast(float, u << 16); }
__device__ __forceinline__ float bfhi(u32 u) { return __builtin_bit_cast(float, u & 0xffff0000u); }
__device__ __forceinline__ float dot2(u32 a, u32 b, float c) {
    return __builtin_amdgcn_fdot2_f32_bf16(__builtin_bit_cast(bf16x2_t, a), __builtin_bit_cast(bf16x2_t, b), c, false);
}
__device__ __forceinline__ void swap32(float& a, float& b) {
    asm volatile("s_nop 1\n\tv_permlane32_swap_b32 %0, %1\n\ts_nop 1" : "+v"(a), "+v"(b));
}
__device__ __forceinline__ float fmx(float a, float b) { return __builtin_elementwise_maximum(a, b); }
__device__ __forceinline__ float xor32_max(float v) { float a = v, b = v; swap32(a, b); return fmx(a, b); }
__device__ __forceinline__ float xor32_sum(float v) { float a = v, b = v; swap32(a, b); return a + b; }
#define BAR() do { asm volatile("s_waitcnt lgkmcnt(0)" ::: "memory"); __builtin_amdgcn_s_barrier(); asm volatile("" ::: "memory"); } while (0)
#define WAIT_VM(n) asm volatile("s_waitcnt vmcnt(" #n ")" ::: "memory")
#define SB() __builtin_amdgcn_sched_barrier(0)

template <class T> __device__ __forceinline__ T lds_ld(const LAS unsigned char* base, int off) { return *(const LAS T*)(base + off); }
template <class T> __device__ __forceinline__ void lds_st(LAS unsigned char* base, int off, T v) { *(LAS T*)(base + off) = v; }

__device__ __forceinline__ void glds4k(const unsigned char* src, u32 voff, u32 dst) {
    u32 keep;
    asm volatile(
        "s_mov_b32 %0, m0\n\t"
        "s_mov_b32 m0, %3\n\t"
        "s_nop 0\n\t"
        "global_load_lds_dwordx4 %1, %2\n\t"
        "global_load_lds_dwordx4 %1, %2 offset:1024\n\t"
        "global_load_lds_dwordx4 %1, %2 offset:2048\n\t"
        "global_load_lds_dwordx4 %1, %2 offset:3072\n\t"
        "s_mov_b32 m0, %0"
        : "=&s"(keep) : "v"(voff), "s"(src), "s"(dst) : "memory");
}

__device__ __forceinline__ void glds1k(const unsigned char* src, u32 voff, u32 dst) {
    u32 keep;
    asm volatile(
        "s_mov_b32 %0, m0\n\t"
        "s_mov_b32 m0, %3\n\t"
        "s_nop 0\n\t"
        "global_load_lds_dwordx4 %1, %2\n\t"
        "s_mov_b32 m0, %0"
        : "=&s"(keep) : "v"(voff), "s"(src), "s"(dst) : "memory");
}
#define PIN(x) asm volatile("" : "+v"(x))

constexpr int PREP_XI = 64 * 8;
constexpr int NF_WQ = 16 * 96, NF_WO = 8 * 64, NF_WG = 16 * 224, NF_WD = 8 * 224;
constexpr int PREP_WI = NF_WQ + NF_WO + NF_WG + NF_WD;
constexpr int PREP_WAVES = PREP_XI + PREP_WI;
constexpr int PREP_WG_TILE = PREP_WAVES / 8;
constexpr int PREP_WG_CS = (1536 + 1792 + 1792) / 64;
constexpr int PREP_WG_FLAGS = 8;
constexpr int PREP_GRID = PREP_WG_TILE + PREP_WG_CS + PREP_WG_FLAGS;
constexpr int PREP_LDS_WAVE = 4608;
__host__ __device__ constexpr int qkv_col(int t) { return t == 0 ? 512 : (t == 1 ? 1024 : 0); }

__global__ void __launch_bounds__(512) prep_kernel(Args a) {
    __shared__ __attribute__((aligned(16))) unsigned char sm[8 * PREP_LDS_WAVE];
    const int tid = threadIdx.x, lane = tid & 63, w = tid >> 6, bid = blockIdx.x;
    unsigned char* ws = a.ws;
    if (bid < PREP_WG_TILE) {
        const int item = bid * 8 + w;
        LAS unsigned char* t = (LAS unsigned char*)sm + w * PREP_LDS_WAVE;
        if (item < PREP_XI) {
            const int tb = item >> 3, g = item & 7;
            const u16* src = a.x + (size_t)(tb * 32) * DM + g * 64;
            u32x4 v[4];
#pragma unroll
            for (int q = 0; q < 4; ++q) v[q] = *(const u32x4*)(src + (size_t)(8 * q + (lane >> 3)) * DM + (lane & 7) * 8);
#pragma unroll
            for (int q = 0; q < 4; ++q) lds_st<u32x4>(t, (8 * q + (lane >> 3)) * 144 + (lane & 7) * 16, v[q]);
            asm volatile("s_waitcnt lgkmcnt(0)" ::: "memory");
            const int h = lane >> 5, r = lane & 31;
#pragma unroll
            for (int kbl = 0; kbl < 4; ++kbl) {
                const u32x2 lo = lds_ld<u32x2>(t, r * 144 + kbl * 32 + 8 * h);
                const u32x2 hi = lds_ld<u32x2>(t, r * 144 + kbl * 32 + 16 + 8 * h);
                u32x4 o = {lo.x, lo.y, hi.x, hi.y};
                *(u32x4*)(ws + WS_XF0 + (size_t)(tb * 32 + 4 * g + kbl) * 1024 + lane * 16) = o;
            }
        } else {
            int f = item - PREP_XI;
            const u16 *pA, *pB; int ld, ks; size_t dst;
            if (f < NF_WQ) {
                const int j = f / 96, rem = f % 96, q = rem >> 4, ksl = rem & 15, c = q >> 1, hf = q & 1;
                ks = 16 * hf + ksl; pA = a.wqkv + qkv_col(c) + 32 * j; pB = pA + 16; ld = 1536; dst = WS_WQ + (size_t)f * 1024;
            } else if ((f -= NF_WQ) < NF_WO) {
                const int cb = f >> 6, rem = f & 63, c = rem >> 4, r2 = rem & 15, kq = r2 >> 2, ks2 = (r2 >> 1) & 1, nt = r2 & 1;
                ks = 8 * kq + 2 * c + ks2; pA = a.wo + 64 * cb + 32 * nt; pB = pA + 16; ld = 512; dst = WS_WO + (size_t)f * 1024;
            } else if ((f -= NF_WO) < NF_WG) {
                const int j = f / 224, rem = f % 224, q = rem >> 4, ksl = rem & 15, c = q >> 1, hf = q & 1;
                ks = 16 * hf + ksl; pA = a.wg + 112 * j + 16 * c; pB = a.wu + 112 * j + 16 * c; ld = FF; dst = WS_WG + (size_t)f * 1024;
            } else {
                f -= NF_WG;
                const int cb = f / 224, rem = f % 224, c = rem >> 4, r2 = rem & 15, kq = r2 >> 2, ks2 = (r2 >> 1) & 1, nt = r2 & 1;
                ks = 28 * kq + 2 * c + ks2; pA = a.wd + 64 * cb + 32 * nt; pB = pA + 16; ld = 512; dst = WS_WD + (size_t)f * 1024;
            }
            const int k = lane >> 2, n8 = lane & 3;
            const u16* src = (n8 < 2 ? pA + n8 * 8 : pB + (n8 - 2) * 8) + (size_t)(16 * ks + k) * ld;
            const u32x4 v = *(const u32x4*)src;
            lds_st<u32x4>(t, k * 64 + n8 * 16, v);
            asm volatile("s_waitcnt lgkmcnt(0)" ::: "memory");
            const int g4 = lane >> 4, h = g4 >> 1, nh = g4 & 1, li = lane & 15, q = li >> 2, p = li & 3;
            const s16x4 lo = __builtin_amdgcn_ds_read_tr16_b64_v4i16((LAS s16x4*)(t + (4 * h + q) * 64 + nh * 32 + p * 8));
            const s16x4 hi = __builtin_amdgcn_ds_read_tr16_b64_v4i16((LAS s16x4*)(t + (8 + 4 * h + q) * 64 + nh * 32 + p * 8));
            const u32x2 l2 = __builtin_bit_cast(u32x2, lo), h2 = __builtin_bit_cast(u32x2, hi);
            u32x4 o = {l2.x, l2.y, h2.x, h2.y};
            *(u32x4*)(ws + dst + lane * 16) = o;
        }
    } else if (bid < PREP_WG_TILE + PREP_WG_CS) {
        const int g = bid - PREP_WG_TILE;
        const u16* src; int ld;
        if (g < 24) { src = a.wqkv + g * 64; ld = 1536; }
        else if (g < 52) { src = a.wg + (g - 24) * 64; ld = FF; }
        else { src = a.wu + (g - 52) * 64; ld = FF; }
        const int rg = lane >> 3, ch = lane & 7;
        u32x4 v[8];
#pragma unroll
        for (int it = 0; it < 8; ++it) v[it] = *(const u32x4*)(src + (size_t)(64 * w + 8 * it + rg) * ld + ch * 8);
        float s[8];
#pragma unroll
        for (int e = 0; e < 8; ++e) s[e] = 0.f;
#pragma unroll
        for (int it = 0; it < 8; ++it) {
            s[0] += bflo(v[it].x); s[1] += bfhi(v[it].x); s[2] += bflo(v[it].y); s[3] += bfhi(v[it].y);
            s[4] += bflo(v[it].z); s[5] += bfhi(v[it].z); s[6] += bflo(v[it].w); s[7] += bfhi(v[it].w);
        }
#pragma unroll
        for (int e = 0; e < 8; ++e) { s[e] += __shfl_xor(s[e], 8); s[e] += __shfl_xor(s[e], 16); s[e] += __shfl_xor(s[e], 32); }
        float* red = (float*)sm;
        if (lane < 8) {
#pragma unroll
            for (int e = 0; e < 8; ++e) red[w * 64 + lane * 8 + e] = s[e];
        }
        __syncthreads();
        if (tid < 64) {
            float t = 0.f;
#pragma unroll
            for (int ww = 0; ww < 8; ++ww) t += red[ww * 64 + tid];
            ((float*)(ws + WS_CS))[g * 64 + tid] = t;
        }
    } else {
        const int g = bid - PREP_WG_TILE - PREP_WG_CS;
        const u32x4 z = {0u, 0u, 0u, 0u};
        for (size_t o = (size_t)(g * 512 + tid) * 16; o < FLAG_BYTES; o += (size_t)PREP_WG_FLAGS * 512 * 16) *(u32x4*)(ws + WS_FLAGS + o) = z;
        if (g == 0 && tid == 0) *(u32*)(ws + WS_TMO) = 0u;
    }
}

#define STAMP(k) do { } while (0)
struct Ctx {
    LAS unsigned char* L;
    u32 Laddr;
    unsigned char* ws;
    __amdgpu_buffer_rsrc_t rs;
    int lane, w, i, j;
    u32 voff;
};

__device__ __forceinline__ u32x4 ld_sc1(const Ctx& c, u32 soff) { return __builtin_amdgcn_raw_buffer_load_b128(c.rs, c.voff, soff, 16); }
__device__ __forceinline__ void st_sc1(const Ctx& c, u32 soff, u32x4 v) { __builtin_amdgcn_raw_buffer_store_b128(v, c.rs, c.voff, soff, 16); }
__device__ __forceinline__ Ctx fresh(const Ctx& c0) {
    Ctx c = c0;
    asm volatile("" : "+s"(c.i), "+s"(c.j), "+s"(c.w));
    asm volatile("" : "+v"(c.lane));
    c.voff = c.lane * 16;
    return c;
}

constexpr int CQ(int t, int hf) { return 2 * t + hf; }
constexpr int CO(int k) { return 6 + k; }
constexpr int CG(int t, int hf) { return 10 + 2 * t + hf; }
constexpr int CD(int k) { return 24 + k; }
constexpr int slot_of(int n) {
    constexpr int tab[38] = {
          0, 2,   1, 4,   3, 5,
          6, 1, 2, 3,
          4, 5,   0, 1,   6, 3,   2, 0,   4, 6,   5, 2,   1, 3,
          4, 0, 5, 6, 1, 2, 3,   4, 0, 5, 6, 1, 2, 0};
    return tab[n];
}
template <int N> __device__ __forceinline__ const unsigned char* chunk_src(const Ctx& c) {
    size_t src;
    const int cb = c.j & 7;
    if (N < 6) src = WS_WQ + (size_t)(c.j * 6 + N) * SLOT;
    else if (N < 10) src = WS_WO + (size_t)(cb * 4 + (N - 6)) * SLOT;
    else if (N < 24) src = WS_WG + (size_t)(c.j * 14 + (N - 10)) * SLOT;
    else src = WS_WD + (size_t)(cb * 14 + (N - 24)) * SLOT;
    return c.ws + src + c.w * 4096;
}
constexpr int chunk_lds(int n) { return n == 37 ? SC_HAND : slot_of(n) * SLOT; }
template <int N> __device__ __forceinline__ void issue_chunk(const Ctx& c) {
    glds4k(chunk_src<N>(c), c.voff, c.Laddr + chunk_lds(N) + c.w * 4096);
}
template <int N> __device__ __forceinline__ void issue_piece(const Ctx& c, int q) {
    if (N >= 0) glds1k(chunk_src<(N < 0 ? 0 : N)>(c) + q * 1024, c.voff, c.Laddr + chunk_lds(N < 0 ? 0 : N) + c.w * 4096 + q * 1024);
}
__device__ __forceinline__ const LAS unsigned char* lds_base(const Ctx& c, int off) {
    u32 a = c.Laddr + off + c.voff;
    asm volatile("" : "+v"(a));
    return (const LAS unsigned char*)(size_t)a;
}

__device__ __forceinline__ u32 flag_off(int layer, int seam, int ci, int cj) {
    return (u32)(WS_FLAGS + (size_t)(((layer * 5 + seam) * 256) + ci * 16 + cj) * 128);
}
__device__ __forceinline__ void signal_group(const Ctx& c, int layer, int seam) {
    if (c.lane < 16) __builtin_amdgcn_raw_buffer_store_b32(1u, c.rs, flag_off(layer, seam, c.i, c.lane) + c.j * 4, 0, 16);
}
__device__ __forceinline__ void poll_flags(const Ctx& c, int layer, int seam, u32 mask) {
    const u32 off = flag_off(layer, seam, c.i, c.j) + (c.lane & 15) * 4;
    const bool need = (mask >> (c.lane & 15)) & 1u;
    for (u32 spins = 0;; ++spins) {
        asm volatile("" ::: "memory");
        const u32 f = __builtin_amdgcn_raw_buffer_load_b32(c.rs, off, 0, 16);
        if (__all(!need || f == 1u)) break;
        if (spins > SPIN_CAP) { if (c.lane == 0) __builtin_amdgcn_raw_buffer_store_b32(1u, c.rs, (u32)WS_TMO, 0, 16); break; }
        __builtin_amdgcn_s_sleep(1);
    }
}

__device__ __forceinline__ int crow(int r, int h) { return (r & 3) + 8 * (r >> 2) + 4 * h; }

__device__ __forceinline__ f32x2 ln_stats(const Ctx& c, int tb, int r) {
    const f32x2 s0 = lds_ld<f32x2>(c.L, SC_ST + (tb * 32 + r) * 8);
    const f32x2 s1 = lds_ld<f32x2>(c.L, SC_ST + 1024 + (tb * 32 + r) * 8);
    const float mean = (s0.x + s1.x) * (1.0f / DM);
    float var = (s0.y + s1.y) * (1.0f / DM) - mean * mean;
    var = var > 0.f ? var : 0.f;
    f32x2 o; o.x = mean; o.y = __builtin_amdgcn_rsqf(var + LN_EPS);
    return o;
}
__device__ __forceinline__ void x_stats(const Ctx& c, const u32x4 (&xf)[16], int tb, int hf) {
    float s1 = 0.f, s2 = 0.f;
#pragma unroll
    for (int ks = 0; ks < 16; ++ks) {
        s1 = dot2(xf[ks].x, BF_ONES, s1); s2 = dot2(xf[ks].x, xf[ks].x, s2);
        s1 = dot2(xf[ks].y, BF_ONES, s1); s2 = dot2(xf[ks].y, xf[ks].y, s2);
        s1 = dot2(xf[ks].z, BF_ONES, s1); s2 = dot2(xf[ks].z, xf[ks].z, s2);
        s1 = dot2(xf[ks].w, BF_ONES, s1); s2 = dot2(xf[ks].w, xf[ks].w, s2);
    }
    s1 = xor32_sum(s1); s2 = xor32_sum(s2);
    if (c.lane < 32) { f32x2 v = {s1, s2}; lds_st<f32x2>(c.L, SC_ST + hf * 1024 + (tb * 32 + c.lane) * 8, v); }
}
__device__ __forceinline__ void hand_write(const Ctx& c, int buf, int tb, const f32x16& acc) {
#pragma unroll
    for (int q = 0; q < 4; ++q) {
        f32x4 v = {acc[4 * q], acc[4 * q + 1], acc[4 * q + 2], acc[4 * q + 3]};
        lds_st<f32x4>(c.L, SC_HAND + (buf * 4 + tb) * 4096 + q * 1024 + c.voff, v);
    }
}
__device__ __forceinline__ f32x16 hand_read(const Ctx& c, int buf, int tb) {
    f32x16 acc;
#pragma unroll
    for (int q = 0; q < 4; ++q) {
        const f32x4 v = lds_ld<f32x4>(c.L, SC_HAND + (buf * 4 + tb) * 4096 + q * 1024 + c.voff);
        acc[4 * q] = v.x; acc[4 * q + 1] = v.y; acc[4 * q + 2] = v.z; acc[4 * q + 3] = v.w;
    }
    return acc;
}
#define LDF(p, k) (*(const LAS u32x4*)((p) + (k) * 1024))

template <bool SWAP, bool PRE, int N0, int N1>
__device__ __forceinline__ void a_tile(const Ctx& c, f32x16& acc, u32x4 (&wf)[8], const LAS unsigned char* cur, const LAS unsigned char* nxt, const u32x4 (&xf)[16]) {
#pragma unroll
    for (int k = 0; k < 16; ++k) {
        const u32x4 wv = wf[k & 7];
        if (k < 8) wf[k & 7] = LDF(cur, k + 8);
        else if (PRE) wf[k & 7] = LDF(nxt, k - 8);
        acc = SWAP ? mfma32(xf[k], wv, acc) : mfma32(wv, xf[k], acc);
        PIN(acc);
        if (k < 4) issue_piece<N0>(c, k);
        else if (k < 8) issue_piece<N1>(c, k - 4);
        SB();
    }
}

struct GS { float ga, gb, t0, t1, ua, ub; };
__device__ __forceinline__ void gg0(GS& s, float aga, float agb, float cga, float cgb, float rstd, float mr) {
    asm volatile("v_mul_f32 %2, %9, %6\n\tv_mul_f32 %3, %9, %7\n\tv_fma_f32 %0, %4, %8, -%2\n\tv_fma_f32 %1, %5, %8, -%3\n\tv_mul_f32 %2, %0, %0"
                 : "=&v"(s.ga), "=&v"(s.gb), "=&v"(s.t0), "=&v"(s.t1) : "v"(aga), "v"(agb), "v"(cga), "v"(cgb), "v"(rstd), "v"(mr));
}
__device__ __forceinline__ void gg1(GS& s, float c2) {
    asm volatile("v_mul_f32 %1, %3, %3\n\tv_fmaak_f32 %0, %4, %0, 0xc0135761\n\tv_fmaak_f32 %1, %4, %1, 0xc0135761\n\t"
                 "v_mul_f32 %0, %2, %0\n\tv_mul_f32 %1, %3, %1\n\tv_exp_f32 %0, %0"
                 : "+v"(s.t0), "=&v"(s.t1) : "v"(s.ga), "v"(s.gb), "v"(c2));
}
__device__ __forceinline__ void gg2(GS& s, float aua, float aub, float cua, float cub, float rstd, float mr) {
    asm volatile("v_exp_f32 %1, %1\n\tv_mul_f32 %2, %9, %6\n\tv_mul_f32 %3, %9, %7\n\tv_fma_f32 %2, %4, %8, -%2\n\tv_fma_f32 %3, %5, %8, -%3\n\tv_add_f32 %0, 1.0, %0"
                 : "+v"(s.t0), "+v"(s.t1), "=&v"(s.ua), "=&v"(s.ub) : "v"(aua), "v"(aub), "v"(cua), "v"(cub), "v"(rstd), "v"(mr));
}
__device__ __forceinline__ u32 gg3(GS& s) {
    u32 o;
    asm volatile("v_add_f32 %2, 1.0, %2\n\tv_rcp_f32 %1, %1\n\tv_mul_f32 %3, %3, %5\n\tv_rcp_f32 %2, %2\n\tv_mul_f32 %4, %4, %6\n\t"
                 "v_mul_f32 %3, %3, %1\n\tv_mul_f32 %4, %4, %2\n\tv_cvt_pk_bf16_f32 %0, %3, %4"
                 : "=&v"(o), "+v"(s.t0), "+v"(s.t1), "+v"(s.ga), "+v"(s.gb) : "v"(s.ua), "v"(s.ub));
    return o;
}
__device__ __forceinline__ u32 lnq2(float a0, float a1, float c0, float c1, float sc, float msc) {
    u32 o; float t0, t1;
    asm volatile("v_mul_f32 %1, %8, %5\n\tv_mul_f32 %2, %8, %6\n\tv_fma_f32 %1, %3, %7, -%1\n\tv_fma_f32 %2, %4, %7, -%2\n\tv_cvt_pk_bf16_f32 %0, %1, %2"
                 : "=v"(o), "=&v"(t0), "=&v"(t1) : "v"(a0), "v"(a1), "v"(c0), "v"(c1), "v"(sc), "v"(msc));
    return o;
}
__device__ __forceinline__ u32 lnv2(float a0, float a1, float m0, float r0, float m1, float r1, float cs) {
    u32 o; float t0, t1;
    asm volatile("v_fma_f32 %1, -%5, %9, %3\n\tv_fma_f32 %2, -%7, %9, %4\n\tv_mul_f32 %1, %1, %6\n\tv_mul_f32 %2, %2, %8\n\tv_cvt_pk_bf16_f32 %0, %1, %2"
                 : "=v"(o), "=&v"(t0), "=&v"(t1) : "v"(a0), "v"(a1), "v"(m0), "v"(r0), "v"(m1), "v"(r1), "v"(cs));
    return o;
}
constexpr u32 GC2_BITS = 0xbdd2d3e7u;

__device__ __forceinline__ float max16(const f32x16& s, float m) {
    float a = fmx(fmx(s[0], s[1]), m), b = fmx(s[2], s[3]);
    a = fmx(fmx(s[4], s[5]), a); b = fmx(fmx(s[6], s[7]), b);
    a = fmx(fmx(s[8], s[9]), a); b = fmx(fmx(s[10], s[11]), b);
    a = fmx(fmx(s[12], s[13]), a); b = fmx(fmx(s[14], s[15]), b);
    return fmx(a, b);
}
template <int NB, int FIRST, int REST0, int MASK, bool FIRSTSEG>
__device__ __forceinline__ void att_segment(f32x16& oacc, float& m, const LAS unsigned char* kb, const LAS unsigned char* vb, const u32x4& qf, int h, int r) {
    const float NEG = -__builtin_inff();
    u32x4 kf[NB];
#pragma unroll
    for (int t = 0; t < NB; ++t) { const int b = t == 0 ? FIRST : REST0 + t - 1; kf[t] = *(const LAS u32x4*)(kb + b * 2048); }
    f32x16 bias = {};
    if (MASK) {
#pragma unroll
        for (int e = 0; e < 16; ++e) bias[e] = (MASK == 1 ? crow(e, h) <= r : crow(e, h) > r) ? 0.f : NEG;
    }
    SB();
    float mx = FIRSTSEG ? NEG : m;
    f32x16 sv[3];
    sv[0] = mfma32(kf[0], qf, bias);
    if (NB > 1) sv[1] = mfma32(kf[1], qf, f32x16{});
    SB();
#pragma unroll
    for (int t = 0; t < NB; ++t) {
        if (t + 2 < NB) sv[(t + 2) % 3] = mfma32(kf[t + 2], qf, f32x16{});
        mx = max16(sv[t % 3], mx);
        SB();
    }
    mx = xor32_max(mx);
    f32x16 negm;
#pragma unroll
    for (int e = 0; e < 16; ++e) { negm[e] = -mx; bias[e] -= mx; }
    SB();
    sv[0] = mfma32(kf[0], qf, MASK ? bias : negm);
    u32x4 v0 = *(const LAS u32x4*)(vb + (FIRST * 2 + 0) * VREC);
    u32x4 v1 = *(const LAS u32x4*)(vb + (FIRST * 2 + 1) * VREC);
    if (!FIRSTSEG) {
        const float f = __builtin_amdgcn_exp2f(m - mx);
#pragma unroll
        for (int e = 0; e < 16; ++e) oacc[e] *= f;
    }
    m = mx;
    SB();
#pragma unroll
    for (int t = 0; t < NB; ++t) {
        u32x4 n0, n1;
        if (t + 1 < NB) {
            sv[(t + 1) & 1] = mfma32(kf[t + 1], qf, negm);
            n0 = *(const LAS u32x4*)(vb + ((REST0 + t) * 2 + 0) * VREC);
            n1 = *(const LAS u32x4*)(vb + ((REST0 + t) * 2 + 1) * VREC);
        }
        SB();
        const f32x16& s = sv[t & 1];
        u32 p[8];
#pragma unroll
        for (int e = 0; e < 4; ++e) p[e] = pack2(__builtin_amdgcn_exp2f(s[2 * e]), __builtin_amdgcn_exp2f(s[2 * e + 1]));
        SB();
        oacc = mfma32(v0, u32x4{p[0], p[1], p[2], p[3]}, oacc);
#pragma unroll
        for (int e = 4; e < 8; ++e) p[e] = pack2(__builtin_amdgcn_exp2f(s[2 * e]), __builtin_amdgcn_exp2f(s[2 * e + 1]));
        SB();
        oacc = mfma32(v1, u32x4{p[4], p[5], p[6], p[7]}, oacc);
        if (t + 1 < NB) { v0 = n0; v1 = n1; }
        SB();
    }
}

__device__ __forceinline__ void phase_qkv_att(const Ctx& c0, int layer) {
    const Ctx c = fresh(c0);
    const int lane = c.lane, w = c.w, tb = w & 3, h = lane >> 5, r = lane & 31;
    const u32 lbase = (u32)(WS_L0 + (size_t)layer * WS_LSTRIDE);
    const u32 xsrc = layer == 0 ? (u32)WS_XF0 : (u32)(WS_L0 + L_X2);
    STAMP(0);
    if (w < 4) {
        WAIT_VM(16);
        BAR();
        STAMP(1);
        u32x4 xf[16];
#pragma unroll
        for (int ks = 0; ks < 16; ++ks) xf[ks] = ld_sc1(c, xsrc + (u32)((4 * c.i + tb) * 32 + ks) * 1024);
        u32x4 wf[8];
        const LAS unsigned char* w0 = lds_base(c, slot_of(CQ(0, 0)) * SLOT);
        const LAS unsigned char* w1 = lds_base(c, slot_of(CQ(1, 0)) * SLOT);
        const LAS unsigned char* w2 = lds_base(c, slot_of(CQ(2, 0)) * SLOT);
#pragma unroll
        for (int k = 0; k < 8; ++k) wf[k] = LDF(w0, k);
        SB();
        {
            f32x16 acc = {};
            a_tile<false, true, -1, -1>(c, acc, wf, w0, w1, xf);
            hand_write(c, 0, tb, acc);
        }
        BAR(); STAMP(2);
        {
            f32x16 acc = {};
            a_tile<true, true, -1, -1>(c, acc, wf, w1, w2, xf);
            hand_write(c, 1, tb, acc);
            x_stats(c, xf, tb, 0);
        }
        BAR(); STAMP(3);
        {
            f32x16 acc = {};
            a_tile<false, false, CO(1), CO(2)>(c, acc, wf, w2, w2, xf);
            hand_write(c, 0, tb, acc);
        }
        BAR(); STAMP(4);
        issue_chunk<CO(3)>(c); issue_chunk<CG(0, 0)>(c);
        BAR(); STAMP(5);
    } else {
        if (layer > 0 && w == 4) poll_flags(c, layer - 1, 0, 0xffffu);
        BAR();
        STAMP(1);
        __builtin_amdgcn_s_sleep(4);
        u32x4 xf[16];
#pragma unroll
        for (int ks = 0; ks < 16; ++ks) xf[ks] = ld_sc1(c, xsrc + (u32)((4 * c.i + tb) * 32 + 16 + ks) * 1024);
        BAR(); STAMP(2);
        f32x16 accK = hand_read(c, 0, tb);
        {
            const LAS unsigned char* wb = lds_base(c, slot_of(CQ(0, 1)) * SLOT);
            u32x4 wf[8];
#pragma unroll
            for (int k = 0; k < 8; ++k) wf[k] = LDF(wb, k);
            SB();
            a_tile<false, false, -1, -1>(c, accK, wf, wb, wb, xf);
        }
        x_stats(c, xf, tb, 1);
        BAR(); STAMP(3);
        const f32x2 ms = ln_stats(c, tb, r);
        if (lane < 32) lds_st<f32x2>(c.L, SC_MS + (tb * 32 + r) * 8, ms);
        const float msk = ms.x * ms.y;
        f32x16 accV = hand_read(c, 1, tb);
        {
            const LAS unsigned char* wb = lds_base(c, slot_of(CQ(1, 1)) * SLOT);
            u32x4 wf[8];
#pragma unroll
            for (int k = 0; k < 8; ++k) wf[k] = LDF(wb, k);
            f32x4 cs[4];
#pragma unroll
            for (int q = 0; q < 4; ++q) cs[q] = lds_ld<f32x4>(c.L, SC_CS + (0 * 32 + 8 * q + 4 * h) * 4);
            u32 o[8];
            SB();
#pragma unroll
            for (int k = 0; k < 16; ++k) {
                const u32x4 wv = wf[k & 7];
                if (k < 8) wf[k & 7] = LDF(wb, k + 8);
                accV = mfma32(xf[k], wv, accV);
                PIN(accV);
                if (k >= 2 && k < 10) { const int e = 2 * (k - 2); o[k - 2] = lnq2(accK[e], accK[e + 1], cs[e >> 2][e & 3], cs[e >> 2][(e & 3) + 1], ms.y, msk); }
                if (k == 10 || k == 11) {
                    const int hh = k - 10;
                    const u32x4 ov = {o[4 * hh], o[4 * hh + 1], o[4 * hh + 2], o[4 * hh + 3]};
                    lds_st<u32x4>(c.L, SC_KS + (tb * 2 + hh) * 1024 + c.voff, ov);
                    st_sc1(c, lbase + (u32)L_KF + (u32)((4 * c.i + tb) * 32 + 2 * c.j + hh) * 1024, ov);
                }
                SB();
            }
        }
        BAR(); STAMP(4);
        f32x16 accQ = hand_read(c, 0, tb);
        {
            const LAS unsigned char* wb = lds_base(c, slot_of(CQ(2, 1)) * SLOT);
            u32x4 wf[8];
#pragma unroll
            for (int k = 0; k < 8; ++k) wf[k] = LDF(wb, k);
            const float csv = lds_ld<float>(c.L, SC_CS + (32 + r) * 4);
            f32x4 mr[8];
#pragma unroll
            for (int q = 0; q < 4; ++q) {
                mr[2 * q] = lds_ld<f32x4>(c.L, SC_MS + (tb * 32 + 8 * q + 4 * h) * 8);
                mr[2 * q + 1] = lds_ld<f32x4>(c.L, SC_MS + (tb * 32 + 8 * q + 4 * h + 2) * 8);
            }
            u32 o[8];
            SB();
#pragma unroll
            for (int k = 0; k < 16; ++k) {
                const u32x4 wv = wf[k & 7];
                if (k < 8) wf[k & 7] = LDF(wb, k + 8);
                accQ = mfma32(wv, xf[k], accQ);
                PIN(accQ);
                if (k >= 2 && k < 10) { const int pi = k - 2; o[pi] = lnv2(accV[2 * pi], accV[2 * pi + 1], mr[pi].x, mr[pi].y, mr[pi].z, mr[pi].w, csv); }
                if (k == 10 || k == 11) {
                    const int sv = k - 10;
                    const u32x4 ov = {o[4 * sv], o[4 * sv + 1], o[4 * sv + 2], o[4 * sv + 3]};
                    lds_st<u32x4>(c.L, SC_VS + (tb * 2 + sv) * VREC + c.voff, ov);
                    st_sc1(c, lbase + (u32)L_VF + (u32)(((4 * c.i + tb) * 16 + c.j) * 2 + sv) * 1024, ov);
                }
                SB();
            }
            if (lane < 2) { u32x4 ones = {BF_ONES, BF_ONES, BF_ONES, BF_ONES}; lds_st<u32x4>(c.L, SC_VS + (tb * 2 + lane) * VREC + 1024, ones); }
        }
        {
            const float sc = ms.y * QSCALE, msc = ms.x * sc;
            f32x4 cs[4];
#pragma unroll
            for (int q = 0; q < 4; ++q) cs[q] = lds_ld<f32x4>(c.L, SC_CS + (64 + 8 * q + 4 * h) * 4);
            float v[16];
#pragma unroll
            for (int e = 0; e < 16; ++e) v[e] = accQ[e] * sc - msc * cs[e >> 2][e & 3];
#pragma unroll
            for (int hh = 0; hh < 2; ++hh) {
                const u32x4 ov = {pack2(v[8 * hh], v[8 * hh + 1]), pack2(v[8 * hh + 2], v[8 * hh + 3]), pack2(v[8 * hh + 4], v[8 * hh + 5]), pack2(v[8 * hh + 6], v[8 * hh + 7])};
                lds_st<u32x4>(c.L, SC_QS + (tb * 2 + hh) * 1024 + c.voff, ov);
            }
        }
        WAIT_VM(0);
        BAR(); STAMP(5);
        if (w == 4 && lane < 2 && c.i + 1 + lane < 16) __builtin_amdgcn_raw_buffer_store_b32(1u, c.rs, flag_off(layer, 1, c.i + 1 + lane, c.j) + lane * 4, 0, 16);
    }
    const int qb = w < 4 ? w : 7 - w, hh = w >> 2;
    const u32x4 qf = lds_ld<u32x4>(c.L, SC_QS + (qb * 2 + hh) * 1024 + c.voff);
    const int vlane = (((lane >> 4) & 1) == hh) ? lane * 16 : 1024;
    f32x16 oacc = {};
    float m = 0.f;
    {
        const LAS unsigned char* kb = c.L + SC_KS + hh * 1024 + c.voff;
        const LAS unsigned char* vb = c.L + SC_VS + vlane;
        if (qb == 0) att_segment<1, 0, 0, 1, true>(oacc, m, kb, vb, qf, h, r);
        else if (qb == 1) att_segment<2, 1, 0, 1, true>(oacc, m, kb, vb, qf, h, r);
        else if (qb == 2) att_segment<3, 2, 0, 1, true>(oacc, m, kb, vb, qf, h, r);
        else att_segment<4, 3, 0, 1, true>(oacc, m, kb, vb, qf, h, r);
    }
    STAMP(6);
    if (c.i > 0) {
        if (w == 7) poll_flags(c, layer, 1, c.i >= 2 ? 3u : 1u);
        BAR();
        STAMP(7);
        {
            const int idx = w;
            const int g = 4 * c.i - 8 + idx;
            if (g >= 0) {
                const u32x4 s0 = ld_sc1(c, lbase + (u32)L_KF + (u32)(g * 32 + 2 * c.j + 0) * 1024);
                const u32x4 s1 = ld_sc1(c, lbase + (u32)L_KF + (u32)(g * 32 + 2 * c.j + 1) * 1024);
                const u32x4 s2 = ld_sc1(c, lbase + (u32)L_VF + (u32)((g * 16 + c.j) * 2 + 0) * 1024);
                const u32x4 s3 = ld_sc1(c, lbase + (u32)L_VF + (u32)((g * 16 + c.j) * 2 + 1) * 1024);
                lds_st<u32x4>(c.L, SC_RK + (idx * 2 + 0) * 1024 + c.voff, s0);
                lds_st<u32x4>(c.L, SC_RK + (idx * 2 + 1) * 1024 + c.voff, s1);
                lds_st<u32x4>(c.L, SC_RV + (idx * 2 + 0) * VREC + c.voff, s2);
                lds_st<u32x4>(c.L, SC_RV + (idx * 2 + 1) * VREC + c.voff, s3);
                if (lane < 2) { u32x4 ones = {BF_ONES, BF_ONES, BF_ONES, BF_ONES}; lds_st<u32x4>(c.L, SC_RV + (idx * 2 + lane) * VREC + 1024, ones); }
            }
        }
        BAR();
        STAMP(8);
        const LAS unsigned char* kb = c.L + SC_RK + hh * 1024 + c.voff;
        const LAS unsigned char* vb = c.L + SC_RV + vlane;
        if (c.i == 1) att_segment<4, 4, 5, 0, false>(oacc, m, kb, vb, qf, h, r);
        else if (qb == 0) att_segment<8, 0, 1, 2, false>(oacc, m, kb, vb, qf, h, r);
        else if (qb == 1) att_segment<7, 1, 2, 2, false>(oacc, m, kb, vb, qf, h, r);
        else if (qb == 2) att_segment<6, 2, 3, 2, false>(oacc, m, kb, vb, qf, h, r);
        else att_segment<5, 3, 4, 2, false>(oacc, m, kb, vb, qf, h, r);
    }
    {
        u32x4 o;
        if (hh == 0) {
            const float il = __builtin_amdgcn_rcpf(oacc[8]);
            o = u32x4{pack2(oacc[0] * il, oacc[1] * il), pack2(oacc[2] * il, oacc[3] * il), pack2(oacc[4] * il, oacc[5] * il), pack2(oacc[6] * il, oacc[7] * il)};
        } else {
            const float il = __builtin_amdgcn_rcpf(oacc[0]);
            o = u32x4{pack2(oacc[8] * il, oacc[9] * il), pack2(oacc[10] * il, oacc[11] * il), pack2(oacc[12] * il, oacc[13] * il), pack2(oacc[14] * il, oacc[15] * il)};
        }
        st_sc1(c, lbase + (u32)L_OF + (u32)((4 * c.i + qb) * 32 + 2 * c.j + hh) * 1024, o);
    }
    STAMP(9);
    WAIT_VM(0);
    BAR();
    if (w == 4) signal_group(c, layer, 2);
    if (w < 4) { issue_chunk<CG(1, 0)>(c); issue_chunk<CG(0, 1)>(c); }
    STAMP(10);
}

__device__ __forceinline__ void reduce4(const Ctx& c, const f32x16& acc0, const f32x16& acc1, int t2, int kq, float (&v)[8]) {
#pragma unroll
    for (int d = 0; d < 4; ++d) {
        if (d != kq) {
            const int slot = kq < d ? kq : kq - 1;
            const int base = SC_RED + ((t2 * 4 + d) * 3 + slot) * 2048 + c.voff;
            const f32x16& a = d < 2 ? acc0 : acc1;
            const int o = (d & 1) * 8;
            lds_st<f32x4>(c.L, base, f32x4{a[o], a[o + 1], a[o + 2], a[o + 3]});
            lds_st<f32x4>(c.L, base + 1024, f32x4{a[o + 4], a[o + 5], a[o + 6], a[o + 7]});
        }
    }
    if (kq == 0) {
#pragma unroll
        for (int e = 0; e < 8; ++e) v[e] = acc0[e];
    } else if (kq == 1) {
#pragma unroll
        for (int e = 0; e < 8; ++e) v[e] = acc0[8 + e];
    } else if (kq == 2) {
#pragma unroll
        for (int e = 0; e < 8; ++e) v[e] = acc1[e];
    } else {
#pragma unroll
        for (int e = 0; e < 8; ++e) v[e] = acc1[8 + e];
    }
    BAR();
#pragma unroll
    for (int s = 0; s < 3; ++s) {
        const int base = SC_RED + ((t2 * 4 + kq) * 3 + s) * 2048 + c.voff;
        const f32x4 lo = lds_ld<f32x4>(c.L, base), hi = lds_ld<f32x4>(c.L, base + 1024);
        v[0] += lo.x; v[1] += lo.y; v[2] += lo.z; v[3] += lo.w; v[4] += hi.x; v[5] += hi.y; v[6] += hi.z; v[7] += hi.w;
    }
}
__device__ __forceinline__ u32x4 add_res(const float (&v)[8], const u32x4& res) {
    return u32x4{pack2(v[0] + bflo(res.x), v[1] + bfhi(res.x)), pack2(v[2] + bflo(res.y), v[3] + bfhi(res.y)),
                 pack2(v[4] + bflo(res.z), v[5] + bfhi(res.z)), pack2(v[6] + bflo(res.w), v[7] + bfhi(res.w))};
}

__device__ __forceinline__ void phase_wo(const Ctx& c0, int layer) {
    const Ctx c = fresh(c0);
    const int w = c.w, t2 = w & 1, kq = w >> 1;
    const int th = c.j >> 3, cb = c.j & 7;
    const int tbk = 4 * c.i + 2 * th + t2;
    const u32 lbase = (u32)(WS_L0 + (size_t)layer * WS_LSTRIDE);
    const int kbo = 4 * cb + kq;
    const u32 rsrc_off = layer == 0 ? (u32)WS_XF0 : (u32)(WS_L0 + L_X2);
    const u32x4 res = ld_sc1(c, rsrc_off + (u32)(tbk * 32 + kbo) * 1024);
    if (w == 4) poll_flags(c, layer, 2, 0xffffu);
    if (w < 4) WAIT_VM(9);
    BAR();
    STAMP(11);
    u32x4 xf[8];
#pragma unroll
    for (int s = 0; s < 8; ++s) xf[s] = ld_sc1(c, lbase + (u32)L_OF + (u32)(tbk * 32 + 8 * kq + s) * 1024);
    const LAS unsigned char* Lw = c.L + kq * 4096 + c.voff;
    u32x4 wf[16];
#pragma unroll
    for (int ch = 0; ch < 4; ++ch)
#pragma unroll
        for (int q = 0; q < 4; ++q) wf[4 * ch + q] = LDF(Lw + slot_of(CO(ch)) * SLOT, q);
    SB();
    f32x16 acc0 = {}, acc1 = {};
#pragma unroll
    for (int s = 0; s < 8; ++s) {
        acc0 = mfma32(wf[2 * s], xf[s], acc0);
        acc1 = mfma32(wf[2 * s + 1], xf[s], acc1);
        SB();
    }
    BAR();
    STAMP(12);
    float v[8];
    reduce4(c, acc0, acc1, t2, kq, v);
    st_sc1(c, lbase + (u32)L_X1 + (u32)(tbk * 32 + kbo) * 1024, add_res(v, res));
    STAMP(13);
    WAIT_VM(0);
    BAR();
    if (w == 4) signal_group(c, layer, 3);
    if (w < 4) { issue_chunk<CG(2, 0)>(c); issue_chunk<CG(1, 1)>(c); issue_chunk<CG(3, 0)>(c); issue_chunk<CG(2, 1)>(c); }
    STAMP(14);
}

constexpr int gu_n0(int T) { return T == 1 ? CG(4, 0) : T == 2 ? CG(3, 1) : T == 3 ? CG(4, 1) : T == 4 ? CG(5, 1) : T == 5 ? CD(0) : T == 6 ? CD(2) : -1; }
constexpr int gu_n1(int T) { return T == 2 ? CG(5, 0) : T == 3 ? CG(6, 0) : T == 4 ? CG(6, 1) : T == 5 ? CD(1) : T == 6 ? CD(3) : -1; }
template <int T> __device__ __forceinline__ void gu_a_step(const Ctx& c, u32x4 (&wf)[8], const u32x4 (&xf)[16], int tb, int layer) {
    const LAS unsigned char* cur = lds_base(c, slot_of(CG(T, 0)) * SLOT);
    const LAS unsigned char* nxt = lds_base(c, slot_of(CG(T < 6 ? T + 1 : T, 0)) * SLOT);
    f32x16 acc = {};
    if (T == 4) STAMP(29);
    a_tile<false, (T < 6), gu_n0(T), gu_n1(T)>(c, acc, wf, cur, nxt, xf);
    if (T == 4) STAMP(30);
    hand_write(c, T & 1, tb, acc);
    if (T == 1) x_stats(c, xf, tb, 0);
    if (T == 2) WAIT_VM(8);
    if (T >= 3) WAIT_VM(8);
    if (T == 4) STAMP(31);
    BAR();
    STAMP(16 + T);
}
template <int T> __device__ __forceinline__ void gu_b_step(const Ctx& c, f32x16& acc, const f32x16& prev, const u32x4 (&xf)[16], int tb, int h,
                                                           float rstd, float mr, u32 hbase, int layer) {
    const LAS unsigned char* wb = lds_base(c, slot_of(CG(T, 1)) * SLOT);
    if (T == 3) STAMP(29);
    acc = hand_read(c, T & 1, tb);
    u32x4 wf[8];
#pragma unroll
    for (int k = 0; k < 8; ++k) wf[k] = LDF(wb, k);
    f32x4 cg[2], cu[2];
    if (T > 0) {
#pragma unroll
        for (int q = 0; q < 2; ++q) {
            cg[q] = lds_ld<f32x4>(c.L, SC_CS + (96 + (T - 1) * 32 + 8 * q + 4 * h) * 4);
            cu[q] = lds_ld<f32x4>(c.L, SC_CS + (96 + (T - 1) * 32 + 16 + 8 * q + 4 * h) * 4);
        }
    }
    const float c2 = __builtin_bit_cast(float, GC2_BITS);
    GS g;
    u32 o[4];
    SB();
#pragma unroll
    for (int k = 0; k < 16; ++k) {
        const u32x4 wv = wf[k & 7];
        if (k < 8) wf[k & 7] = LDF(wb, k + 8);
        acc = mfma32(wv, xf[k], acc);
        PIN(acc);
        if (T > 0) {
            const int p = k >> 2, e = 2 * p;
            if ((k & 3) == 0) gg0(g, prev[e], prev[e + 1], cg[e >> 2][e & 3], cg[e >> 2][(e & 3) + 1], rstd, mr);
            if ((k & 3) == 1) gg1(g, c2);
            if ((k & 3) == 2) gg2(g, prev[8 + e], prev[8 + e + 1], cu[e >> 2][e & 3], cu[e >> 2][(e & 3) + 1], rstd, mr);
            if ((k & 3) == 3) o[p] = gg3(g);
        }
        SB();
    }
    if (T == 3) STAMP(30);
    if (T > 0) st_sc1(c, hbase + (u32)(T - 1) * 1024, u32x4{o[0], o[1], o[2], o[3]});
    if (T == 3) STAMP(31);
    if (T == 0) x_stats(c, xf, tb, 1);
    if (T < 6) { BAR(); STAMP(17 + T); }
}

__device__ __forceinline__ void phase_gu(const Ctx& c0, int layer) {
    const Ctx c = fresh(c0);
    const int lane = c.lane, w = c.w, tb = w & 3, h = lane >> 5, r = lane & 31;
    const u32 lbase = (u32)(WS_L0 + (size_t)layer * WS_LSTRIDE);
    if (w < 4) {
        WAIT_VM(16);
        BAR();
        STAMP(15);
        u32x4 xf[16];
#pragma unroll
        for (int ks = 0; ks < 16; ++ks) xf[ks] = ld_sc1(c, lbase + (u32)L_X1 + (u32)((4 * c.i + tb) * 32 + ks) * 1024);
        u32x4 wf[8];
#pragma unroll
        for (int k = 0; k < 8; ++k) wf[k] = LDF(lds_base(c, slot_of(CG(0, 0)) * SLOT), k);
        SB();
        gu_a_step<0>(c, wf, xf, tb, layer); gu_a_step<1>(c, wf, xf, tb, layer); gu_a_step<2>(c, wf, xf, tb, layer); gu_a_step<3>(c, wf, xf, tb, layer);
        gu_a_step<4>(c, wf, xf, tb, layer); gu_a_step<5>(c, wf, xf, tb, layer); gu_a_step<6>(c, wf, xf, tb, layer);
        issue_chunk<CD(4)>(c); issue_chunk<CD(5)>(c);
        BAR();
        STAMP(23);
        issue_chunk<CD(6)>(c);
    } else {
        if (w == 4) poll_flags(c, layer, 3, 0xffffu);
        BAR();
        STAMP(15);
        __builtin_amdgcn_s_sleep(4);
        u32x4 xf[16];
#pragma unroll
        for (int ks = 0; ks < 16; ++ks) xf[ks] = ld_sc1(c, lbase + (u32)L_X1 + (u32)((4 * c.i + tb) * 32 + 16 + ks) * 1024);
        BAR();
        STAMP(16);
        const u32 hbase = lbase + (u32)L_HF + (u32)((4 * c.i + tb) * 112 + 7 * c.j) * 1024;
        f32x16 a0, a1;
        gu_b_step<0>(c, a0, a1, xf, tb, h, 0.f, 0.f, hbase, layer);
        const f32x2 ms = ln_stats(c, tb, r);
        const float mr = ms.x * ms.y;
        gu_b_step<1>(c, a1, a0, xf, tb, h, ms.y, mr, hbase, layer);
        gu_b_step<2>(c, a0, a1, xf, tb, h, ms.y, mr, hbase, layer);
        gu_b_step<3>(c, a1, a0, xf, tb, h, ms.y, mr, hbase, layer);
        gu_b_step<4>(c, a0, a1, xf, tb, h, ms.y, mr, hbase, layer);
        gu_b_step<5>(c, a1, a0, xf, tb, h, ms.y, mr, hbase, layer);
        gu_b_step<6>(c, a0, a1, xf, tb, h, ms.y, mr, hbase, layer);
        {
            f32x4 cg[2], cu[2];
#pragma unroll
            for (int q = 0; q < 2; ++q) {
                cg[q] = lds_ld<f32x4>(c.L, SC_CS + (96 + 6 * 32 + 8 * q + 4 * h) * 4);
                cu[q] = lds_ld<f32x4>(c.L, SC_CS + (96 + 6 * 32 + 16 + 8 * q + 4 * h) * 4);
            }
            const float c2 = __builtin_bit_cast(float, GC2_BITS);
            asm volatile("s_nop 15" : "+v"(a0));
            GS g[4];
            u32 o[4];
#pragma unroll
            for (int p = 0; p < 4; ++p) { const int e = 2 * p; gg0(g[p], a0[e], a0[e + 1], cg[e >> 2][e & 3], cg[e >> 2][(e & 3) + 1], ms.y, mr); }
#pragma unroll
            for (int p = 0; p < 4; ++p) gg1(g[p], c2);
#pragma unroll
            for (int p = 0; p < 4; ++p) { const int e = 2 * p; gg2(g[p], a0[8 + e], a0[8 + e + 1], cu[e >> 2][e & 3], cu[e >> 2][(e & 3) + 1], ms.y, mr); }
#pragma unroll
            for (int p = 0; p < 4; ++p) o[p] = gg3(g[p]);
            st_sc1(c, hbase + 6u * 1024, u32x4{o[0], o[1], o[2], o[3]});
        }
        WAIT_VM(0);
        BAR();
        STAMP(23);
        if (w == 4) signal_group(c, layer, 4);
    }
}

__device__ __forceinline__ void phase_dn(const Ctx& c0, int layer, const Args& a) {
    const Ctx c = fresh(c0);
    const int lane = c.lane, w = c.w, t2 = w & 1, kq = w >> 1, h = lane >> 5, r = lane & 31;
    const int th = c.j >> 3, cb = c.j & 7;
    const int tbk = 4 * c.i + 2 * th + t2;
    const u32 lbase = (u32)(WS_L0 + (size_t)layer * WS_LSTRIDE);
    const int kbo = 4 * cb + kq;
    const u32 xsrc = lbase + (u32)L_HF + (u32)(tbk * 112 + 28 * kq) * 1024;
    if (w < 4) WAIT_VM(4);
    BAR();
    u32x4 wr[24];
#pragma unroll
    for (int ch = 0; ch < 6; ++ch) {
        const LAS unsigned char* wb = lds_base(c, chunk_lds(CD(ch)) + kq * 4096);
#pragma unroll
        for (int q = 0; q < 4; ++q) wr[4 * ch + q] = LDF(wb, q);
    }
    BAR();
    if (w < 4) {
        issue_chunk<CD(7)>(c); issue_chunk<CD(8)>(c); issue_chunk<CD(9)>(c); issue_chunk<CD(10)>(c);
        issue_chunk<CD(11)>(c); issue_chunk<CD(12)>(c); issue_chunk<CD(13)>(c);
    }
    const u32x4 res = ld_sc1(c, lbase + (u32)L_X1 + (u32)(tbk * 32 + kbo) * 1024);
    if (w == 4) poll_flags(c, layer, 4, 0xffffu);
    BAR();
    STAMP(25);
    u32x4 xa[12], xb[16];
#pragma unroll
    for (int s = 0; s < 12; ++s) xa[s] = ld_sc1(c, xsrc + (u32)s * 1024);
    SB();
    f32x16 acc0 = {}, acc1 = {};
#pragma unroll
    for (int s = 0; s < 12; ++s) {
        acc0 = mfma32(wr[2 * s], xa[s], acc0);
        acc1 = mfma32(wr[2 * s + 1], xa[s], acc1);
        PIN(acc0); PIN(acc1);
        if (s >= 2 && s < 10) { xb[2 * (s - 2)] = ld_sc1(c, xsrc + (u32)(12 + 2 * (s - 2)) * 1024); xb[2 * (s - 2) + 1] = ld_sc1(c, xsrc + (u32)(13 + 2 * (s - 2)) * 1024); }
        SB();
    }
    WAIT_VM(16);
    BAR();
    STAMP(24);
    {
        const LAS unsigned char* wb[8];
#pragma unroll
        for (int ch = 0; ch < 8; ++ch) wb[ch] = lds_base(c, chunk_lds(CD(6 + ch)) + kq * 4096);
        u32x4 wf[8];
#pragma unroll
        for (int q = 0; q < 8; ++q) wf[q] = LDF(wb[q >> 2], q & 3);
        SB();
#pragma unroll
        for (int s = 0; s < 16; ++s) {
            const u32x4 w0 = wf[(2 * s) & 7], w1 = wf[(2 * s + 1) & 7];
            if (2 * s + 8 < 32) {
                wf[(2 * s) & 7] = LDF(wb[(2 * s + 8) >> 2], (2 * s + 8) & 3);
                wf[(2 * s + 1) & 7] = LDF(wb[(2 * s + 9) >> 2], (2 * s + 9) & 3);
            }
            acc0 = mfma32(w0, xb[s], acc0);
            acc1 = mfma32(w1, xb[s], acc1);
            PIN(acc0); PIN(acc1);
            SB();
        }
    }
    BAR();
    STAMP(26);
    float v[8];
    reduce4(c, acc0, acc1, t2, kq, v);
    const u32x4 o = add_res(v, res);
    if (layer == 1) {
        u16* dst = a.out + (size_t)(tbk * 32 + r) * DM + kbo * 16 + 4 * h;
        *(u32x2*)dst = u32x2{o.x, o.y};
        *(u32x2*)(dst + 8) = u32x2{o.z, o.w};
        STAMP(27);
    } else {
        st_sc1(c, lbase + (u32)L_X2 + (u32)(tbk * 32 + kbo) * 1024, o);
        STAMP(27);
        WAIT_VM(0);
        BAR();
        if (w == 4) signal_group(c, layer, 0);
        if (w < 4) {
            issue_chunk<CQ(0, 0)>(c); issue_chunk<CQ(1, 0)>(c); issue_chunk<CQ(0, 1)>(c); issue_chunk<CQ(2, 0)>(c);
            issue_chunk<CQ(1, 1)>(c); issue_chunk<CQ(2, 1)>(c); issue_chunk<CO(0)>(c);
        }
        STAMP(28);
    }
}

__global__ void __launch_bounds__(512, 2) mega_kernel(Args a) {
    extern __shared__ __attribute__((aligned(16))) unsigned char lds[];
    Ctx c;
    c.L = (LAS unsigned char*)lds;
    c.Laddr = (u32)(size_t)c.L;
    c.ws = a.ws;
    c.rs = __builtin_amdgcn_make_buffer_rsrc((void*)a.ws, 0, (int)WS_END, 0x00020000);
    const int tid = threadIdx.x;
    c.lane = tid & 63;
    c.w = __builtin_amdgcn_readfirstlane(tid >> 6);
    const int bid = blockIdx.x;
    c.i = 2 * (bid & 7) + (bid >> 7);
    c.j = (bid >> 3) & 15;
    c.voff = c.lane * 16;
    if (c.w >= 4) {
        __builtin_amdgcn_s_setprio(1);
        const float* cs = (const float*)(a.ws + WS_CS);
        for (int e0 = tid - 256; e0 < 320; e0 += 256) {
            float v;
            if (e0 < 96) v = cs[qkv_col(e0 >> 5) + 32 * c.j + (e0 & 31)];
            else { const int e = e0 - 96, t = e >> 5, n = e & 31; v = cs[1536 + (n >> 4) * 1792 + 112 * c.j + 16 * t + (n & 15)]; }
            lds_st<float>(c.L, SC_CS + e0 * 4, v);
        }
    } else {
        issue_chunk<CQ(0, 0)>(c); issue_chunk<CQ(1, 0)>(c); issue_chunk<CQ(0, 1)>(c); issue_chunk<CQ(2, 0)>(c);
        issue_chunk<CQ(1, 1)>(c); issue_chunk<CQ(2, 1)>(c); issue_chunk<CO(0)>(c);
    }
    for (int layer = 0; layer < 2; ++layer) {
        phase_qkv_att(c, layer);
        phase_wo(c, layer);
        phase_gu(c, layer);
        phase_dn(c, layer, a);
    }
}

}

extern "C" void kernel_launch(void* const* d_in, const int* in_sizes, int n_in, void* d_out, int out_size, void* d_ws, size_t ws_size,
                              hipStream_t stream) {
    Args a;
    a.x = (const u16*)d_in[0]; a.wqkv = (const u16*)d_in[1]; a.wo = (const u16*)d_in[2];
    a.wg = (const u16*)d_in[4]; a.wu = (const u16*)d_in[5]; a.wd = (const u16*)d_in[6];
    a.out = (u16*)d_out; a.ws = (unsigned char*)d_ws;
    (void)hipFuncSetAttribute((const void*)mega_kernel, hipFuncAttributeMaxDynamicSharedMemorySize, LDS_BYTES);
    prep_kernel<<<PREP_GRID, 512, 0, stream>>>(a);
    mega_kernel<<<256, 512, LDS_BYTES, stream>>>(a);
}
```

```cpp
#include <hip/hip_runtime.h>
#include <hip/hip_bf16.h>

#define LAS __attribute__((address_space(3)))
#define GAS __attribute__((address_space(1)))
typedef unsigned u32;
typedef unsigned short u16;
typedef u32 u32x4 __attribute__((ext_vector_type(4)));
typedef u32 u32x2 __attribute__((ext_vector_type(2)));
typedef float f32x16 __attribute__((ext_vector_type(16)));
typedef float f32x4 __attribute__((ext_vector_type(4)));
typedef float f32x2 __attribute__((ext_vector_type(2)));
typedef __bf16 bf16x8_t __attribute__((ext_vector_type(8)));
typedef __bf16 bf16x2_t __attribute__((ext_vector_type(2)));
typedef short s16x4 __attribute__((ext_vector_type(4)));

namespace {

constexpr int SEQ = 2048, DM = 512, FF = 1792;
constexpr size_t KiB = 1024, MiB = 1024 * 1024;
constexpr size_t WS_FLAGS = 0;
constexpr size_t FLAG_BYTES = 2 * 5 * 256 * 128;
constexpr size_t WS_TMO = 448 * KiB;
constexpr size_t WS_CS = 512 * KiB;
constexpr size_t WS_XF0 = 1 * MiB;
constexpr size_t WS_WQ = 3 * MiB;
constexpr size_t WS_WO = 4 * MiB + 512 * KiB;
constexpr size_t WS_WG = 5 * MiB;
constexpr size_t WS_WD = 9 * MiB;
constexpr size_t WS_L0 = 16 * MiB, WS_LSTRIDE = 32 * MiB;
constexpr size_t L_KF = 0, L_VF = 2 * MiB, L_OF = 4 * MiB, L_X1 = 6 * MiB, L_X2 = 8 * MiB, L_HF = 10 * MiB;
constexpr size_t WS_END = WS_L0 + 2 * WS_LSTRIDE;

constexpr int NSLOT = 8, SLOT = 16384;
constexpr int LDS_SCR = NSLOT * SLOT;
constexpr int VREC = 1040;
constexpr int SC_RV = LDS_SCR;
constexpr int SC_VS = SC_RV + 16640;
constexpr int SC_ST = SC_VS + 8320;
constexpr int SC_CS = SC_ST + 1024;
constexpr int SC_MK = SC_CS + 1280;
constexpr int LDS_BYTES = SC_MK + 4096;
constexpr int SC_KS = 0 * SLOT;
constexpr int SC_QS = 0 * SLOT + 8192;
constexpr int SC_RK = 1 * SLOT;
static_assert(LDS_BYTES <= 163840, "LDS");
__device__ __forceinline__ int red_off(int q) { return q < 5 ? 2 * SLOT + q * 6144 : 6 * SLOT + (q - 5) * 6144; }

constexpr float LN_EPS = 1e-5f;
constexpr float QSCALE = 0.25f * 1.4426950408889634f;
constexpr u32 SPIN_CAP = 1u << 17;
constexpr u32 BF_ONES = 0x3f803f80u;

struct Args {
    const u16* x; const u16* wqkv; const u16* wo; const u16* wg; const u16* wu; const u16* wd;
    u16* out; unsigned char* ws;
};

__device__ __forceinline__ f32x16 mfma32(u32x4 a, u32x4 b, f32x16 c) {
    return __builtin_amdgcn_mfma_f32_32x32x16_bf16(__builtin_bit_cast(bf16x8_t, a), __builtin_bit_cast(bf16x8_t, b), c, 0, 0, 0);
}
__device__ __forceinline__ u32 pack2(float lo, float hi) {
    f32x2 v = {lo, hi};
    return __builtin_bit_cast(u32, __builtin_convertvector(v, bf16x2_t));
}
__device__ __forceinline__ float bflo(u32 u) { return __builtin_bit_cast(float, u << 16); }
__device__ __forceinline__ float bfhi(u32 u) { return __builtin_bit_cast(float, u & 0xffff0000u); }
__device__ __forceinline__ float dot2(u32 a, u32 b, float c) {
    return __builtin_amdgcn_fdot2_f32_bf16(__builtin_bit_cast(bf16x2_t, a), __builtin_bit_cast(bf16x2_t, b), c, false);
}
__device__ __forceinline__ void swap32(float& a, float& b) {
    asm volatile("s_nop 1\n\tv_permlane32_swap_b32 %0, %1\n\ts_nop 1" : "+v"(a), "+v"(b));
}
__device__ __forceinline__ float fmx(float a, float b) { return __builtin_elementwise_maximum(a, b); }
__device__ __forceinline__ float xor32_max(float v) { float a = v, b = v; swap32(a, b); return fmx(a, b); }
__device__ __forceinline__ float xor32_sum(float v) { float a = v, b = v; swap32(a, b); return a + b; }
#define BAR() do { asm volatile("s_waitcnt lgkmcnt(0)" ::: "memory"); __builtin_amdgcn_s_barrier(); asm volatile("" ::: "memory"); } while (0)
#define BARL() do { asm volatile("" ::: "memory"); __builtin_amdgcn_s_barrier(); asm volatile("" ::: "memory"); } while (0)
#define WAIT_VM(n) asm volatile("s_waitcnt vmcnt(" #n ")" ::: "memory")
#define SB() __builtin_amdgcn_sched_barrier(0)

template <class T> __device__ __forceinline__ T lds_ld(const LAS unsigned char* base, int off) { return *(const LAS T*)(base + off); }
template <class T> __device__ __forceinline__ void lds_st(LAS unsigned char* base, int off, T v) { *(LAS T*)(base + off) = v; }

__device__ __forceinline__ void glds4k(const unsigned char* src, u32 voff, u32 dst) {
    u32 keep;
    asm volatile(
        "s_mov_b32 %0, m0\n\t"
        "s_mov_b32 m0, %3\n\t"
        "s_nop 0\n\t"
        "global_load_lds_dwordx4 %1, %2\n\t"
        "global_load_lds_dwordx4 %1, %2 offset:1024\n\t"
        "global_load_lds_dwordx4 %1, %2 offset:2048\n\t"
        "global_load_lds_dwordx4 %1, %2 offset:3072\n\t"
        "s_mov_b32 m0, %0"
        : "=&s"(keep) : "v"(voff), "s"(src), "s"(dst) : "memory");
}

__device__ __forceinline__ void glds1k(const unsigned char* src, u32 voff, u32 dst) {
    u32 keep;
    asm volatile(
        "s_mov_b32 %0, m0\n\t"
        "s_mov_b32 m0, %3\n\t"
        "s_nop 0\n\t"
        "global_load_lds_dwordx4 %1, %2\n\t"
        "s_mov_b32 m0, %0"
        : "=&s"(keep) : "v"(voff), "s"(src), "s"(dst) : "memory");
}
#define PIN(x) asm volatile("" : "+v"(x))

constexpr int PREP_XI = 64 * 8;
constexpr int NF_WQ = 16 * 96, NF_WO = 8 * 64, NF_WG = 16 * 224, NF_WD = 8 * 224;
constexpr int PREP_WI = NF_WQ + NF_WO + NF_WG + NF_WD;
constexpr int PREP_WAVES = PREP_XI + PREP_WI;
constexpr int PREP_IPW = 4;
constexpr int PREP_WG_TILE = PREP_WAVES / (8 * PREP_IPW);
constexpr int PREP_WG_CS = (1536 + 1792 + 1792) / 64;
constexpr int PREP_WG_FLAGS = 8;
constexpr int PREP_GRID = PREP_WG_TILE + PREP_WG_CS + PREP_WG_FLAGS;
constexpr int PREP_LDS_WAVE = 4608;
__host__ __device__ constexpr int qkv_col(int t) { return t == 0 ? 512 : (t == 1 ? 1024 : 0); }

struct WItem { const u16* src; size_t dst; };
__device__ __forceinline__ WItem w_item(const Args& a, int f, int lane) {
    const u16 *pA, *pB; int ld, ks; size_t dst;
    if (f < NF_WQ) {
        const int j = f / 96, rem = f % 96, q = rem >> 4, ksl = rem & 15, c = q >> 1, hf = q & 1;
        ks = 16 * hf + ksl; pA = a.wqkv + qkv_col(c) + 32 * j; pB = pA + 16; ld = 1536; dst = WS_WQ + (size_t)f * 1024;
    } else if ((f -= NF_WQ) < NF_WO) {
        const int cb = f >> 6, rem = f & 63, c = rem >> 4, r2 = rem & 15, kq = r2 >> 2, ks2 = (r2 >> 1) & 1, nt = r2 & 1;
        ks = 8 * kq + 2 * c + ks2; pA = a.wo + 64 * cb + 32 * nt; pB = pA + 16; ld = 512; dst = WS_WO + (size_t)f * 1024;
    } else if ((f -= NF_WO) < NF_WG) {
        const int j = f / 224, rem = f % 224, q = rem >> 4, ksl = rem & 15, c = q >> 1, hf = q & 1;
        ks = 16 * hf + ksl; pA = a.wg + 112 * j + 16 * c; pB = a.wu + 112 * j + 16 * c; ld = FF; dst = WS_WG + (size_t)f * 1024;
    } else {
        f -= NF_WG;
        const int cb = f / 224, rem = f % 224, c = rem >> 4, r2 = rem & 15, kq = r2 >> 2, ks2 = (r2 >> 1) & 1, nt = r2 & 1;
        ks = 28 * kq + 2 * c + ks2; pA = a.wd + 64 * cb + 32 * nt; pB = pA + 16; ld = 512; dst = WS_WD + (size_t)f * 1024;
    }
    const int k = lane >> 2, n8 = lane & 3;
    WItem it;
    it.src = (n8 < 2 ? pA + n8 * 8 : pB + (n8 - 2) * 8) + (size_t)(16 * ks + k) * ld;
    it.dst = dst;
    return it;
}

__global__ void __launch_bounds__(512) prep_kernel(Args a) {
    __shared__ __attribute__((aligned(16))) unsigned char sm[8 * PREP_LDS_WAVE];
    const int tid = threadIdx.x, lane = tid & 63, w = tid >> 6;
    const int b0 = blockIdx.x;
    const int bid = b0 < PREP_WG_CS + PREP_WG_FLAGS ? PREP_WG_TILE + b0 : b0 - (PREP_WG_CS + PREP_WG_FLAGS);
    unsigned char* ws = a.ws;
    if (bid < PREP_WG_TILE) {
        LAS unsigned char* t = (LAS unsigned char*)sm + w * PREP_LDS_WAVE;
        const int wv = bid * 8 + w;
        if (wv < PREP_XI / 1) {
            const int item = wv;
            const int tb = item >> 3, g = item & 7;
            const u16* src = a.x + (size_t)(tb * 32) * DM + g * 64;
            u32x4 v[4];
#pragma unroll
            for (int q = 0; q < 4; ++q) v[q] = *(const u32x4*)(src + (size_t)(8 * q + (lane >> 3)) * DM + (lane & 7) * 8);
#pragma unroll
            for (int q = 0; q < 4; ++q) lds_st<u32x4>(t, (8 * q + (lane >> 3)) * 144 + (lane & 7) * 16, v[q]);
            asm volatile("s_waitcnt lgkmcnt(0)" ::: "memory");
            const int h = lane >> 5, r = lane & 31;
#pragma unroll
            for (int kbl = 0; kbl < 4; ++kbl) {
                const u32x2 lo = lds_ld<u32x2>(t, r * 144 + kbl * 32 + 8 * h);
                const u32x2 hi = lds_ld<u32x2>(t, r * 144 + kbl * 32 + 16 + 8 * h);
                u32x4 o = {lo.x, lo.y, hi.x, hi.y};
                *(u32x4*)(ws + WS_XF0 + (size_t)(tb * 32 + 4 * g + kbl) * 1024 + lane * 16) = o;
            }
        } else {
            constexpr int NW = PREP_WG_TILE * 8 - PREP_XI;
            constexpr int NIT = (PREP_WI + NW - 1) / NW;
            const int f0 = wv - PREP_XI;
            WItem it[NIT]; u32x4 v[NIT];
#pragma unroll
            for (int q = 0; q < NIT; ++q) { const int f = f0 + q * NW; it[q] = w_item(a, f < PREP_WI ? f : f0, lane); v[q] = *(const u32x4*)it[q].src; }
            const int k = lane >> 2, n8 = lane & 3;
            const int g4 = lane >> 4, h = g4 >> 1, nh = g4 & 1, li = lane & 15, qq = li >> 2, p = li & 3;
#pragma unroll
            for (int q = 0; q < NIT; ++q) {
                if (f0 + q * NW < PREP_WI) {
                    lds_st<u32x4>(t, k * 64 + n8 * 16, v[q]);
                    asm volatile("s_waitcnt lgkmcnt(0)" ::: "memory");
                    const s16x4 lo = __builtin_amdgcn_ds_read_tr16_b64_v4i16((LAS s16x4*)(t + (4 * h + qq) * 64 + nh * 32 + p * 8));
                    const s16x4 hi = __builtin_amdgcn_ds_read_tr16_b64_v4i16((LAS s16x4*)(t + (8 + 4 * h + qq) * 64 + nh * 32 + p * 8));
                    const u32x2 l2 = __builtin_bit_cast(u32x2, lo), h2 = __builtin_bit_cast(u32x2, hi);
                    u32x4 o = {l2.x, l2.y, h2.x, h2.y};
                    *(u32x4*)(ws + it[q].dst + lane * 16) = o;
                    asm volatile("s_waitcnt lgkmcnt(0)" ::: "memory");
                }
            }
        }
    } else if (bid < PREP_WG_TILE + PREP_WG_CS) {
        const int g = bid - PREP_WG_TILE;
        const u16* src; int ld;
        if (g < 24) { src = a.wqkv + g * 64; ld = 1536; }
        else if (g < 52) { src = a.wg + (g - 24) * 64; ld = FF; }
        else { src = a.wu + (g - 52) * 64; ld = FF; }
        const int rg = lane >> 3, ch = lane & 7;
        u32x4 v[8];
#pragma unroll
        for (int it = 0; it < 8; ++it) v[it] = *(const u32x4*)(src + (size_t)(64 * w + 8 * it + rg) * ld + ch * 8);
        float s[8];
#pragma unroll
        for (int e = 0; e < 8; ++e) s[e] = 0.f;
#pragma unroll
        for (int it = 0; it < 8; ++it) {
            s[0] += bflo(v[it].x); s[1] += bfhi(v[it].x); s[2] += bflo(v[it].y); s[3] += bfhi(v[it].y);
            s[4] += bflo(v[it].z); s[5] += bfhi(v[it].z); s[6] += bflo(v[it].w); s[7] += bfhi(v[it].w);
        }
#pragma unroll
        for (int e = 0; e < 8; ++e) { s[e] += __shfl_xor(s[e], 8); s[e] += __shfl_xor(s[e], 16); s[e] += __shfl_xor(s[e], 32); }
        float* red = (float*)sm;
        if (lane < 8) {
#pragma unroll
            for (int e = 0; e < 8; ++e) red[w * 64 + lane * 8 + e] = s[e];
        }
        __syncthreads();
        if (tid < 64) {
            float t = 0.f;
#pragma unroll
            for (int ww = 0; ww < 8; ++ww) t += red[ww * 64 + tid];
            ((float*)(ws + WS_CS))[g * 64 + tid] = t;
        }
    } else {
        const int g = bid - PREP_WG_TILE - PREP_WG_CS;
        const u32x4 z = {0u, 0u, 0u, 0u};
        for (size_t o = (size_t)(g * 512 + tid) * 16; o < FLAG_BYTES; o += (size_t)PREP_WG_FLAGS * 512 * 16) *(u32x4*)(ws + WS_FLAGS + o) = z;
        if (g == 0 && tid == 0) *(u32*)(ws + WS_TMO) = 0u;
    }
}

#define STAMP(k) do { } while (0)
struct Ctx {
    LAS unsigned char* L;
    u32 Laddr;
    unsigned char* ws;
    __amdgpu_buffer_rsrc_t rs;
    int lane, w, i, j;
    u32 voff;
};

__device__ __forceinline__ u32x4 ld_sc1(const Ctx& c, u32 soff) { return __builtin_amdgcn_raw_buffer_load_b128(c.rs, c.voff, soff, 16); }
__device__ __forceinline__ void st_sc1(const Ctx& c, u32 soff, u32x4 v) { __builtin_amdgcn_raw_buffer_store_b128(v, c.rs, c.voff, soff, 16); }
__device__ __forceinline__ Ctx fresh(const Ctx& c0) {
    Ctx c = c0;
    asm volatile("" : "+s"(c.i), "+s"(c.j), "+s"(c.w));
    asm volatile("" : "+v"(c.lane));
    c.voff = c.lane * 16;
    return c;
}

constexpr int CQ(int t, int hf) { return 2 * t + hf; }
constexpr int CO(int k) { return 6 + k; }
constexpr int CG(int t, int hf) { return 10 + 2 * t + hf; }
constexpr int CD(int k) { return 24 + k; }
constexpr int slot_of(int n) {
    constexpr int tab[38] = {
          0, 1,   2, 3,   4, 5,
          6, 7, 2, 3,
          4, 5,   0, 1,   2, 3,   6, 7,   4, 5,   0, 1,   2, 3,
          6, 7, 4, 5, 0, 1, 2, 3,   6, 7, 4, 5, 0, 1};
    return tab[n];
}
template <int N> __device__ __forceinline__ const unsigned char* chunk_src(const Ctx& c) {
    size_t src;
    const int cb = c.j & 7;
    if (N < 6) src = WS_WQ + (size_t)(c.j * 6 + N) * SLOT;
    else if (N < 10) src = WS_WO + (size_t)(cb * 4 + (N - 6)) * SLOT;
    else if (N < 24) src = WS_WG + (size_t)(c.j * 14 + (N - 10)) * SLOT;
    else src = WS_WD + (size_t)(cb * 14 + (N - 24)) * SLOT;
    return c.ws + src + (c.w & 3) * 4096;
}
constexpr int chunk_lds(int n) { return slot_of(n) * SLOT; }
template <int N> __device__ __forceinline__ void issue_chunk(const Ctx& c) {
    glds4k(chunk_src<N>(c), c.voff, c.Laddr + chunk_lds(N) + (c.w & 3) * 4096);
}
__device__ __forceinline__ const LAS unsigned char* lds_base(const Ctx& c, int off) {
    u32 a = c.Laddr + off + c.voff;
    asm volatile("" : "+v"(a));
    return (const LAS unsigned char*)(size_t)a;
}

__device__ __forceinline__ u32 flag_off(int layer, int seam, int ci, int cj) {
    return (u32)(WS_FLAGS + (size_t)(((layer * 5 + seam) * 256) + ci * 16 + cj) * 128);
}
__device__ __forceinline__ void signal_group(const Ctx& c, int layer, int seam) {
    if (c.lane < 16) __builtin_amdgcn_raw_buffer_store_b32(1u, c.rs, flag_off(layer, seam, c.i, c.lane) + c.j * 4, 0, 16);
}
__device__ __forceinline__ void poll_flags(const Ctx& c, int layer, int seam, u32 mask) {
    const u32 off = flag_off(layer, seam, c.i, c.j) + (c.lane & 15) * 4;
    const bool need = (mask >> (c.lane & 15)) & 1u;
    for (u32 spins = 0;; ++spins) {
        asm volatile("" ::: "memory");
        const u32 f = __builtin_amdgcn_raw_buffer_load_b32(c.rs, off, 0, 16);
        if (__all(!need || f == 1u)) break;
        if (spins > SPIN_CAP) { if (c.lane == 0) __builtin_amdgcn_raw_buffer_store_b32(1u, c.rs, (u32)WS_TMO, 0, 16); break; }
        __builtin_amdgcn_s_sleep(1);
    }
}

__device__ __forceinline__ int crow(int r, int h) { return (r & 3) + 8 * (r >> 2) + 4 * h; }

#define LDF(p, k) (*(const LAS u32x4*)((p) + (k) * 1024))

template <bool SWAP, bool PRE, class F>
__device__ __forceinline__ void c_tile(f32x16& acc, u32x4 (&wf)[8], const LAS unsigned char* lo, const LAS unsigned char* hi, const LAS unsigned char* nxt,
                                       const u32x4 (&xf)[32], F&& filler) {
#pragma unroll
    for (int k = 0; k < 32; ++k) {
        const u32x4 wv = wf[k & 7];
        if (k + 8 < 16) wf[k & 7] = LDF(lo, k + 8);
        else if (k + 8 < 32) wf[k & 7] = LDF(hi, k + 8 - 16);
        else if (PRE) wf[k & 7] = LDF(nxt, k + 8 - 32);
        acc = SWAP ? mfma32(xf[k], wv, acc) : mfma32(wv, xf[k], acc);
        PIN(acc);
        filler(k);
        SB();
    }
}
template <bool PRE>
__device__ __forceinline__ f32x2 c_tile0(const Ctx& c, f32x16& acc, u32x4 (&wf)[8], const LAS unsigned char* lo, const LAS unsigned char* hi,
                                         const LAS unsigned char* nxt, u32x4 (&xf)[32], u32 xsoff) {
#pragma unroll
    for (int k = 0; k < 14; ++k) xf[k] = ld_sc1(c, xsoff + (u32)k * 1024);
    SB();
    float s1[4] = {0.f, 0.f, 0.f, 0.f}, s2[4] = {0.f, 0.f, 0.f, 0.f};
#pragma unroll
    for (int k = 0; k < 32; ++k) {
        const u32x4 wv = wf[k & 7];
        if (k + 14 < 32) xf[k + 14] = ld_sc1(c, xsoff + (u32)(k + 14) * 1024);
        if (k + 8 < 16) wf[k & 7] = LDF(lo, k + 8);
        else if (k + 8 < 32) wf[k & 7] = LDF(hi, k + 8 - 16);
        else if (PRE) wf[k & 7] = LDF(nxt, k + 8 - 32);
        acc = mfma32(wv, xf[k], acc);
        PIN(acc);
        s1[0] = dot2(xf[k].x, BF_ONES, s1[0]); s2[0] = dot2(xf[k].x, xf[k].x, s2[0]);
        s1[1] = dot2(xf[k].y, BF_ONES, s1[1]); s2[1] = dot2(xf[k].y, xf[k].y, s2[1]);
        s1[2] = dot2(xf[k].z, BF_ONES, s1[2]); s2[2] = dot2(xf[k].z, xf[k].z, s2[2]);
        s1[3] = dot2(xf[k].w, BF_ONES, s1[3]); s2[3] = dot2(xf[k].w, xf[k].w, s2[3]);
        asm volatile("" : "+v"(s1[0]), "+v"(s1[1]), "+v"(s1[2]), "+v"(s1[3]), "+v"(s2[0]), "+v"(s2[1]), "+v"(s2[2]), "+v"(s2[3]));
        SB();
    }
    const float a = xor32_sum((s1[0] + s1[1]) + (s1[2] + s1[3])), b = xor32_sum((s2[0] + s2[1]) + (s2[2] + s2[3]));
    const float mean = a * (1.0f / DM);
    float var = b * (1.0f / DM) - mean * mean;
    var = var > 0.f ? var : 0.f;
    f32x2 o; o.x = mean; o.y = __builtin_amdgcn_rsqf(var + LN_EPS);
    return o;
}

struct GS { float g0, g1, t0, t1, u0, u1; };
struct GK { float mean, A, B, ir2; };
__device__ __forceinline__ void geglu_sub(GS& s, u32& o, int q, float ag0, float ag1, float au0, float au1, float cg0, float cg1, float cu0, float cu1, const GK& k) {
    if (q == 0) asm volatile("v_fma_f32 %0, -%7, %5, %3\n\tv_fma_f32 %1, -%7, %6, %4\n\tv_mul_f32 %2, %0, %0"
                             : "=&v"(s.g0), "=&v"(s.g1), "=&v"(s.t0) : "v"(ag0), "v"(ag1), "v"(cg0), "v"(cg1), "v"(k.mean));
    if (q == 1) asm volatile("v_mul_f32 %1, %2, %2\n\tv_fma_f32 %0, %0, %4, %3\n\tv_fma_f32 %1, %1, %4, %3"
                             : "+v"(s.t0), "=&v"(s.t1) : "v"(s.g1), "v"(k.A), "v"(k.B));
    if (q == 2) asm volatile("v_mul_f32 %0, %2, %0\n\tv_mul_f32 %1, %3, %1\n\tv_exp_f32 %0, %0"
                             : "+v"(s.t0), "+v"(s.t1) : "v"(s.g0), "v"(s.g1));
    if (q == 3) asm volatile("v_exp_f32 %0, %0\n\tv_fma_f32 %1, -%7, %5, %3\n\tv_fma_f32 %2, -%7, %6, %4"
                             : "+v"(s.t1), "=&v"(s.u0), "=&v"(s.u1) : "v"(au0), "v"(au1), "v"(cu0), "v"(cu1), "v"(k.mean));
    if (q == 4) asm volatile("v_fma_f32 %0, %0, %4, %4\n\tv_mul_f32 %2, %2, %3\n\tv_fma_f32 %1, %1, %4, %4"
                             : "+v"(s.t0), "+v"(s.t1), "+v"(s.g0) : "v"(s.u0), "v"(k.ir2));
    if (q == 5) asm volatile("v_rcp_f32 %0, %0\n\tv_mul_f32 %2, %2, %3\n\tv_rcp_f32 %1, %1"
                             : "+v"(s.t0), "+v"(s.t1), "+v"(s.g1) : "v"(s.u1));
    if (q == 6) asm volatile("v_mul_f32 %1, %1, %3\n\tv_mul_f32 %2, %2, %4\n\tv_cvt_pk_bf16_f32 %0, %1, %2"
                             : "=&v"(o), "+v"(s.g0), "+v"(s.g1) : "v"(s.t0), "v"(s.t1));
}
struct LS { float t0, t1; };
__device__ __forceinline__ void lnq_a(LS& s, float a0, float c0, float c1, float sc, float msc) {
    asm volatile("v_mul_f32 %0, %6, %3\n\tv_mul_f32 %1, %6, %4\n\tv_fma_f32 %0, %2, %5, -%0"
                 : "=&v"(s.t0), "=&v"(s.t1) : "v"(a0), "v"(c0), "v"(c1), "v"(sc), "v"(msc));
}
__device__ __forceinline__ u32 lnq_b(LS& s, float a1, float sc) {
    u32 o;
    asm volatile("v_fma_f32 %1, %3, %4, -%1\n\tv_cvt_pk_bf16_f32 %0, %2, %1" : "=v"(o), "+v"(s.t1) : "v"(s.t0), "v"(a1), "v"(sc));
    return o;
}
__device__ __forceinline__ void lnv_a(LS& s, float a0, float a1, float m0, float m1, float r0, float cs) {
    asm volatile("v_fma_f32 %0, -%4, %7, %2\n\tv_fma_f32 %1, -%5, %7, %3\n\tv_mul_f32 %0, %0, %6"
                 : "=&v"(s.t0), "=&v"(s.t1) : "v"(a0), "v"(a1), "v"(m0), "v"(m1), "v"(r0), "v"(cs));
}
__device__ __forceinline__ u32 lnv_b(LS& s, float r1) {
    u32 o;
    asm volatile("v_mul_f32 %1, %1, %3\n\tv_cvt_pk_bf16_f32 %0, %2, %1" : "=v"(o), "+v"(s.t1) : "v"(s.t0), "v"(r1));
    return o;
}
__device__ __forceinline__ u32x4 pack8(const float* v) { return u32x4{pack2(v[0], v[1]), pack2(v[2], v[3]), pack2(v[4], v[5]), pack2(v[6], v[7])}; }

__device__ __forceinline__ float max16(const f32x16& s, float m) {
    float a = fmx(fmx(s[0], s[1]), m), b = fmx(s[2], s[3]);
    a = fmx(fmx(s[4], s[5]), a); b = fmx(fmx(s[6], s[7]), b);
    a = fmx(fmx(s[8], s[9]), a); b = fmx(fmx(s[10], s[11]), b);
    a = fmx(fmx(s[12], s[13]), a); b = fmx(fmx(s[14], s[15]), b);
    return fmx(a, b);
}
__device__ __forceinline__ float smul(float a, float b) { float r; asm("v_mul_f32 %0, %1, %2" : "=v"(r) : "v"(a), "v"(b)); return r; }
struct MaskF { u32x4 alo, ahi, ilo, ihi; };
__device__ __forceinline__ f32x16 add_mask(const MaskF& mf, f32x16 s) { s = mfma32(mf.alo, mf.ilo, s); return mfma32(mf.ahi, mf.ihi, s); }
template <int NB, int FIRST, int REST0, int MASK, bool FIRSTSEG>
__device__ __forceinline__ void att_segment(f32x16& oacc, float& m, const LAS unsigned char* kb, const LAS unsigned char* vb, const LAS unsigned char* mk, const u32x4& qf) {
    u32x4 kf[NB];
#pragma unroll
    for (int t = 0; t < NB; ++t) { const int b = t == 0 ? FIRST : REST0 + t - 1; kf[t] = *(const LAS u32x4*)(kb + b * 2048); }
    MaskF mf;
    if (MASK) {
        mf.alo = LDF(mk, 0); mf.ahi = LDF(mk, 1); mf.ilo = LDF(mk, 2); mf.ihi = LDF(mk, 3);
        if (MASK == 2) { mf.alo ^= 0xff7fff7fu; mf.ahi ^= 0xff7fff7fu; }
    }
    SB();
    float mx = FIRSTSEG ? -__builtin_inff() : m;
    f32x16 sv[4];
    sv[0] = mfma32(kf[0], qf, f32x16{});
    if (NB > 1) sv[1] = mfma32(kf[1], qf, f32x16{});
    if (MASK) sv[0] = add_mask(mf, sv[0]);
    SB();
#pragma unroll
    for (int t = 0; t < NB; ++t) {
        const int u = NB > 1 ? (t == 0 ? 1 : t == 1 ? 0 : t) : 0;
        if (t + 2 < NB) sv[(t + 2) & 3] = mfma32(kf[t + 2], qf, f32x16{});
        mx = max16(sv[u & 3], mx);
        SB();
    }
    mx = xor32_max(mx);
    f32x16 negm;
#pragma unroll
    for (int e = 0; e < 16; ++e) negm[e] = -mx;
    SB();
    sv[0] = mfma32(kf[0], qf, negm);
    if (MASK) sv[0] = add_mask(mf, sv[0]);
    u32x4 v0 = *(const LAS u32x4*)(vb + (FIRST * 2 + 0) * VREC);
    u32x4 v1 = *(const LAS u32x4*)(vb + (FIRST * 2 + 1) * VREC);
    if (NB > 1) sv[1] = mfma32(kf[1], qf, negm);
    if (!FIRSTSEG) {
        float f = __builtin_amdgcn_exp2f(m - mx);
        asm volatile("s_nop 1" : "+v"(f));
#pragma unroll
        for (int e = 0; e < 16; ++e) oacc[e] = smul(oacc[e], f);
    }
    m = mx;
    SB();
#pragma unroll
    for (int t = 0; t < NB; ++t) {
        u32x4 n0, n1;
        if (t + 1 < NB) {
            if (t > 0) sv[(t + 1) & 1] = mfma32(kf[t + 1], qf, negm);
            n0 = *(const LAS u32x4*)(vb + ((REST0 + t) * 2 + 0) * VREC);
            n1 = *(const LAS u32x4*)(vb + ((REST0 + t) * 2 + 1) * VREC);
        }
        SB();
        const f32x16& s = sv[t & 1];
        u32 p[8];
#pragma unroll
        for (int e = 0; e < 4; ++e) p[e] = pack2(__builtin_amdgcn_exp2f(s[2 * e]), __builtin_amdgcn_exp2f(s[2 * e + 1]));
        SB();
        oacc = mfma32(v0, u32x4{p[0], p[1], p[2], p[3]}, oacc);
#pragma unroll
        for (int e = 4; e < 8; ++e) p[e] = pack2(__builtin_amdgcn_exp2f(s[2 * e]), __builtin_amdgcn_exp2f(s[2 * e + 1]));
        SB();
        oacc = mfma32(v1, u32x4{p[4], p[5], p[6], p[7]}, oacc);
        if (t + 1 < NB) { v0 = n0; v1 = n1; }
        SB();
    }
}

__device__ __forceinline__ void phase_qkv_att(const Ctx& c0, int layer) {
    const Ctx c = fresh(c0);
    const int lane = c.lane, w = c.w, tb = w & 3, h = lane >> 5, r = lane & 31;
    const u32 lbase = (u32)(WS_L0 + (size_t)layer * WS_LSTRIDE);
    const u32 xsrc = layer == 0 ? (u32)WS_XF0 : (u32)(WS_L0 + L_X2);
    STAMP(0);
    if (w < 4) {
        if (layer > 0 && w == 0) poll_flags(c, layer - 1, 0, 0xffffu);
        BAR();
        STAMP(1);
        u32x4 xf[32], wf[8];
        const LAS unsigned char* wK = lds_base(c, slot_of(CQ(0, 0)) * SLOT);
        const LAS unsigned char* wV = lds_base(c, slot_of(CQ(1, 0)) * SLOT);
        const LAS unsigned char* wQ = lds_base(c, slot_of(CQ(2, 0)) * SLOT);
#pragma unroll
        for (int k = 0; k < 8; ++k) wf[k] = LDF(wK, k);
        f32x16 accK = {}, accV = {}, accQ = {};
        const f32x2 ms = c_tile0<true>(c, accK, wf, wK, wK + SLOT, wV, xf, xsrc + (u32)((4 * c.i + tb) * 32) * 1024);
        if (lane < 32) lds_st<f32x2>(c.L, SC_ST + (tb * 32 + r) * 8, ms);
        const float msk = ms.x * ms.y;
        f32x4 cs[4];
#pragma unroll
        for (int q = 0; q < 4; ++q) cs[q] = lds_ld<f32x4>(c.L, SC_CS + (0 * 32 + 8 * q + 4 * h) * 4);
        BARL(); STAMP(2);
        f32x4 mr[8];
        float csv;
        {
            u32 o[8]; LS ls;
            c_tile<true, true>(accV, wf, wV, wV + SLOT, wQ, xf, [&](int k) {
                if (k >= 2 && k < 18) {
                    const int pi = (k - 2) >> 1, e = 2 * pi;
                    if (((k - 2) & 1) == 0) lnq_a(ls, accK[e], cs[e >> 2][e & 3], cs[e >> 2][(e & 3) + 1], ms.y, msk);
                    else o[pi] = lnq_b(ls, accK[e + 1], ms.y);
                }
                if (k == 18 || k == 20) {
                    const int hh = (k - 18) >> 1;
                    const u32x4 ov = {o[4 * hh], o[4 * hh + 1], o[4 * hh + 2], o[4 * hh + 3]};
                    lds_st<u32x4>(c.L, SC_KS + (tb * 2 + hh) * 1024 + c.voff, ov);
                    st_sc1(c, lbase + (u32)L_KF + (u32)((4 * c.i + tb) * 32 + 2 * c.j + hh) * 1024, ov);
                }
                if (k >= 22 && k < 30) { const int q = (k - 22) >> 1; mr[k - 22] = lds_ld<f32x4>(c.L, SC_ST + (tb * 32 + 8 * q + 4 * h + 2 * ((k - 22) & 1)) * 8); }
                if (k == 30) csv = lds_ld<float>(c.L, SC_CS + (32 + r) * 4);
            });
        }
        BARL();
        {
            u32 o[8]; LS ls;
            c_tile<false, false>(accQ, wf, wQ, wQ + SLOT, wQ, xf, [&](int k) {
                if (k >= 2 && k < 18) {
                    const int pi = (k - 2) >> 1;
                    if (((k - 2) & 1) == 0) lnv_a(ls, accV[2 * pi], accV[2 * pi + 1], mr[pi].x, mr[pi].z, mr[pi].y, csv);
                    else o[pi] = lnv_b(ls, mr[pi].w);
                }
                if (k == 18 || k == 20) {
                    const int sv = (k - 18) >> 1;
                    const u32x4 ov = {o[4 * sv], o[4 * sv + 1], o[4 * sv + 2], o[4 * sv + 3]};
                    lds_st<u32x4>(c.L, SC_VS + (tb * 2 + sv) * VREC + c.voff, ov);
                    st_sc1(c, lbase + (u32)L_VF + (u32)(((4 * c.i + tb) * 16 + c.j) * 2 + sv) * 1024, ov);
                }
                if (k >= 24 && k < 28) cs[k - 24] = lds_ld<f32x4>(c.L, SC_CS + (64 + 8 * (k - 24) + 4 * h) * 4);
            });
            if (lane < 2) { u32x4 ones = {BF_ONES, BF_ONES, BF_ONES, BF_ONES}; lds_st<u32x4>(c.L, SC_VS + (tb * 2 + lane) * VREC + 1024, ones); }
        }
        {
            const float sc = ms.y * QSCALE, msc = ms.x * sc;
            asm volatile("s_nop 15" : "+v"(accQ));
            u32 o[8]; LS ls[8];
#pragma unroll
            for (int pi = 0; pi < 8; ++pi) { const int e = 2 * pi; lnq_a(ls[pi], accQ[e], cs[e >> 2][e & 3], cs[e >> 2][(e & 3) + 1], sc, msc); }
#pragma unroll
            for (int pi = 0; pi < 8; ++pi) o[pi] = lnq_b(ls[pi], accQ[2 * pi + 1], sc);
#pragma unroll
            for (int hh = 0; hh < 2; ++hh) lds_st<u32x4>(c.L, SC_QS + (tb * 2 + hh) * 1024 + c.voff, u32x4{o[4 * hh], o[4 * hh + 1], o[4 * hh + 2], o[4 * hh + 3]});
        }
        WAIT_VM(0);
        BAR(); STAMP(5);
        if (w == 0 && lane < 2 && c.i + 1 + lane < 16) __builtin_amdgcn_raw_buffer_store_b32(1u, c.rs, flag_off(layer, 1, c.i + 1 + lane, c.j) + lane * 4, 0, 16);
    } else {
        WAIT_VM(16);
        BAR();
        WAIT_VM(0);
        BAR();
        BAR();
        issue_chunk<CO(2)>(c); issue_chunk<CO(3)>(c);
        BAR();
        issue_chunk<CG(0, 0)>(c); issue_chunk<CG(0, 1)>(c);
    }
    const int qb = w < 4 ? w : 7 - w, hh = w >> 2;
    const u32x4 qf = lds_ld<u32x4>(c.L, SC_QS + (qb * 2 + hh) * 1024 + c.voff);
    const int vlane = (((lane >> 4) & 1) == hh) ? lane * 16 : 1024;
    f32x16 oacc = {};
    float m = 0.f;
    const LAS unsigned char* mk = lds_base(c, SC_MK);
    {
        const LAS unsigned char* kb = c.L + SC_KS + hh * 1024 + c.voff;
        const LAS unsigned char* vb = c.L + SC_VS + vlane;
        if (qb == 0) att_segment<1, 0, 0, 1, true>(oacc, m, kb, vb, mk, qf);
        else if (qb == 1) att_segment<2, 1, 0, 1, true>(oacc, m, kb, vb, mk, qf);
        else if (qb == 2) att_segment<3, 2, 0, 1, true>(oacc, m, kb, vb, mk, qf);
        else att_segment<4, 3, 0, 1, true>(oacc, m, kb, vb, mk, qf);
    }
    STAMP(6);
    if (c.i > 0) {
        if (w == 0) poll_flags(c, layer, 1, c.i >= 2 ? 3u : 1u);
        BAR();
        STAMP(7);
        {
            const int idx = w;
            const int g = 4 * c.i - 8 + idx;
            if (g >= 0) {
                const u32x4 s0 = ld_sc1(c, lbase + (u32)L_KF + (u32)(g * 32 + 2 * c.j + 0) * 1024);
                const u32x4 s1 = ld_sc1(c, lbase + (u32)L_KF + (u32)(g * 32 + 2 * c.j + 1) * 1024);
                const u32x4 s2 = ld_sc1(c, lbase + (u32)L_VF + (u32)((g * 16 + c.j) * 2 + 0) * 1024);
                const u32x4 s3 = ld_sc1(c, lbase + (u32)L_VF + (u32)((g * 16 + c.j) * 2 + 1) * 1024);
                lds_st<u32x4>(c.L, SC_RK + (idx * 2 + 0) * 1024 + c.voff, s0);
                lds_st<u32x4>(c.L, SC_RK + (idx * 2 + 1) * 1024 + c.voff, s1);
                lds_st<u32x4>(c.L, SC_RV + (idx * 2 + 0) * VREC + c.voff, s2);
                lds_st<u32x4>(c.L, SC_RV + (idx * 2 + 1) * VREC + c.voff, s3);
                if (lane < 2) { u32x4 ones = {BF_ONES, BF_ONES, BF_ONES, BF_ONES}; lds_st<u32x4>(c.L, SC_RV + (idx * 2 + lane) * VREC + 1024, ones); }
            }
        }
        BAR();
        STAMP(8);
        const LAS unsigned char* kb = c.L + SC_RK + hh * 1024 + c.voff;
        const LAS unsigned char* vb = c.L + SC_RV + vlane;
        if (c.i == 1) att_segment<4, 4, 5, 0, false>(oacc, m, kb, vb, mk, qf);
        else if (qb == 0) att_segment<8, 0, 1, 2, false>(oacc, m, kb, vb, mk, qf);
        else if (qb == 1) att_segment<7, 1, 2, 2, false>(oacc, m, kb, vb, mk, qf);
        else if (qb == 2) att_segment<6, 2, 3, 2, false>(oacc, m, kb, vb, mk, qf);
        else att_segment<5, 3, 4, 2, false>(oacc, m, kb, vb, mk, qf);
    }
    {
        u32x4 o;
        if (hh == 0) {
            float il = __builtin_amdgcn_rcpf(oacc[8]);
            asm volatile("s_nop 1" : "+v"(il));
            o = u32x4{pack2(smul(oacc[0], il), smul(oacc[1], il)), pack2(smul(oacc[2], il), smul(oacc[3], il)), pack2(smul(oacc[4], il), smul(oacc[5], il)), pack2(smul(oacc[6], il), smul(oacc[7], il))};
        } else {
            float il = __builtin_amdgcn_rcpf(oacc[0]);
            asm volatile("s_nop 1" : "+v"(il));
            o = u32x4{pack2(smul(oacc[8], il), smul(oacc[9], il)), pack2(smul(oacc[10], il), smul(oacc[11], il)), pack2(smul(oacc[12], il), smul(oacc[13], il)), pack2(smul(oacc[14], il), smul(oacc[15], il))};
        }
        st_sc1(c, lbase + (u32)L_OF + (u32)((4 * c.i + qb) * 32 + 2 * c.j + hh) * 1024, o);
    }
    STAMP(9);
    WAIT_VM(0);
    BAR();
    if (w == 0) signal_group(c, layer, 2);
    if (w >= 4) { issue_chunk<CG(1, 0)>(c); issue_chunk<CG(1, 1)>(c); }
    STAMP(10);
}

__device__ __forceinline__ void reduce4(const Ctx& c, const f32x16& acc0, const f32x16& acc1, int t2, int kq, float (&v)[8]) {
#pragma unroll
    for (int d = 0; d < 4; ++d) {
        if (d != kq) {
            const int slot = kq < d ? kq : kq - 1;
            const int base = red_off(t2 * 4 + d) + slot * 2048 + c.voff;
            const f32x16& a = d < 2 ? acc0 : acc1;
            const int o = (d & 1) * 8;
            lds_st<f32x4>(c.L, base, f32x4{a[o], a[o + 1], a[o + 2], a[o + 3]});
            lds_st<f32x4>(c.L, base + 1024, f32x4{a[o + 4], a[o + 5], a[o + 6], a[o + 7]});
        }
    }
    if (kq == 0) {
#pragma unroll
        for (int e = 0; e < 8; ++e) v[e] = acc0[e];
    } else if (kq == 1) {
#pragma unroll
        for (int e = 0; e < 8; ++e) v[e] = acc0[8 + e];
    } else if (kq == 2) {
#pragma unroll
        for (int e = 0; e < 8; ++e) v[e] = acc1[e];
    } else {
#pragma unroll
        for (int e = 0; e < 8; ++e) v[e] = acc1[8 + e];
    }
    BAR();
#pragma unroll
    for (int s = 0; s < 3; ++s) {
        const int base = red_off(t2 * 4 + kq) + s * 2048 + c.voff;
        const f32x4 lo = lds_ld<f32x4>(c.L, base), hi = lds_ld<f32x4>(c.L, base + 1024);
        v[0] += lo.x; v[1] += lo.y; v[2] += lo.z; v[3] += lo.w; v[4] += hi.x; v[5] += hi.y; v[6] += hi.z; v[7] += hi.w;
    }
}
__device__ __forceinline__ u32x4 add_res(const float (&v)[8], const u32x4& res) {
    return u32x4{pack2(v[0] + bflo(res.x), v[1] + bfhi(res.x)), pack2(v[2] + bflo(res.y), v[3] + bfhi(res.y)),
                 pack2(v[4] + bflo(res.z), v[5] + bfhi(res.z)), pack2(v[6] + bflo(res.w), v[7] + bfhi(res.w))};
}

__device__ __forceinline__ void phase_wo(const Ctx& c0, int layer) {
    const Ctx c = fresh(c0);
    const int w = c.w, t2 = w & 1, kq = w >> 1;
    const int th = c.j >> 3, cb = c.j & 7;
    const int tbk = 4 * c.i + 2 * th + t2;
    const u32 lbase = (u32)(WS_L0 + (size_t)layer * WS_LSTRIDE);
    const int kbo = 4 * cb + kq;
    const u32 rsrc_off = layer == 0 ? (u32)WS_XF0 : (u32)(WS_L0 + L_X2);
    const u32x4 res = ld_sc1(c, rsrc_off + (u32)(tbk * 32 + kbo) * 1024);
    if (w == 0) poll_flags(c, layer, 2, 0xffffu);
    if (w >= 4) WAIT_VM(9);
    BAR();
    STAMP(11);
    u32x4 xf[8];
#pragma unroll
    for (int s = 0; s < 8; ++s) xf[s] = ld_sc1(c, lbase + (u32)L_OF + (u32)(tbk * 32 + 8 * kq + s) * 1024);
    u32x4 wf[16];
#pragma unroll
    for (int ch = 0; ch < 4; ++ch) {
        const LAS unsigned char* wb = lds_base(c, slot_of(CO(ch)) * SLOT + kq * 4096);
#pragma unroll
        for (int q = 0; q < 4; ++q) wf[4 * ch + q] = LDF(wb, q);
    }
    SB();
    f32x16 acc0 = {}, acc1 = {};
#pragma unroll
    for (int s = 0; s < 8; ++s) {
        acc0 = mfma32(wf[2 * s], xf[s], acc0);
        acc1 = mfma32(wf[2 * s + 1], xf[s], acc1);
        SB();
    }
    BAR();
    STAMP(12);
    float v[8];
    reduce4(c, acc0, acc1, t2, kq, v);
    st_sc1(c, lbase + (u32)L_X1 + (u32)(tbk * 32 + kbo) * 1024, add_res(v, res));
    STAMP(13);
    WAIT_VM(0);
    BAR();
    if (w == 0) signal_group(c, layer, 3);
    STAMP(14);
}

template <int T> __device__ __forceinline__ void gu_tile(const Ctx& c, f32x16& acc, const f32x16& prev, u32x4 (&wf)[8], const u32x4 (&xf)[32], int h,
                                                         f32x4 (&cs)[4], const GK& gk, u32 hbase) {
    const LAS unsigned char* cur = lds_base(c, slot_of(CG(T, 0)) * SLOT);
    const LAS unsigned char* nxt = lds_base(c, slot_of(CG(T < 6 ? T + 1 : T, 0)) * SLOT);
    GS g;
    u32 o[4];
    acc = f32x16{};
    SB();
    c_tile<false, (T < 6)>(acc, wf, cur, cur + SLOT, nxt, xf, [&](int k) {
        const int p = k >> 3, e = 2 * p, q2 = p >> 1, e2 = e & 3;
        if ((k & 7) < 7) geglu_sub(g, o[p], k & 7, prev[e], prev[e + 1], prev[8 + e], prev[8 + e + 1], cs[q2][e2], cs[q2][e2 + 1], cs[2 + q2][e2], cs[2 + q2][e2 + 1], gk);
        if (k == 15) cs[0] = lds_ld<f32x4>(c.L, SC_CS + (96 + T * 32 + 4 * h) * 4);
        if (k == 15) cs[2] = lds_ld<f32x4>(c.L, SC_CS + (96 + T * 32 + 16 + 4 * h) * 4);
        if (k == 31) cs[1] = lds_ld<f32x4>(c.L, SC_CS + (96 + T * 32 + 8 + 4 * h) * 4);
        if (k == 31) cs[3] = lds_ld<f32x4>(c.L, SC_CS + (96 + T * 32 + 16 + 8 + 4 * h) * 4);
    });
    st_sc1(c, hbase + (u32)(T - 1) * 1024, u32x4{o[0], o[1], o[2], o[3]});
    if (T < 6) BARL();
}

__device__ __forceinline__ void phase_gu(const Ctx& c0, int layer) {
    const Ctx c = fresh(c0);
    const int lane = c.lane, w = c.w, tb = w & 3, h = lane >> 5;
    const u32 lbase = (u32)(WS_L0 + (size_t)layer * WS_LSTRIDE);
    if (w < 4) {
        if (w == 0) poll_flags(c, layer, 3, 0xffffu);
        BAR();
        STAMP(15);
        u32x4 xf[32], wf[8];
        const LAS unsigned char* w0 = lds_base(c, slot_of(CG(0, 0)) * SLOT);
        const LAS unsigned char* w1 = lds_base(c, slot_of(CG(1, 0)) * SLOT);
#pragma unroll
        for (int k = 0; k < 8; ++k) wf[k] = LDF(w0, k);
        f32x16 a0 = {}, a1;
        const f32x2 ms = c_tile0<true>(c, a0, wf, w0, w0 + SLOT, w1, xf, lbase + (u32)L_X1 + (u32)((4 * c.i + tb) * 32) * 1024);
        constexpr float GC1 = -2.0f * 0.7978845608028654f * 1.4426950408889634f, GC2 = GC1 * 0.044715f;
        GK gk; gk.mean = ms.x; gk.A = ms.y * GC1; gk.B = ms.y * ms.y * ms.y * GC2; gk.ir2 = 1.0f / (ms.y * ms.y);
        f32x4 cs[4];
#pragma unroll
        for (int q = 0; q < 4; ++q) cs[q] = lds_ld<f32x4>(c.L, SC_CS + (96 + (q >> 1) * 16 + 8 * (q & 1) + 4 * h) * 4);
        BARL(); STAMP(16);
        const u32 hbase = lbase + (u32)L_HF + (u32)((4 * c.i + tb) * 112 + 7 * c.j) * 1024;
        gu_tile<1>(c, a1, a0, wf, xf, h, cs, gk, hbase);
        gu_tile<2>(c, a0, a1, wf, xf, h, cs, gk, hbase);
        gu_tile<3>(c, a1, a0, wf, xf, h, cs, gk, hbase);
        gu_tile<4>(c, a0, a1, wf, xf, h, cs, gk, hbase);
        gu_tile<5>(c, a1, a0, wf, xf, h, cs, gk, hbase);
        gu_tile<6>(c, a0, a1, wf, xf, h, cs, gk, hbase);
        STAMP(22);
        {
            asm volatile("s_nop 15" : "+v"(a0));
            GS g[4];
            u32 o[4];
#pragma unroll
            for (int q = 0; q < 7; ++q)
#pragma unroll
                for (int p = 0; p < 4; ++p) {
                    const int e = 2 * p, q2 = p >> 1, e2 = e & 3;
                    geglu_sub(g[p], o[p], q, a0[e], a0[e + 1], a0[8 + e], a0[8 + e + 1], cs[q2][e2], cs[q2][e2 + 1], cs[2 + q2][e2], cs[2 + q2][e2 + 1], gk);
                }
            st_sc1(c, hbase + 6u * 1024, u32x4{o[0], o[1], o[2], o[3]});
        }
        STAMP(21);
        WAIT_VM(0);
        BAR();
        STAMP(23);
        if (w == 0) signal_group(c, layer, 4);
    } else {
        WAIT_VM(0);
        BAR();
        __builtin_amdgcn_s_sleep(16);
        issue_chunk<CG(2, 0)>(c); issue_chunk<CG(2, 1)>(c); issue_chunk<CG(3, 0)>(c); issue_chunk<CG(3, 1)>(c);
        WAIT_VM(8);
        BAR();
        issue_chunk<CG(4, 0)>(c); issue_chunk<CG(4, 1)>(c);
        WAIT_VM(8);
        BAR();
        issue_chunk<CG(5, 0)>(c); issue_chunk<CG(5, 1)>(c);
        WAIT_VM(8);
        BAR();
        issue_chunk<CG(6, 0)>(c); issue_chunk<CG(6, 1)>(c);
        WAIT_VM(8);
        BAR();
        issue_chunk<CD(0)>(c); issue_chunk<CD(1)>(c);
        WAIT_VM(8);
        BAR();
        issue_chunk<CD(2)>(c); issue_chunk<CD(3)>(c);
        BAR();
        issue_chunk<CD(4)>(c); issue_chunk<CD(5)>(c);
        BAR();
        issue_chunk<CD(6)>(c); issue_chunk<CD(7)>(c);
    }
}

__device__ __forceinline__ void phase_dn(const Ctx& c0, int layer, const Args& a) {
    const Ctx c = fresh(c0);
    const int lane = c.lane, w = c.w, t2 = w & 1, kq = w >> 1, h = lane >> 5, r = lane & 31;
    const int th = c.j >> 3, cb = c.j & 7;
    const int tbk = 4 * c.i + 2 * th + t2;
    const u32 lbase = (u32)(WS_L0 + (size_t)layer * WS_LSTRIDE);
    const int kbo = 4 * cb + kq;
    const u32 xsrc = lbase + (u32)L_HF + (u32)(tbk * 112 + 28 * kq) * 1024;
    if (w >= 4) WAIT_VM(8);
    BAR();
    u32x4 wr[24];
#pragma unroll
    for (int ch = 0; ch < 6; ++ch) {
        const LAS unsigned char* wb = lds_base(c, chunk_lds(CD(ch)) + kq * 4096);
#pragma unroll
        for (int q = 0; q < 4; ++q) wr[4 * ch + q] = LDF(wb, q);
    }
    BAR();
    if (w >= 4) {
        issue_chunk<CD(8)>(c); issue_chunk<CD(9)>(c); issue_chunk<CD(10)>(c);
        issue_chunk<CD(11)>(c); issue_chunk<CD(12)>(c); issue_chunk<CD(13)>(c);
    }
    const u32x4 res = ld_sc1(c, lbase + (u32)L_X1 + (u32)(tbk * 32 + kbo) * 1024);
    if (w == 0) poll_flags(c, layer, 4, 0xffffu);
    BAR();
    STAMP(25);
    constexpr int XD = 10;
    u32x4 xf[28];
#pragma unroll
    for (int s = 0; s < XD; ++s) xf[s] = ld_sc1(c, xsrc + (u32)s * 1024);
    SB();
    f32x16 acc0 = {}, acc1 = {};
#pragma unroll
    for (int s = 0; s < 12; ++s) {
        xf[s + XD] = ld_sc1(c, xsrc + (u32)(s + XD) * 1024);
        acc0 = mfma32(wr[2 * s], xf[s], acc0);
        acc1 = mfma32(wr[2 * s + 1], xf[s], acc1);
        PIN(acc0); PIN(acc1);
        SB();
    }
    WAIT_VM(22);
    BARL();
    STAMP(24);
    {
        const LAS unsigned char* wb[8];
#pragma unroll
        for (int ch = 0; ch < 8; ++ch) wb[ch] = lds_base(c, chunk_lds(CD(6 + ch)) + kq * 4096);
        u32x4 wf[8];
#pragma unroll
        for (int q = 0; q < 8; ++q) wf[q] = LDF(wb[q >> 2], q & 3);
        SB();
#pragma unroll
        for (int s = 0; s < 16; ++s) {
            const u32x4 w0 = wf[(2 * s) & 7], w1 = wf[(2 * s + 1) & 7];
            if (12 + s + XD < 28) xf[12 + s + XD] = ld_sc1(c, xsrc + (u32)(12 + s + XD) * 1024);
            if (2 * s + 8 < 32) {
                wf[(2 * s) & 7] = LDF(wb[(2 * s + 8) >> 2], (2 * s + 8) & 3);
                wf[(2 * s + 1) & 7] = LDF(wb[(2 * s + 9) >> 2], (2 * s + 9) & 3);
            }
            acc0 = mfma32(w0, xf[12 + s], acc0);
            acc1 = mfma32(w1, xf[12 + s], acc1);
            PIN(acc0); PIN(acc1);
            SB();
        }
    }
    BAR();
    STAMP(26);
    float v[8];
    reduce4(c, acc0, acc1, t2, kq, v);
    const u32x4 o = add_res(v, res);
    if (layer == 1) {
        u16* dst = a.out + (size_t)(tbk * 32 + r) * DM + kbo * 16 + 4 * h;
        *(u32x2*)dst = u32x2{o.x, o.y};
        *(u32x2*)(dst + 8) = u32x2{o.z, o.w};
        STAMP(27);
    } else {
        st_sc1(c, lbase + (u32)L_X2 + (u32)(tbk * 32 + kbo) * 1024, o);
        STAMP(27);
        WAIT_VM(0);
        BAR();
        if (w == 0) signal_group(c, layer, 0);
        if (w >= 4) {
            issue_chunk<CQ(0, 0)>(c); issue_chunk<CQ(0, 1)>(c); issue_chunk<CQ(1, 0)>(c); issue_chunk<CQ(1, 1)>(c);
            issue_chunk<CQ(2, 0)>(c); issue_chunk<CQ(2, 1)>(c); issue_chunk<CO(0)>(c); issue_chunk<CO(1)>(c);
        }
        STAMP(28);
    }
}

__global__ void __launch_bounds__(512, 2) mega_kernel(Args a) {
    extern __shared__ __attribute__((aligned(16))) unsigned char lds[];
    Ctx c;
    c.L = (LAS unsigned char*)lds;
    c.Laddr = (u32)(size_t)c.L;
    c.ws = a.ws;
    c.rs = __builtin_amdgcn_make_buffer_rsrc((void*)a.ws, 0, (int)WS_END, 0x00020000);
    const int tid = threadIdx.x;
    c.lane = tid & 63;
    c.w = __builtin_amdgcn_readfirstlane(tid >> 6);
    const int bid = blockIdx.x;
    c.i = 2 * (bid & 7) + (bid >> 7);
    c.j = (bid >> 3) & 15;
    c.voff = c.lane * 16;
    if (c.w < 4) {
        {
            const int hh = c.lane >> 5, row = c.lane & 31, half = c.w & 1;
            u32 d[4];
#pragma unroll
            for (int q = 0; q < 4; ++q) {
                const int c0 = 16 * half + 8 * (q >> 1) + 4 * hh + 2 * (q & 1), c1 = c0 + 1;
                const u32 lo = c.w < 2 ? (row > c0 ? 0xff7fu : 0u) : (row == c0 ? 0x3f80u : 0u);
                const u32 hi = c.w < 2 ? (row > c1 ? 0xff7fu : 0u) : (row == c1 ? 0x3f80u : 0u);
                d[q] = lo | (hi << 16);
            }
            lds_st<u32x4>(c.L, SC_MK + c.w * 1024 + c.voff, u32x4{d[0], d[1], d[2], d[3]});
        }
        const float* cs = (const float*)(a.ws + WS_CS);
        for (int e0 = tid; e0 < 320; e0 += 256) {
            float v;
            if (e0 < 96) v = cs[qkv_col(e0 >> 5) + 32 * c.j + (e0 & 31)];
            else { const int e = e0 - 96, t = e >> 5, n = e & 31; v = cs[1536 + (n >> 4) * 1792 + 112 * c.j + 16 * t + (n & 15)]; }
            lds_st<float>(c.L, SC_CS + e0 * 4, v);
        }
    } else {
        issue_chunk<CQ(0, 0)>(c); issue_chunk<CQ(0, 1)>(c); issue_chunk<CQ(1, 0)>(c); issue_chunk<CQ(1, 1)>(c);
        issue_chunk<CQ(2, 0)>(c); issue_chunk<CQ(2, 1)>(c); issue_chunk<CO(0)>(c); issue_chunk<CO(1)>(c);
    }
    for (int layer = 0; layer < 2; ++layer) {
        phase_qkv_att(c, layer);
        phase_wo(c, layer);
        phase_gu(c, layer);
        phase_dn(c, layer, a);
    }
}

}

extern "C" void kernel_launch(void* const* d_in, const int* in_sizes, int n_in, void* d_out, int out_size, void* d_ws, size_t ws_size,
                              hipStream_t stream) {
    Args a;
    a.x = (const u16*)d_in[0]; a.wqkv = (const u16*)d_in[1]; a.wo = (const u16*)d_in[2];
    a.wg = (const u16*)d_in[4]; a.wu = (const u16*)d_in[5]; a.wd = (const u16*)d_in[6];
    a.out = (u16*)d_out; a.ws = (unsigned char*)d_ws;
    (void)hipFuncSetAttribute((const void*)mega_kernel, hipFuncAttributeMaxDynamicSharedMemorySize, LDS_BYTES);
    prep_kernel<<<PREP_GRID, 512, 0, stream>>>(a);
    mega_kernel<<<256, 512, LDS_BYTES, stream>>>(a);
}
```

```cpp
#include <hip/hip_runtime.h>
#include <hip/hip_bf16.h>

#define LAS __attribute__((address_space(3)))
#define GAS __attribute__((address_space(1)))
typedef unsigned u32;
typedef unsigned short u16;
typedef u32 u32x4 __attribute__((ext_vector_type(4)));
typedef u32 u32x2 __attribute__((ext_vector_type(2)));
typedef float f32x16 __attribute__((ext_vector_type(16)));
typedef float f32x4 __attribute__((ext_vector_type(4)));
typedef float f32x2 __attribute__((ext_vector_type(2)));
typedef __bf16 bf16x8_t __attribute__((ext_vector_type(8)));
typedef __bf16 bf16x2_t __attribute__((ext_vector_type(2)));
typedef short s16x4 __attribute__((ext_vector_type(4)));

namespace {

constexpr int SEQ = 2048, DM = 512, FF = 1792;
constexpr size_t KiB = 1024, MiB = 1024 * 1024;
constexpr size_t WS_FLAGS = 1 * MiB;
constexpr size_t WS_EPOCH = 640 * KiB;
constexpr size_t WS_STARTED = WS_EPOCH + 4096;
constexpr size_t WS_TMO = 768 * KiB;
constexpr size_t WS_WO = 4 * MiB + 512 * KiB;
constexpr size_t WS_WG = 5 * MiB;
constexpr size_t WS_WD = 9 * MiB;
constexpr size_t WS_L0 = 16 * MiB, WS_LSTRIDE = 32 * MiB;
constexpr size_t L_KF = 0, L_VF = 2 * MiB, L_OF = 4 * MiB, L_X1 = 6 * MiB, L_X2 = 8 * MiB, L_HF = 10 * MiB, L_X2N = 18 * MiB;
constexpr size_t WS_END = WS_L0 + 2 * WS_LSTRIDE;

constexpr int NSLOT = 8, SLOT = 16384;
constexpr int LDS_SCR = NSLOT * SLOT;
constexpr int VREC = 1040;
constexpr int SC_RV = LDS_SCR;
constexpr int SC_VS = SC_RV + 16640;
constexpr int SC_ST = SC_VS + 8320;
constexpr int SC_CS = SC_ST + 1024;
constexpr int SC_MK = SC_CS + 1280;
constexpr int LDS_BYTES = SC_MK + 4096;
constexpr int SC_KS = 0 * SLOT;
constexpr int SC_QS = 0 * SLOT + 8192;
constexpr int SC_RK = 1 * SLOT;
static_assert(LDS_BYTES <= 163840, "LDS");
__device__ __forceinline__ int red_off(int q) { return q < 5 ? 2 * SLOT + q * 6144 : 6 * SLOT + (q - 5) * 6144; }

constexpr float LN_EPS = 1e-5f;
constexpr float QSCALE = 0.25f * 1.4426950408889634f;
constexpr u32 SPIN_CAP = 1u << 17;
constexpr u32 BF_ONES = 0x3f803f80u;

struct Args {
    const u16* x; const u16* wqkv; const u16* wo; const u16* wg; const u16* wu; const u16* wd;
    u16* out; unsigned char* ws;
};

__device__ __forceinline__ f32x16 mfma32(u32x4 a, u32x4 b, f32x16 c) {
    return __builtin_amdgcn_mfma_f32_32x32x16_bf16(__builtin_bit_cast(bf16x8_t, a), __builtin_bit_cast(bf16x8_t, b), c, 0, 0, 0);
}
__device__ __forceinline__ u32 pack2(float lo, float hi) {
    f32x2 v = {lo, hi};
    return __builtin_bit_cast(u32, __builtin_convertvector(v, bf16x2_t));
}
__device__ __forceinline__ float bflo(u32 u) { return __builtin_bit_cast(float, u << 16); }
__device__ __forceinline__ float bfhi(u32 u) { return __builtin_bit_cast(float, u & 0xffff0000u); }
__device__ __forceinline__ float dot2(u32 a, u32 b, float c) {
    return __builtin_amdgcn_fdot2_f32_bf16(__builtin_bit_cast(bf16x2_t, a), __builtin_bit_cast(bf16x2_t, b), c, false);
}
__device__ __forceinline__ void swap32(float& a, float& b) {
    asm volatile("s_nop 1\n\tv_permlane32_swap_b32 %0, %1\n\ts_nop 1" : "+v"(a), "+v"(b));
}
__device__ __forceinline__ float fmx(float a, float b) { return __builtin_elementwise_maximum(a, b); }
__device__ __forceinline__ float xor32_max(float v) { float a = v, b = v; swap32(a, b); return fmx(a, b); }
__device__ __forceinline__ float xor32_sum(float v) { float a = v, b = v; swap32(a, b); return a + b; }
#define BAR() do { asm volatile("s_waitcnt lgkmcnt(0)" ::: "memory"); __builtin_amdgcn_s_barrier(); asm volatile("" ::: "memory"); } while (0)
#define BARL() do { asm volatile("" ::: "memory"); __builtin_amdgcn_s_barrier(); asm volatile("" ::: "memory"); } while (0)
#define WAIT_VM(n) asm volatile("s_waitcnt vmcnt(" #n ")" ::: "memory")
#define SB() __builtin_amdgcn_sched_barrier(0)

template <class T> __device__ __forceinline__ T lds_ld(const LAS unsigned char* base, int off) { return *(const LAS T*)(base + off); }
template <class T> __device__ __forceinline__ void lds_st(LAS unsigned char* base, int off, T v) { *(LAS T*)(base + off) = v; }

__device__ __forceinline__ void glds4k(const unsigned char* src, u32 voff, u32 dst) {
    u32 keep;
    asm volatile(
        "s_mov_b32 %0, m0\n\t"
        "s_mov_b32 m0, %3\n\t"
        "s_nop 0\n\t"
        "global_load_lds_dwordx4 %1, %2 sc1\n\t"
        "global_load_lds_dwordx4 %1, %2 offset:1024 sc1\n\t"
        "global_load_lds_dwordx4 %1, %2 offset:2048 sc1\n\t"
        "global_load_lds_dwordx4 %1, %2 offset:3072 sc1\n\t"
        "s_mov_b32 m0, %0"
        : "=&s"(keep) : "v"(voff), "s"(src), "s"(dst) : "memory");
}

__device__ __forceinline__ void glds1k(const unsigned char* src, u32 voff, u32 dst) {
    u32 keep;
    asm volatile(
        "s_mov_b32 %0, m0\n\t"
        "s_mov_b32 m0, %3\n\t"
        "s_nop 0\n\t"
        "global_load_lds_dwordx4 %1, %2\n\t"
        "s_mov_b32 m0, %0"
        : "=&s"(keep) : "v"(voff), "s"(src), "s"(dst) : "memory");
}
#define PIN(x) asm volatile("" : "+v"(x))

constexpr int NF_WO = 8 * 64, NF_WG = 16 * 224, NF_WD = 8 * 224;
__host__ __device__ constexpr int qkv_col(int t) { return t == 0 ? 512 : (t == 1 ? 1024 : 0); }
struct WItem { const u16* src; size_t dst; };
__device__ __forceinline__ WItem w_item(const Args& a, int f, int lane) {
    const u16 *pA, *pB; int ld, ks; size_t dst;
    if (f < NF_WO) {
        const int cb = f >> 6, rem = f & 63, c = rem >> 4, r2 = rem & 15, kq = r2 >> 2, ks2 = (r2 >> 1) & 1, nt = r2 & 1;
        ks = 8 * kq + 2 * c + ks2; pA = a.wo + 64 * cb + 32 * nt; pB = pA + 16; ld = 512; dst = WS_WO + (size_t)f * 1024;
    } else if ((f -= NF_WO) < NF_WG) {
        const int j = f / 224, rem = f % 224, q = rem >> 4, ksl = rem & 15, c = q >> 1, hf = q & 1;
        ks = 16 * hf + ksl; pA = a.wg + 112 * j + 16 * c; pB = a.wu + 112 * j + 16 * c; ld = FF; dst = WS_WG + (size_t)f * 1024;
    } else {
        f -= NF_WG;
        const int cb = f / 224, rem = f % 224, c = rem >> 4, r2 = rem & 15, kq = r2 >> 2, ks2 = (r2 >> 1) & 1, nt = r2 & 1;
        ks = 28 * kq + 2 * c + ks2; pA = a.wd + 64 * cb + 32 * nt; pB = pA + 16; ld = 512; dst = WS_WD + (size_t)f * 1024;
    }
    const int k = lane >> 2, n8 = lane & 3;
    WItem it;
    it.src = (n8 < 2 ? pA + n8 * 8 : pB + (n8 - 2) * 8) + (size_t)(16 * ks + k) * ld;
    it.dst = dst;
    return it;
}

#define STAMP(k) do { } while (0)
struct Ctx {
    LAS unsigned char* L;
    u32 Laddr;
    unsigned char* ws;
    __amdgpu_buffer_rsrc_t rs;
    int lane, w, i, j;
    u32 voff;
    u32 tag;
};

__device__ __forceinline__ u32x4 ld_sc1(const Ctx& c, u32 soff) { return __builtin_amdgcn_raw_buffer_load_b128(c.rs, c.voff, soff, 16); }
__device__ __forceinline__ void st_sc1(const Ctx& c, u32 soff, u32x4 v) { __builtin_amdgcn_raw_buffer_store_b128(v, c.rs, c.voff, soff, 16); }
__device__ __forceinline__ Ctx fresh(const Ctx& c0) {
    Ctx c = c0;
    asm volatile("" : "+s"(c.i), "+s"(c.j), "+s"(c.w), "+s"(c.tag));
    asm volatile("" : "+v"(c.lane));
    c.voff = c.lane * 16;
    return c;
}

constexpr int CQ(int t, int hf) { return 2 * t + hf; }
constexpr int CO(int k) { return 6 + k; }
constexpr int CG(int t, int hf) { return 10 + 2 * t + hf; }
constexpr int CD(int k) { return 24 + k; }
constexpr int slot_of(int n) {
    constexpr int tab[38] = {
          0, 1,   2, 3,   4, 5,
          6, 7, 2, 3,
          4, 5,   0, 1,   2, 3,   6, 7,   4, 5,   0, 1,   2, 3,
          6, 7, 4, 5, 0, 1, 2, 3,   6, 7, 4, 5, 0, 1};
    return tab[n];
}
template <int N> __device__ __forceinline__ const unsigned char* chunk_src(const Ctx& c) {
    size_t src;
    const int cb = c.j & 7;
    if (N < 10) src = WS_WO + (size_t)(cb * 4 + (N - 6)) * SLOT;
    else if (N < 24) src = WS_WG + (size_t)(c.j * 14 + (N - 10)) * SLOT;
    else src = WS_WD + (size_t)(cb * 14 + (N - 24)) * SLOT;
    return c.ws + src + (c.w & 3) * 4096;
}
constexpr int chunk_lds(int n) { return slot_of(n) * SLOT; }
template <int N> __device__ __forceinline__ void issue_chunk(const Ctx& c) {
    glds4k(chunk_src<N>(c), c.voff, c.Laddr + chunk_lds(N) + (c.w & 3) * 4096);
}
__device__ __forceinline__ const LAS unsigned char* lds_base(const Ctx& c, int off) {
    u32 a = c.Laddr + off + c.voff;
    asm volatile("" : "+v"(a));
    return (const LAS unsigned char*)(size_t)a;
}

__device__ __forceinline__ u32 flag_off(int layer, int seam, int ci, int cj) {
    return (u32)(WS_FLAGS + (size_t)(((layer * 8 + seam) * 256) + ci * 16 + cj) * 512);
}
__device__ __forceinline__ u32 flag_tag(const Ctx& c, int layer, int seam) { return c.tag + (u32)(layer * 8 + seam); }
__device__ __forceinline__ void flag_store(const Ctx& c, int layer, int seam, int ci, int cj, int word) {
    __builtin_amdgcn_raw_buffer_store_b32(flag_tag(c, layer, seam), c.rs, flag_off(layer, seam, ci, cj) + word * 4, 0, 16);
}
__device__ __forceinline__ void signal_group(const Ctx& c, int layer, int seam) {
    if (c.lane < 16) flag_store(c, layer, seam, c.i, c.lane, c.j);
}
template <int NWORDS> __device__ __forceinline__ void poll_flags(const Ctx& c, int layer, int seam, int nwords = NWORDS) {
    const u32 off = flag_off(layer, seam, c.i, c.j) + c.lane * 4;
    const u32 tag = flag_tag(c, layer, seam);
    for (u32 spins = 0;; ++spins) {
        asm volatile("" ::: "memory");
        const u32 f0 = __builtin_amdgcn_raw_buffer_load_b32(c.rs, off, 0, 16);
        bool ok = c.lane >= nwords || f0 == tag;
        if (NWORDS > 64) { const u32 f1 = __builtin_amdgcn_raw_buffer_load_b32(c.rs, off + 256, 0, 16); ok = ok && f1 == tag; }
        if (__all(ok)) break;
        if (spins > SPIN_CAP) { if (c.lane == 0) __builtin_amdgcn_raw_buffer_store_b32(1u, c.rs, (u32)WS_TMO, 0, 16); break; }
        __builtin_amdgcn_s_sleep(1);
    }
}

__device__ __forceinline__ int crow(int r, int h) { return (r & 3) + 8 * (r >> 2) + 4 * h; }

#define LDF(p, k) (*(const LAS u32x4*)((p) + (k) * 1024))
__device__ __forceinline__ u32x4 ldf_tr(const LAS unsigned char* p, int k) {
    const u32x2 lo = __builtin_bit_cast(u32x2, __builtin_amdgcn_ds_read_tr16_b64_v4i16((LAS s16x4*)(p + k * 1024)));
    const u32x2 hi = __builtin_bit_cast(u32x2, __builtin_amdgcn_ds_read_tr16_b64_v4i16((LAS s16x4*)(p + k * 1024 + 256)));
    return u32x4{lo.x, lo.y, hi.x, hi.y};
}
template <bool TR> __device__ __forceinline__ u32x4 ldf(const LAS unsigned char* p, int k) { if constexpr (TR) return ldf_tr(p, k); else return LDF(p, k); }

template <bool SWAP, bool PRE, bool TR, class F>
__device__ __forceinline__ void c_tile(f32x16& acc, u32x4 (&wf)[8], const LAS unsigned char* lo, const LAS unsigned char* hi, const LAS unsigned char* nxt,
                                       const u32x4 (&xf)[32], F&& filler) {
#pragma unroll
    for (int k = 0; k < 32; ++k) {
        const u32x4 wv = wf[k & 7];
        if (k + 8 < 16) wf[k & 7] = ldf<TR>(lo, k + 8);
        else if (k + 8 < 32) wf[k & 7] = ldf<TR>(hi, k + 8 - 16);
        else if (PRE) wf[k & 7] = ldf<TR>(nxt, k + 8 - 32);
        acc = SWAP ? mfma32(xf[k], wv, acc) : mfma32(wv, xf[k], acc);
        PIN(acc);
        filler(k);
        SB();
    }
}
template <bool PRE, bool TR, bool STAGE>
__device__ __forceinline__ f32x2 c_tile0(const Ctx& c, f32x16& acc, u32x4 (&wf)[8], const LAS unsigned char* lo, const LAS unsigned char* hi,
                                         const LAS unsigned char* nxt, u32x4 (&xf)[32], __amdgpu_buffer_rsrc_t xrs, u32 xsoff) {
    const int lane = c.lane;
    u32x4 raw[STAGE ? 32 : 1];
    const u32 rvoff = (u32)(lane >> 3) * 1024u + (u32)(lane & 7) * 16u;
    LAS unsigned char* st = c.L + 6 * SLOT + (c.w & 3) * 8192;
    const int wr_off = (lane >> 3) * 128 + (((lane & 7) ^ ((lane >> 3) & 7)) * 16);
    const int rd_row = (lane & 31) * 128, rd_sw = lane & 7, rd_h = lane >> 5;
#define XLD(k) __builtin_amdgcn_raw_buffer_load_b128(xrs, c.voff, xsoff + (u32)(k) * 1024u, 16)
#define RLD(q) __builtin_amdgcn_raw_buffer_load_b128(xrs, rvoff, xsoff + (u32)((q) & 3) * 8192u + (u32)((q) >> 2) * 128u, 0)
#define STG(g) do { _Pragma("unroll") for (int q = 0; q < 4; ++q) lds_st<u32x4>(st + ((g) & 1) * 4096, q * 1024 + wr_off, raw[4 * (g) + q]); \
                    _Pragma("unroll") for (int q = 0; q < 4; ++q) xf[4 * (g) + q] = lds_ld<u32x4>(st + ((g) & 1) * 4096, rd_row + (((2 * q + rd_h) ^ rd_sw) * 16)); } while (0)
    if (STAGE) {
#pragma unroll
        for (int q = 0; q < 16; ++q) raw[q] = RLD(q);
        SB();
        STG(0);
    } else {
#pragma unroll
        for (int k = 0; k < 14; ++k) xf[k] = XLD(k);
    }
    SB();
    float s1[4] = {0.f, 0.f, 0.f, 0.f}, s2[4] = {0.f, 0.f, 0.f, 0.f};
#pragma unroll
    for (int k = 0; k < 32; ++k) {
        if (STAGE) {
            if ((k & 3) == 0) {
                const int g = k >> 2;
                if (4 * (g + 4) < 32) {
#pragma unroll
                    for (int q = 0; q < 4; ++q) raw[4 * (g + 4) + q] = RLD(4 * (g + 4) + q);
                }
                if (g + 1 < 8) STG(g + 1);
            }
        } else {
            if (k + 14 < 32) xf[k + 14] = XLD(k + 14);
        }
        const u32x4 wv = wf[k & 7];
        if (k + 8 < 16) wf[k & 7] = ldf<TR>(lo, k + 8);
        else if (k + 8 < 32) wf[k & 7] = ldf<TR>(hi, k + 8 - 16);
        else if (PRE) wf[k & 7] = ldf<TR>(nxt, k + 8 - 32);
        acc = mfma32(wv, xf[k], acc);
        PIN(acc);
        s1[0] = dot2(xf[k].x, BF_ONES, s1[0]); s2[0] = dot2(xf[k].x, xf[k].x, s2[0]);
        s1[1] = dot2(xf[k].y, BF_ONES, s1[1]); s2[1] = dot2(xf[k].y, xf[k].y, s2[1]);
        s1[2] = dot2(xf[k].z, BF_ONES, s1[2]); s2[2] = dot2(xf[k].z, xf[k].z, s2[2]);
        s1[3] = dot2(xf[k].w, BF_ONES, s1[3]); s2[3] = dot2(xf[k].w, xf[k].w, s2[3]);
        asm volatile("" : "+v"(s1[0]), "+v"(s1[1]), "+v"(s1[2]), "+v"(s1[3]), "+v"(s2[0]), "+v"(s2[1]), "+v"(s2[2]), "+v"(s2[3]));
        SB();
    }
    const float a = xor32_sum((s1[0] + s1[1]) + (s1[2] + s1[3])), b = xor32_sum((s2[0] + s2[1]) + (s2[2] + s2[3]));
    const float mean = a * (1.0f / DM);
    float var = b * (1.0f / DM) - mean * mean;
    var = var > 0.f ? var : 0.f;
    f32x2 o; o.x = mean; o.y = __builtin_amdgcn_rsqf(var + LN_EPS);
    return o;
#undef XLD
#undef RLD
#undef STG
}

struct GS { float g0, g1, t0, t1, u0, u1; };
struct GK { float mean, A, B, ir2; };
__device__ __forceinline__ void geglu_sub(GS& s, u32& o, int q, float ag0, float ag1, float au0, float au1, float cg0, float cg1, float cu0, float cu1, const GK& k) {
    if (q == 0) asm volatile("v_fma_f32 %0, -%7, %5, %3\n\tv_fma_f32 %1, -%7, %6, %4\n\tv_mul_f32 %2, %0, %0"
                             : "=&v"(s.g0), "=&v"(s.g1), "=&v"(s.t0) : "v"(ag0), "v"(ag1), "v"(cg0), "v"(cg1), "v"(k.mean));
    if (q == 1) asm volatile("v_mul_f32 %1, %2, %2\n\tv_fma_f32 %0, %0, %4, %3\n\tv_fma_f32 %1, %1, %4, %3"
                             : "+v"(s.t0), "=&v"(s.t1) : "v"(s.g1), "v"(k.A), "v"(k.B));
    if (q == 2) asm volatile("v_mul_f32 %0, %2, %0\n\tv_mul_f32 %1, %3, %1\n\tv_exp_f32 %0, %0"
                             : "+v"(s.t0), "+v"(s.t1) : "v"(s.g0), "v"(s.g1));
    if (q == 3) asm volatile("v_exp_f32 %0, %0\n\tv_fma_f32 %1, -%7, %5, %3\n\tv_fma_f32 %2, -%7, %6, %4"
                             : "+v"(s.t1), "=&v"(s.u0), "=&v"(s.u1) : "v"(au0), "v"(au1), "v"(cu0), "v"(cu1), "v"(k.mean));
    if (q == 4) asm volatile("v_fma_f32 %0, %0, %4, %4\n\tv_mul_f32 %2, %2, %3\n\tv_fma_f32 %1, %1, %4, %4"
                             : "+v"(s.t0), "+v"(s.t1), "+v"(s.g0) : "v"(s.u0), "v"(k.ir2));
    if (q == 5) asm volatile("v_rcp_f32 %0, %0\n\tv_mul_f32 %2, %2, %3\n\tv_rcp_f32 %1, %1"
                             : "+v"(s.t0), "+v"(s.t1), "+v"(s.g1) : "v"(s.u1));
    if (q == 6) asm volatile("v_mul_f32 %1, %1, %3\n\tv_mul_f32 %2, %2, %4\n\tv_cvt_pk_bf16_f32 %0, %1, %2"
                             : "=&v"(o), "+v"(s.g0), "+v"(s.g1) : "v"(s.t0), "v"(s.t1));
}
struct LS { float t0, t1; };
__device__ __forceinline__ void lnq_a(LS& s, float a0, float c0, float c1, float sc, float msc) {
    asm volatile("v_mul_f32 %0, %6, %3\n\tv_mul_f32 %1, %6, %4\n\tv_fma_f32 %0, %2, %5, -%0"
                 : "=&v"(s.t0), "=&v"(s.t1) : "v"(a0), "v"(c0), "v"(c1), "v"(sc), "v"(msc));
}
__device__ __forceinline__ u32 lnq_b(LS& s, float a1, float sc) {
    u32 o;
    asm volatile("v_fma_f32 %1, %3, %4, -%1\n\tv_cvt_pk_bf16_f32 %0, %2, %1" : "=v"(o), "+v"(s.t1) : "v"(s.t0), "v"(a1), "v"(sc));
    return o;
}
__device__ __forceinline__ void lnv_a(LS& s, float a0, float a1, float m0, float m1, float r0, float cs) {
    asm volatile("v_fma_f32 %0, -%4, %7, %2\n\tv_fma_f32 %1, -%5, %7, %3\n\tv_mul_f32 %0, %0, %6"
                 : "=&v"(s.t0), "=&v"(s.t1) : "v"(a0), "v"(a1), "v"(m0), "v"(m1), "v"(r0), "v"(cs));
}
__device__ __forceinline__ u32 lnv_b(LS& s, float r1) {
    u32 o;
    asm volatile("v_mul_f32 %1, %1, %3\n\tv_cvt_pk_bf16_f32 %0, %2, %1" : "=v"(o), "+v"(s.t1) : "v"(s.t0), "v"(r1));
    return o;
}
__device__ __forceinline__ u32x4 pack8(const float* v) { return u32x4{pack2(v[0], v[1]), pack2(v[2], v[3]), pack2(v[4], v[5]), pack2(v[6], v[7])}; }

__device__ __forceinline__ float max16(const f32x16& s, float m) {
    float a = fmx(fmx(s[0], s[1]), m), b = fmx(s[2], s[3]);
    a = fmx(fmx(s[4], s[5]), a); b = fmx(fmx(s[6], s[7]), b);
    a = fmx(fmx(s[8], s[9]), a); b = fmx(fmx(s[10], s[11]), b);
    a = fmx(fmx(s[12], s[13]), a); b = fmx(fmx(s[14], s[15]), b);
    return fmx(a, b);
}
__device__ __forceinline__ float smul(float a, float b) { float r; asm("v_mul_f32 %0, %1, %2" : "=v"(r) : "v"(a), "v"(b)); return r; }
struct MaskF { u32x4 alo, ahi, ilo, ihi; };
__device__ __forceinline__ f32x16 add_mask(const MaskF& mf, f32x16 s) { s = mfma32(mf.alo, mf.ilo, s); return mfma32(mf.ahi, mf.ihi, s); }
template <int NB, int FIRST, int REST0, int MASK, bool FIRSTSEG>
__device__ __forceinline__ void att_segment(f32x16& oacc, float& m, const LAS unsigned char* kb, const LAS unsigned char* vb, const LAS unsigned char* mk, const u32x4& qf) {
    u32x4 kf[NB];
#pragma unroll
    for (int t = 0; t < NB; ++t) { const int b = t == 0 ? FIRST : REST0 + t - 1; kf[t] = *(const LAS u32x4*)(kb + b * 2048); }
    MaskF mf;
    if (MASK) {
        mf.alo = LDF(mk, 0); mf.ahi = LDF(mk, 1); mf.ilo = LDF(mk, 2); mf.ihi = LDF(mk, 3);
        if (MASK == 2) { mf.alo ^= 0xff7fff7fu; mf.ahi ^= 0xff7fff7fu; }
    }
    SB();
    float mx = FIRSTSEG ? -__builtin_inff() : m;
    f32x16 sv[4];
    sv[0] = mfma32(kf[0], qf, f32x16{});
    if (NB > 1) sv[1] = mfma32(kf[1], qf, f32x16{});
    if (MASK) sv[0] = add_mask(mf, sv[0]);
    SB();
#pragma unroll
    for (int t = 0; t < NB; ++t) {
        const int u = NB > 1 ? (t == 0 ? 1 : t == 1 ? 0 : t) : 0;
        if (t + 2 < NB) sv[(t + 2) & 3] = mfma32(kf[t + 2], qf, f32x16{});
        mx = max16(sv[u & 3], mx);
        SB();
    }
    mx = xor32_max(mx);
    f32x16 negm;
#pragma unroll
    for (int e = 0; e < 16; ++e) negm[e] = -mx;
    SB();
    sv[0] = mfma32(kf[0], qf, negm);
    if (MASK) sv[0] = add_mask(mf, sv[0]);
    u32x4 v0 = *(const LAS u32x4*)(vb + (FIRST * 2 + 0) * VREC);
    u32x4 v1 = *(const LAS u32x4*)(vb + (FIRST * 2 + 1) * VREC);
    if (NB > 1) sv[1] = mfma32(kf[1], qf, negm);
    if (!FIRSTSEG) {
        float f = __builtin_amdgcn_exp2f(m - mx);
        asm volatile("s_nop 1" : "+v"(f));
#pragma unroll
        for (int e = 0; e < 16; ++e) oacc[e] = smul(oacc[e], f);
    }
    m = mx;
    SB();
#pragma unroll
    for (int t = 0; t < NB; ++t) {
        u32x4 n0, n1;
        if (t + 1 < NB) {
            if (t > 0) sv[(t + 1) & 1] = mfma32(kf[t + 1], qf, negm);
            n0 = *(const LAS u32x4*)(vb + ((REST0 + t) * 2 + 0) * VREC);
            n1 = *(const LAS u32x4*)(vb + ((REST0 + t) * 2 + 1) * VREC);
        }
        SB();
        const f32x16& s = sv[t & 1];
        u32 p[8];
#pragma unroll
        for (int e = 0; e < 4; ++e) p[e] = pack2(__builtin_amdgcn_exp2f(s[2 * e]), __builtin_amdgcn_exp2f(s[2 * e + 1]));
        SB();
        oacc = mfma32(v0, u32x4{p[0], p[1], p[2], p[3]}, oacc);
#pragma unroll
        for (int e = 4; e < 8; ++e) p[e] = pack2(__builtin_amdgcn_exp2f(s[2 * e]), __builtin_amdgcn_exp2f(s[2 * e + 1]));
        SB();
        oacc = mfma32(v1, u32x4{p[4], p[5], p[6], p[7]}, oacc);
        if (t + 1 < NB) { v0 = n0; v1 = n1; }
        SB();
    }
}

__device__ __forceinline__ void issue_qkv_tile(const Ctx& c, const Args& a, int t) {
    const u32 voff = (u32)(c.lane >> 2) * 3072u + (u32)(c.lane & 3) * 16u;
    const unsigned char* src = (const unsigned char*)(a.wqkv + qkv_col(t) + 32 * c.j) + (size_t)(c.w & 3) * 8 * 16 * 3072;
    const u32 dst = c.Laddr + slot_of(CQ(t, 0)) * SLOT + (c.w & 3) * 8192;
#pragma unroll
    for (int q = 0; q < 8; ++q) glds1k(src + (size_t)q * 16 * 3072, voff, dst + q * 1024);
}
template <bool TR> __device__ __forceinline__ void tile_colsum(const Ctx& c, const LAS unsigned char* lo, const LAS unsigned char* hi, int csidx) {
    float s[4] = {0.f, 0.f, 0.f, 0.f};
#pragma unroll
    for (int hf = 0; hf < 2; ++hf) {
        u32x4 f[16];
#pragma unroll
        for (int k = 0; k < 16; ++k) f[k] = ldf<TR>(hf ? hi : lo, k);
        SB();
#pragma unroll
        for (int k = 0; k < 16; ++k) {
            s[0] = dot2(f[k].x, BF_ONES, s[0]); s[1] = dot2(f[k].y, BF_ONES, s[1]); s[2] = dot2(f[k].z, BF_ONES, s[2]); s[3] = dot2(f[k].w, BF_ONES, s[3]);
        }
        SB();
    }
    const float t = xor32_sum((s[0] + s[1]) + (s[2] + s[3]));
    if (c.lane < 32) lds_st<float>(c.L, SC_CS + (csidx + c.lane) * 4, t);
}
__device__ __forceinline__ int tr_lane_off(int lane) {
    const int g4 = lane >> 4, h = g4 >> 1, nh = g4 & 1, li = lane & 15;
    return (8 * h + (li >> 2)) * 64 + nh * 32 + (li & 3) * 8;
}
__device__ __forceinline__ const LAS unsigned char* lds_base_raw(const Ctx& c, int off) {
    u32 a = c.Laddr + off;
    asm volatile("" : "+v"(a));
    return (const LAS unsigned char*)(size_t)a;
}

__device__ __forceinline__ void phase_qkv_att(const Ctx& c0, int layer, const Args& a) {
    const Ctx c = fresh(c0);
    const int lane = c.lane, w = c.w, tb = w & 3, h = lane >> 5, r = lane & 31;
    const u32 lbase = (u32)(WS_L0 + (size_t)layer * WS_LSTRIDE);
    const int tro = tr_lane_off(lane);
    STAMP(0);
    if (w < 4) {
        if (layer > 0 && w == 0) poll_flags<64>(c, layer - 1, 0, 16);
        BAR();
        STAMP(1);
        u32x4 xf[32], wf[8];
        const LAS unsigned char* wK = lds_base_raw(c, slot_of(CQ(0, 0)) * SLOT + tro);
        const LAS unsigned char* wV = lds_base_raw(c, slot_of(CQ(1, 0)) * SLOT + tro);
        const LAS unsigned char* wQ = lds_base_raw(c, slot_of(CQ(2, 0)) * SLOT + tro);
#pragma unroll
        for (int k = 0; k < 8; ++k) wf[k] = ldf_tr(wK, k);
        f32x16 accK = {}, accV = {}, accQ = {};
        f32x2 ms;
        if (layer == 0) ms = c_tile0<true, true, true>(c, accK, wf, wK, wK + SLOT, wV, xf, __builtin_amdgcn_make_buffer_rsrc((void*)a.x, 0, SEQ * DM * 2, 0x00020000),
                                                        (u32)((4 * c.i + tb) * 32 * 1024));
        else ms = c_tile0<true, true, false>(c, accK, wf, wK, wK + SLOT, wV, xf, c.rs, (u32)(WS_L0 + L_X2N) + (u32)((4 * c.i + tb) * 32) * 1024);
        if (lane < 32) lds_st<f32x2>(c.L, SC_ST + (tb * 32 + r) * 8, ms);
        const float msk = ms.x * ms.y;
        BARL(); STAMP(2);
        f32x4 cs[4];
#pragma unroll
        for (int q = 0; q < 4; ++q) cs[q] = lds_ld<f32x4>(c.L, SC_CS + (0 * 32 + 8 * q + 4 * h) * 4);
        f32x4 mr[8];
        float csv;
        {
            u32 o[8]; LS ls;
            c_tile<true, true, true>(accV, wf, wV, wV + SLOT, wQ, xf, [&](int k) {
                if (k >= 4 && k < 20) {
                    const int pi = (k - 4) >> 1, e = 2 * pi;
                    if (((k - 4) & 1) == 0) lnq_a(ls, accK[e], cs[e >> 2][e & 3], cs[e >> 2][(e & 3) + 1], ms.y, msk);
                    else o[pi] = lnq_b(ls, accK[e + 1], ms.y);
                }
                if (k == 20 || k == 21) {
                    const int hh = k - 20;
                    const u32x4 ov = {o[4 * hh], o[4 * hh + 1], o[4 * hh + 2], o[4 * hh + 3]};
                    lds_st<u32x4>(c.L, SC_KS + (tb * 2 + hh) * 1024 + c.voff, ov);
                    st_sc1(c, lbase + (u32)L_KF + (u32)((4 * c.i + tb) * 32 + 2 * c.j + hh) * 1024, ov);
                }
                if (k >= 22 && k < 30) { const int q = (k - 22) >> 1; mr[k - 22] = lds_ld<f32x4>(c.L, SC_ST + (tb * 32 + 8 * q + 4 * h + 2 * ((k - 22) & 1)) * 8); }
                if (k == 30) csv = lds_ld<float>(c.L, SC_CS + (32 + r) * 4);
            });
        }
        BARL();
        {
            u32 o[8]; LS ls;
            c_tile<false, false, true>(accQ, wf, wQ, wQ + SLOT, wQ, xf, [&](int k) {
                if (k >= 2 && k < 18) {
                    const int pi = (k - 2) >> 1;
                    if (((k - 2) & 1) == 0) lnv_a(ls, accV[2 * pi], accV[2 * pi + 1], mr[pi].x, mr[pi].z, mr[pi].y, csv);
                    else o[pi] = lnv_b(ls, mr[pi].w);
                }
                if (k == 18 || k == 20) {
                    const int sv = (k - 18) >> 1;
                    const u32x4 ov = {o[4 * sv], o[4 * sv + 1], o[4 * sv + 2], o[4 * sv + 3]};
                    lds_st<u32x4>(c.L, SC_VS + (tb * 2 + sv) * VREC + c.voff, ov);
                    st_sc1(c, lbase + (u32)L_VF + (u32)(((4 * c.i + tb) * 16 + c.j) * 2 + sv) * 1024, ov);
                }
                if (k >= 24 && k < 28) cs[k - 24] = lds_ld<f32x4>(c.L, SC_CS + (64 + 8 * (k - 24) + 4 * h) * 4);
            });
            if (lane < 2) { u32x4 ones = {BF_ONES, BF_ONES, BF_ONES, BF_ONES}; lds_st<u32x4>(c.L, SC_VS + (tb * 2 + lane) * VREC + 1024, ones); }
        }
        {
            const float sc = ms.y * QSCALE, msc = ms.x * sc;
            asm volatile("s_nop 15" : "+v"(accQ));
            u32 o[8]; LS ls[8];
#pragma unroll
            for (int pi = 0; pi < 8; ++pi) { const int e = 2 * pi; lnq_a(ls[pi], accQ[e], cs[e >> 2][e & 3], cs[e >> 2][(e & 3) + 1], sc, msc); }
#pragma unroll
            for (int pi = 0; pi < 8; ++pi) o[pi] = lnq_b(ls[pi], accQ[2 * pi + 1], sc);
#pragma unroll
            for (int hh = 0; hh < 2; ++hh) lds_st<u32x4>(c.L, SC_QS + (tb * 2 + hh) * 1024 + c.voff, u32x4{o[4 * hh], o[4 * hh + 1], o[4 * hh + 2], o[4 * hh + 3]});
        }
        WAIT_VM(0);
        BAR(); STAMP(5);
        if (w == 0 && lane < 2 && c.i + 1 + lane < 16) flag_store(c, layer, 1, c.i + 1 + lane, c.j, lane);
    } else {
        WAIT_VM(8);
        BAR();
        const int cb = c.j & 7, sidx = c.i * 2 + (c.j >> 3);
        if (layer == 0) {
            LAS unsigned char* t = c.L + SC_VS + (w & 3) * 1024;
            WItem it[6]; u32x4 v[6];
#pragma unroll
            for (int q = 0; q < 6; ++q) {
                const int n = (w & 3) + 4 * q;
                int f;
                if (n < 14) { const int ks = 2 * c.i + (n & 1), cc = n >> 1; f = NF_WO + c.j * 224 + (2 * cc + (ks >> 4)) * 16 + (ks & 15); }
                else if (n < 21) f = NF_WO + NF_WG + cb * 224 + sidx * 7 + (n - 14);
                else f = cb * 64 + sidx * 2 + ((n - 21) & 1);
                it[q] = w_item(a, f, lane);
                v[q] = *(const u32x4*)it[q].src;
            }
            const int k = lane >> 2, n8 = lane & 3;
            const int g4 = lane >> 4, hh = g4 >> 1, nh = g4 & 1, li = lane & 15, qq = li >> 2, p = li & 3;
#pragma unroll
            for (int q = 0; q < 6; ++q) {
                if ((w & 3) + 4 * q < 23) {
                    lds_st<u32x4>(t, k * 64 + n8 * 16, v[q]);
                    asm volatile("s_waitcnt lgkmcnt(0)" ::: "memory");
                    const s16x4 lo = __builtin_amdgcn_ds_read_tr16_b64_v4i16((LAS s16x4*)(t + (4 * hh + qq) * 64 + nh * 32 + p * 8));
                    const s16x4 hi = __builtin_amdgcn_ds_read_tr16_b64_v4i16((LAS s16x4*)(t + (8 + 4 * hh + qq) * 64 + nh * 32 + p * 8));
                    const u32x2 l2 = __builtin_bit_cast(u32x2, lo), h2 = __builtin_bit_cast(u32x2, hi);
                    __builtin_amdgcn_raw_buffer_store_b128(u32x4{l2.x, l2.y, h2.x, h2.y}, c.rs, c.voff, (u32)it[q].dst, 16);
                    asm volatile("s_waitcnt lgkmcnt(0)" ::: "memory");
                }
            }
        }
        WAIT_VM(0);
        if (layer == 0 && w < 7) {
            const int t = w - 4;
            const LAS unsigned char* wb = lds_base_raw(c, (t == 0 ? slot_of(CQ(0, 0)) : t == 1 ? slot_of(CQ(1, 0)) : slot_of(CQ(2, 0))) * SLOT + tro);
            tile_colsum<true>(c, wb, wb + SLOT, 32 * t);
        }
        BAR();
        if (layer == 0 && w == 4) {
            if (lane < 16) flag_store(c, 0, 5, lane, c.j, c.i);
            if (lane < 32) flag_store(c, 0, 6, lane >> 1, cb + 8 * (lane & 1), sidx);
        }
        BAR();
        if (layer == 0) poll_flags<64>(c, 0, 6, 32);
        issue_chunk<CO(0)>(c); issue_chunk<CO(1)>(c); issue_chunk<CO(2)>(c); issue_chunk<CO(3)>(c);
        BAR();
        if (layer == 0) poll_flags<64>(c, 0, 5, 16);
        issue_chunk<CG(0, 0)>(c); issue_chunk<CG(0, 1)>(c);
    }
    const int qb = w < 4 ? w : 7 - w, hh = w >> 2;
    const u32x4 qf = lds_ld<u32x4>(c.L, SC_QS + (qb * 2 + hh) * 1024 + c.voff);
    const int vlane = (((lane >> 4) & 1) == hh) ? lane * 16 : 1024;
    f32x16 oacc = {};
    float m = 0.f;
    const LAS unsigned char* mk = lds_base(c, SC_MK);
    {
        const LAS unsigned char* kb = c.L + SC_KS + hh * 1024 + c.voff;
        const LAS unsigned char* vb = c.L + SC_VS + vlane;
        if (qb == 0) att_segment<1, 0, 0, 1, true>(oacc, m, kb, vb, mk, qf);
        else if (qb == 1) att_segment<2, 1, 0, 1, true>(oacc, m, kb, vb, mk, qf);
        else if (qb == 2) att_segment<3, 2, 0, 1, true>(oacc, m, kb, vb, mk, qf);
        else att_segment<4, 3, 0, 1, true>(oacc, m, kb, vb, mk, qf);
    }
    STAMP(6);
    if (c.i > 0) {
        if (w == 0) poll_flags<64>(c, layer, 1, c.i >= 2 ? 2 : 1);
        BAR();
        STAMP(7);
        {
            const int idx = w;
            const int g = 4 * c.i - 8 + idx;
            if (g >= 0) {
                const u32x4 s0 = ld_sc1(c, lbase + (u32)L_KF + (u32)(g * 32 + 2 * c.j + 0) * 1024);
                const u32x4 s1 = ld_sc1(c, lbase + (u32)L_KF + (u32)(g * 32 + 2 * c.j + 1) * 1024);
                const u32x4 s2 = ld_sc1(c, lbase + (u32)L_VF + (u32)((g * 16 + c.j) * 2 + 0) * 1024);
                const u32x4 s3 = ld_sc1(c, lbase + (u32)L_VF + (u32)((g * 16 + c.j) * 2 + 1) * 1024);
                lds_st<u32x4>(c.L, SC_RK + (idx * 2 + 0) * 1024 + c.voff, s0);
                lds_st<u32x4>(c.L, SC_RK + (idx * 2 + 1) * 1024 + c.voff, s1);
                lds_st<u32x4>(c.L, SC_RV + (idx * 2 + 0) * VREC + c.voff, s2);
                lds_st<u32x4>(c.L, SC_RV + (idx * 2 + 1) * VREC + c.voff, s3);
                if (lane < 2) { u32x4 ones = {BF_ONES, BF_ONES, BF_ONES, BF_ONES}; lds_st<u32x4>(c.L, SC_RV + (idx * 2 + lane) * VREC + 1024, ones); }
            }
        }
        BAR();
        STAMP(8);
        const LAS unsigned char* kb = c.L + SC_RK + hh * 1024 + c.voff;
        const LAS unsigned char* vb = c.L + SC_RV + vlane;
        if (c.i == 1) att_segment<4, 4, 5, 0, false>(oacc, m, kb, vb, mk, qf);
        else if (qb == 0) att_segment<8, 0, 1, 2, false>(oacc, m, kb, vb, mk, qf);
        else if (qb == 1) att_segment<7, 1, 2, 2, false>(oacc, m, kb, vb, mk, qf);
        else if (qb == 2) att_segment<6, 2, 3, 2, false>(oacc, m, kb, vb, mk, qf);
        else att_segment<5, 3, 4, 2, false>(oacc, m, kb, vb, mk, qf);
    }
    {
        u32x4 o;
        if (hh == 0) {
            float il = __builtin_amdgcn_rcpf(oacc[8]);
            asm volatile("s_nop 1" : "+v"(il));
            o = u32x4{pack2(smul(oacc[0], il), smul(oacc[1], il)), pack2(smul(oacc[2], il), smul(oacc[3], il)), pack2(smul(oacc[4], il), smul(oacc[5], il)), pack2(smul(oacc[6], il), smul(oacc[7], il))};
        } else {
            float il = __builtin_amdgcn_rcpf(oacc[0]);
            asm volatile("s_nop 1" : "+v"(il));
            o = u32x4{pack2(smul(oacc[8], il), smul(oacc[9], il)), pack2(smul(oacc[10], il), smul(oacc[11], il)), pack2(smul(oacc[12], il), smul(oacc[13], il)), pack2(smul(oacc[14], il), smul(oacc[15], il))};
        }
        st_sc1(c, lbase + (u32)L_OF + (u32)((4 * c.i + qb) * 32 + 2 * c.j + hh) * 1024, o);
    }
    STAMP(9);
    WAIT_VM(0);
    BAR();
    if (w == 0) signal_group(c, layer, 2);
    if (w >= 4) { issue_chunk<CG(1, 0)>(c); issue_chunk<CG(1, 1)>(c); }
    STAMP(10);
}

__device__ __forceinline__ void red_write(const Ctx& c, const f32x16& acc0, const f32x16& acc1, int t2, int kq) {
#pragma unroll
    for (int d = 0; d < 4; ++d) {
        const int fin = d & 1;
        if (kq != fin) {
            const int slot = kq < fin ? kq : kq - 1;
            const int base = red_off(t2 * 4 + d) + slot * 2048 + c.voff;
            const f32x16& a = d < 2 ? acc0 : acc1;
            const int o = (d & 1) * 8;
            lds_st<f32x4>(c.L, base, f32x4{a[o], a[o + 1], a[o + 2], a[o + 3]});
            lds_st<f32x4>(c.L, base + 1024, f32x4{a[o + 4], a[o + 5], a[o + 6], a[o + 7]});
        }
    }
}
__device__ __forceinline__ void red_read(const Ctx& c, const f32x16& acc0, const f32x16& acc1, int t2, int kq, float (&v)[2][8]) {
#pragma unroll
    for (int e = 0; e < 8; ++e) { v[0][e] = kq ? acc0[8 + e] : acc0[e]; v[1][e] = kq ? acc1[8 + e] : acc1[e]; }
#pragma unroll
    for (int x = 0; x < 2; ++x)
#pragma unroll
        for (int s = 0; s < 3; ++s) {
            const int base = red_off(t2 * 4 + 2 * x + kq) + s * 2048 + c.voff;
            const f32x4 lo = lds_ld<f32x4>(c.L, base), hi = lds_ld<f32x4>(c.L, base + 1024);
            v[x][0] += lo.x; v[x][1] += lo.y; v[x][2] += lo.z; v[x][3] += lo.w; v[x][4] += hi.x; v[x][5] += hi.y; v[x][6] += hi.z; v[x][7] += hi.w;
        }
}
__device__ __forceinline__ u32x4 add_res(const float (&v)[8], const u32x4& res) {
    return u32x4{pack2(v[0] + bflo(res.x), v[1] + bfhi(res.x)), pack2(v[2] + bflo(res.y), v[3] + bfhi(res.y)),
                 pack2(v[4] + bflo(res.z), v[5] + bfhi(res.z)), pack2(v[6] + bflo(res.w), v[7] + bfhi(res.w))};
}

__device__ __forceinline__ void phase_wo(const Ctx& c0, int layer, const Args& a) {
    const Ctx c = fresh(c0);
    const int w = c.w, t2 = w & 1, kq = w >> 1;
    const int th = c.j >> 3, cb = c.j & 7;
    const int tbk = 4 * c.i + 2 * th + t2;
    const u32 lbase = (u32)(WS_L0 + (size_t)layer * WS_LSTRIDE);
    u32x4 res[2];
    if (w < 4) {
#pragma unroll
        for (int x = 0; x < 2; ++x) {
            const int kbo = 4 * cb + 2 * x + kq;
            if (layer == 0) {
                const u16* src = a.x + (size_t)(tbk * 32 + (c.lane & 31)) * DM + kbo * 16 + 4 * (c.lane >> 5);
                const u32x2 lo = *(const u32x2*)src, hi = *(const u32x2*)(src + 8);
                res[x] = u32x4{lo.x, lo.y, hi.x, hi.y};
            } else {
                res[x] = ld_sc1(c, (u32)(WS_L0 + L_X2) + (u32)(tbk * 32 + kbo) * 1024);
            }
        }
        if (w == 0) poll_flags<64>(c, layer, 2, 16);
    } else {
        WAIT_VM(8);
    }
    BAR();
    STAMP(11);
    u32x4 xf[8];
#pragma unroll
    for (int s = 0; s < 8; ++s) xf[s] = ld_sc1(c, lbase + (u32)L_OF + (u32)(tbk * 32 + 8 * kq + s) * 1024);
    u32x4 wf[16];
#pragma unroll
    for (int ch = 0; ch < 4; ++ch) {
        const LAS unsigned char* wb = lds_base(c, slot_of(CO(ch)) * SLOT + kq * 4096);
#pragma unroll
        for (int q = 0; q < 4; ++q) wf[4 * ch + q] = LDF(wb, q);
    }
    SB();
    f32x16 acc0 = {}, acc1 = {};
#pragma unroll
    for (int s = 0; s < 8; ++s) {
        acc0 = mfma32(wf[2 * s], xf[s], acc0);
        acc1 = mfma32(wf[2 * s + 1], xf[s], acc1);
        SB();
    }
    BAR();
    STAMP(12);
    red_write(c, acc0, acc1, t2, kq);
    BAR();
    if (w < 4) {
        float v[2][8];
        red_read(c, acc0, acc1, t2, kq, v);
#pragma unroll
        for (int x = 0; x < 2; ++x) st_sc1(c, lbase + (u32)L_X1 + (u32)(tbk * 32 + 4 * cb + 2 * x + kq) * 1024, add_res(v[x], res[x]));
        STAMP(13);
        WAIT_VM(0);
    }
    BAR();
    if (w == 0) signal_group(c, layer, 3);
    STAMP(14);
}

template <int T> __device__ __forceinline__ void gu_tile(const Ctx& c, f32x16& acc, const f32x16& prev, u32x4 (&wf)[8], const u32x4 (&xf)[32], int h,
                                                         f32x4 (&cs)[4], const GK& gk, u32 hbase) {
    const LAS unsigned char* cur = lds_base(c, slot_of(CG(T, 0)) * SLOT);
    const LAS unsigned char* nxt = lds_base(c, slot_of(CG(T < 6 ? T + 1 : T, 0)) * SLOT);
    GS g;
    u32 o[4];
    acc = f32x16{};
    SB();
    c_tile<false, (T < 6), false>(acc, wf, cur, cur + SLOT, nxt, xf, [&](int k) {
        const int p = k >> 3, e = 2 * p, q2 = p >> 1, e2 = e & 3;
        if ((k & 7) < 7) geglu_sub(g, o[p], k & 7, prev[e], prev[e + 1], prev[8 + e], prev[8 + e + 1], cs[q2][e2], cs[q2][e2 + 1], cs[2 + q2][e2], cs[2 + q2][e2 + 1], gk);
        if (k == 15) cs[0] = lds_ld<f32x4>(c.L, SC_CS + (96 + T * 32 + 4 * h) * 4);
        if (k == 15) cs[2] = lds_ld<f32x4>(c.L, SC_CS + (96 + T * 32 + 16 + 4 * h) * 4);
        if (k == 31) cs[1] = lds_ld<f32x4>(c.L, SC_CS + (96 + T * 32 + 8 + 4 * h) * 4);
        if (k == 31) cs[3] = lds_ld<f32x4>(c.L, SC_CS + (96 + T * 32 + 16 + 8 + 4 * h) * 4);
    });
    st_sc1(c, hbase + (u32)(T - 1) * 1024, u32x4{o[0], o[1], o[2], o[3]});
    if (T < 6) BARL();
}

__device__ __forceinline__ void phase_gu(const Ctx& c0, int layer) {
    const Ctx c = fresh(c0);
    const int lane = c.lane, w = c.w, tb = w & 3, h = lane >> 5;
    const u32 lbase = (u32)(WS_L0 + (size_t)layer * WS_LSTRIDE);
    if (w < 4) {
        if (w == 0) poll_flags<64>(c, layer, 3, 16);
        BAR();
        STAMP(15);
        u32x4 xf[32], wf[8];
        const LAS unsigned char* w0 = lds_base(c, slot_of(CG(0, 0)) * SLOT);
        const LAS unsigned char* w1 = lds_base(c, slot_of(CG(1, 0)) * SLOT);
#pragma unroll
        for (int k = 0; k < 8; ++k) wf[k] = LDF(w0, k);
        f32x16 a0 = {}, a1;
        const f32x2 ms = c_tile0<true, false, false>(c, a0, wf, w0, w0 + SLOT, w1, xf, c.rs, lbase + (u32)L_X1 + (u32)((4 * c.i + tb) * 32) * 1024);
        constexpr float GC1 = -2.0f * 0.7978845608028654f * 1.4426950408889634f, GC2 = GC1 * 0.044715f;
        GK gk; gk.mean = ms.x; gk.A = ms.y * GC1; gk.B = ms.y * ms.y * ms.y * GC2; gk.ir2 = 1.0f / (ms.y * ms.y);
        BARL(); STAMP(16);
        f32x4 cs[4];
#pragma unroll
        for (int q = 0; q < 4; ++q) cs[q] = lds_ld<f32x4>(c.L, SC_CS + (96 + (q >> 1) * 16 + 8 * (q & 1) + 4 * h) * 4);
        const u32 hbase = lbase + (u32)L_HF + (u32)((4 * c.i + tb) * 112 + 7 * c.j) * 1024;
        gu_tile<1>(c, a1, a0, wf, xf, h, cs, gk, hbase);
        gu_tile<2>(c, a0, a1, wf, xf, h, cs, gk, hbase);
        gu_tile<3>(c, a1, a0, wf, xf, h, cs, gk, hbase);
        gu_tile<4>(c, a0, a1, wf, xf, h, cs, gk, hbase);
        gu_tile<5>(c, a1, a0, wf, xf, h, cs, gk, hbase);
        gu_tile<6>(c, a0, a1, wf, xf, h, cs, gk, hbase);
        STAMP(22);
        {
            asm volatile("s_nop 15" : "+v"(a0));
            GS g[4];
            u32 o[4];
#pragma unroll
            for (int q = 0; q < 7; ++q)
#pragma unroll
                for (int p = 0; p < 4; ++p) {
                    const int e = 2 * p, q2 = p >> 1, e2 = e & 3;
                    geglu_sub(g[p], o[p], q, a0[e], a0[e + 1], a0[8 + e], a0[8 + e + 1], cs[q2][e2], cs[q2][e2 + 1], cs[2 + q2][e2], cs[2 + q2][e2 + 1], gk);
                }
            st_sc1(c, hbase + 6u * 1024, u32x4{o[0], o[1], o[2], o[3]});
        }
        STAMP(21);
        WAIT_VM(0);
        BAR();
        STAMP(23);
        if (w == 0) signal_group(c, layer, 4);
    } else {
#define GU_CS(T) do { if (layer == 0 && (w & 3) == ((T) & 3)) { const LAS unsigned char* wb_ = lds_base(c, slot_of(CG(T, 0)) * SLOT); tile_colsum<false>(c, wb_, wb_ + SLOT, 96 + 32 * (T)); } } while (0)
        WAIT_VM(0);
        BAR();
        __builtin_amdgcn_s_sleep(16);
        issue_chunk<CG(2, 0)>(c); issue_chunk<CG(2, 1)>(c); issue_chunk<CG(3, 0)>(c); issue_chunk<CG(3, 1)>(c);
        GU_CS(0); GU_CS(1);
        if (layer == 1 && w == 7 && c.i == 0 && c.j == 0) {
            for (u32 spins = 0;; ++spins) {
                asm volatile("" ::: "memory");
                const u32x4 f = __builtin_amdgcn_raw_buffer_load_b128(c.rs, c.voff, (u32)WS_STARTED, 16);
                if (__all(f.x == c.tag && f.y == c.tag && f.z == c.tag && f.w == c.tag)) { if (lane == 0) __builtin_amdgcn_raw_buffer_store_b32(c.tag + 31u, c.rs, (u32)WS_EPOCH, 0, 16); break; }
                if (spins > SPIN_CAP) break;
                __builtin_amdgcn_s_sleep(1);
            }
        }
        WAIT_VM(8);
        BAR();
        issue_chunk<CG(4, 0)>(c); issue_chunk<CG(4, 1)>(c);
        GU_CS(2);
        WAIT_VM(8);
        BAR();
        issue_chunk<CG(5, 0)>(c); issue_chunk<CG(5, 1)>(c);
        GU_CS(3);
        WAIT_VM(8);
        BAR();
        issue_chunk<CG(6, 0)>(c); issue_chunk<CG(6, 1)>(c);
        GU_CS(4);
        WAIT_VM(8);
        BAR();
        issue_chunk<CD(0)>(c); issue_chunk<CD(1)>(c);
        GU_CS(5);
        WAIT_VM(8);
        BAR();
        issue_chunk<CD(2)>(c); issue_chunk<CD(3)>(c);
        GU_CS(6);
        BAR();
        issue_chunk<CD(4)>(c); issue_chunk<CD(5)>(c);
        BAR();
        issue_chunk<CD(6)>(c); issue_chunk<CD(7)>(c);
#undef GU_CS
    }
}

__device__ __forceinline__ void phase_dn(const Ctx& c0, int layer, const Args& a) {
    const Ctx c = fresh(c0);
    const int lane = c.lane, w = c.w, t2 = w & 1, kq = w >> 1, h = lane >> 5, r = lane & 31;
    const int th = c.j >> 3, cb = c.j & 7;
    const int tbk = 4 * c.i + 2 * th + t2;
    const u32 lbase = (u32)(WS_L0 + (size_t)layer * WS_LSTRIDE);
    const u32 xsrc = lbase + (u32)L_HF + (u32)(tbk * 112 + 28 * kq) * 1024;
    if (w >= 4) WAIT_VM(8);
    BAR();
    u32x4 wr[24];
#pragma unroll
    for (int ch = 0; ch < 6; ++ch) {
        const LAS unsigned char* wb = lds_base(c, chunk_lds(CD(ch)) + kq * 4096);
#pragma unroll
        for (int q = 0; q < 4; ++q) wr[4 * ch + q] = LDF(wb, q);
    }
    BAR();
    if (w >= 4) {
        issue_chunk<CD(8)>(c); issue_chunk<CD(9)>(c); issue_chunk<CD(10)>(c);
        issue_chunk<CD(11)>(c); issue_chunk<CD(12)>(c); issue_chunk<CD(13)>(c);
    }
    u32x4 res[2];
    if (w < 4) {
        res[0] = ld_sc1(c, lbase + (u32)L_X1 + (u32)(tbk * 32 + 4 * cb + kq) * 1024);
        res[1] = ld_sc1(c, lbase + (u32)L_X1 + (u32)(tbk * 32 + 4 * cb + 2 + kq) * 1024);
    }
    if (w == 0) poll_flags<64>(c, layer, 4, 16);
    BAR();
    STAMP(25);
    constexpr int XD = 10;
    u32x4 xf[28];
#pragma unroll
    for (int s = 0; s < XD; ++s) xf[s] = ld_sc1(c, xsrc + (u32)s * 1024);
    SB();
    f32x16 acc0 = {}, acc1 = {};
#pragma unroll
    for (int s = 0; s < 12; ++s) {
        xf[s + XD] = ld_sc1(c, xsrc + (u32)(s + XD) * 1024);
        acc0 = mfma32(wr[2 * s], xf[s], acc0);
        acc1 = mfma32(wr[2 * s + 1], xf[s], acc1);
        PIN(acc0); PIN(acc1);
        SB();
    }
    WAIT_VM(22);
    BARL();
    STAMP(24);
    {
        const LAS unsigned char* wb[8];
#pragma unroll
        for (int ch = 0; ch < 8; ++ch) wb[ch] = lds_base(c, chunk_lds(CD(6 + ch)) + kq * 4096);
        u32x4 wf[8];
#pragma unroll
        for (int q = 0; q < 8; ++q) wf[q] = LDF(wb[q >> 2], q & 3);
        SB();
#pragma unroll
        for (int s = 0; s < 16; ++s) {
            const u32x4 w0 = wf[(2 * s) & 7], w1 = wf[(2 * s + 1) & 7];
            if (12 + s + XD < 28) xf[12 + s + XD] = ld_sc1(c, xsrc + (u32)(12 + s + XD) * 1024);
            if (2 * s + 8 < 32) {
                wf[(2 * s) & 7] = LDF(wb[(2 * s + 8) >> 2], (2 * s + 8) & 3);
                wf[(2 * s + 1) & 7] = LDF(wb[(2 * s + 9) >> 2], (2 * s + 9) & 3);
            }
            acc0 = mfma32(w0, xf[12 + s], acc0);
            acc1 = mfma32(w1, xf[12 + s], acc1);
            PIN(acc0); PIN(acc1);
            SB();
        }
    }
    BAR();
    STAMP(26);
    red_write(c, acc0, acc1, t2, kq);
    BAR();
    if (w >= 4) {
        if (layer == 0) issue_qkv_tile(c, a, 0);
    } else {
        float v[2][8];
        red_read(c, acc0, acc1, t2, kq, v);
#pragma unroll
        for (int x = 0; x < 2; ++x) {
            const int kbo = 4 * cb + 2 * x + kq;
            const u32x4 o = add_res(v[x], res[x]);
            const auto r0 = __builtin_amdgcn_permlane32_swap(o.x, o.z, false, false);
            const auto r1 = __builtin_amdgcn_permlane32_swap(o.y, o.w, false, false);
            const u32x4 on = u32x4{r0[0], r1[0], r0[1], r1[1]};
            if (layer == 1) {
                *(u32x4*)(a.out + (size_t)(tbk * 32 + r) * DM + kbo * 16 + 8 * h) = on;
            } else {
                st_sc1(c, lbase + (u32)L_X2N + (u32)(tbk * 32 + kbo) * 1024, on);
                st_sc1(c, lbase + (u32)L_X2 + (u32)(tbk * 32 + kbo) * 1024, o);
            }
        }
        STAMP(27);
        if (layer == 0) WAIT_VM(0);
    }
    if (layer == 0) {
        BAR();
        if (w == 0) signal_group(c, layer, 0);
        if (w >= 4) { issue_qkv_tile(c, a, 1); issue_qkv_tile(c, a, 2); }
        STAMP(28);
    }
}

__global__ void __launch_bounds__(512, 2) mega_kernel(Args a) {
    extern __shared__ __attribute__((aligned(16))) unsigned char lds[];
    Ctx c;
    c.L = (LAS unsigned char*)lds;
    c.Laddr = (u32)(size_t)c.L;
    c.ws = a.ws;
    c.rs = __builtin_amdgcn_make_buffer_rsrc((void*)a.ws, 0, (int)WS_END, 0x00020000);
    const int tid = threadIdx.x;
    c.lane = tid & 63;
    c.w = __builtin_amdgcn_readfirstlane(tid >> 6);
    const int bid = blockIdx.x;
    c.i = 2 * (bid & 7) + (bid >> 7);
    c.j = (bid >> 3) & 15;
    c.voff = c.lane * 16;
    {
        const u32 e = __builtin_amdgcn_raw_buffer_load_b32(c.rs, 0, (u32)WS_EPOCH, 16);
        c.tag = 0;
        if (c.w >= 4) { issue_qkv_tile(c, a, 0); issue_qkv_tile(c, a, 1); issue_qkv_tile(c, a, 2); }
        c.tag = (u32)__builtin_amdgcn_readfirstlane((int)e) + 1u;
        if (tid == 0) __builtin_amdgcn_raw_buffer_store_b32(c.tag, c.rs, (u32)bid * 4u, (u32)WS_STARTED, 16);
    }
    if (c.w < 4) {
        const int hh = c.lane >> 5, row = c.lane & 31, half = c.w & 1;
        u32 d[4];
#pragma unroll
        for (int q = 0; q < 4; ++q) {
            const int c0 = 16 * half + 8 * (q >> 1) + 4 * hh + 2 * (q & 1), c1 = c0 + 1;
            const u32 lo = c.w < 2 ? (row > c0 ? 0xff7fu : 0u) : (row == c0 ? 0x3f80u : 0u);
            const u32 hi = c.w < 2 ? (row > c1 ? 0xff7fu : 0u) : (row == c1 ? 0x3f80u : 0u);
            d[q] = lo | (hi << 16);
        }
        lds_st<u32x4>(c.L, SC_MK + c.w * 1024 + c.voff, u32x4{d[0], d[1], d[2], d[3]});
    }
    for (int layer = 0; layer < 2; ++layer) {
        phase_qkv_att(c, layer, a);
        phase_wo(c, layer, a);
        phase_gu(c, layer);
        phase_dn(c, layer, a);
    }
}

}

extern "C" void kernel_launch(void* const* d_in, const int* in_sizes, int n_in, void* d_out, int out_size, void* d_ws, size_t ws_size,
                              hipStream_t stream) {
    Args a;
    a.x = (const u16*)d_in[0]; a.wqkv = (const u16*)d_in[1]; a.wo = (const u16*)d_in[2];
    a.wg = (const u16*)d_in[4]; a.wu = (const u16*)d_in[5]; a.wd = (const u16*)d_in[6];
    a.out = (u16*)d_out; a.ws = (unsigned char*)d_ws;
    (void)hipFuncSetAttribute((const void*)mega_kernel, hipFuncAttributeMaxDynamicSharedMemorySize, LDS_BYTES);
    mega_kernel<<<256, 512, LDS_BYTES, stream>>>(a);
}
```

```cpp
#include <hip/hip_runtime.h>
#include <hip/hip_bf16.h>

#define LAS __attribute__((address_space(3)))
#define GAS __attribute__((address_space(1)))
typedef unsigned u32;
typedef unsigned short u16;
typedef u32 u32x4 __attribute__((ext_vector_type(4)));
typedef u32 u32x2 __attribute__((ext_vector_type(2)));
typedef float f32x16 __attribute__((ext_vector_type(16)));
typedef float f32x4 __attribute__((ext_vector_type(4)));
typedef float f32x2 __attribute__((ext_vector_type(2)));
typedef __bf16 bf16x8_t __attribute__((ext_vector_type(8)));
typedef __bf16 bf16x2_t __attribute__((ext_vector_type(2)));
typedef short s16x4 __attribute__((ext_vector_type(4)));

namespace {

constexpr int SEQ = 2048, DM = 512, FF = 1792;
constexpr size_t KiB = 1024, MiB = 1024 * 1024;
constexpr size_t WS_FLAGS = 1 * MiB;
constexpr size_t WS_EPOCH = 640 * KiB;
constexpr size_t WS_STARTED = WS_EPOCH + 4096;
constexpr size_t WS_TMO = 768 * KiB;
constexpr size_t WS_WO = 4 * MiB + 512 * KiB;
constexpr size_t WS_WG = 5 * MiB;
constexpr size_t WS_WD = 9 * MiB;
constexpr size_t WS_L0 = 16 * MiB, WS_LSTRIDE = 32 * MiB;
constexpr size_t L_KF = 0, L_VF = 2 * MiB, L_OF = 4 * MiB, L_X1 = 6 * MiB, L_X2 = 8 * MiB, L_HF = 10 * MiB, L_X2N = 18 * MiB;
constexpr size_t WS_END = WS_L0 + 2 * WS_LSTRIDE;

constexpr int NSLOT = 8, SLOT = 16384;
constexpr int LDS_SCR = NSLOT * SLOT;
constexpr int VREC = 1040;
constexpr int SC_RV = LDS_SCR;
constexpr int SC_VS = SC_RV + 16640;
constexpr int SC_ST = SC_VS + 8320;
constexpr int SC_CS = SC_ST + 1024;
constexpr int SC_MK = SC_CS + 1280;
constexpr int LDS_BYTES = SC_MK + 4096;
constexpr int SC_KS = 0 * SLOT;
constexpr int SC_QS = 0 * SLOT + 8192;
constexpr int SC_RK = 1 * SLOT;
static_assert(LDS_BYTES <= 163840, "LDS");
__device__ __forceinline__ int red_off(int q) { return q < 5 ? 2 * SLOT + q * 6144 : 6 * SLOT + (q - 5) * 6144; }

constexpr float LN_EPS = 1e-5f;
constexpr float QSCALE = 0.25f * 1.4426950408889634f;
constexpr u32 SPIN_CAP = 1u << 17;
constexpr u32 BF_ONES = 0x3f803f80u;

struct Args {
    const u16* x; const u16* wqkv; const u16* wo; const u16* wg; const u16* wu; const u16* wd;
    u16* out; unsigned char* ws;
};

__device__ __forceinline__ f32x16 mfma32(u32x4 a, u32x4 b, f32x16 c) {
    return __builtin_amdgcn_mfma_f32_32x32x16_bf16(__builtin_bit_cast(bf16x8_t, a), __builtin_bit_cast(bf16x8_t, b), c, 0, 0, 0);
}
__device__ __forceinline__ u32 pack2(float lo, float hi) {
    f32x2 v = {lo, hi};
    return __builtin_bit_cast(u32, __builtin_convertvector(v, bf16x2_t));
}
__device__ __forceinline__ float bflo(u32 u) { return __builtin_bit_cast(float, u << 16); }
__device__ __forceinline__ float bfhi(u32 u) { return __builtin_bit_cast(float, u & 0xffff0000u); }
__device__ __forceinline__ float dot2(u32 a, u32 b, float c) {
    return __builtin_amdgcn_fdot2_f32_bf16(__builtin_bit_cast(bf16x2_t, a), __builtin_bit_cast(bf16x2_t, b), c, false);
}
__device__ __forceinline__ void swap32(float& a, float& b) {
    asm volatile("s_nop 1\n\tv_permlane32_swap_b32 %0, %1\n\ts_nop 1" : "+v"(a), "+v"(b));
}
__device__ __forceinline__ float fmx(float a, float b) { return __builtin_elementwise_maximum(a, b); }
__device__ __forceinline__ float xor32_max(float v) { float a = v, b = v; swap32(a, b); return fmx(a, b); }
__device__ __forceinline__ float xor32_sum(float v) { float a = v, b = v; swap32(a, b); return a + b; }
#define BAR() do { asm volatile("s_waitcnt lgkmcnt(0)" ::: "memory"); __builtin_amdgcn_s_barrier(); asm volatile("" ::: "memory"); } while (0)
#define BARL() do { asm volatile("" ::: "memory"); __builtin_amdgcn_s_barrier(); asm volatile("" ::: "memory"); } while (0)
#define WAIT_VM(n) asm volatile("s_waitcnt vmcnt(" #n ")" ::: "memory")
#define SB() __builtin_amdgcn_sched_barrier(0)

template <class T> __device__ __forceinline__ T lds_ld(const LAS unsigned char* base, int off) { return *(const LAS T*)(base + off); }
template <class T> __device__ __forceinline__ void lds_st(LAS unsigned char* base, int off, T v) { *(LAS T*)(base + off) = v; }

__device__ __forceinline__ void glds4k(const unsigned char* src, u32 voff, u32 dst) {
    u32 keep;
    asm volatile(
        "s_mov_b32 %0, m0\n\t"
        "s_mov_b32 m0, %3\n\t"
        "s_nop 0\n\t"
        "global_load_lds_dwordx4 %1, %2 sc1\n\t"
        "global_load_lds_dwordx4 %1, %2 offset:1024 sc1\n\t"
        "global_load_lds_dwordx4 %1, %2 offset:2048 sc1\n\t"
        "global_load_lds_dwordx4 %1, %2 offset:3072 sc1\n\t"
        "s_mov_b32 m0, %0"
        : "=&s"(keep) : "v"(voff), "s"(src), "s"(dst) : "memory");
}

__device__ __forceinline__ void glds1k(const unsigned char* src, u32 voff, u32 dst) {
    u32 keep;
    asm volatile(
        "s_mov_b32 %0, m0\n\t"
        "s_mov_b32 m0, %3\n\t"
        "s_nop 0\n\t"
        "global_load_lds_dwordx4 %1, %2\n\t"
        "s_mov_b32 m0, %0"
        : "=&s"(keep) : "v"(voff), "s"(src), "s"(dst) : "memory");
}
#define PIN(x) asm volatile("" : "+v"(x))

constexpr int NF_WO = 8 * 64, NF_WG = 16 * 224, NF_WD = 8 * 224;
__host__ __device__ constexpr int qkv_col(int t) { return t == 0 ? 512 : (t == 1 ? 1024 : 0); }
struct WItem { const u16* src; size_t dst; };
__device__ __forceinline__ WItem w_item(const Args& a, int f, int lane) {
    const u16 *pA, *pB; int ld, ks; size_t dst;
    if (f < NF_WO) {
        const int cb = f >> 6, rem = f & 63, c = rem >> 4, r2 = rem & 15, kq = r2 >> 2, ks2 = (r2 >> 1) & 1, nt = r2 & 1;
        ks = 8 * kq + 2 * c + ks2; pA = a.wo + 64 * cb + 32 * nt; pB = pA + 16; ld = 512; dst = WS_WO + (size_t)f * 1024;
    } else if ((f -= NF_WO) < NF_WG) {
        const int j = f / 224, rem = f % 224, q = rem >> 4, ksl = rem & 15, c = q >> 1, hf = q & 1;
        ks = 16 * hf + ksl; pA = a.wg + 112 * j + 16 * c; pB = a.wu + 112 * j + 16 * c; ld = FF; dst = WS_WG + (size_t)f * 1024;
    } else {
        f -= NF_WG;
        const int cb = f / 224, rem = f % 224, c = rem >> 4, r2 = rem & 15, kq = r2 >> 2, ks2 = (r2 >> 1) & 1, nt = r2 & 1;
        ks = 28 * kq + 2 * c + ks2; pA = a.wd + 64 * cb + 32 * nt; pB = pA + 16; ld = 512; dst = WS_WD + (size_t)f * 1024;
    }
    const int k = lane >> 2, n8 = lane & 3;
    WItem it;
    it.src = (n8 < 2 ? pA + n8 * 8 : pB + (n8 - 2) * 8) + (size_t)(16 * ks + k) * ld;
    it.dst = dst;
    return it;
}

#define STAMP(k) do { } while (0)
struct Ctx {
    LAS unsigned char* L;
    u32 Laddr;
    unsigned char* ws;
    __amdgpu_buffer_rsrc_t rs;
    int lane, w, i, j;
    u32 voff;
    u32 tag;
};

__device__ __forceinline__ u32x4 ld_sc1(const Ctx& c, u32 soff) { return __builtin_amdgcn_raw_buffer_load_b128(c.rs, c.voff, soff, 16); }
__device__ __forceinline__ void st_sc1(const Ctx& c, u32 soff, u32x4 v) { __builtin_amdgcn_raw_buffer_store_b128(v, c.rs, c.voff, soff, 16); }
__device__ __forceinline__ Ctx fresh(const Ctx& c0) {
    Ctx c = c0;
    asm volatile("" : "+s"(c.i), "+s"(c.j), "+s"(c.w), "+s"(c.tag));
    asm volatile("" : "+v"(c.lane));
    c.voff = c.lane * 16;
    return c;
}

constexpr int CQ(int t, int hf) { return 2 * t + hf; }
constexpr int CO(int k) { return 6 + k; }
constexpr int CG(int t, int hf) { return 10 + 2 * t + hf; }
constexpr int CD(int k) { return 24 + k; }
constexpr int slot_of(int n) {
    constexpr int tab[38] = {
          0, 1,   2, 3,   4, 5,
          6, 7, 2, 3,
          4, 5,   0, 1,   2, 3,   6, 7,   4, 5,   0, 1,   2, 3,
          6, 7, 4, 5, 0, 1, 2, 3,   6, 7, 4, 5, 0, 1};
    return tab[n];
}
template <int N> __device__ __forceinline__ const unsigned char* chunk_src(const Ctx& c) {
    size_t src;
    const int cb = c.j & 7;
    if (N < 10) src = WS_WO + (size_t)(cb * 4 + (N - 6)) * SLOT;
    else if (N < 24) src = WS_WG + (size_t)(c.j * 14 + (N - 10)) * SLOT;
    else src = WS_WD + (size_t)(cb * 14 + (N - 24)) * SLOT;
    return c.ws + src + (c.w & 3) * 4096;
}
constexpr int chunk_lds(int n) { return slot_of(n) * SLOT; }
template <int N> __device__ __forceinline__ void issue_chunk(const Ctx& c) {
    glds4k(chunk_src<N>(c), c.voff, c.Laddr + chunk_lds(N) + (c.w & 3) * 4096);
}
__device__ __forceinline__ const LAS unsigned char* lds_base(const Ctx& c, int off) {
    u32 a = c.Laddr + off + c.voff;
    asm volatile("" : "+v"(a));
    return (const LAS unsigned char*)(size_t)a;
}

__device__ __forceinline__ u32 flag_off(int layer, int seam, int ci, int cj) {
    return (u32)(WS_FLAGS + (size_t)(((layer * 8 + seam) * 256) + ci * 16 + cj) * 512);
}
__device__ __forceinline__ u32 flag_tag(const Ctx& c, int layer, int seam) { return c.tag + (u32)(layer * 8 + seam); }
__device__ __forceinline__ void flag_store(const Ctx& c, int layer, int seam, int ci, int cj, int word) {
    __builtin_amdgcn_raw_buffer_store_b32(flag_tag(c, layer, seam), c.rs, flag_off(layer, seam, ci, cj) + word * 4, 0, 16);
}
__device__ __forceinline__ void signal_group(const Ctx& c, int layer, int seam) {
    if (c.lane < 16) flag_store(c, layer, seam, c.i, c.lane, c.j);
}
template <int NWORDS> __device__ __forceinline__ void poll_flags(const Ctx& c, int layer, int seam, int nwords = NWORDS) {
    const u32 off = flag_off(layer, seam, c.i, c.j) + c.lane * 4;
    const u32 tag = flag_tag(c, layer, seam);
    for (u32 spins = 0;; ++spins) {
        asm volatile("" ::: "memory");
        const u32 f0 = __builtin_amdgcn_raw_buffer_load_b32(c.rs, off, 0, 16);
        bool ok = c.lane >= nwords || f0 == tag;
        if (NWORDS > 64) { const u32 f1 = __builtin_amdgcn_raw_buffer_load_b32(c.rs, off + 256, 0, 16); ok = ok && f1 == tag; }
        if (__all(ok)) break;
        if (spins > SPIN_CAP) { if (c.lane == 0) __builtin_amdgcn_raw_buffer_store_b32(1u, c.rs, (u32)WS_TMO, 0, 16); break; }
        __builtin_amdgcn_s_sleep(1);
    }
}

__device__ __forceinline__ int crow(int r, int h) { return (r & 3) + 8 * (r >> 2) + 4 * h; }

#define LDF(p, k) (*(const LAS u32x4*)((p) + (k) * 1024))
__device__ __forceinline__ u32x4 ldf_tr(const LAS unsigned char* p, int k) {
    const u32x2 lo = __builtin_bit_cast(u32x2, __builtin_amdgcn_ds_read_tr16_b64_v4i16((LAS s16x4*)(p + k * 1024)));
    const u32x2 hi = __builtin_bit_cast(u32x2, __builtin_amdgcn_ds_read_tr16_b64_v4i16((LAS s16x4*)(p + k * 1024 + 256)));
    return u32x4{lo.x, lo.y, hi.x, hi.y};
}
template <bool TR> __device__ __forceinline__ u32x4 ldf(const LAS unsigned char* p, int k) { if constexpr (TR) return ldf_tr(p, k); else return LDF(p, k); }

template <bool SWAP, bool PRE, bool TR, class F>
__device__ __forceinline__ void c_tile(f32x16& acc, u32x4 (&wf)[8], const LAS unsigned char* lo, const LAS unsigned char* hi, const LAS unsigned char* nxt,
                                       const u32x4 (&xf)[32], F&& filler) {
#pragma unroll
    for (int k = 0; k < 32; ++k) {
        const u32x4 wv = wf[k & 7];
        if (k + 8 < 16) wf[k & 7] = ldf<TR>(lo, k + 8);
        else if (k + 8 < 32) wf[k & 7] = ldf<TR>(hi, k + 8 - 16);
        else if (PRE) wf[k & 7] = ldf<TR>(nxt, k + 8 - 32);
        acc = SWAP ? mfma32(xf[k], wv, acc) : mfma32(wv, xf[k], acc);
        PIN(acc);
        filler(k);
        SB();
    }
}
template <bool PRE, bool TR, bool STAGE>
__device__ __forceinline__ f32x2 c_tile0(const Ctx& c, f32x16& acc, u32x4 (&wf)[8], const LAS unsigned char* lo, const LAS unsigned char* hi,
                                         const LAS unsigned char* nxt, u32x4 (&xf)[32], __amdgpu_buffer_rsrc_t xrs, u32 xsoff) {
    const int lane = c.lane;
    u32x4 raw[STAGE ? 32 : 1];
    const u32 rvoff = (u32)(lane >> 3) * 1024u + (u32)(lane & 7) * 16u;
    LAS unsigned char* st = c.L + 6 * SLOT + (c.w & 3) * 8192;
    const int wr_off = (lane >> 3) * 128 + (((lane & 7) ^ ((lane >> 3) & 7)) * 16);
    const int rd_row = (lane & 31) * 128, rd_sw = lane & 7, rd_h = lane >> 5;
#define XLD(k) __builtin_amdgcn_raw_buffer_load_b128(xrs, c.voff, xsoff + (u32)(k) * 1024u, 16)
#define RLD(q) __builtin_amdgcn_raw_buffer_load_b128(xrs, rvoff, xsoff + (u32)((q) & 3) * 8192u + (u32)((q) >> 2) * 128u, 0)
#define STG(g) do { _Pragma("unroll") for (int q = 0; q < 4; ++q) lds_st<u32x4>(st + ((g) & 1) * 4096, q * 1024 + wr_off, raw[4 * (g) + q]); \
                    _Pragma("unroll") for (int q = 0; q < 4; ++q) xf[4 * (g) + q] = lds_ld<u32x4>(st + ((g) & 1) * 4096, rd_row + (((2 * q + rd_h) ^ rd_sw) * 16)); } while (0)
    if (STAGE) {
#pragma unroll
        for (int q = 0; q < 16; ++q) raw[q] = RLD(q);
        SB();
        STG(0);
    } else {
#pragma unroll
        for (int k = 0; k < 14; ++k) xf[k] = XLD(k);
    }
    SB();
    float s1[4] = {0.f, 0.f, 0.f, 0.f}, s2[4] = {0.f, 0.f, 0.f, 0.f};
#pragma unroll
    for (int k = 0; k < 32; ++k) {
        if (STAGE) {
            if ((k & 3) == 0) {
                const int g = k >> 2;
                if (4 * (g + 4) < 32) {
#pragma unroll
                    for (int q = 0; q < 4; ++q) raw[4 * (g + 4) + q] = RLD(4 * (g + 4) + q);
                }
                if (g + 1 < 8) STG(g + 1);
            }
        } else {
            if (k + 14 < 32) xf[k + 14] = XLD(k + 14);
        }
        const u32x4 wv = wf[k & 7];
        if (k + 8 < 16) wf[k & 7] = ldf<TR>(lo, k + 8);
        else if (k + 8 < 32) wf[k & 7] = ldf<TR>(hi, k + 8 - 16);
        else if (PRE) wf[k & 7] = ldf<TR>(nxt, k + 8 - 32);
        acc = mfma32(wv, xf[k], acc);
        PIN(acc);
        s1[0] = dot2(xf[k].x, BF_ONES, s1[0]); s2[0] = dot2(xf[k].x, xf[k].x, s2[0]);
        s1[1] = dot2(xf[k].y, BF_ONES, s1[1]); s2[1] = dot2(xf[k].y, xf[k].y, s2[1]);
        s1[2] = dot2(xf[k].z, BF_ONES, s1[2]); s2[2] = dot2(xf[k].z, xf[k].z, s2[2]);
        s1[3] = dot2(xf[k].w, BF_ONES, s1[3]); s2[3] = dot2(xf[k].w, xf[k].w, s2[3]);
        asm volatile("" : "+v"(s1[0]), "+v"(s1[1]), "+v"(s1[2]), "+v"(s1[3]), "+v"(s2[0]), "+v"(s2[1]), "+v"(s2[2]), "+v"(s2[3]));
        SB();
    }
    const float a = xor32_sum((s1[0] + s1[1]) + (s1[2] + s1[3])), b = xor32_sum((s2[0] + s2[1]) + (s2[2] + s2[3]));
    const float mean = a * (1.0f / DM);
    float var = b * (1.0f / DM) - mean * mean;
    var = var > 0.f ? var : 0.f;
    f32x2 o; o.x = mean; o.y = __builtin_amdgcn_rsqf(var + LN_EPS);
    return o;
#undef XLD
#undef RLD
#undef STG
}

struct GS { float g0, g1, t0, t1, u0, u1; };
struct GK { float mean, A, B, ir2; };
__device__ __forceinline__ void geglu_sub(GS& s, u32& o, int q, float ag0, float ag1, float au0, float au1, float cg0, float cg1, float cu0, float cu1, const GK& k) {
    if (q == 0) asm volatile("v_fma_f32 %0, -%7, %5, %3\n\tv_fma_f32 %1, -%7, %6, %4\n\tv_mul_f32 %2, %0, %0"
                             : "=&v"(s.g0), "=&v"(s.g1), "=&v"(s.t0) : "v"(ag0), "v"(ag1), "v"(cg0), "v"(cg1), "v"(k.mean));
    if (q == 1) asm volatile("v_mul_f32 %1, %2, %2\n\tv_fma_f32 %0, %0, %4, %3\n\tv_fma_f32 %1, %1, %4, %3"
                             : "+v"(s.t0), "=&v"(s.t1) : "v"(s.g1), "v"(k.A), "v"(k.B));
    if (q == 2) asm volatile("v_mul_f32 %0, %2, %0\n\tv_mul_f32 %1, %3, %1\n\tv_exp_f32 %0, %0"
                             : "+v"(s.t0), "+v"(s.t1) : "v"(s.g0), "v"(s.g1));
    if (q == 3) asm volatile("v_exp_f32 %0, %0\n\tv_fma_f32 %1, -%7, %5, %3\n\tv_fma_f32 %2, -%7, %6, %4"
                             : "+v"(s.t1), "=&v"(s.u0), "=&v"(s.u1) : "v"(au0), "v"(au1), "v"(cu0), "v"(cu1), "v"(k.mean));
    if (q == 4) asm volatile("v_fma_f32 %0, %0, %4, %4\n\tv_mul_f32 %2, %2, %3\n\tv_fma_f32 %1, %1, %4, %4"
                             : "+v"(s.t0), "+v"(s.t1), "+v"(s.g0) : "v"(s.u0), "v"(k.ir2));
    if (q == 5) asm volatile("v_rcp_f32 %0, %0\n\tv_mul_f32 %2, %2, %3\n\tv_rcp_f32 %1, %1"
                             : "+v"(s.t0), "+v"(s.t1), "+v"(s.g1) : "v"(s.u1));
    if (q == 6) asm volatile("v_mul_f32 %1, %1, %3\n\tv_mul_f32 %2, %2, %4\n\tv_cvt_pk_bf16_f32 %0, %1, %2"
                             : "=&v"(o), "+v"(s.g0), "+v"(s.g1) : "v"(s.t0), "v"(s.t1));
}
struct LS { float t0, t1; };
__device__ __forceinline__ void lnq_a(LS& s, float a0, float c0, float c1, float sc, float msc) {
    asm volatile("v_mul_f32 %0, %6, %3\n\tv_mul_f32 %1, %6, %4\n\tv_fma_f32 %0, %2, %5, -%0"
                 : "=&v"(s.t0), "=&v"(s.t1) : "v"(a0), "v"(c0), "v"(c1), "v"(sc), "v"(msc));
}
__device__ __forceinline__ u32 lnq_b(LS& s, float a1, float sc) {
    u32 o;
    asm volatile("v_fma_f32 %1, %3, %4, -%1\n\tv_cvt_pk_bf16_f32 %0, %2, %1" : "=v"(o), "+v"(s.t1) : "v"(s.t0), "v"(a1), "v"(sc));
    return o;
}
__device__ __forceinline__ void lnv_a(LS& s, float a0, float a1, float m0, float m1, float r0, float cs) {
    asm volatile("v_fma_f32 %0, -%4, %7, %2\n\tv_fma_f32 %1, -%5, %7, %3\n\tv_mul_f32 %0, %0, %6"
                 : "=&v"(s.t0), "=&v"(s.t1) : "v"(a0), "v"(a1), "v"(m0), "v"(m1), "v"(r0), "v"(cs));
}
__device__ __forceinline__ u32 lnv_b(LS& s, float r1) {
    u32 o;
    asm volatile("v_mul_f32 %1, %1, %3\n\tv_cvt_pk_bf16_f32 %0, %2, %1" : "=v"(o), "+v"(s.t1) : "v"(s.t0), "v"(r1));
    return o;
}
__device__ __forceinline__ u32x4 pack8(const float* v) { return u32x4{pack2(v[0], v[1]), pack2(v[2], v[3]), pack2(v[4], v[5]), pack2(v[6], v[7])}; }

__device__ __forceinline__ float max16(const f32x16& s, float m) {
    float a = fmx(fmx(s[0], s[1]), m), b = fmx(s[2], s[3]);
    a = fmx(fmx(s[4], s[5]), a); b = fmx(fmx(s[6], s[7]), b);
    a = fmx(fmx(s[8], s[9]), a); b = fmx(fmx(s[10], s[11]), b);
    a = fmx(fmx(s[12], s[13]), a); b = fmx(fmx(s[14], s[15]), b);
    return fmx(a, b);
}
__device__ __forceinline__ float smul(float a, float b) { float r; asm("v_mul_f32 %0, %1, %2" : "=v"(r) : "v"(a), "v"(b)); return r; }
struct MaskF { u32x4 alo, ahi, ilo, ihi; };
__device__ __forceinline__ f32x16 add_mask(const MaskF& mf, f32x16 s) { s = mfma32(mf.alo, mf.ilo, s); return mfma32(mf.ahi, mf.ihi, s); }
template <int NB, int FIRST, int REST0, int MASK, bool FIRSTSEG>
__device__ __forceinline__ void att_segment(f32x16& oacc, float& m, const LAS unsigned char* kb, const LAS unsigned char* vb, const LAS unsigned char* mk, const u32x4& qf) {
    u32x4 kf[NB];
#pragma unroll
    for (int t = 0; t < NB; ++t) { const int b = t == 0 ? FIRST : REST0 + t - 1; kf[t] = *(const LAS u32x4*)(kb + b * 2048); }
    MaskF mf;
    if (MASK) {
        mf.alo = LDF(mk, 0); mf.ahi = LDF(mk, 1); mf.ilo = LDF(mk, 2); mf.ihi = LDF(mk, 3);
        if (MASK == 2) { mf.alo ^= 0xff7fff7fu; mf.ahi ^= 0xff7fff7fu; }
    }
    SB();
    float mx = FIRSTSEG ? -__builtin_inff() : m;
    f32x16 sv[4];
    sv[0] = mfma32(kf[0], qf, f32x16{});
    if (NB > 1) sv[1] = mfma32(kf[1], qf, f32x16{});
    if (MASK) sv[0] = add_mask(mf, sv[0]);
    SB();
#pragma unroll
    for (int t = 0; t < NB; ++t) {
        const int u = NB > 1 ? (t == 0 ? 1 : t == 1 ? 0 : t) : 0;
        if (t + 2 < NB) sv[(t + 2) & 3] = mfma32(kf[t + 2], qf, f32x16{});
        mx = max16(sv[u & 3], mx);
        SB();
    }
    mx = xor32_max(mx);
    f32x16 negm;
#pragma unroll
    for (int e = 0; e < 16; ++e) negm[e] = -mx;
    SB();
    sv[0] = mfma32(kf[0], qf, negm);
    if (MASK) sv[0] = add_mask(mf, sv[0]);
    u32x4 v0 = *(const LAS u32x4*)(vb + (FIRST * 2 + 0) * VREC);
    u32x4 v1 = *(const LAS u32x4*)(vb + (FIRST * 2 + 1) * VREC);
    if (NB > 1) sv[1] = mfma32(kf[1], qf, negm);
    if (!FIRSTSEG) {
        float f = __builtin_amdgcn_exp2f(m - mx);
        asm volatile("s_nop 1" : "+v"(f));
#pragma unroll
        for (int e = 0; e < 16; ++e) oacc[e] = smul(oacc[e], f);
    }
    m = mx;
    SB();
#pragma unroll
    for (int t = 0; t < NB; ++t) {
        u32x4 n0, n1;
        if (t + 1 < NB) {
            if (t > 0) sv[(t + 1) & 1] = mfma32(kf[t + 1], qf, negm);
            n0 = *(const LAS u32x4*)(vb + ((REST0 + t) * 2 + 0) * VREC);
            n1 = *(const LAS u32x4*)(vb + ((REST0 + t) * 2 + 1) * VREC);
        }
        SB();
        const f32x16& s = sv[t & 1];
        u32 p[8];
#pragma unroll
        for (int e = 0; e < 4; ++e) p[e] = pack2(__builtin_amdgcn_exp2f(s[2 * e]), __builtin_amdgcn_exp2f(s[2 * e + 1]));
        SB();
        oacc = mfma32(v0, u32x4{p[0], p[1], p[2], p[3]}, oacc);
#pragma unroll
        for (int e = 4; e < 8; ++e) p[e] = pack2(__builtin_amdgcn_exp2f(s[2 * e]), __builtin_amdgcn_exp2f(s[2 * e + 1]));
        SB();
        oacc = mfma32(v1, u32x4{p[4], p[5], p[6], p[7]}, oacc);
        if (t + 1 < NB) { v0 = n0; v1 = n1; }
        SB();
    }
}

__device__ __forceinline__ void issue_qkv_tile(const Ctx& c, const Args& a, int t) {
    const u32 voff = (u32)(c.lane >> 2) * 3072u + (u32)(c.lane & 3) * 16u;
    const unsigned char* src = (const unsigned char*)(a.wqkv + qkv_col(t) + 32 * c.j) + (size_t)(c.w & 3) * 8 * 16 * 3072;
    const u32 dst = c.Laddr + slot_of(CQ(t, 0)) * SLOT + (c.w & 3) * 8192;
#pragma unroll
    for (int q = 0; q < 8; ++q) glds1k(src + (size_t)q * 16 * 3072, voff, dst + q * 1024);
}
template <bool TR> __device__ __forceinline__ void tile_colsum(const Ctx& c, const LAS unsigned char* lo, const LAS unsigned char* hi, int csidx) {
    float s[4] = {0.f, 0.f, 0.f, 0.f};
#pragma unroll
    for (int hf = 0; hf < 2; ++hf) {
        u32x4 f[16];
#pragma unroll
        for (int k = 0; k < 16; ++k) f[k] = ldf<TR>(hf ? hi : lo, k);
        SB();
#pragma unroll
        for (int k = 0; k < 16; ++k) {
            s[0] = dot2(f[k].x, BF_ONES, s[0]); s[1] = dot2(f[k].y, BF_ONES, s[1]); s[2] = dot2(f[k].z, BF_ONES, s[2]); s[3] = dot2(f[k].w, BF_ONES, s[3]);
        }
        SB();
    }
    const float t = xor32_sum((s[0] + s[1]) + (s[2] + s[3]));
    if (c.lane < 32) lds_st<float>(c.L, SC_CS + (csidx + c.lane) * 4, t);
}
__device__ __forceinline__ int tr_lane_off(int lane) {
    const int g4 = lane >> 4, h = g4 >> 1, nh = g4 & 1, li = lane & 15;
    return (8 * h + (li >> 2)) * 64 + nh * 32 + (li & 3) * 8;
}
__device__ __forceinline__ const LAS unsigned char* lds_base_raw(const Ctx& c, int off) {
    u32 a = c.Laddr + off;
    asm volatile("" : "+v"(a));
    return (const LAS unsigned char*)(size_t)a;
}

__device__ __forceinline__ void phase_qkv_att(const Ctx& c0, int layer, const Args& a) {
    const Ctx c = fresh(c0);
    const int lane = c.lane, w = c.w, tb = w & 3, h = lane >> 5, r = lane & 31;
    const u32 lbase = (u32)(WS_L0 + (size_t)layer * WS_LSTRIDE);
    const int tro = tr_lane_off(lane);
    STAMP(0);
    if (w < 4) {
        if (layer > 0 && w == 0) poll_flags<64>(c, layer - 1, 0, 16);
        BAR();
        STAMP(1);
        u32x4 xf[32], wf[8];
        const LAS unsigned char* wK = lds_base_raw(c, slot_of(CQ(0, 0)) * SLOT + tro);
        const LAS unsigned char* wV = lds_base_raw(c, slot_of(CQ(1, 0)) * SLOT + tro);
        const LAS unsigned char* wQ = lds_base_raw(c, slot_of(CQ(2, 0)) * SLOT + tro);
#pragma unroll
        for (int k = 0; k < 8; ++k) wf[k] = ldf_tr(wK, k);
        f32x16 accK = {}, accV = {}, accQ = {};
        f32x2 ms;
        if (layer == 0) ms = c_tile0<true, true, true>(c, accK, wf, wK, wK + SLOT, wV, xf, __builtin_amdgcn_make_buffer_rsrc((void*)a.x, 0, SEQ * DM * 2, 0x00020000),
                                                        (u32)((4 * c.i + tb) * 32 * 1024));
        else ms = c_tile0<true, true, false>(c, accK, wf, wK, wK + SLOT, wV, xf, c.rs, (u32)(WS_L0 + L_X2N) + (u32)((4 * c.i + tb) * 32) * 1024);
        if (lane < 32) lds_st<f32x2>(c.L, SC_ST + (tb * 32 + r) * 8, ms);
        const float msk = ms.x * ms.y;
        BARL(); STAMP(2);
        f32x4 cs[4];
#pragma unroll
        for (int q = 0; q < 4; ++q) cs[q] = lds_ld<f32x4>(c.L, SC_CS + (0 * 32 + 8 * q + 4 * h) * 4);
        f32x4 mr[8];
        float csv;
        {
            u32 o[8]; LS ls;
            c_tile<true, true, true>(accV, wf, wV, wV + SLOT, wQ, xf, [&](int k) {
                if (k >= 4 && k < 20) {
                    const int pi = (k - 4) >> 1, e = 2 * pi;
                    if (((k - 4) & 1) == 0) lnq_a(ls, accK[e], cs[e >> 2][e & 3], cs[e >> 2][(e & 3) + 1], ms.y, msk);
                    else o[pi] = lnq_b(ls, accK[e + 1], ms.y);
                }
                if (k == 20 || k == 21) {
                    const int hh = k - 20;
                    const u32x4 ov = {o[4 * hh], o[4 * hh + 1], o[4 * hh + 2], o[4 * hh + 3]};
                    lds_st<u32x4>(c.L, SC_KS + (tb * 2 + hh) * 1024 + c.voff, ov);
                    st_sc1(c, lbase + (u32)L_KF + (u32)((4 * c.i + tb) * 32 + 2 * c.j + hh) * 1024, ov);
                }
                if (k >= 22 && k < 30) { const int q = (k - 22) >> 1; mr[k - 22] = lds_ld<f32x4>(c.L, SC_ST + (tb * 32 + 8 * q + 4 * h + 2 * ((k - 22) & 1)) * 8); }
                if (k == 30) csv = lds_ld<float>(c.L, SC_CS + (32 + r) * 4);
            });
        }
        BARL();
        {
            u32 o[8]; LS ls;
            c_tile<false, false, true>(accQ, wf, wQ, wQ + SLOT, wQ, xf, [&](int k) {
                if (k >= 2 && k < 18) {
                    const int pi = (k - 2) >> 1;
                    if (((k - 2) & 1) == 0) lnv_a(ls, accV[2 * pi], accV[2 * pi + 1], mr[pi].x, mr[pi].z, mr[pi].y, csv);
                    else o[pi] = lnv_b(ls, mr[pi].w);
                }
                if (k == 18 || k == 20) {
                    const int sv = (k - 18) >> 1;
                    const u32x4 ov = {o[4 * sv], o[4 * sv + 1], o[4 * sv + 2], o[4 * sv + 3]};
                    lds_st<u32x4>(c.L, SC_VS + (tb * 2 + sv) * VREC + c.voff, ov);
                    st_sc1(c, lbase + (u32)L_VF + (u32)(((4 * c.i + tb) * 16 + c.j) * 2 + sv) * 1024, ov);
                }
                if (k >= 24 && k < 28) cs[k - 24] = lds_ld<f32x4>(c.L, SC_CS + (64 + 8 * (k - 24) + 4 * h) * 4);
            });
            if (lane < 2) { u32x4 ones = {BF_ONES, BF_ONES, BF_ONES, BF_ONES}; lds_st<u32x4>(c.L, SC_VS + (tb * 2 + lane) * VREC + 1024, ones); }
        }
        {
            const float sc = ms.y * QSCALE, msc = ms.x * sc;
            asm volatile("s_nop 15" : "+v"(accQ));
            u32 o[8]; LS ls[8];
#pragma unroll
            for (int pi = 0; pi < 8; ++pi) { const int e = 2 * pi; lnq_a(ls[pi], accQ[e], cs[e >> 2][e & 3], cs[e >> 2][(e & 3) + 1], sc, msc); }
#pragma unroll
            for (int pi = 0; pi < 8; ++pi) o[pi] = lnq_b(ls[pi], accQ[2 * pi + 1], sc);
#pragma unroll
            for (int hh = 0; hh < 2; ++hh) lds_st<u32x4>(c.L, SC_QS + (tb * 2 + hh) * 1024 + c.voff, u32x4{o[4 * hh], o[4 * hh + 1], o[4 * hh + 2], o[4 * hh + 3]});
        }
        WAIT_VM(0);
        BAR(); STAMP(5);
        if (w == 0 && lane < 2 && c.i + 1 + lane < 16) flag_store(c, layer, 1, c.i + 1 + lane, c.j, lane);
    } else {
        WAIT_VM(8);
        BAR();
        const int cb = c.j & 7, sidx = c.i * 2 + (c.j >> 3);
        if (layer == 0) {
            LAS unsigned char* t = c.L + SC_VS + (w & 3) * 1024;
            WItem it[6]; u32x4 v[6];
#pragma unroll
            for (int q = 0; q < 6; ++q) {
                const int n = (w & 3) + 4 * q;
                int f;
                if (n < 14) { const int ks = 2 * c.i + (n & 1), cc = n >> 1; f = NF_WO + c.j * 224 + (2 * cc + (ks >> 4)) * 16 + (ks & 15); }
                else if (n < 21) f = NF_WO + NF_WG + cb * 224 + sidx * 7 + (n - 14);
                else f = cb * 64 + sidx * 2 + ((n - 21) & 1);
                it[q] = w_item(a, f, lane);
                v[q] = *(const u32x4*)it[q].src;
            }
            const int k = lane >> 2, n8 = lane & 3;
            const int g4 = lane >> 4, hh = g4 >> 1, nh = g4 & 1, li = lane & 15, qq = li >> 2, p = li & 3;
#pragma unroll
            for (int q = 0; q < 6; ++q) {
                if ((w & 3) + 4 * q < 23) {
                    lds_st<u32x4>(t, k * 64 + n8 * 16, v[q]);
                    asm volatile("s_waitcnt lgkmcnt(0)" ::: "memory");
                    const s16x4 lo = __builtin_amdgcn_ds_read_tr16_b64_v4i16((LAS s16x4*)(t + (4 * hh + qq) * 64 + nh * 32 + p * 8));
                    const s16x4 hi = __builtin_amdgcn_ds_read_tr16_b64_v4i16((LAS s16x4*)(t + (8 + 4 * hh + qq) * 64 + nh * 32 + p * 8));
                    const u32x2 l2 = __builtin_bit_cast(u32x2, lo), h2 = __builtin_bit_cast(u32x2, hi);
                    __builtin_amdgcn_raw_buffer_store_b128(u32x4{l2.x, l2.y, h2.x, h2.y}, c.rs, c.voff, (u32)it[q].dst, 16);
                    asm volatile("s_waitcnt lgkmcnt(0)" ::: "memory");
                }
            }
        }
        WAIT_VM(0);
        if (layer == 0 && w < 7) {
            const int t = w - 4;
            const LAS unsigned char* wb = lds_base_raw(c, (t == 0 ? slot_of(CQ(0, 0)) : t == 1 ? slot_of(CQ(1, 0)) : slot_of(CQ(2, 0))) * SLOT + tro);
            tile_colsum<true>(c, wb, wb + SLOT, 32 * t);
        }
        BAR();
        if (layer == 0 && w == 4) {
            if (lane < 16) flag_store(c, 0, 5, lane, c.j, c.i);
            if (lane < 32) flag_store(c, 0, 6, lane >> 1, cb + 8 * (lane & 1), sidx);
        }
        BAR();
        if (layer == 0) poll_flags<64>(c, 0, 6, 32);
        issue_chunk<CO(0)>(c); issue_chunk<CO(1)>(c); issue_chunk<CO(2)>(c); issue_chunk<CO(3)>(c);
        BAR();
        if (layer == 0) poll_flags<64>(c, 0, 5, 16);
        issue_chunk<CG(0, 0)>(c); issue_chunk<CG(0, 1)>(c);
    }
    const int qb = w < 4 ? w : 7 - w, hh = w >> 2;
    const u32x4 qf = lds_ld<u32x4>(c.L, SC_QS + (qb * 2 + hh) * 1024 + c.voff);
    const int vlane = (((lane >> 4) & 1) == hh) ? lane * 16 : 1024;
    f32x16 oacc = {};
    float m = 0.f;
    const LAS unsigned char* mk = lds_base(c, SC_MK);
    {
        const LAS unsigned char* kb = c.L + SC_KS + hh * 1024 + c.voff;
        const LAS unsigned char* vb = c.L + SC_VS + vlane;
        if (qb == 0) att_segment<1, 0, 0, 1, true>(oacc, m, kb, vb, mk, qf);
        else if (qb == 1) att_segment<2, 1, 0, 1, true>(oacc, m, kb, vb, mk, qf);
        else if (qb == 2) att_segment<3, 2, 0, 1, true>(oacc, m, kb, vb, mk, qf);
        else att_segment<4, 3, 0, 1, true>(oacc, m, kb, vb, mk, qf);
    }
    STAMP(6);
    if (c.i > 0) {
        if (w == 0) poll_flags<64>(c, layer, 1, c.i >= 2 ? 2 : 1);
        BAR();
        STAMP(7);
        {
            const int idx = w;
            const int g = 4 * c.i - 8 + idx;
            if (g >= 0) {
                const u32x4 s0 = ld_sc1(c, lbase + (u32)L_KF + (u32)(g * 32 + 2 * c.j + 0) * 1024);
                const u32x4 s1 = ld_sc1(c, lbase + (u32)L_KF + (u32)(g * 32 + 2 * c.j + 1) * 1024);
                const u32x4 s2 = ld_sc1(c, lbase + (u32)L_VF + (u32)((g * 16 + c.j) * 2 + 0) * 1024);
                const u32x4 s3 = ld_sc1(c, lbase + (u32)L_VF + (u32)((g * 16 + c.j) * 2 + 1) * 1024);
                lds_st<u32x4>(c.L, SC_RK + (idx * 2 + 0) * 1024 + c.voff, s0);
                lds_st<u32x4>(c.L, SC_RK + (idx * 2 + 1) * 1024 + c.voff, s1);
                lds_st<u32x4>(c.L, SC_RV + (idx * 2 + 0) * VREC + c.voff, s2);
                lds_st<u32x4>(c.L, SC_RV + (idx * 2 + 1) * VREC + c.voff, s3);
                if (lane < 2) { u32x4 ones = {BF_ONES, BF_ONES, BF_ONES, BF_ONES}; lds_st<u32x4>(c.L, SC_RV + (idx * 2 + lane) * VREC + 1024, ones); }
            }
        }
        BAR();
        STAMP(8);
        const LAS unsigned char* kb = c.L + SC_RK + hh * 1024 + c.voff;
        const LAS unsigned char* vb = c.L + SC_RV + vlane;
        if (c.i == 1) att_segment<4, 4, 5, 0, false>(oacc, m, kb, vb, mk, qf);
        else if (qb == 0) att_segment<8, 0, 1, 2, false>(oacc, m, kb, vb, mk, qf);
        else if (qb == 1) att_segment<7, 1, 2, 2, false>(oacc, m, kb, vb, mk, qf);
        else if (qb == 2) att_segment<6, 2, 3, 2, false>(oacc, m, kb, vb, mk, qf);
        else att_segment<5, 3, 4, 2, false>(oacc, m, kb, vb, mk, qf);
    }
    {
        u32x4 o;
        if (hh == 0) {
            float il = __builtin_amdgcn_rcpf(oacc[8]);
            asm volatile("s_nop 1" : "+v"(il));
            o = u32x4{pack2(smul(oacc[0], il), smul(oacc[1], il)), pack2(smul(oacc[2], il), smul(oacc[3], il)), pack2(smul(oacc[4], il), smul(oacc[5], il)), pack2(smul(oacc[6], il), smul(oacc[7], il))};
        } else {
            float il = __builtin_amdgcn_rcpf(oacc[0]);
            asm volatile("s_nop 1" : "+v"(il));
            o = u32x4{pack2(smul(oacc[8], il), smul(oacc[9], il)), pack2(smul(oacc[10], il), smul(oacc[11], il)), pack2(smul(oacc[12], il), smul(oacc[13], il)), pack2(smul(oacc[14], il), smul(oacc[15], il))};
        }
        st_sc1(c, lbase + (u32)L_OF + (u32)((4 * c.i + qb) * 32 + 2 * c.j + hh) * 1024, o);
    }
    STAMP(9);
    WAIT_VM(0);
    BAR();
    if (w == 0) signal_group(c, layer, 2);
    if (w >= 4) { issue_chunk<CG(1, 0)>(c); issue_chunk<CG(1, 1)>(c); }
    STAMP(10);
}

__device__ __forceinline__ void red_write(const Ctx& c, const f32x16& acc0, const f32x16& acc1, int t2, int kq) {
#pragma unroll
    for (int d = 0; d < 4; ++d) {
        const int fin = d & 1;
        if (kq != fin) {
            const int slot = kq < fin ? kq : kq - 1;
            const int base = red_off(t2 * 4 + d) + slot * 2048 + c.voff;
            const f32x16& a = d < 2 ? acc0 : acc1;
            const int o = (d & 1) * 8;
            lds_st<f32x4>(c.L, base, f32x4{a[o], a[o + 1], a[o + 2], a[o + 3]});
            lds_st<f32x4>(c.L, base + 1024, f32x4{a[o + 4], a[o + 5], a[o + 6], a[o + 7]});
        }
    }
}
__device__ __forceinline__ void red_read(const Ctx& c, const f32x16& acc0, const f32x16& acc1, int t2, int kq, float (&v)[2][8]) {
#pragma unroll
    for (int e = 0; e < 8; ++e) { v[0][e] = kq ? acc0[8 + e] : acc0[e]; v[1][e] = kq ? acc1[8 + e] : acc1[e]; }
#pragma unroll
    for (int x = 0; x < 2; ++x)
#pragma unroll
        for (int s = 0; s < 3; ++s) {
            const int base = red_off(t2 * 4 + 2 * x + kq) + s * 2048 + c.voff;
            const f32x4 lo = lds_ld<f32x4>(c.L, base), hi = lds_ld<f32x4>(c.L, base + 1024);
            v[x][0] += lo.x; v[x][1] += lo.y; v[x][2] += lo.z; v[x][3] += lo.w; v[x][4] += hi.x; v[x][5] += hi.y; v[x][6] += hi.z; v[x][7] += hi.w;
        }
}
__device__ __forceinline__ u32x4 add_res(const float (&v)[8], const u32x4& res) {
    return u32x4{pack2(v[0] + bflo(res.x), v[1] + bfhi(res.x)), pack2(v[2] + bflo(res.y), v[3] + bfhi(res.y)),
                 pack2(v[4] + bflo(res.z), v[5] + bfhi(res.z)), pack2(v[6] + bflo(res.w), v[7] + bfhi(res.w))};
}

__device__ __forceinline__ void phase_wo(const Ctx& c0, int layer, const Args& a) {
    const Ctx c = fresh(c0);
    const int w = c.w, t2 = w & 1, kq = w >> 1;
    const int th = c.j >> 3, cb = c.j & 7;
    const int tbk = 4 * c.i + 2 * th + t2;
    const u32 lbase = (u32)(WS_L0 + (size_t)layer * WS_LSTRIDE);
    u32x4 res[2];
    if (w < 4) {
#pragma unroll
        for (int x = 0; x < 2; ++x) {
            const int kbo = 4 * cb + 2 * x + kq;
            if (layer == 0) {
                const u16* src = a.x + (size_t)(tbk * 32 + (c.lane & 31)) * DM + kbo * 16 + 4 * (c.lane >> 5);
                const u32x2 lo = *(const u32x2*)src, hi = *(const u32x2*)(src + 8);
                res[x] = u32x4{lo.x, lo.y, hi.x, hi.y};
            } else {
                res[x] = ld_sc1(c, (u32)(WS_L0 + L_X2) + (u32)(tbk * 32 + kbo) * 1024);
            }
        }
        if (w == 0) poll_flags<64>(c, layer, 2, 16);
    } else {
        WAIT_VM(8);
    }
    BAR();
    STAMP(11);
    u32x4 xf[8];
#pragma unroll
    for (int s = 0; s < 8; ++s) xf[s] = ld_sc1(c, lbase + (u32)L_OF + (u32)(tbk * 32 + 8 * kq + s) * 1024);
    u32x4 wf[16];
#pragma unroll
    for (int ch = 0; ch < 4; ++ch) {
        const LAS unsigned char* wb = lds_base(c, slot_of(CO(ch)) * SLOT + kq * 4096);
#pragma unroll
        for (int q = 0; q < 4; ++q) wf[4 * ch + q] = LDF(wb, q);
    }
    SB();
    f32x16 acc0 = {}, acc1 = {};
#pragma unroll
    for (int s = 0; s < 8; ++s) {
        acc0 = mfma32(wf[2 * s], xf[s], acc0);
        acc1 = mfma32(wf[2 * s + 1], xf[s], acc1);
        SB();
    }
    BAR();
    STAMP(12);
    red_write(c, acc0, acc1, t2, kq);
    BAR();
    if (w < 4) {
        float v[2][8];
        red_read(c, acc0, acc1, t2, kq, v);
#pragma unroll
        for (int x = 0; x < 2; ++x) st_sc1(c, lbase + (u32)L_X1 + (u32)(tbk * 32 + 4 * cb + 2 * x + kq) * 1024, add_res(v[x], res[x]));
        STAMP(13);
        WAIT_VM(0);
    }
    BAR();
    if (w == 0) signal_group(c, layer, 3);
    STAMP(14);
}

__device__ __forceinline__ int gu_slot(int T) { return (int)((0x02046204u >> (4 * (T & 7))) & 15u); }
__device__ __forceinline__ void gu_tile(const Ctx& c, int T, f32x16& acc, const f32x16& prev, u32x4 (&wf)[8], const u32x4 (&xf)[32], int h,
                                        f32x4 (&cs)[4], const GK& gk, u32 hbase) {
    const LAS unsigned char* cur = lds_base(c, gu_slot(T) * SLOT);
    const LAS unsigned char* nxt = lds_base(c, gu_slot(T < 6 ? T + 1 : T) * SLOT);
    const LAS unsigned char* csb = lds_base_raw(c, SC_CS + (96 + T * 32 + 4 * h) * 4);
    GS g;
    u32 o[4];
    acc = f32x16{};
    SB();
    c_tile<false, true, false>(acc, wf, cur, cur + SLOT, nxt, xf, [&](int k) {
        const int p = k >> 3, e = 2 * p, q2 = p >> 1, e2 = e & 3;
        if ((k & 7) < 7) geglu_sub(g, o[p], k & 7, prev[e], prev[e + 1], prev[8 + e], prev[8 + e + 1], cs[q2][e2], cs[q2][e2 + 1], cs[2 + q2][e2], cs[2 + q2][e2 + 1], gk);
        if (k == 15) cs[0] = *(const LAS f32x4*)(csb);
        if (k == 15) cs[2] = *(const LAS f32x4*)(csb + 64);
        if (k == 31) cs[1] = *(const LAS f32x4*)(csb + 32);
        if (k == 31) cs[3] = *(const LAS f32x4*)(csb + 96);
    });
    st_sc1(c, hbase + (u32)(T - 1) * 1024, u32x4{o[0], o[1], o[2], o[3]});
}
static_assert(slot_of(CG(0, 0)) == 4 && slot_of(CG(1, 0)) == 0 && slot_of(CG(2, 0)) == 2 && slot_of(CG(3, 0)) == 6 && slot_of(CG(4, 0)) == 4 && slot_of(CG(5, 0)) == 0 && slot_of(CG(6, 0)) == 2, "gu_slot");

__device__ __forceinline__ void phase_gu(const Ctx& c0, int layer) {
    const Ctx c = fresh(c0);
    const int lane = c.lane, w = c.w, tb = w & 3, h = lane >> 5;
    const u32 lbase = (u32)(WS_L0 + (size_t)layer * WS_LSTRIDE);
    if (w < 4) {
        if (w == 0) poll_flags<64>(c, layer, 3, 16);
        BAR();
        STAMP(15);
        u32x4 xf[32], wf[8];
        const LAS unsigned char* w0 = lds_base(c, slot_of(CG(0, 0)) * SLOT);
        const LAS unsigned char* w1 = lds_base(c, slot_of(CG(1, 0)) * SLOT);
#pragma unroll
        for (int k = 0; k < 8; ++k) wf[k] = LDF(w0, k);
        f32x16 a0 = {}, a1;
        const f32x2 ms = c_tile0<true, false, false>(c, a0, wf, w0, w0 + SLOT, w1, xf, c.rs, lbase + (u32)L_X1 + (u32)((4 * c.i + tb) * 32) * 1024);
        constexpr float GC1 = -2.0f * 0.7978845608028654f * 1.4426950408889634f, GC2 = GC1 * 0.044715f;
        GK gk; gk.mean = ms.x; gk.A = ms.y * GC1; gk.B = ms.y * ms.y * ms.y * GC2; gk.ir2 = 1.0f / (ms.y * ms.y);
        BARL(); STAMP(16);
        f32x4 cs[4];
#pragma unroll
        for (int q = 0; q < 4; ++q) cs[q] = lds_ld<f32x4>(c.L, SC_CS + (96 + (q >> 1) * 16 + 8 * (q & 1) + 4 * h) * 4);
        const u32 hbase = lbase + (u32)L_HF + (u32)((4 * c.i + tb) * 112 + 7 * c.j) * 1024;
#pragma unroll 1
        for (int T = 1; T < 7; T += 2) {
            gu_tile(c, T, a1, a0, wf, xf, h, cs, gk, hbase);
            BARL();
            gu_tile(c, T + 1, a0, a1, wf, xf, h, cs, gk, hbase);
            if (T < 5) BARL();
        }
        STAMP(22);
        {
            asm volatile("s_nop 15" : "+v"(a0));
            GS g[4];
            u32 o[4];
#pragma unroll
            for (int q = 0; q < 7; ++q)
#pragma unroll
                for (int p = 0; p < 4; ++p) {
                    const int e = 2 * p, q2 = p >> 1, e2 = e & 3;
                    geglu_sub(g[p], o[p], q, a0[e], a0[e + 1], a0[8 + e], a0[8 + e + 1], cs[q2][e2], cs[q2][e2 + 1], cs[2 + q2][e2], cs[2 + q2][e2 + 1], gk);
                }
            st_sc1(c, hbase + 6u * 1024, u32x4{o[0], o[1], o[2], o[3]});
        }
        STAMP(21);
        WAIT_VM(0);
        BAR();
        STAMP(23);
        if (w == 0) signal_group(c, layer, 4);
    } else {
#define GU_CS(T) do { if (layer == 0 && (w & 3) == ((T) & 3)) { const LAS unsigned char* wb_ = lds_base(c, slot_of(CG(T, 0)) * SLOT); tile_colsum<false>(c, wb_, wb_ + SLOT, 96 + 32 * (T)); } } while (0)
        WAIT_VM(0);
        BAR();
        __builtin_amdgcn_s_sleep(16);
        issue_chunk<CG(2, 0)>(c); issue_chunk<CG(2, 1)>(c); issue_chunk<CG(3, 0)>(c); issue_chunk<CG(3, 1)>(c);
        GU_CS(0); GU_CS(1);
        if (layer == 1 && w == 7 && c.i == 0 && c.j == 0) {
            for (u32 spins = 0;; ++spins) {
                asm volatile("" ::: "memory");
                const u32x4 f = __builtin_amdgcn_raw_buffer_load_b128(c.rs, c.voff, (u32)WS_STARTED, 16);
                if (__all(f.x == c.tag && f.y == c.tag && f.z == c.tag && f.w == c.tag)) { if (lane == 0) __builtin_amdgcn_raw_buffer_store_b32(c.tag + 31u, c.rs, (u32)WS_EPOCH, 0, 16); break; }
                if (spins > SPIN_CAP) break;
                __builtin_amdgcn_s_sleep(1);
            }
        }
        WAIT_VM(8);
        BAR();
        issue_chunk<CG(4, 0)>(c); issue_chunk<CG(4, 1)>(c);
        GU_CS(2);
        WAIT_VM(8);
        BAR();
        issue_chunk<CG(5, 0)>(c); issue_chunk<CG(5, 1)>(c);
        GU_CS(3);
        WAIT_VM(8);
        BAR();
        issue_chunk<CG(6, 0)>(c); issue_chunk<CG(6, 1)>(c);
        GU_CS(4);
        WAIT_VM(8);
        BAR();
        issue_chunk<CD(0)>(c); issue_chunk<CD(1)>(c);
        GU_CS(5);
        WAIT_VM(8);
        BAR();
        issue_chunk<CD(2)>(c); issue_chunk<CD(3)>(c);
        GU_CS(6);
        BAR();
        issue_chunk<CD(4)>(c); issue_chunk<CD(5)>(c);
        BAR();
        issue_chunk<CD(6)>(c); issue_chunk<CD(7)>(c);
#undef GU_CS
    }
}

__device__ __forceinline__ void phase_dn(const Ctx& c0, int layer, const Args& a) {
    const Ctx c = fresh(c0);
    const int lane = c.lane, w = c.w, t2 = w & 1, kq = w >> 1, h = lane >> 5, r = lane & 31;
    const int th = c.j >> 3, cb = c.j & 7;
    const int tbk = 4 * c.i + 2 * th + t2;
    const u32 lbase = (u32)(WS_L0 + (size_t)layer * WS_LSTRIDE);
    const u32 xsrc = lbase + (u32)L_HF + (u32)(tbk * 112 + 28 * kq) * 1024;
    if (w >= 4) WAIT_VM(8);
    BAR();
    u32x4 wr[24];
#pragma unroll
    for (int ch = 0; ch < 6; ++ch) {
        const LAS unsigned char* wb = lds_base(c, chunk_lds(CD(ch)) + kq * 4096);
#pragma unroll
        for (int q = 0; q < 4; ++q) wr[4 * ch + q] = LDF(wb, q);
    }
    BAR();
    if (w >= 4) {
        issue_chunk<CD(8)>(c); issue_chunk<CD(9)>(c); issue_chunk<CD(10)>(c);
        issue_chunk<CD(11)>(c); issue_chunk<CD(12)>(c); issue_chunk<CD(13)>(c);
    }
    u32x4 res[2];
    if (w < 4) {
        res[0] = ld_sc1(c, lbase + (u32)L_X1 + (u32)(tbk * 32 + 4 * cb + kq) * 1024);
        res[1] = ld_sc1(c, lbase + (u32)L_X1 + (u32)(tbk * 32 + 4 * cb + 2 + kq) * 1024);
    }
    if (w == 0) poll_flags<64>(c, layer, 4, 16);
    BAR();
    STAMP(25);
    constexpr int XD = 10;
    u32x4 xf[28];
#pragma unroll
    for (int s = 0; s < XD; ++s) xf[s] = ld_sc1(c, xsrc + (u32)s * 1024);
    SB();
    f32x16 acc0 = {}, acc1 = {};
#pragma unroll
    for (int s = 0; s < 12; ++s) {
        xf[s + XD] = ld_sc1(c, xsrc + (u32)(s + XD) * 1024);
        acc0 = mfma32(wr[2 * s], xf[s], acc0);
        acc1 = mfma32(wr[2 * s + 1], xf[s], acc1);
        PIN(acc0); PIN(acc1);
        SB();
    }
    WAIT_VM(22);
    BARL();
    STAMP(24);
    {
        const LAS unsigned char* wb[8];
#pragma unroll
        for (int ch = 0; ch < 8; ++ch) wb[ch] = lds_base(c, chunk_lds(CD(6 + ch)) + kq * 4096);
        u32x4 wf[8];
#pragma unroll
        for (int q = 0; q < 8; ++q) wf[q] = LDF(wb[q >> 2], q & 3);
        SB();
#pragma unroll
        for (int s = 0; s < 16; ++s) {
            const u32x4 w0 = wf[(2 * s) & 7], w1 = wf[(2 * s + 1) & 7];
            if (12 + s + XD < 28) xf[12 + s + XD] = ld_sc1(c, xsrc + (u32)(12 + s + XD) * 1024);
            if (2 * s + 8 < 32) {
                wf[(2 * s) & 7] = LDF(wb[(2 * s + 8) >> 2], (2 * s + 8) & 3);
                wf[(2 * s + 1) & 7] = LDF(wb[(2 * s + 9) >> 2], (2 * s + 9) & 3);
            }
            acc0 = mfma32(w0, xf[12 + s], acc0);
            acc1 = mfma32(w1, xf[12 + s], acc1);
            PIN(acc0); PIN(acc1);
            SB();
        }
    }
    BAR();
    STAMP(26);
    red_write(c, acc0, acc1, t2, kq);
    BAR();
    if (w >= 4) {
        if (layer == 0) issue_qkv_tile(c, a, 0);
    } else {
        float v[2][8];
        red_read(c, acc0, acc1, t2, kq, v);
#pragma unroll
        for (int x = 0; x < 2; ++x) {
            const int kbo = 4 * cb + 2 * x + kq;
            const u32x4 o = add_res(v[x], res[x]);
            const auto r0 = __builtin_amdgcn_permlane32_swap(o.x, o.z, false, false);
            const auto r1 = __builtin_amdgcn_permlane32_swap(o.y, o.w, false, false);
            const u32x4 on = u32x4{r0[0], r1[0], r0[1], r1[1]};
            if (layer == 1) {
                *(u32x4*)(a.out + (size_t)(tbk * 32 + r) * DM + kbo * 16 + 8 * h) = on;
            } else {
                st_sc1(c, lbase + (u32)L_X2N + (u32)(tbk * 32 + kbo) * 1024, on);
                st_sc1(c, lbase + (u32)L_X2 + (u32)(tbk * 32 + kbo) * 1024, o);
            }
        }
        STAMP(27);
        if (layer == 0) WAIT_VM(0);
    }
    if (layer == 0) {
        BAR();
        if (w == 0) signal_group(c, layer, 0);
        if (w >= 4) { issue_qkv_tile(c, a, 1); issue_qkv_tile(c, a, 2); }
        STAMP(28);
    }
}

__global__ void __launch_bounds__(512, 2) mega_kernel(Args a) {
    extern __shared__ __attribute__((aligned(16))) unsigned char lds[];
    Ctx c;
    c.L = (LAS unsigned char*)lds;
    c.Laddr = (u32)(size_t)c.L;
    c.ws = a.ws;
    c.rs = __builtin_amdgcn_make_buffer_rsrc((void*)a.ws, 0, (int)WS_END, 0x00020000);
    const int tid = threadIdx.x;
    c.lane = tid & 63;
    c.w = __builtin_amdgcn_readfirstlane(tid >> 6);
    const int bid = blockIdx.x;
    c.i = 2 * (bid & 7) + (bid >> 7);
    c.j = (bid >> 3) & 15;
    c.voff = c.lane * 16;
    {
        const u32 e = __builtin_amdgcn_raw_buffer_load_b32(c.rs, 0, (u32)WS_EPOCH, 16);
        c.tag = 0;
        if (c.w >= 4) { issue_qkv_tile(c, a, 0); issue_qkv_tile(c, a, 1); issue_qkv_tile(c, a, 2); }
        c.tag = (u32)__builtin_amdgcn_readfirstlane((int)e) + 1u;
        if (tid == 0) __builtin_amdgcn_raw_buffer_store_b32(c.tag, c.rs, (u32)bid * 4u, (u32)WS_STARTED, 16);
    }
    if (c.w < 4) {
        const int hh = c.lane >> 5, row = c.lane & 31, half = c.w & 1;
        u32 d[4];
#pragma unroll
        for (int q = 0; q < 4; ++q) {
            const int c0 = 16 * half + 8 * (q >> 1) + 4 * hh + 2 * (q & 1), c1 = c0 + 1;
            const u32 lo = c.w < 2 ? (row > c0 ? 0xff7fu : 0u) : (row == c0 ? 0x3f80u : 0u);
            const u32 hi = c.w < 2 ? (row > c1 ? 0xff7fu : 0u) : (row == c1 ? 0x3f80u : 0u);
            d[q] = lo | (hi << 16);
        }
        lds_st<u32x4>(c.L, SC_MK + c.w * 1024 + c.voff, u32x4{d[0], d[1], d[2], d[3]});
    }
    for (int layer = 0; layer < 2; ++layer) {
        phase_qkv_att(c, layer, a);
        phase_wo(c, layer, a);
        phase_gu(c, layer);
        phase_dn(c, layer, a);
    }
}

}

extern "C" void kernel_launch(void* const* d_in, const int* in_sizes, int n_in, void* d_out, int out_size, void* d_ws, size_t ws_size,
                              hipStream_t stream) {
    Args a;
    a.x = (const u16*)d_in[0]; a.wqkv = (const u16*)d_in[1]; a.wo = (const u16*)d_in[2];
    a.wg = (const u16*)d_in[4]; a.wu = (const u16*)d_in[5]; a.wd = (const u16*)d_in[6];
    a.out = (u16*)d_out; a.ws = (unsigned char*)d_ws;
    (void)hipFuncSetAttribute((const void*)mega_kernel, hipFuncAttributeMaxDynamicSharedMemorySize, LDS_BYTES);
    mega_kernel<<<256, 512, LDS_BYTES, stream>>>(a);
}
```

```cpp
#define PROFILE 1
#include <cstdio>
#include <hip/hip_runtime.h>
#include <hip/hip_bf16.h>

#define LAS __attribute__((address_space(3)))
#define GAS __attribute__((address_space(1)))
typedef unsigned u32;
typedef unsigned short u16;
typedef u32 u32x4 __attribute__((ext_vector_type(4)));
typedef u32 u32x2 __attribute__((ext_vector_type(2)));
typedef float f32x16 __attribute__((ext_vector_type(16)));
typedef float f32x4 __attribute__((ext_vector_type(4)));
typedef float f32x2 __attribute__((ext_vector_type(2)));
typedef __bf16 bf16x8_t __attribute__((ext_vector_type(8)));
typedef __bf16 bf16x2_t __attribute__((ext_vector_type(2)));
typedef short s16x4 __attribute__((ext_vector_type(4)));

namespace {

constexpr int SEQ = 2048, DM = 512, FF = 1792;
constexpr size_t KiB = 1024, MiB = 1024 * 1024;
constexpr size_t WS_FLAGS = 1 * MiB;
constexpr size_t WS_EPOCH = 640 * KiB;
constexpr size_t WS_STARTED = WS_EPOCH + 4096;
constexpr size_t WS_TMO = 768 * KiB;
constexpr size_t WS_WO = 4 * MiB + 512 * KiB;
constexpr size_t WS_WG = 5 * MiB;
constexpr size_t WS_WD = 9 * MiB;
constexpr size_t WS_L0 = 16 * MiB, WS_LSTRIDE = 32 * MiB;
constexpr size_t L_KF = 0, L_VF = 2 * MiB, L_OF = 4 * MiB, L_X1 = 6 * MiB, L_X2 = 8 * MiB, L_HF = 10 * MiB, L_X2N = 18 * MiB;
constexpr size_t WS_END = WS_L0 + 2 * WS_LSTRIDE;

constexpr int NSLOT = 8, SLOT = 16384;
constexpr int LDS_SCR = NSLOT * SLOT;
constexpr int VREC = 1040;
constexpr int SC_RV = LDS_SCR;
constexpr int SC_VS = SC_RV + 16640;
constexpr int SC_ST = SC_VS + 8320;
constexpr int SC_CS = SC_ST + 1024;
constexpr int SC_MK = SC_CS + 1280;
constexpr int LDS_BYTES = SC_MK + 4096;
constexpr int SC_KS = 0 * SLOT;
constexpr int SC_QS = 0 * SLOT + 8192;
constexpr int SC_RK = 1 * SLOT;
static_assert(LDS_BYTES <= 163840, "LDS");
__device__ __forceinline__ int red_off(int q) { return q < 5 ? 2 * SLOT + q * 6144 : 6 * SLOT + (q - 5) * 6144; }

constexpr float LN_EPS = 1e-5f;
constexpr float QSCALE = 0.25f * 1.4426950408889634f;
constexpr u32 SPIN_CAP = 1u << 17;
constexpr u32 BF_ONES = 0x3f803f80u;

struct Args {
    const u16* x; const u16* wqkv; const u16* wo; const u16* wg; const u16* wu; const u16* wd;
    u16* out; unsigned char* ws;
};

__device__ __forceinline__ f32x16 mfma32(u32x4 a, u32x4 b, f32x16 c) {
    return __builtin_amdgcn_mfma_f32_32x32x16_bf16(__builtin_bit_cast(bf16x8_t, a), __builtin_bit_cast(bf16x8_t, b), c, 0, 0, 0);
}
__device__ __forceinline__ u32 pack2(float lo, float hi) {
    f32x2 v = {lo, hi};
    return __builtin_bit_cast(u32, __builtin_convertvector(v, bf16x2_t));
}
__device__ __forceinline__ float bflo(u32 u) { return __builtin_bit_cast(float, u << 16); }
__device__ __forceinline__ float bfhi(u32 u) { return __builtin_bit_cast(float, u & 0xffff0000u); }
__device__ __forceinline__ float dot2(u32 a, u32 b, float c) {
    return __builtin_amdgcn_fdot2_f32_bf16(__builtin_bit_cast(bf16x2_t, a), __builtin_bit_cast(bf16x2_t, b), c, false);
}
__device__ __forceinline__ void swap32(float& a, float& b) {
    asm volatile("s_nop 1\n\tv_permlane32_swap_b32 %0, %1\n\ts_nop 1" : "+v"(a), "+v"(b));
}
__device__ __forceinline__ float fmx(float a, float b) { return __builtin_elementwise_maximum(a, b); }
__device__ __forceinline__ float xor32_max(float v) { float a = v, b = v; swap32(a, b); return fmx(a, b); }
__device__ __forceinline__ float xor32_sum(float v) { float a = v, b = v; swap32(a, b); return a + b; }
#define BAR() do { asm volatile("s_waitcnt lgkmcnt(0)" ::: "memory"); __builtin_amdgcn_s_barrier(); asm volatile("" ::: "memory"); } while (0)
#define BARL() do { asm volatile("" ::: "memory"); __builtin_amdgcn_s_barrier(); asm volatile("" ::: "memory"); } while (0)
#define WAIT_VM(n) asm volatile("s_waitcnt vmcnt(" #n ")" ::: "memory")
#define SB() __builtin_amdgcn_sched_barrier(0)

template <class T> __device__ __forceinline__ T lds_ld(const LAS unsigned char* base, int off) { return *(const LAS T*)(base + off); }
template <class T> __device__ __forceinline__ void lds_st(LAS unsigned char* base, int off, T v) { *(LAS T*)(base + off) = v; }

__device__ __forceinline__ void glds4k(const unsigned char* src, u32 voff, u32 dst) {
    u32 keep;
    asm volatile(
        "s_mov_b32 %0, m0\n\t"
        "s_mov_b32 m0, %3\n\t"
        "s_nop 0\n\t"
        "global_load_lds_dwordx4 %1, %2 sc1\n\t"
        "global_load_lds_dwordx4 %1, %2 offset:1024 sc1\n\t"
        "global_load_lds_dwordx4 %1, %2 offset:2048 sc1\n\t"
        "global_load_lds_dwordx4 %1, %2 offset:3072 sc1\n\t"
        "s_mov_b32 m0, %0"
        : "=&s"(keep) : "v"(voff), "s"(src), "s"(dst) : "memory");
}

__device__ __forceinline__ void glds1k(const unsigned char* src, u32 voff, u32 dst) {
    u32 keep;
    asm volatile(
        "s_mov_b32 %0, m0\n\t"
        "s_mov_b32 m0, %3\n\t"
        "s_nop 0\n\t"
        "global_load_lds_dwordx4 %1, %2\n\t"
        "s_mov_b32 m0, %0"
        : "=&s"(keep) : "v"(voff), "s"(src), "s"(dst) : "memory");
}
#define PIN(x) asm volatile("" : "+v"(x))

constexpr int NF_WO = 8 * 64, NF_WG = 16 * 224, NF_WD = 8 * 224;
__host__ __device__ constexpr int qkv_col(int t) { return t == 0 ? 512 : (t == 1 ? 1024 : 0); }
struct WItem { const u16* src; size_t dst; };
__device__ __forceinline__ WItem w_item(const Args& a, int f, int lane) {
    const u16 *pA, *pB; int ld, ks; size_t dst;
    if (f < NF_WO) {
        const int cb = f >> 6, rem = f & 63, c = rem >> 4, r2 = rem & 15, kq = r2 >> 2, ks2 = (r2 >> 1) & 1, nt = r2 & 1;
        ks = 8 * kq + 2 * c + ks2; pA = a.wo + 64 * cb + 32 * nt; pB = pA + 16; ld = 512; dst = WS_WO + (size_t)f * 1024;
    } else if ((f -= NF_WO) < NF_WG) {
        const int j = f / 224, rem = f % 224, q = rem >> 4, ksl = rem & 15, c = q >> 1, hf = q & 1;
        ks = 16 * hf + ksl; pA = a.wg + 112 * j + 16 * c; pB = a.wu + 112 * j + 16 * c; ld = FF; dst = WS_WG + (size_t)f * 1024;
    } else {
        f -= NF_WG;
        const int cb = f / 224, rem = f % 224, c = rem >> 4, r2 = rem & 15, kq = r2 >> 2, ks2 = (r2 >> 1) & 1, nt = r2 & 1;
        ks = 28 * kq + 2 * c + ks2; pA = a.wd + 64 * cb + 32 * nt; pB = pA + 16; ld = 512; dst = WS_WD + (size_t)f * 1024;
    }
    const int k = lane >> 2, n8 = lane & 3;
    WItem it;
    it.src = (n8 < 2 ? pA + n8 * 8 : pB + (n8 - 2) * 8) + (size_t)(16 * ks + k) * ld;
    it.dst = dst;
    return it;
}

constexpr size_t WS_PROF = 12 * MiB;
#define STAMP(k) do { if (c.w == 0) { const unsigned long long t_ = __builtin_amdgcn_s_memrealtime(), y_ = __builtin_amdgcn_s_memtime(); \
    const u32 o_ = (u32)WS_PROF + (u32)(blockIdx.x * 2 * 64 + layer * 32 + (k)) * 8u; \
    if (c.lane == 0) { __builtin_amdgcn_raw_buffer_store_b64(u32x2{(u32)t_, (u32)(t_ >> 32)}, c.rs, 0, o_, 0); \
                       __builtin_amdgcn_raw_buffer_store_b64(u32x2{(u32)y_, (u32)(y_ >> 32)}, c.rs, 0, o_ + (u32)MiB, 0); } } } while (0)
struct Ctx {
    LAS unsigned char* L;
    u32 Laddr;
    unsigned char* ws;
    __amdgpu_buffer_rsrc_t rs;
    int lane, w, i, j;
    u32 voff;
    u32 tag;
};

__device__ __forceinline__ u32x4 ld_sc1(const Ctx& c, u32 soff) { return __builtin_amdgcn_raw_buffer_load_b128(c.rs, c.voff, soff, 16); }
__device__ __forceinline__ void st_sc1(const Ctx& c, u32 soff, u32x4 v) { __builtin_amdgcn_raw_buffer_store_b128(v, c.rs, c.voff, soff, 16); }
__device__ __forceinline__ Ctx fresh(const Ctx& c0) {
    Ctx c = c0;
    asm volatile("" : "+s"(c.i), "+s"(c.j), "+s"(c.w), "+s"(c.tag));
    asm volatile("" : "+v"(c.lane));
    c.voff = c.lane * 16;
    return c;
}

constexpr int CQ(int t, int hf) { return 2 * t + hf; }
constexpr int CO(int k) { return 6 + k; }
constexpr int CG(int t, int hf) { return 10 + 2 * t + hf; }
constexpr int CD(int k) { return 24 + k; }
constexpr int slot_of(int n) {
    constexpr int tab[38] = {
          0, 1,   2, 3,   4, 5,
          6, 7, 2, 3,
          4, 5,   0, 1,   2, 3,   6, 7,   4, 5,   0, 1,   2, 3,
          6, 7, 4, 5, 0, 1, 2, 3,   6, 7, 4, 5, 0, 1};
    return tab[n];
}
template <int N> __device__ __forceinline__ const unsigned char* chunk_src(const Ctx& c) {
    size_t src;
    const int cb = c.j & 7;
    if (N < 10) src = WS_WO + (size_t)(cb * 4 + (N - 6)) * SLOT;
    else if (N < 24) src = WS_WG + (size_t)(c.j * 14 + (N - 10)) * SLOT;
    else src = WS_WD + (size_t)(cb * 14 + (N - 24)) * SLOT;
    return c.ws + src + (c.w & 3) * 4096;
}
constexpr int chunk_lds(int n) { return slot_of(n) * SLOT; }
template <int N> __device__ __forceinline__ void issue_chunk(const Ctx& c) {
    glds4k(chunk_src<N>(c), c.voff, c.Laddr + chunk_lds(N) + (c.w & 3) * 4096);
}
__device__ __forceinline__ const LAS unsigned char* lds_base(const Ctx& c, int off) {
    u32 a = c.Laddr + off + c.voff;
    asm volatile("" : "+v"(a));
    return (const LAS unsigned char*)(size_t)a;
}

__device__ __forceinline__ u32 flag_off(int layer, int seam, int ci, int cj) {
    return (u32)(WS_FLAGS + (size_t)(((layer * 8 + seam) * 256) + ci * 16 + cj) * 512);
}
__device__ __forceinline__ u32 flag_tag(const Ctx& c, int layer, int seam) { return c.tag + (u32)(layer * 8 + seam); }
__device__ __forceinline__ void flag_store(const Ctx& c, int layer, int seam, int ci, int cj, int word) {
    __builtin_amdgcn_raw_buffer_store_b32(flag_tag(c, layer, seam), c.rs, flag_off(layer, seam, ci, cj) + word * 4, 0, 16);
}
__device__ __forceinline__ void signal_group(const Ctx& c, int layer, int seam) {
    if (c.lane < 16) flag_store(c, layer, seam, c.i, c.lane, c.j);
}
template <int NWORDS> __device__ __forceinline__ void poll_flags(const Ctx& c, int layer, int seam, int nwords = NWORDS) {
    const u32 off = flag_off(layer, seam, c.i, c.j) + c.lane * 4;
    const u32 tag = flag_tag(c, layer, seam);
    for (u32 spins = 0;; ++spins) {
        asm volatile("" ::: "memory");
        const u32 f0 = __builtin_amdgcn_raw_buffer_load_b32(c.rs, off, 0, 16);
        bool ok = c.lane >= nwords || f0 == tag;
        if (NWORDS > 64) { const u32 f1 = __builtin_amdgcn_raw_buffer_load_b32(c.rs, off + 256, 0, 16); ok = ok && f1 == tag; }
        if (__all(ok)) break;
        if (spins > SPIN_CAP) { if (c.lane == 0) __builtin_amdgcn_raw_buffer_store_b32(1u, c.rs, (u32)WS_TMO, 0, 16); break; }
        __builtin_amdgcn_s_sleep(1);
    }
}

__device__ __forceinline__ int crow(int r, int h) { return (r & 3) + 8 * (r >> 2) + 4 * h; }

#define LDF(p, k) (*(const LAS u32x4*)((p) + (k) * 1024))
__device__ __forceinline__ u32x4 ldf_tr(const LAS unsigned char* p, int k) {
    const u32x2 lo = __builtin_bit_cast(u32x2, __builtin_amdgcn_ds_read_tr16_b64_v4i16((LAS s16x4*)(p + k * 1024)));
    const u32x2 hi = __builtin_bit_cast(u32x2, __builtin_amdgcn_ds_read_tr16_b64_v4i16((LAS s16x4*)(p + k * 1024 + 256)));
    return u32x4{lo.x, lo.y, hi.x, hi.y};
}
template <bool TR> __device__ __forceinline__ u32x4 ldf(const LAS unsigned char* p, int k) { if constexpr (TR) return ldf_tr(p, k); else return LDF(p, k); }

template <bool SWAP, bool PRE, bool TR, class F>
__device__ __forceinline__ void c_tile(f32x16& acc, u32x4 (&wf)[8], const LAS unsigned char* lo, const LAS unsigned char* hi, const LAS unsigned char* nxt,
                                       const u32x4 (&xf)[32], F&& filler) {
#pragma unroll
    for (int k = 0; k < 32; ++k) {
        const u32x4 wv = wf[k & 7];
        if (k + 8 < 16) wf[k & 7] = ldf<TR>(lo, k + 8);
        else if (k + 8 < 32) wf[k & 7] = ldf<TR>(hi, k + 8 - 16);
        else if (PRE) wf[k & 7] = ldf<TR>(nxt, k + 8 - 32);
        acc = SWAP ? mfma32(xf[k], wv, acc) : mfma32(wv, xf[k], acc);
        PIN(acc);
        filler(k);
        SB();
    }
}
template <bool PRE, bool TR, bool STAGE>
__device__ __forceinline__ f32x2 c_tile0(const Ctx& c, f32x16& acc, u32x4 (&wf)[8], const LAS unsigned char* lo, const LAS unsigned char* hi,
                                         const LAS unsigned char* nxt, u32x4 (&xf)[32], __amdgpu_buffer_rsrc_t xrs, u32 xsoff) {
    const int lane = c.lane;
    u32x4 raw[STAGE ? 32 : 1];
    const u32 rvoff = (u32)(lane >> 3) * 1024u + (u32)(lane & 7) * 16u;
    LAS unsigned char* st = c.L + 6 * SLOT + (c.w & 3) * 8192;
    const int wr_off = (lane >> 3) * 128 + (((lane & 7) ^ ((lane >> 3) & 7)) * 16);
    const int rd_row = (lane & 31) * 128, rd_sw = lane & 7, rd_h = lane >> 5;
#define XLD(k) __builtin_amdgcn_raw_buffer_load_b128(xrs, c.voff, xsoff + (u32)(k) * 1024u, 16)
#define RLD(q) __builtin_amdgcn_raw_buffer_load_b128(xrs, rvoff, xsoff + (u32)((q) & 3) * 8192u + (u32)((q) >> 2) * 128u, 0)
#define STG(g) do { _Pragma("unroll") for (int q = 0; q < 4; ++q) lds_st<u32x4>(st + ((g) & 1) * 4096, q * 1024 + wr_off, raw[4 * (g) + q]); \
                    _Pragma("unroll") for (int q = 0; q < 4; ++q) xf[4 * (g) + q] = lds_ld<u32x4>(st + ((g) & 1) * 4096, rd_row + (((2 * q + rd_h) ^ rd_sw) * 16)); } while (0)
    if (STAGE) {
#pragma unroll
        for (int q = 0; q < 16; ++q) raw[q] = RLD(q);
        SB();
        STG(0);
    } else {
#pragma unroll
        for (int k = 0; k < 14; ++k) xf[k] = XLD(k);
    }
    SB();
    float s1[4] = {0.f, 0.f, 0.f, 0.f}, s2[4] = {0.f, 0.f, 0.f, 0.f};
#pragma unroll
    for (int k = 0; k < 32; ++k) {
        if (STAGE) {
            if ((k & 3) == 0) {
                const int g = k >> 2;
                if (4 * (g + 4) < 32) {
#pragma unroll
                    for (int q = 0; q < 4; ++q) raw[4 * (g + 4) + q] = RLD(4 * (g + 4) + q);
                }
                if (g + 1 < 8) STG(g + 1);
            }
        } else {
            if (k + 14 < 32) xf[k + 14] = XLD(k + 14);
        }
        const u32x4 wv = wf[k & 7];
        if (k + 8 < 16) wf[k & 7] = ldf<TR>(lo, k + 8);
        else if (k + 8 < 32) wf[k & 7] = ldf<TR>(hi, k + 8 - 16);
        else if (PRE) wf[k & 7] = ldf<TR>(nxt, k + 8 - 32);
        acc = mfma32(wv, xf[k], acc);
        PIN(acc);
        s1[0] = dot2(xf[k].x, BF_ONES, s1[0]); s2[0] = dot2(xf[k].x, xf[k].x, s2[0]);
        s1[1] = dot2(xf[k].y, BF_ONES, s1[1]); s2[1] = dot2(xf[k].y, xf[k].y, s2[1]);
        s1[2] = dot2(xf[k].z, BF_ONES, s1[2]); s2[2] = dot2(xf[k].z, xf[k].z, s2[2]);
        s1[3] = dot2(xf[k].w, BF_ONES, s1[3]); s2[3] = dot2(xf[k].w, xf[k].w, s2[3]);
        asm volatile("" : "+v"(s1[0]), "+v"(s1[1]), "+v"(s1[2]), "+v"(s1[3]), "+v"(s2[0]), "+v"(s2[1]), "+v"(s2[2]), "+v"(s2[3]));
        SB();
    }
    const float a = xor32_sum((s1[0] + s1[1]) + (s1[2] + s1[3])), b = xor32_sum((s2[0] + s2[1]) + (s2[2] + s2[3]));
    const float mean = a * (1.0f / DM);
    float var = b * (1.0f / DM) - mean * mean;
    var = var > 0.f ? var : 0.f;
    f32x2 o; o.x = mean; o.y = __builtin_amdgcn_rsqf(var + LN_EPS);
    return o;
#undef XLD
#undef RLD
#undef STG
}

struct GS { float g0, g1, t0, t1, u0, u1; };
struct GK { float mean, A, B, ir2; };
__device__ __forceinline__ void geglu_sub(GS& s, u32& o, int q, float ag0, float ag1, float au0, float au1, float cg0, float cg1, float cu0, float cu1, const GK& k) {
    if (q == 0) asm volatile("v_fma_f32 %0, -%7, %5, %3\n\tv_fma_f32 %1, -%7, %6, %4\n\tv_mul_f32 %2, %0, %0"
                             : "=&v"(s.g0), "=&v"(s.g1), "=&v"(s.t0) : "v"(ag0), "v"(ag1), "v"(cg0), "v"(cg1), "v"(k.mean));
    if (q == 1) asm volatile("v_mul_f32 %1, %2, %2\n\tv_fma_f32 %0, %0, %4, %3\n\tv_fma_f32 %1, %1, %4, %3"
                             : "+v"(s.t0), "=&v"(s.t1) : "v"(s.g1), "v"(k.A), "v"(k.B));
    if (q == 2) asm volatile("v_mul_f32 %0, %2, %0\n\tv_mul_f32 %1, %3, %1\n\tv_exp_f32 %0, %0"
                             : "+v"(s.t0), "+v"(s.t1) : "v"(s.g0), "v"(s.g1));
    if (q == 3) asm volatile("v_exp_f32 %0, %0\n\tv_fma_f32 %1, -%7, %5, %3\n\tv_fma_f32 %2, -%7, %6, %4"
                             : "+v"(s.t1), "=&v"(s.u0), "=&v"(s.u1) : "v"(au0), "v"(au1), "v"(cu0), "v"(cu1), "v"(k.mean));
    if (q == 4) asm volatile("v_fma_f32 %0, %0, %4, %4\n\tv_mul_f32 %2, %2, %3\n\tv_fma_f32 %1, %1, %4, %4"
                             : "+v"(s.t0), "+v"(s.t1), "+v"(s.g0) : "v"(s.u0), "v"(k.ir2));
    if (q == 5) asm volatile("v_rcp_f32 %0, %0\n\tv_mul_f32 %2, %2, %3\n\tv_rcp_f32 %1, %1"
                             : "+v"(s.t0), "+v"(s.t1), "+v"(s.g1) : "v"(s.u1));
    if (q == 6) asm volatile("v_mul_f32 %1, %1, %3\n\tv_mul_f32 %2, %2, %4\n\tv_cvt_pk_bf16_f32 %0, %1, %2"
                             : "=&v"(o), "+v"(s.g0), "+v"(s.g1) : "v"(s.t0), "v"(s.t1));
}
struct LS { float t0, t1; };
__device__ __forceinline__ void lnq_a(LS& s, float a0, float c0, float c1, float sc, float msc) {
    asm volatile("v_mul_f32 %0, %6, %3\n\tv_mul_f32 %1, %6, %4\n\tv_fma_f32 %0, %2, %5, -%0"
                 : "=&v"(s.t0), "=&v"(s.t1) : "v"(a0), "v"(c0), "v"(c1), "v"(sc), "v"(msc));
}
__device__ __forceinline__ u32 lnq_b(LS& s, float a1, float sc) {
    u32 o;
    asm volatile("v_fma_f32 %1, %3, %4, -%1\n\tv_cvt_pk_bf16_f32 %0, %2, %1" : "=v"(o), "+v"(s.t1) : "v"(s.t0), "v"(a1), "v"(sc));
    return o;
}
__device__ __forceinline__ void lnv_a(LS& s, float a0, float a1, float m0, float m1, float r0, float cs) {
    asm volatile("v_fma_f32 %0, -%4, %7, %2\n\tv_fma_f32 %1, -%5, %7, %3\n\tv_mul_f32 %0, %0, %6"
                 : "=&v"(s.t0), "=&v"(s.t1) : "v"(a0), "v"(a1), "v"(m0), "v"(m1), "v"(r0), "v"(cs));
}
__device__ __forceinline__ u32 lnv_b(LS& s, float r1) {
    u32 o;
    asm volatile("v_mul_f32 %1, %1, %3\n\tv_cvt_pk_bf16_f32 %0, %2, %1" : "=v"(o), "+v"(s.t1) : "v"(s.t0), "v"(r1));
    return o;
}
__device__ __forceinline__ u32x4 pack8(const float* v) { return u32x4{pack2(v[0], v[1]), pack2(v[2], v[3]), pack2(v[4], v[5]), pack2(v[6], v[7])}; }

__device__ __forceinline__ float max16(const f32x16& s, float m) {
    float a = fmx(fmx(s[0], s[1]), m), b = fmx(s[2], s[3]);
    a = fmx(fmx(s[4], s[5]), a); b = fmx(fmx(s[6], s[7]), b);
    a = fmx(fmx(s[8], s[9]), a); b = fmx(fmx(s[10], s[11]), b);
    a = fmx(fmx(s[12], s[13]), a); b = fmx(fmx(s[14], s[15]), b);
    return fmx(a, b);
}
__device__ __forceinline__ float smul(float a, float b) { float r; asm("v_mul_f32 %0, %1, %2" : "=v"(r) : "v"(a), "v"(b)); return r; }
struct MaskF { u32x4 alo, ahi, ilo, ihi; };
__device__ __forceinline__ f32x16 add_mask(const MaskF& mf, f32x16 s) { s = mfma32(mf.alo, mf.ilo, s); return mfma32(mf.ahi, mf.ihi, s); }
template <int NB, int FIRST, int REST0, int MASK, bool FIRSTSEG>
__device__ __forceinline__ void att_segment(f32x16& oacc, float& m, const LAS unsigned char* kb, const LAS unsigned char* vb, const LAS unsigned char* mk, const u32x4& qf) {
    u32x4 kf[NB];
#pragma unroll
    for (int t = 0; t < NB; ++t) { const int b = t == 0 ? FIRST : REST0 + t - 1; kf[t] = *(const LAS u32x4*)(kb + b * 2048); }
    MaskF mf;
    if (MASK) {
        mf.alo = LDF(mk, 0); mf.ahi = LDF(mk, 1); mf.ilo = LDF(mk, 2); mf.ihi = LDF(mk, 3);
        if (MASK == 2) { mf.alo ^= 0xff7fff7fu; mf.ahi ^= 0xff7fff7fu; }
    }
    SB();
    float mx = FIRSTSEG ? -__builtin_inff() : m;
    f32x16 sv[4];
    sv[0] = mfma32(kf[0], qf, f32x16{});
    if (NB > 1) sv[1] = mfma32(kf[1], qf, f32x16{});
    if (MASK) sv[0] = add_mask(mf, sv[0]);
    SB();
#pragma unroll
    for (int t = 0; t < NB; ++t) {
        const int u = NB > 1 ? (t == 0 ? 1 : t == 1 ? 0 : t) : 0;
        if (t + 2 < NB) sv[(t + 2) & 3] = mfma32(kf[t + 2], qf, f32x16{});
        mx = max16(sv[u & 3], mx);
        SB();
    }
    mx = xor32_max(mx);
    f32x16 negm;
#pragma unroll
    for (int e = 0; e < 16; ++e) negm[e] = -mx;
    SB();
    sv[0] = mfma32(kf[0], qf, negm);
    if (MASK) sv[0] = add_mask(mf, sv[0]);
    u32x4 v0 = *(const LAS u32x4*)(vb + (FIRST * 2 + 0) * VREC);
    u32x4 v1 = *(const LAS u32x4*)(vb + (FIRST * 2 + 1) * VREC);
    if (NB > 1) sv[1] = mfma32(kf[1], qf, negm);
    if (!FIRSTSEG) {
        float f = __builtin_amdgcn_exp2f(m - mx);
        asm volatile("s_nop 1" : "+v"(f));
#pragma unroll
        for (int e = 0; e < 16; ++e) oacc[e] = smul(oacc[e], f);
    }
    m = mx;
    SB();
#pragma unroll
    for (int t = 0; t < NB; ++t) {
        u32x4 n0, n1;
        if (t + 1 < NB) {
            if (t > 0) sv[(t + 1) & 1] = mfma32(kf[t + 1], qf, negm);
            n0 = *(const LAS u32x4*)(vb + ((REST0 + t) * 2 + 0) * VREC);
            n1 = *(const LAS u32x4*)(vb + ((REST0 + t) * 2 + 1) * VREC);
        }
        SB();
        const f32x16& s = sv[t & 1];
        u32 p[8];
#pragma unroll
        for (int e = 0; e < 4; ++e) p[e] = pack2(__builtin_amdgcn_exp2f(s[2 * e]), __builtin_amdgcn_exp2f(s[2 * e + 1]));
        SB();
        oacc = mfma32(v0, u32x4{p[0], p[1], p[2], p[3]}, oacc);
#pragma unroll
        for (int e = 4; e < 8; ++e) p[e] = pack2(__builtin_amdgcn_exp2f(s[2 * e]), __builtin_amdgcn_exp2f(s[2 * e + 1]));
        SB();
        oacc = mfma32(v1, u32x4{p[4], p[5], p[6], p[7]}, oacc);
        if (t + 1 < NB) { v0 = n0; v1 = n1; }
        SB();
    }
}

__device__ __forceinline__ void issue_qkv_tile(const Ctx& c, const Args& a, int t) {
    const u32 voff = (u32)(c.lane >> 2) * 3072u + (u32)(c.lane & 3) * 16u;
    const unsigned char* src = (const unsigned char*)(a.wqkv + qkv_col(t) + 32 * c.j) + (size_t)(c.w & 3) * 8 * 16 * 3072;
    const u32 dst = c.Laddr + slot_of(CQ(t, 0)) * SLOT + (c.w & 3) * 8192;
#pragma unroll
    for (int q = 0; q < 8; ++q) glds1k(src + (size_t)q * 16 * 3072, voff, dst + q * 1024);
}
template <bool TR> __device__ __forceinline__ void tile_colsum(const Ctx& c, const LAS unsigned char* lo, const LAS unsigned char* hi, int csidx) {
    float s[4] = {0.f, 0.f, 0.f, 0.f};
#pragma unroll
    for (int hf = 0; hf < 2; ++hf) {
        u32x4 f[16];
#pragma unroll
        for (int k = 0; k < 16; ++k) f[k] = ldf<TR>(hf ? hi : lo, k);
        SB();
#pragma unroll
        for (int k = 0; k < 16; ++k) {
            s[0] = dot2(f[k].x, BF_ONES, s[0]); s[1] = dot2(f[k].y, BF_ONES, s[1]); s[2] = dot2(f[k].z, BF_ONES, s[2]); s[3] = dot2(f[k].w, BF_ONES, s[3]);
        }
        SB();
    }
    const float t = xor32_sum((s[0] + s[1]) + (s[2] + s[3]));
    if (c.lane < 32) lds_st<float>(c.L, SC_CS + (csidx + c.lane) * 4, t);
}
__device__ __forceinline__ int tr_lane_off(int lane) {
    const int g4 = lane >> 4, h = g4 >> 1, nh = g4 & 1, li = lane & 15;
    return (8 * h + (li >> 2)) * 64 + nh * 32 + (li & 3) * 8;
}
__device__ __forceinline__ const LAS unsigned char* lds_base_raw(const Ctx& c, int off) {
    u32 a = c.Laddr + off;
    asm volatile("" : "+v"(a));
    return (const LAS unsigned char*)(size_t)a;
}

__device__ __forceinline__ void phase_qkv_att(const Ctx& c0, int layer, const Args& a) {
    const Ctx c = fresh(c0);
    const int lane = c.lane, w = c.w, tb = w & 3, h = lane >> 5, r = lane & 31;
    const u32 lbase = (u32)(WS_L0 + (size_t)layer * WS_LSTRIDE);
    const int tro = tr_lane_off(lane);
    STAMP(0);
    if (w < 4) {
        if (layer > 0 && w == 0) poll_flags<64>(c, layer - 1, 0, 16);
        BAR();
        STAMP(1);
        u32x4 xf[32], wf[8];
        const LAS unsigned char* wK = lds_base_raw(c, slot_of(CQ(0, 0)) * SLOT + tro);
        const LAS unsigned char* wV = lds_base_raw(c, slot_of(CQ(1, 0)) * SLOT + tro);
        const LAS unsigned char* wQ = lds_base_raw(c, slot_of(CQ(2, 0)) * SLOT + tro);
#pragma unroll
        for (int k = 0; k < 8; ++k) wf[k] = ldf_tr(wK, k);
        f32x16 accK = {}, accV = {}, accQ = {};
        f32x2 ms;
        if (layer == 0) ms = c_tile0<true, true, true>(c, accK, wf, wK, wK + SLOT, wV, xf, __builtin_amdgcn_make_buffer_rsrc((void*)a.x, 0, SEQ * DM * 2, 0x00020000),
                                                        (u32)((4 * c.i + tb) * 32 * 1024));
        else ms = c_tile0<true, true, false>(c, accK, wf, wK, wK + SLOT, wV, xf, c.rs, (u32)(WS_L0 + L_X2N) + (u32)((4 * c.i + tb) * 32) * 1024);
        if (lane < 32) lds_st<f32x2>(c.L, SC_ST + (tb * 32 + r) * 8, ms);
        const float msk = ms.x * ms.y;
        BARL(); STAMP(2);
        f32x4 cs[4];
#pragma unroll
        for (int q = 0; q < 4; ++q) cs[q] = lds_ld<f32x4>(c.L, SC_CS + (0 * 32 + 8 * q + 4 * h) * 4);
        f32x4 mr[8];
        float csv;
        {
            u32 o[8]; LS ls;
            c_tile<true, true, true>(accV, wf, wV, wV + SLOT, wQ, xf, [&](int k) {
                if (k >= 4 && k < 20) {
                    const int pi = (k - 4) >> 1, e = 2 * pi;
                    if (((k - 4) & 1) == 0) lnq_a(ls, accK[e], cs[e >> 2][e & 3], cs[e >> 2][(e & 3) + 1], ms.y, msk);
                    else o[pi] = lnq_b(ls, accK[e + 1], ms.y);
                }
                if (k == 20 || k == 21) {
                    const int hh = k - 20;
                    const u32x4 ov = {o[4 * hh], o[4 * hh + 1], o[4 * hh + 2], o[4 * hh + 3]};
                    lds_st<u32x4>(c.L, SC_KS + (tb * 2 + hh) * 1024 + c.voff, ov);
                    st_sc1(c, lbase + (u32)L_KF + (u32)((4 * c.i + tb) * 32 + 2 * c.j + hh) * 1024, ov);
                }
                if (k >= 22 && k < 30) { const int q = (k - 22) >> 1; mr[k - 22] = lds_ld<f32x4>(c.L, SC_ST + (tb * 32 + 8 * q + 4 * h + 2 * ((k - 22) & 1)) * 8); }
                if (k == 30) csv = lds_ld<float>(c.L, SC_CS + (32 + r) * 4);
            });
        }
        BARL();
        {
            u32 o[8]; LS ls;
            c_tile<false, false, true>(accQ, wf, wQ, wQ + SLOT, wQ, xf, [&](int k) {
                if (k >= 2 && k < 18) {
                    const int pi = (k - 2) >> 1;
                    if (((k - 2) & 1) == 0) lnv_a(ls, accV[2 * pi], accV[2 * pi + 1], mr[pi].x, mr[pi].z, mr[pi].y, csv);
                    else o[pi] = lnv_b(ls, mr[pi].w);
                }
                if (k == 18 || k == 20) {
                    const int sv = (k - 18) >> 1;
                    const u32x4 ov = {o[4 * sv], o[4 * sv + 1], o[4 * sv + 2], o[4 * sv + 3]};
                    lds_st<u32x4>(c.L, SC_VS + (tb * 2 + sv) * VREC + c.voff, ov);
                    st_sc1(c, lbase + (u32)L_VF + (u32)(((4 * c.i + tb) * 16 + c.j) * 2 + sv) * 1024, ov);
                }
                if (k >= 24 && k < 28) cs[k - 24] = lds_ld<f32x4>(c.L, SC_CS + (64 + 8 * (k - 24) + 4 * h) * 4);
            });
            if (lane < 2) { u32x4 ones = {BF_ONES, BF_ONES, BF_ONES, BF_ONES}; lds_st<u32x4>(c.L, SC_VS + (tb * 2 + lane) * VREC + 1024, ones); }
        }
        {
            const float sc = ms.y * QSCALE, msc = ms.x * sc;
            asm volatile("s_nop 15" : "+v"(accQ));
            u32 o[8]; LS ls[8];
#pragma unroll
            for (int pi = 0; pi < 8; ++pi) { const int e = 2 * pi; lnq_a(ls[pi], accQ[e], cs[e >> 2][e & 3], cs[e >> 2][(e & 3) + 1], sc, msc); }
#pragma unroll
            for (int pi = 0; pi < 8; ++pi) o[pi] = lnq_b(ls[pi], accQ[2 * pi + 1], sc);
#pragma unroll
            for (int hh = 0; hh < 2; ++hh) lds_st<u32x4>(c.L, SC_QS + (tb * 2 + hh) * 1024 + c.voff, u32x4{o[4 * hh], o[4 * hh + 1], o[4 * hh + 2], o[4 * hh + 3]});
        }
        WAIT_VM(0);
        BAR(); STAMP(5);
        if (w == 0 && lane < 2 && c.i + 1 + lane < 16) flag_store(c, layer, 1, c.i + 1 + lane, c.j, lane);
    } else {
        WAIT_VM(8);
        BAR();
        const int cb = c.j & 7, sidx = c.i * 2 + (c.j >> 3);
        if (layer == 0) {
            LAS unsigned char* t = c.L + SC_VS + (w & 3) * 1024;
            WItem it[6]; u32x4 v[6];
#pragma unroll
            for (int q = 0; q < 6; ++q) {
                const int n = (w & 3) + 4 * q;
                int f;
                if (n < 14) { const int ks = 2 * c.i + (n & 1), cc = n >> 1; f = NF_WO + c.j * 224 + (2 * cc + (ks >> 4)) * 16 + (ks & 15); }
                else if (n < 21) f = NF_WO + NF_WG + cb * 224 + sidx * 7 + (n - 14);
                else f = cb * 64 + sidx * 2 + ((n - 21) & 1);
                it[q] = w_item(a, f, lane);
                v[q] = *(const u32x4*)it[q].src;
            }
            const int k = lane >> 2, n8 = lane & 3;
            const int g4 = lane >> 4, hh = g4 >> 1, nh = g4 & 1, li = lane & 15, qq = li >> 2, p = li & 3;
#pragma unroll
            for (int q = 0; q < 6; ++q) {
                if ((w & 3) + 4 * q < 23) {
                    lds_st<u32x4>(t, k * 64 + n8 * 16, v[q]);
                    asm volatile("s_waitcnt lgkmcnt(0)" ::: "memory");
                    const s16x4 lo = __builtin_amdgcn_ds_read_tr16_b64_v4i16((LAS s16x4*)(t + (4 * hh + qq) * 64 + nh * 32 + p * 8));
                    const s16x4 hi = __builtin_amdgcn_ds_read_tr16_b64_v4i16((LAS s16x4*)(t + (8 + 4 * hh + qq) * 64 + nh * 32 + p * 8));
                    const u32x2 l2 = __builtin_bit_cast(u32x2, lo), h2 = __builtin_bit_cast(u32x2, hi);
                    __builtin_amdgcn_raw_buffer_store_b128(u32x4{l2.x, l2.y, h2.x, h2.y}, c.rs, c.voff, (u32)it[q].dst, 16);
                    asm volatile("s_waitcnt lgkmcnt(0)" ::: "memory");
                }
            }
        }
        WAIT_VM(0);
        if (layer == 0 && w < 7) {
            const int t = w - 4;
            const LAS unsigned char* wb = lds_base_raw(c, (t == 0 ? slot_of(CQ(0, 0)) : t == 1 ? slot_of(CQ(1, 0)) : slot_of(CQ(2, 0))) * SLOT + tro);
            tile_colsum<true>(c, wb, wb + SLOT, 32 * t);
        }
        BAR();
        if (layer == 0 && w == 4) {
            if (lane < 16) flag_store(c, 0, 5, lane, c.j, c.i);
            if (lane < 32) flag_store(c, 0, 6, lane >> 1, cb + 8 * (lane & 1), sidx);
        }
        BAR();
        if (layer == 0) poll_flags<64>(c, 0, 6, 32);
        issue_chunk<CO(0)>(c); issue_chunk<CO(1)>(c); issue_chunk<CO(2)>(c); issue_chunk<CO(3)>(c);
        BAR();
        if (layer == 0) poll_flags<64>(c, 0, 5, 16);
        issue_chunk<CG(0, 0)>(c); issue_chunk<CG(0, 1)>(c);
    }
    const int qb = w < 4 ? w : 7 - w, hh = w >> 2;
    const u32x4 qf = lds_ld<u32x4>(c.L, SC_QS + (qb * 2 + hh) * 1024 + c.voff);
    const int vlane = (((lane >> 4) & 1) == hh) ? lane * 16 : 1024;
    f32x16 oacc = {};
    float m = 0.f;
    const LAS unsigned char* mk = lds_base(c, SC_MK);
    {
        const LAS unsigned char* kb = c.L + SC_KS + hh * 1024 + c.voff;
        const LAS unsigned char* vb = c.L + SC_VS + vlane;
        if (qb == 0) att_segment<1, 0, 0, 1, true>(oacc, m, kb, vb, mk, qf);
        else if (qb == 1) att_segment<2, 1, 0, 1, true>(oacc, m, kb, vb, mk, qf);
        else if (qb == 2) att_segment<3, 2, 0, 1, true>(oacc, m, kb, vb, mk, qf);
        else att_segment<4, 3, 0, 1, true>(oacc, m, kb, vb, mk, qf);
    }
    STAMP(6);
    if (c.i > 0) {
        if (w == 0) poll_flags<64>(c, layer, 1, c.i >= 2 ? 2 : 1);
        BAR();
        STAMP(7);
        {
            const int idx = w;
            const int g = 4 * c.i - 8 + idx;
            if (g >= 0) {
                const u32x4 s0 = ld_sc1(c, lbase + (u32)L_KF + (u32)(g * 32 + 2 * c.j + 0) * 1024);
                const u32x4 s1 = ld_sc1(c, lbase + (u32)L_KF + (u32)(g * 32 + 2 * c.j + 1) * 1024);
                const u32x4 s2 = ld_sc1(c, lbase + (u32)L_VF + (u32)((g * 16 + c.j) * 2 + 0) * 1024);
                const u32x4 s3 = ld_sc1(c, lbase + (u32)L_VF + (u32)((g * 16 + c.j) * 2 + 1) * 1024);
                lds_st<u32x4>(c.L, SC_RK + (idx * 2 + 0) * 1024 + c.voff, s0);
                lds_st<u32x4>(c.L, SC_RK + (idx * 2 + 1) * 1024 + c.voff, s1);
                lds_st<u32x4>(c.L, SC_RV + (idx * 2 + 0) * VREC + c.voff, s2);
                lds_st<u32x4>(c.L, SC_RV + (idx * 2 + 1) * VREC + c.voff, s3);
                if (lane < 2) { u32x4 ones = {BF_ONES, BF_ONES, BF_ONES, BF_ONES}; lds_st<u32x4>(c.L, SC_RV + (idx * 2 + lane) * VREC + 1024, ones); }
            }
        }
        BAR();
        STAMP(8);
        const LAS unsigned char* kb = c.L + SC_RK + hh * 1024 + c.voff;
        const LAS unsigned char* vb = c.L + SC_RV + vlane;
        if (c.i == 1) att_segment<4, 4, 5, 0, false>(oacc, m, kb, vb, mk, qf);
        else if (qb == 0) att_segment<8, 0, 1, 2, false>(oacc, m, kb, vb, mk, qf);
        else if (qb == 1) att_segment<7, 1, 2, 2, false>(oacc, m, kb, vb, mk, qf);
        else if (qb == 2) att_segment<6, 2, 3, 2, false>(oacc, m, kb, vb, mk, qf);
        else att_segment<5, 3, 4, 2, false>(oacc, m, kb, vb, mk, qf);
    }
    {
        u32x4 o;
        if (hh == 0) {
            float il = __builtin_amdgcn_rcpf(oacc[8]);
            asm volatile("s_nop 1" : "+v"(il));
            o = u32x4{pack2(smul(oacc[0], il), smul(oacc[1], il)), pack2(smul(oacc[2], il), smul(oacc[3], il)), pack2(smul(oacc[4], il), smul(oacc[5], il)), pack2(smul(oacc[6], il), smul(oacc[7], il))};
        } else {
            float il = __builtin_amdgcn_rcpf(oacc[0]);
            asm volatile("s_nop 1" : "+v"(il));
            o = u32x4{pack2(smul(oacc[8], il), smul(oacc[9], il)), pack2(smul(oacc[10], il), smul(oacc[11], il)), pack2(smul(oacc[12], il), smul(oacc[13], il)), pack2(smul(oacc[14], il), smul(oacc[15], il))};
        }
        st_sc1(c, lbase + (u32)L_OF + (u32)((4 * c.i + qb) * 32 + 2 * c.j + hh) * 1024, o);
    }
    STAMP(9);
    WAIT_VM(0);
    BAR();
    if (w == 0) signal_group(c, layer, 2);
    if (w >= 4) { issue_chunk<CG(1, 0)>(c); issue_chunk<CG(1, 1)>(c); }
    STAMP(10);
}

__device__ __forceinline__ void red_write(const Ctx& c, const f32x16& acc0, const f32x16& acc1, int t2, int kq) {
#pragma unroll
    for (int d = 0; d < 4; ++d) {
        const int fin = d & 1;
        if (kq != fin) {
            const int slot = kq < fin ? kq : kq - 1;
            const int base = red_off(t2 * 4 + d) + slot * 2048 + c.voff;
            const f32x16& a = d < 2 ? acc0 : acc1;
            const int o = (d & 1) * 8;
            lds_st<f32x4>(c.L, base, f32x4{a[o], a[o + 1], a[o + 2], a[o + 3]});
            lds_st<f32x4>(c.L, base + 1024, f32x4{a[o + 4], a[o + 5], a[o + 6], a[o + 7]});
        }
    }
}
__device__ __forceinline__ void red_read(const Ctx& c, const f32x16& acc0, const f32x16& acc1, int t2, int kq, float (&v)[2][8]) {
#pragma unroll
    for (int e = 0; e < 8; ++e) { v[0][e] = kq ? acc0[8 + e] : acc0[e]; v[1][e] = kq ? acc1[8 + e] : acc1[e]; }
#pragma unroll
    for (int x = 0; x < 2; ++x)
#pragma unroll
        for (int s = 0; s < 3; ++s) {
            const int base = red_off(t2 * 4 + 2 * x + kq) + s * 2048 + c.voff;
            const f32x4 lo = lds_ld<f32x4>(c.L, base), hi = lds_ld<f32x4>(c.L, base + 1024);
            v[x][0] += lo.x; v[x][1] += lo.y; v[x][2] += lo.z; v[x][3] += lo.w; v[x][4] += hi.x; v[x][5] += hi.y; v[x][6] += hi.z; v[x][7] += hi.w;
        }
}
__device__ __forceinline__ u32x4 add_res(const float (&v)[8], const u32x4& res) {
    return u32x4{pack2(v[0] + bflo(res.x), v[1] + bfhi(res.x)), pack2(v[2] + bflo(res.y), v[3] + bfhi(res.y)),
                 pack2(v[4] + bflo(res.z), v[5] + bfhi(res.z)), pack2(v[6] + bflo(res.w), v[7] + bfhi(res.w))};
}

__device__ __forceinline__ void phase_wo(const Ctx& c0, int layer, const Args& a) {
    const Ctx c = fresh(c0);
    const int w = c.w, t2 = w & 1, kq = w >> 1;
    const int th = c.j >> 3, cb = c.j & 7;
    const int tbk = 4 * c.i + 2 * th + t2;
    const u32 lbase = (u32)(WS_L0 + (size_t)layer * WS_LSTRIDE);
    u32x4 res[2];
    if (w < 4) {
#pragma unroll
        for (int x = 0; x < 2; ++x) {
            const int kbo = 4 * cb + 2 * x + kq;
            if (layer == 0) {
                const u16* src = a.x + (size_t)(tbk * 32 + (c.lane & 31)) * DM + kbo * 16 + 4 * (c.lane >> 5);
                const u32x2 lo = *(const u32x2*)src, hi = *(const u32x2*)(src + 8);
                res[x] = u32x4{lo.x, lo.y, hi.x, hi.y};
            } else {
                res[x] = ld_sc1(c, (u32)(WS_L0 + L_X2) + (u32)(tbk * 32 + kbo) * 1024);
            }
        }
        if (w == 0) poll_flags<64>(c, layer, 2, 16);
    } else {
        WAIT_VM(8);
    }
    BAR();
    STAMP(11);
    u32x4 xf[8];
#pragma unroll
    for (int s = 0; s < 8; ++s) xf[s] = ld_sc1(c, lbase + (u32)L_OF + (u32)(tbk * 32 + 8 * kq + s) * 1024);
    u32x4 wf[16];
#pragma unroll
    for (int ch = 0; ch < 4; ++ch) {
        const LAS unsigned char* wb = lds_base(c, slot_of(CO(ch)) * SLOT + kq * 4096);
#pragma unroll
        for (int q = 0; q < 4; ++q) wf[4 * ch + q] = LDF(wb, q);
    }
    SB();
    f32x16 acc0 = {}, acc1 = {};
#pragma unroll
    for (int s = 0; s < 8; ++s) {
        acc0 = mfma32(wf[2 * s], xf[s], acc0);
        acc1 = mfma32(wf[2 * s + 1], xf[s], acc1);
        SB();
    }
    BAR();
    STAMP(12);
    red_write(c, acc0, acc1, t2, kq);
    BAR();
    if (w < 4) {
        float v[2][8];
        red_read(c, acc0, acc1, t2, kq, v);
#pragma unroll
        for (int x = 0; x < 2; ++x) st_sc1(c, lbase + (u32)L_X1 + (u32)(tbk * 32 + 4 * cb + 2 * x + kq) * 1024, add_res(v[x], res[x]));
        STAMP(13);
        WAIT_VM(0);
    }
    BAR();
    if (w == 0) signal_group(c, layer, 3);
    STAMP(14);
}

__device__ __forceinline__ int gu_slot(int T) { return (int)((0x02046204u >> (4 * (T & 7))) & 15u); }
__device__ __forceinline__ void gu_tile(const Ctx& c, int T, f32x16& acc, const f32x16& prev, u32x4 (&wf)[8], const u32x4 (&xf)[32], int h,
                                        f32x4 (&cs)[4], const GK& gk, u32 hbase) {
    const LAS unsigned char* cur = lds_base(c, gu_slot(T) * SLOT);
    const LAS unsigned char* nxt = lds_base(c, gu_slot(T < 6 ? T + 1 : T) * SLOT);
    const LAS unsigned char* csb = lds_base_raw(c, SC_CS + (96 + T * 32 + 4 * h) * 4);
    GS g;
    u32 o[4];
    acc = f32x16{};
    SB();
    c_tile<false, true, false>(acc, wf, cur, cur + SLOT, nxt, xf, [&](int k) {
        const int p = k >> 3, e = 2 * p, q2 = p >> 1, e2 = e & 3;
        if ((k & 7) < 7) geglu_sub(g, o[p], k & 7, prev[e], prev[e + 1], prev[8 + e], prev[8 + e + 1], cs[q2][e2], cs[q2][e2 + 1], cs[2 + q2][e2], cs[2 + q2][e2 + 1], gk);
        if (k == 15) cs[0] = *(const LAS f32x4*)(csb);
        if (k == 15) cs[2] = *(const LAS f32x4*)(csb + 64);
        if (k == 31) cs[1] = *(const LAS f32x4*)(csb + 32);
        if (k == 31) cs[3] = *(const LAS f32x4*)(csb + 96);
    });
    st_sc1(c, hbase + (u32)(T - 1) * 1024, u32x4{o[0], o[1], o[2], o[3]});
}
static_assert(slot_of(CG(0, 0)) == 4 && slot_of(CG(1, 0)) == 0 && slot_of(CG(2, 0)) == 2 && slot_of(CG(3, 0)) == 6 && slot_of(CG(4, 0)) == 4 && slot_of(CG(5, 0)) == 0 && slot_of(CG(6, 0)) == 2, "gu_slot");

__device__ __forceinline__ void phase_gu(const Ctx& c0, int layer) {
    const Ctx c = fresh(c0);
    const int lane = c.lane, w = c.w, tb = w & 3, h = lane >> 5;
    const u32 lbase = (u32)(WS_L0 + (size_t)layer * WS_LSTRIDE);
    if (w < 4) {
        if (w == 0) poll_flags<64>(c, layer, 3, 16);
        BAR();
        STAMP(15);
        u32x4 xf[32], wf[8];
        const LAS unsigned char* w0 = lds_base(c, slot_of(CG(0, 0)) * SLOT);
        const LAS unsigned char* w1 = lds_base(c, slot_of(CG(1, 0)) * SLOT);
#pragma unroll
        for (int k = 0; k < 8; ++k) wf[k] = LDF(w0, k);
        f32x16 a0 = {}, a1;
        const f32x2 ms = c_tile0<true, false, false>(c, a0, wf, w0, w0 + SLOT, w1, xf, c.rs, lbase + (u32)L_X1 + (u32)((4 * c.i + tb) * 32) * 1024);
        constexpr float GC1 = -2.0f * 0.7978845608028654f * 1.4426950408889634f, GC2 = GC1 * 0.044715f;
        GK gk; gk.mean = ms.x; gk.A = ms.y * GC1; gk.B = ms.y * ms.y * ms.y * GC2; gk.ir2 = 1.0f / (ms.y * ms.y);
        BARL(); STAMP(16);
        f32x4 cs[4];
#pragma unroll
        for (int q = 0; q < 4; ++q) cs[q] = lds_ld<f32x4>(c.L, SC_CS + (96 + (q >> 1) * 16 + 8 * (q & 1) + 4 * h) * 4);
        const u32 hbase = lbase + (u32)L_HF + (u32)((4 * c.i + tb) * 112 + 7 * c.j) * 1024;
#pragma unroll 1
        for (int T = 1; T < 7; T += 2) {
            gu_tile(c, T, a1, a0, wf, xf, h, cs, gk, hbase);
            BARL();
            gu_tile(c, T + 1, a0, a1, wf, xf, h, cs, gk, hbase);
            if (T < 5) BARL();
        }
        STAMP(22);
        {
            asm volatile("s_nop 15" : "+v"(a0));
            GS g[4];
            u32 o[4];
#pragma unroll
            for (int q = 0; q < 7; ++q)
#pragma unroll
                for (int p = 0; p < 4; ++p) {
                    const int e = 2 * p, q2 = p >> 1, e2 = e & 3;
                    geglu_sub(g[p], o[p], q, a0[e], a0[e + 1], a0[8 + e], a0[8 + e + 1], cs[q2][e2], cs[q2][e2 + 1], cs[2 + q2][e2], cs[2 + q2][e2 + 1], gk);
                }
            st_sc1(c, hbase + 6u * 1024, u32x4{o[0], o[1], o[2], o[3]});
        }
        STAMP(21);
        WAIT_VM(0);
        BAR();
        STAMP(23);
        if (w == 0) signal_group(c, layer, 4);
    } else {
#define GU_CS(T) do { if (layer == 0 && (w & 3) == ((T) & 3)) { const LAS unsigned char* wb_ = lds_base(c, slot_of(CG(T, 0)) * SLOT); tile_colsum<false>(c, wb_, wb_ + SLOT, 96 + 32 * (T)); } } while (0)
        WAIT_VM(0);
        BAR();
        __builtin_amdgcn_s_sleep(16);
        issue_chunk<CG(2, 0)>(c); issue_chunk<CG(2, 1)>(c); issue_chunk<CG(3, 0)>(c); issue_chunk<CG(3, 1)>(c);
        GU_CS(0); GU_CS(1);
        if (layer == 1 && w == 7 && c.i == 0 && c.j == 0) {
            for (u32 spins = 0;; ++spins) {
                asm volatile("" ::: "memory");
                const u32x4 f = __builtin_amdgcn_raw_buffer_load_b128(c.rs, c.voff, (u32)WS_STARTED, 16);
                if (__all(f.x == c.tag && f.y == c.tag && f.z == c.tag && f.w == c.tag)) { if (lane == 0) __builtin_amdgcn_raw_buffer_store_b32(c.tag + 31u, c.rs, (u32)WS_EPOCH, 0, 16); break; }
                if (spins > SPIN_CAP) break;
                __builtin_amdgcn_s_sleep(1);
            }
        }
        WAIT_VM(8);
        BAR();
        issue_chunk<CG(4, 0)>(c); issue_chunk<CG(4, 1)>(c);
        GU_CS(2);
        WAIT_VM(8);
        BAR();
        issue_chunk<CG(5, 0)>(c); issue_chunk<CG(5, 1)>(c);
        GU_CS(3);
        WAIT_VM(8);
        BAR();
        issue_chunk<CG(6, 0)>(c); issue_chunk<CG(6, 1)>(c);
        GU_CS(4);
        WAIT_VM(8);
        BAR();
        issue_chunk<CD(0)>(c); issue_chunk<CD(1)>(c);
        GU_CS(5);
        WAIT_VM(8);
        BAR();
        issue_chunk<CD(2)>(c); issue_chunk<CD(3)>(c);
        GU_CS(6);
        BAR();
        issue_chunk<CD(4)>(c); issue_chunk<CD(5)>(c);
        BAR();
        issue_chunk<CD(6)>(c); issue_chunk<CD(7)>(c);
#undef GU_CS
    }
}

__device__ __forceinline__ void phase_dn(const Ctx& c0, int layer, const Args& a) {
    const Ctx c = fresh(c0);
    const int lane = c.lane, w = c.w, t2 = w & 1, kq = w >> 1, h = lane >> 5, r = lane & 31;
    const int th = c.j >> 3, cb = c.j & 7;
    const int tbk = 4 * c.i + 2 * th + t2;
    const u32 lbase = (u32)(WS_L0 + (size_t)layer * WS_LSTRIDE);
    const u32 xsrc = lbase + (u32)L_HF + (u32)(tbk * 112 + 28 * kq) * 1024;
    if (w >= 4) WAIT_VM(8);
    BAR();
    u32x4 wr[24];
#pragma unroll
    for (int ch = 0; ch < 6; ++ch) {
        const LAS unsigned char* wb = lds_base(c, chunk_lds(CD(ch)) + kq * 4096);
#pragma unroll
        for (int q = 0; q < 4; ++q) wr[4 * ch + q] = LDF(wb, q);
    }
    BAR();
    if (w >= 4) {
        issue_chunk<CD(8)>(c); issue_chunk<CD(9)>(c); issue_chunk<CD(10)>(c);
        issue_chunk<CD(11)>(c); issue_chunk<CD(12)>(c); issue_chunk<CD(13)>(c);
    }
    u32x4 res[2];
    if (w < 4) {
        res[0] = ld_sc1(c, lbase + (u32)L_X1 + (u32)(tbk * 32 + 4 * cb + kq) * 1024);
        res[1] = ld_sc1(c, lbase + (u32)L_X1 + (u32)(tbk * 32 + 4 * cb + 2 + kq) * 1024);
    }
    if (w == 0) poll_flags<64>(c, layer, 4, 16);
    BAR();
    STAMP(25);
    constexpr int XD = 10;
    u32x4 xf[28];
#pragma unroll
    for (int s = 0; s < XD; ++s) xf[s] = ld_sc1(c, xsrc + (u32)s * 1024);
    SB();
    f32x16 acc0 = {}, acc1 = {};
#pragma unroll
    for (int s = 0; s < 12; ++s) {
        xf[s + XD] = ld_sc1(c, xsrc + (u32)(s + XD) * 1024);
        acc0 = mfma32(wr[2 * s], xf[s], acc0);
        acc1 = mfma32(wr[2 * s + 1], xf[s], acc1);
        PIN(acc0); PIN(acc1);
        SB();
    }
    WAIT_VM(22);
    BARL();
    STAMP(24);
    {
        const LAS unsigned char* wb[8];
#pragma unroll
        for (int ch = 0; ch < 8; ++ch) wb[ch] = lds_base(c, chunk_lds(CD(6 + ch)) + kq * 4096);
        u32x4 wf[8];
#pragma unroll
        for (int q = 0; q < 8; ++q) wf[q] = LDF(wb[q >> 2], q & 3);
        SB();
#pragma unroll
        for (int s = 0; s < 16; ++s) {
            const u32x4 w0 = wf[(2 * s) & 7], w1 = wf[(2 * s + 1) & 7];
            if (12 + s + XD < 28) xf[12 + s + XD] = ld_sc1(c, xsrc + (u32)(12 + s + XD) * 1024);
            if (2 * s + 8 < 32) {
                wf[(2 * s) & 7] = LDF(wb[(2 * s + 8) >> 2], (2 * s + 8) & 3);
                wf[(2 * s + 1) & 7] = LDF(wb[(2 * s + 9) >> 2], (2 * s + 9) & 3);
            }
            acc0 = mfma32(w0, xf[12 + s], acc0);
            acc1 = mfma32(w1, xf[12 + s], acc1);
            PIN(acc0); PIN(acc1);
            SB();
        }
    }
    BAR();
    STAMP(26);
    red_write(c, acc0, acc1, t2, kq);
    BAR();
    if (w >= 4) {
        if (layer == 0) issue_qkv_tile(c, a, 0);
    } else {
        float v[2][8];
        red_read(c, acc0, acc1, t2, kq, v);
#pragma unroll
        for (int x = 0; x < 2; ++x) {
            const int kbo = 4 * cb + 2 * x + kq;
            const u32x4 o = add_res(v[x], res[x]);
            const auto r0 = __builtin_amdgcn_permlane32_swap(o.x, o.z, false, false);
            const auto r1 = __builtin_amdgcn_permlane32_swap(o.y, o.w, false, false);
            const u32x4 on = u32x4{r0[0], r1[0], r0[1], r1[1]};
            if (layer == 1) {
                *(u32x4*)(a.out + (size_t)(tbk * 32 + r) * DM + kbo * 16 + 8 * h) = on;
            } else {
                st_sc1(c, lbase + (u32)L_X2N + (u32)(tbk * 32 + kbo) * 1024, on);
                st_sc1(c, lbase + (u32)L_X2 + (u32)(tbk * 32 + kbo) * 1024, o);
            }
        }
        STAMP(27);
        if (layer == 0) WAIT_VM(0);
    }
    if (layer == 0) {
        BAR();
        if (w == 0) signal_group(c, layer, 0);
        if (w >= 4) { issue_qkv_tile(c, a, 1); issue_qkv_tile(c, a, 2); }
        STAMP(28);
    }
}

__global__ void __launch_bounds__(512, 2) mega_kernel(Args a) {
    extern __shared__ __attribute__((aligned(16))) unsigned char lds[];
    Ctx c;
    c.L = (LAS unsigned char*)lds;
    c.Laddr = (u32)(size_t)c.L;
    c.ws = a.ws;
    c.rs = __builtin_amdgcn_make_buffer_rsrc((void*)a.ws, 0, (int)WS_END, 0x00020000);
    const int tid = threadIdx.x;
    c.lane = tid & 63;
    c.w = __builtin_amdgcn_readfirstlane(tid >> 6);
    const int bid = blockIdx.x;
    c.i = 2 * (bid & 7) + (bid >> 7);
    c.j = (bid >> 3) & 15;
    c.voff = c.lane * 16;
    {
        const u32 e = __builtin_amdgcn_raw_buffer_load_b32(c.rs, 0, (u32)WS_EPOCH, 16);
        c.tag = 0;
        if (c.w >= 4) { issue_qkv_tile(c, a, 0); issue_qkv_tile(c, a, 1); issue_qkv_tile(c, a, 2); }
        c.tag = (u32)__builtin_amdgcn_readfirstlane((int)e) + 1u;
        if (tid == 0) __builtin_amdgcn_raw_buffer_store_b32(c.tag, c.rs, (u32)bid * 4u, (u32)WS_STARTED, 16);
    }
    if (c.w < 4) {
        const int hh = c.lane >> 5, row = c.lane & 31, half = c.w & 1;
        u32 d[4];
#pragma unroll
        for (int q = 0; q < 4; ++q) {
            const int c0 = 16 * half + 8 * (q >> 1) + 4 * hh + 2 * (q & 1), c1 = c0 + 1;
            const u32 lo = c.w < 2 ? (row > c0 ? 0xff7fu : 0u) : (row == c0 ? 0x3f80u : 0u);
            const u32 hi = c.w < 2 ? (row > c1 ? 0xff7fu : 0u) : (row == c1 ? 0x3f80u : 0u);
            d[q] = lo | (hi << 16);
        }
        lds_st<u32x4>(c.L, SC_MK + c.w * 1024 + c.voff, u32x4{d[0], d[1], d[2], d[3]});
    }
    for (int layer = 0; layer < 2; ++layer) {
        phase_qkv_att(c, layer, a);
        phase_wo(c, layer, a);
        phase_gu(c, layer);
        phase_dn(c, layer, a);
    }
}

__global__ void print_kernel(const unsigned char* ws) {
    if (threadIdx.x == 0) {
        const unsigned long long* t = (const unsigned long long*)(ws + WS_PROF + (size_t)(72 * 2) * 64 * 8);
#define D(k) (int)(t[k] - t[0])
        printf("TLa %d %d %d %d %d %d\n", D(1), D(2), D(5), D(6), D(7), D(8));
        printf("TLb %d %d %d %d %d %d\n", D(10), D(11), D(14), D(15), D(16), D(23));
        printf("TLc %d %d %d %d %d %d\n", D(25), D(26), D(28), D(33), D(34), D(37));
        printf("TLd %d %d %d %d %d %d\n", D(40), D(42), D(43), D(46), D(47), D(48));
        printf("TLe %d %d %d %d\n", D(55), D(57), D(58), D(59));
    }
}
}

extern "C" void kernel_launch(void* const* d_in, const int* in_sizes, int n_in, void* d_out, int out_size, void* d_ws, size_t ws_size,
                              hipStream_t stream) {
    Args a;
    a.x = (const u16*)d_in[0]; a.wqkv = (const u16*)d_in[1]; a.wo = (const u16*)d_in[2];
    a.wg = (const u16*)d_in[4]; a.wu = (const u16*)d_in[5]; a.wd = (const u16*)d_in[6];
    a.out = (u16*)d_out; a.ws = (unsigned char*)d_ws;
    (void)hipFuncSetAttribute((const void*)mega_kernel, hipFuncAttributeMaxDynamicSharedMemorySize, LDS_BYTES);
    mega_kernel<<<256, 512, LDS_BYTES, stream>>>(a);
    print_kernel<<<1, 64, 0, stream>>>((const unsigned char*)d_ws);
}
```

```cpp
#include <hip/hip_runtime.h>
#include <hip/hip_bf16.h>

#define LAS __attribute__((address_space(3)))
#define GAS __attribute__((address_space(1)))
typedef unsigned u32;
typedef unsigned short u16;
typedef u32 u32x4 __attribute__((ext_vector_type(4)));
typedef u32 u32x2 __attribute__((ext_vector_type(2)));
typedef float f32x16 __attribute__((ext_vector_type(16)));
typedef float f32x4 __attribute__((ext_vector_type(4)));
typedef float f32x2 __attribute__((ext_vector_type(2)));
typedef __bf16 bf16x8_t __attribute__((ext_vector_type(8)));
typedef __bf16 bf16x2_t __attribute__((ext_vector_type(2)));
typedef short s16x4 __attribute__((ext_vector_type(4)));

namespace {

constexpr int SEQ = 2048, DM = 512, FF = 1792;
constexpr size_t KiB = 1024, MiB = 1024 * 1024;
constexpr size_t WS_FLAGS = 1 * MiB;
constexpr size_t WS_EPOCH = 640 * KiB;
constexpr size_t WS_STARTED = WS_EPOCH + 4096;
constexpr size_t WS_TMO = 768 * KiB;
constexpr size_t WS_CSP = 3 * MiB + 256 * KiB;
constexpr size_t WS_WO = 4 * MiB + 512 * KiB;
constexpr size_t WS_WG = 5 * MiB;
constexpr size_t WS_WD = 9 * MiB;
constexpr size_t WS_L0 = 16 * MiB, WS_LSTRIDE = 32 * MiB;
constexpr size_t L_KF = 0, L_VF = 2 * MiB, L_OF = 4 * MiB, L_X1 = 6 * MiB, L_X2 = 8 * MiB, L_HF = 10 * MiB, L_X2N = 18 * MiB;
constexpr size_t WS_END = WS_L0 + 2 * WS_LSTRIDE;

constexpr int NSLOT = 8, SLOT = 16384;
constexpr int LDS_SCR = NSLOT * SLOT;
constexpr int VREC = 1040;
constexpr int SC_RV = LDS_SCR;
constexpr int SC_VS = SC_RV + 16640;
constexpr int SC_ST = SC_VS + 8320;
constexpr int SC_CS = SC_ST + 1024;
constexpr int SC_MK = SC_CS + 1280;
constexpr int LDS_BYTES = SC_MK + 4096;
constexpr int SC_KS = 0 * SLOT;
constexpr int SC_QS = 0 * SLOT + 8192;
constexpr int SC_RK = 1 * SLOT;
static_assert(LDS_BYTES <= 163840, "LDS");
__device__ __forceinline__ int red_off(int q) { return q < 5 ? 2 * SLOT + q * 6144 : 6 * SLOT + (q - 5) * 6144; }

constexpr float LN_EPS = 1e-5f;
constexpr float QSCALE = 0.25f * 1.4426950408889634f;
constexpr u32 SPIN_CAP = 1u << 17;
constexpr u32 BF_ONES = 0x3f803f80u;

struct Args {
    const u16* x; const u16* wqkv; const u16* wo; const u16* wg; const u16* wu; const u16* wd;
    u16* out; unsigned char* ws;
};

__device__ __forceinline__ f32x16 mfma32(u32x4 a, u32x4 b, f32x16 c) {
    return __builtin_amdgcn_mfma_f32_32x32x16_bf16(__builtin_bit_cast(bf16x8_t, a), __builtin_bit_cast(bf16x8_t, b), c, 0, 0, 0);
}
__device__ __forceinline__ u32 pack2(float lo, float hi) {
    f32x2 v = {lo, hi};
    return __builtin_bit_cast(u32, __builtin_convertvector(v, bf16x2_t));
}
__device__ __forceinline__ float bflo(u32 u) { return __builtin_bit_cast(float, u << 16); }
__device__ __forceinline__ float bfhi(u32 u) { return __builtin_bit_cast(float, u & 0xffff0000u); }
__device__ __forceinline__ float dot2(u32 a, u32 b, float c) {
    return __builtin_amdgcn_fdot2_f32_bf16(__builtin_bit_cast(bf16x2_t, a), __builtin_bit_cast(bf16x2_t, b), c, false);
}
__device__ __forceinline__ void swap32(float& a, float& b) {
    asm volatile("s_nop 1\n\tv_permlane32_swap_b32 %0, %1\n\ts_nop 1" : "+v"(a), "+v"(b));
}
__device__ __forceinline__ float fmx(float a, float b) { return __builtin_elementwise_maximum(a, b); }
__device__ __forceinline__ float xor32_max(float v) { float a = v, b = v; swap32(a, b); return fmx(a, b); }
__device__ __forceinline__ float xor32_sum(float v) { float a = v, b = v; swap32(a, b); return a + b; }
#define BAR() do { asm volatile("s_waitcnt lgkmcnt(0)" ::: "memory"); __builtin_amdgcn_s_barrier(); asm volatile("" ::: "memory"); } while (0)
#define BARL() do { asm volatile("" ::: "memory"); __builtin_amdgcn_s_barrier(); asm volatile("" ::: "memory"); } while (0)
#define WAIT_VM(n) asm volatile("s_waitcnt vmcnt(" #n ")" ::: "memory")
#define SB() __builtin_amdgcn_sched_barrier(0)

template <class T> __device__ __forceinline__ T lds_ld(const LAS unsigned char* base, int off) { return *(const LAS T*)(base + off); }
template <class T> __device__ __forceinline__ void lds_st(LAS unsigned char* base, int off, T v) { *(LAS T*)(base + off) = v; }

__device__ __forceinline__ void glds4k(const unsigned char* src, u32 voff, u32 dst) {
    u32 keep;
    asm volatile(
        "s_mov_b32 %0, m0\n\t"
        "s_mov_b32 m0, %3\n\t"
        "s_nop 0\n\t"
        "global_load_lds_dwordx4 %1, %2 sc1\n\t"
        "global_load_lds_dwordx4 %1, %2 offset:1024 sc1\n\t"
        "global_load_lds_dwordx4 %1, %2 offset:2048 sc1\n\t"
        "global_load_lds_dwordx4 %1, %2 offset:3072 sc1\n\t"
        "s_mov_b32 m0, %0"
        : "=&s"(keep) : "v"(voff), "s"(src), "s"(dst) : "memory");
}

__device__ __forceinline__ void glds1k(const unsigned char* src, u32 voff, u32 dst) {
    u32 keep;
    asm volatile(
        "s_mov_b32 %0, m0\n\t"
        "s_mov_b32 m0, %3\n\t"
        "s_nop 0\n\t"
        "global_load_lds_dwordx4 %1, %2\n\t"
        "s_mov_b32 m0, %0"
        : "=&s"(keep) : "v"(voff), "s"(src), "s"(dst) : "memory");
}
#define PIN(x) asm volatile("" : "+v"(x))

constexpr int NF_WO = 8 * 64, NF_WG = 16 * 224, NF_WD = 8 * 224;
__host__ __device__ constexpr int qkv_col(int t) { return t == 0 ? 512 : (t == 1 ? 1024 : 0); }
struct WItem { const u16* src; size_t dst; };
__device__ __forceinline__ WItem w_item(const Args& a, int f, int lane) {
    const u16 *pA, *pB; int ld, ks; size_t dst;
    if (f < NF_WO) {
        const int cb = f >> 6, rem = f & 63, c = rem >> 4, r2 = rem & 15, kq = r2 >> 2, ks2 = (r2 >> 1) & 1, nt = r2 & 1;
        ks = 8 * kq + 2 * c + ks2; pA = a.wo + 64 * cb + 32 * nt; pB = pA + 16; ld = 512; dst = WS_WO + (size_t)f * 1024;
    } else if ((f -= NF_WO) < NF_WG) {
        const int j = f / 224, rem = f % 224, q = rem >> 4, ksl = rem & 15, c = q >> 1, hf = q & 1;
        ks = 16 * hf + ksl; pA = a.wg + 112 * j + 16 * c; pB = a.wu + 112 * j + 16 * c; ld = FF; dst = WS_WG + (size_t)f * 1024;
    } else {
        f -= NF_WG;
        const int cb = f / 224, rem = f % 224, c = rem >> 4, r2 = rem & 15, kq = r2 >> 2, ks2 = (r2 >> 1) & 1, nt = r2 & 1;
        ks = 28 * kq + 2 * c + ks2; pA = a.wd + 64 * cb + 32 * nt; pB = pA + 16; ld = 512; dst = WS_WD + (size_t)f * 1024;
    }
    const int k = lane >> 2, n8 = lane & 3;
    WItem it;
    it.src = (n8 < 2 ? pA + n8 * 8 : pB + (n8 - 2) * 8) + (size_t)(16 * ks + k) * ld;
    it.dst = dst;
    return it;
}

#define STAMP(k) do { } while (0)
struct Ctx {
    LAS unsigned char* L;
    u32 Laddr;
    unsigned char* ws;
    __amdgpu_buffer_rsrc_t rs;
    int lane, w, i, j;
    u32 voff;
    u32 tag;
};

__device__ __forceinline__ u32x4 ld_sc1(const Ctx& c, u32 soff) { return __builtin_amdgcn_raw_buffer_load_b128(c.rs, c.voff, soff, 16); }
__device__ __forceinline__ void st_sc1(const Ctx& c, u32 soff, u32x4 v) { __builtin_amdgcn_raw_buffer_store_b128(v, c.rs, c.voff, soff, 16); }
__device__ __forceinline__ Ctx fresh(const Ctx& c0) {
    Ctx c = c0;
    asm volatile("" : "+s"(c.i), "+s"(c.j), "+s"(c.w), "+s"(c.tag));
    asm volatile("" : "+v"(c.lane));
    c.voff = c.lane * 16;
    return c;
}

constexpr int CQ(int t, int hf) { return 2 * t + hf; }
constexpr int CO(int k) { return 6 + k; }
constexpr int CG(int t, int hf) { return 10 + 2 * t + hf; }
constexpr int CD(int k) { return 24 + k; }
constexpr int slot_of(int n) {
    constexpr int tab[38] = {
          0, 1,   2, 3,   4, 5,
          6, 7, 2, 3,
          4, 5,   0, 1,   2, 3,   6, 7,   4, 5,   0, 1,   2, 3,
          6, 7, 4, 5, 0, 1, 2, 3,   6, 7, 4, 5, 0, 1};
    return tab[n];
}
template <int N> __device__ __forceinline__ const unsigned char* chunk_src(const Ctx& c) {
    size_t src;
    const int cb = c.j & 7;
    if (N < 10) src = WS_WO + (size_t)(cb * 4 + (N - 6)) * SLOT;
    else if (N < 24) src = WS_WG + (size_t)(c.j * 14 + (N - 10)) * SLOT;
    else src = WS_WD + (size_t)(cb * 14 + (N - 24)) * SLOT;
    return c.ws + src + (c.w & 3) * 4096;
}
constexpr int chunk_lds(int n) { return slot_of(n) * SLOT; }
template <int N> __device__ __forceinline__ void issue_chunk(const Ctx& c) {
    glds4k(chunk_src<N>(c), c.voff, c.Laddr + chunk_lds(N) + (c.w & 3) * 4096);
}
__device__ __forceinline__ const LAS unsigned char* lds_base(const Ctx& c, int off) {
    u32 a = c.Laddr + off + c.voff;
    asm volatile("" : "+v"(a));
    return (const LAS unsigned char*)(size_t)a;
}

__device__ __forceinline__ u32 flag_off(int layer, int seam, int ci, int cj) {
    return (u32)(WS_FLAGS + (size_t)(((layer * 8 + seam) * 256) + ci * 16 + cj) * 512);
}
__device__ __forceinline__ u32 flag_tag(const Ctx& c, int layer, int seam) { return c.tag + (u32)(layer * 8 + seam); }
__device__ __forceinline__ void flag_store(const Ctx& c, int layer, int seam, int ci, int cj, int word) {
    __builtin_amdgcn_raw_buffer_store_b32(flag_tag(c, layer, seam), c.rs, flag_off(layer, seam, ci, cj) + word * 4, 0, 16);
}
__device__ __forceinline__ void signal_group(const Ctx& c, int layer, int seam) {
    if (c.lane < 16) flag_store(c, layer, seam, c.i, c.lane, c.j);
}
template <int NWORDS> __device__ __forceinline__ void poll_flags(const Ctx& c, int layer, int seam, int nwords = NWORDS) {
    const u32 off = flag_off(layer, seam, c.i, c.j) + c.lane * 4;
    const u32 tag = flag_tag(c, layer, seam);
    for (u32 spins = 0;; ++spins) {
        asm volatile("" ::: "memory");
        const u32 f0 = __builtin_amdgcn_raw_buffer_load_b32(c.rs, off, 0, 16);
        bool ok = c.lane >= nwords || f0 == tag;
        if (NWORDS > 64) { const u32 f1 = __builtin_amdgcn_raw_buffer_load_b32(c.rs, off + 256, 0, 16); ok = ok && f1 == tag; }
        if (__all(ok)) break;
        if (spins > SPIN_CAP) { if (c.lane == 0) __builtin_amdgcn_raw_buffer_store_b32(1u, c.rs, (u32)WS_TMO, 0, 16); break; }
        __builtin_amdgcn_s_sleep(1);
    }
}

__device__ __forceinline__ int crow(int r, int h) { return (r & 3) + 8 * (r >> 2) + 4 * h; }

#define LDF(p, k) (*(const LAS u32x4*)((p) + (k) * 1024))
__device__ __forceinline__ u32x4 ldf_tr(const LAS unsigned char* p, int k) {
    const u32x2 lo = __builtin_bit_cast(u32x2, __builtin_amdgcn_ds_read_tr16_b64_v4i16((LAS s16x4*)(p + k * 1024)));
    const u32x2 hi = __builtin_bit_cast(u32x2, __builtin_amdgcn_ds_read_tr16_b64_v4i16((LAS s16x4*)(p + k * 1024 + 256)));
    return u32x4{lo.x, lo.y, hi.x, hi.y};
}
template <bool TR> __device__ __forceinline__ u32x4 ldf(const LAS unsigned char* p, int k) { if constexpr (TR) return ldf_tr(p, k); else return LDF(p, k); }

template <bool SWAP, bool PRE, bool TR, class F>
__device__ __forceinline__ void c_tile(f32x16& acc, u32x4 (&wf)[8], const LAS unsigned char* lo, const LAS unsigned char* hi, const LAS unsigned char* nxt,
                                       const u32x4 (&xf)[32], F&& filler) {
#pragma unroll
    for (int k = 0; k < 32; ++k) {
        const u32x4 wv = wf[k & 7];
        if (k + 8 < 16) wf[k & 7] = ldf<TR>(lo, k + 8);
        else if (k + 8 < 32) wf[k & 7] = ldf<TR>(hi, k + 8 - 16);
        else if (PRE) wf[k & 7] = ldf<TR>(nxt, k + 8 - 32);
        acc = SWAP ? mfma32(xf[k], wv, acc) : mfma32(wv, xf[k], acc);
        PIN(acc);
        filler(k);
        SB();
    }
}
template <bool PRE, bool TR, bool STAGE, class W>
__device__ __forceinline__ f32x2 c_tile0(const Ctx& c, f32x16& acc, u32x4 (&wf)[8], const LAS unsigned char* lo, const LAS unsigned char* hi,
                                         const LAS unsigned char* nxt, u32x4 (&xf)[32], __amdgpu_buffer_rsrc_t xrs, u32 xsoff, W&& wait_ready) {
    const int lane = c.lane;
    u32x4 raw[STAGE ? 32 : 1];
    const u32 rvoff = (u32)(lane >> 3) * 1024u + (u32)(lane & 7) * 16u;
    LAS unsigned char* st = c.L + 6 * SLOT + (c.w & 3) * 8192;
    const int wr_off = (lane >> 3) * 128 + (((lane & 7) ^ ((lane >> 3) & 7)) * 16);
    const int rd_row = (lane & 31) * 128, rd_sw = lane & 7, rd_h = lane >> 5;
#define XLD(k) __builtin_amdgcn_raw_buffer_load_b128(xrs, c.voff, xsoff + (u32)(k) * 1024u, 16)
#define RLD(q) __builtin_amdgcn_raw_buffer_load_b128(xrs, rvoff, xsoff + (u32)((q) & 3) * 8192u + (u32)((q) >> 2) * 128u, 0)
#define STG(g) do { _Pragma("unroll") for (int q = 0; q < 4; ++q) lds_st<u32x4>(st + ((g) & 1) * 4096, q * 1024 + wr_off, raw[4 * (g) + q]); \
                    _Pragma("unroll") for (int q = 0; q < 4; ++q) xf[4 * (g) + q] = lds_ld<u32x4>(st + ((g) & 1) * 4096, rd_row + (((2 * q + rd_h) ^ rd_sw) * 16)); } while (0)
    if (STAGE) {
#pragma unroll
        for (int q = 0; q < 16; ++q) raw[q] = RLD(q);
        SB();
        wait_ready();
        STG(0);
    } else {
        wait_ready();
#pragma unroll
        for (int k = 0; k < 14; ++k) xf[k] = XLD(k);
    }
#pragma unroll
    for (int k = 0; k < 8; ++k) wf[k] = ldf<TR>(lo, k);
    SB();
    float s1[4] = {0.f, 0.f, 0.f, 0.f}, s2[4] = {0.f, 0.f, 0.f, 0.f};
#pragma unroll
    for (int k = 0; k < 32; ++k) {
        if (STAGE) {
            if ((k & 3) == 0) {
                const int g = k >> 2;
                if (4 * (g + 4) < 32) {
#pragma unroll
                    for (int q = 0; q < 4; ++q) raw[4 * (g + 4) + q] = RLD(4 * (g + 4) + q);
                }
                if (g + 1 < 8) STG(g + 1);
            }
        } else {
            if (k + 14 < 32) xf[k + 14] = XLD(k + 14);
        }
        const u32x4 wv = wf[k & 7];
        if (k + 8 < 16) wf[k & 7] = ldf<TR>(lo, k + 8);
        else if (k + 8 < 32) wf[k & 7] = ldf<TR>(hi, k + 8 - 16);
        else if (PRE) wf[k & 7] = ldf<TR>(nxt, k + 8 - 32);
        acc = mfma32(wv, xf[k], acc);
        PIN(acc);
        s1[0] = dot2(xf[k].x, BF_ONES, s1[0]); s2[0] = dot2(xf[k].x, xf[k].x, s2[0]);
        s1[1] = dot2(xf[k].y, BF_ONES, s1[1]); s2[1] = dot2(xf[k].y, xf[k].y, s2[1]);
        s1[2] = dot2(xf[k].z, BF_ONES, s1[2]); s2[2] = dot2(xf[k].z, xf[k].z, s2[2]);
        s1[3] = dot2(xf[k].w, BF_ONES, s1[3]); s2[3] = dot2(xf[k].w, xf[k].w, s2[3]);
        asm volatile("" : "+v"(s1[0]), "+v"(s1[1]), "+v"(s1[2]), "+v"(s1[3]), "+v"(s2[0]), "+v"(s2[1]), "+v"(s2[2]), "+v"(s2[3]));
        if (TR && k == 3) BARL();
        SB();
    }
    const float a = xor32_sum((s1[0] + s1[1]) + (s1[2] + s1[3])), b = xor32_sum((s2[0] + s2[1]) + (s2[2] + s2[3]));
    const float mean = a * (1.0f / DM);
    float var = b * (1.0f / DM) - mean * mean;
    var = var > 0.f ? var : 0.f;
    f32x2 o; o.x = mean; o.y = __builtin_amdgcn_rsqf(var + LN_EPS);
    return o;
#undef XLD
#undef RLD
#undef STG
}

struct GS { float g0, g1, t0, t1, u0, u1; };
struct GK { float mean, A, B, ir2; };
__device__ __forceinline__ void geglu_sub(GS& s, u32& o, int q, float ag0, float ag1, float au0, float au1, float cg0, float cg1, float cu0, float cu1, const GK& k) {
    if (q == 0) asm volatile("v_fma_f32 %0, -%7, %5, %3\n\tv_fma_f32 %1, -%7, %6, %4\n\tv_mul_f32 %2, %0, %0"
                             : "=&v"(s.g0), "=&v"(s.g1), "=&v"(s.t0) : "v"(ag0), "v"(ag1), "v"(cg0), "v"(cg1), "v"(k.mean));
    if (q == 1) asm volatile("v_mul_f32 %1, %2, %2\n\tv_fma_f32 %0, %0, %4, %3\n\tv_fma_f32 %1, %1, %4, %3"
                             : "+v"(s.t0), "=&v"(s.t1) : "v"(s.g1), "v"(k.A), "v"(k.B));
    if (q == 2) asm volatile("v_mul_f32 %0, %2, %0\n\tv_mul_f32 %1, %3, %1\n\tv_exp_f32 %0, %0"
                             : "+v"(s.t0), "+v"(s.t1) : "v"(s.g0), "v"(s.g1));
    if (q == 3) asm volatile("v_exp_f32 %0, %0\n\tv_fma_f32 %1, -%7, %5, %3\n\tv_fma_f32 %2, -%7, %6, %4"
                             : "+v"(s.t1), "=&v"(s.u0), "=&v"(s.u1) : "v"(au0), "v"(au1), "v"(cu0), "v"(cu1), "v"(k.mean));
    if (q == 4) asm volatile("v_fma_f32 %0, %0, %4, %4\n\tv_mul_f32 %2, %2, %3\n\tv_fma_f32 %1, %1, %4, %4"
                             : "+v"(s.t0), "+v"(s.t1), "+v"(s.g0) : "v"(s.u0), "v"(k.ir2));
    if (q == 5) asm volatile("v_rcp_f32 %0, %0\n\tv_mul_f32 %2, %2, %3\n\tv_rcp_f32 %1, %1"
                             : "+v"(s.t0), "+v"(s.t1), "+v"(s.g1) : "v"(s.u1));
    if (q == 6) asm volatile("v_mul_f32 %1, %1, %3\n\tv_mul_f32 %2, %2, %4\n\tv_cvt_pk_bf16_f32 %0, %1, %2"
                             : "=&v"(o), "+v"(s.g0), "+v"(s.g1) : "v"(s.t0), "v"(s.t1));
}
struct LS { float t0, t1; };
__device__ __forceinline__ void lnq_a(LS& s, float a0, float c0, float c1, float sc, float msc) {
    asm volatile("v_mul_f32 %0, %6, %3\n\tv_mul_f32 %1, %6, %4\n\tv_fma_f32 %0, %2, %5, -%0"
                 : "=&v"(s.t0), "=&v"(s.t1) : "v"(a0), "v"(c0), "v"(c1), "v"(sc), "v"(msc));
}
__device__ __forceinline__ u32 lnq_b(LS& s, float a1, float sc) {
    u32 o;
    asm volatile("v_fma_f32 %1, %3, %4, -%1\n\tv_cvt_pk_bf16_f32 %0, %2, %1" : "=v"(o), "+v"(s.t1) : "v"(s.t0), "v"(a1), "v"(sc));
    return o;
}
__device__ __forceinline__ void lnv_a(LS& s, float a0, float a1, float m0, float m1, float r0, float cs) {
    asm volatile("v_fma_f32 %0, -%4, %7, %2\n\tv_fma_f32 %1, -%5, %7, %3\n\tv_mul_f32 %0, %0, %6"
                 : "=&v"(s.t0), "=&v"(s.t1) : "v"(a0), "v"(a1), "v"(m0), "v"(m1), "v"(r0), "v"(cs));
}
__device__ __forceinline__ u32 lnv_b(LS& s, float r1) {
    u32 o;
    asm volatile("v_mul_f32 %1, %1, %3\n\tv_cvt_pk_bf16_f32 %0, %2, %1" : "=v"(o), "+v"(s.t1) : "v"(s.t0), "v"(r1));
    return o;
}
__device__ __forceinline__ u32x4 pack8(const float* v) { return u32x4{pack2(v[0], v[1]), pack2(v[2], v[3]), pack2(v[4], v[5]), pack2(v[6], v[7])}; }

__device__ __forceinline__ float max16(const f32x16& s, float m) {
    float a = fmx(fmx(s[0], s[1]), m), b = fmx(s[2], s[3]);
    a = fmx(fmx(s[4], s[5]), a); b = fmx(fmx(s[6], s[7]), b);
    a = fmx(fmx(s[8], s[9]), a); b = fmx(fmx(s[10], s[11]), b);
    a = fmx(fmx(s[12], s[13]), a); b = fmx(fmx(s[14], s[15]), b);
    return fmx(a, b);
}
__device__ __forceinline__ float smul(float a, float b) { float r; asm("v_mul_f32 %0, %1, %2" : "=v"(r) : "v"(a), "v"(b)); return r; }
struct MaskF { u32x4 alo, ahi, ilo, ihi; };
__device__ __forceinline__ f32x16 add_mask(const MaskF& mf, f32x16 s) { s = mfma32(mf.alo, mf.ilo, s); return mfma32(mf.ahi, mf.ihi, s); }
template <int NB, int FIRST, int REST0, int MASK, bool FIRSTSEG>
__device__ __forceinline__ void att_segment(f32x16& oacc, float& m, const LAS unsigned char* kb, const LAS unsigned char* vb, const LAS unsigned char* mk, const u32x4& qf) {
    u32x4 kf[NB];
#pragma unroll
    for (int t = 0; t < NB; ++t) { const int b = t == 0 ? FIRST : REST0 + t - 1; kf[t] = *(const LAS u32x4*)(kb + b * 2048); }
    MaskF mf;
    if (MASK) {
        mf.alo = LDF(mk, 0); mf.ahi = LDF(mk, 1); mf.ilo = LDF(mk, 2); mf.ihi = LDF(mk, 3);
        if (MASK == 2) { mf.alo ^= 0xff7fff7fu; mf.ahi ^= 0xff7fff7fu; }
    }
    SB();
    float mx = FIRSTSEG ? -__builtin_inff() : m;
    f32x16 sv[4];
    sv[0] = mfma32(kf[0], qf, f32x16{});
    if (NB > 1) sv[1] = mfma32(kf[1], qf, f32x16{});
    if (MASK) sv[0] = add_mask(mf, sv[0]);
    SB();
#pragma unroll
    for (int t = 0; t < NB; ++t) {
        const int u = NB > 1 ? (t == 0 ? 1 : t == 1 ? 0 : t) : 0;
        if (t + 2 < NB) sv[(t + 2) & 3] = mfma32(kf[t + 2], qf, f32x16{});
        mx = max16(sv[u & 3], mx);
        SB();
    }
    mx = xor32_max(mx);
    f32x16 negm;
#pragma unroll
    for (int e = 0; e < 16; ++e) negm[e] = -mx;
    SB();
    sv[0] = mfma32(kf[0], qf, negm);
    if (MASK) sv[0] = add_mask(mf, sv[0]);
    u32x4 v0 = *(const LAS u32x4*)(vb + (FIRST * 2 + 0) * VREC);
    u32x4 v1 = *(const LAS u32x4*)(vb + (FIRST * 2 + 1) * VREC);
    if (NB > 1) sv[1] = mfma32(kf[1], qf, negm);
    if (!FIRSTSEG) {
        float f = __builtin_amdgcn_exp2f(m - mx);
        asm volatile("s_nop 1" : "+v"(f));
#pragma unroll
        for (int e = 0; e < 16; ++e) oacc[e] = smul(oacc[e], f);
    }
    m = mx;
    SB();
#pragma unroll
    for (int t = 0; t < NB; ++t) {
        u32x4 n0, n1;
        if (t + 1 < NB) {
            if (t > 0) sv[(t + 1) & 1] = mfma32(kf[t + 1], qf, negm);
            n0 = *(const LAS u32x4*)(vb + ((REST0 + t) * 2 + 0) * VREC);
            n1 = *(const LAS u32x4*)(vb + ((REST0 + t) * 2 + 1) * VREC);
        }
        SB();
        const f32x16& s = sv[t & 1];
        u32 p[8];
#pragma unroll
        for (int e = 0; e < 4; ++e) p[e] = pack2(__builtin_amdgcn_exp2f(s[2 * e]), __builtin_amdgcn_exp2f(s[2 * e + 1]));
        SB();
        oacc = mfma32(v0, u32x4{p[0], p[1], p[2], p[3]}, oacc);
#pragma unroll
        for (int e = 4; e < 8; ++e) p[e] = pack2(__builtin_amdgcn_exp2f(s[2 * e]), __builtin_amdgcn_exp2f(s[2 * e + 1]));
        SB();
        oacc = mfma32(v1, u32x4{p[4], p[5], p[6], p[7]}, oacc);
        if (t + 1 < NB) { v0 = n0; v1 = n1; }
        SB();
    }
}

__device__ __forceinline__ void issue_qkv_tile(const Ctx& c, const Args& a, int t) {
    const u32 voff = (u32)(c.lane >> 2) * 3072u + (u32)(c.lane & 3) * 16u;
    const unsigned char* src = (const unsigned char*)(a.wqkv + qkv_col(t) + 32 * c.j) + (size_t)(c.w & 3) * 8 * 16 * 3072;
    const u32 dst = c.Laddr + slot_of(CQ(t, 0)) * SLOT + (c.w & 3) * 8192;
#pragma unroll
    for (int q = 0; q < 8; ++q) glds1k(src + (size_t)q * 16 * 3072, voff, dst + q * 1024);
}
template <bool TR> __device__ __forceinline__ void tile_colsum(const Ctx& c, const LAS unsigned char* lo, const LAS unsigned char* hi, int csidx) {
    float s[4] = {0.f, 0.f, 0.f, 0.f};
#pragma unroll
    for (int hf = 0; hf < 2; ++hf) {
        u32x4 f[16];
#pragma unroll
        for (int k = 0; k < 16; ++k) f[k] = ldf<TR>(hf ? hi : lo, k);
        SB();
#pragma unroll
        for (int k = 0; k < 16; ++k) {
            s[0] = dot2(f[k].x, BF_ONES, s[0]); s[1] = dot2(f[k].y, BF_ONES, s[1]); s[2] = dot2(f[k].z, BF_ONES, s[2]); s[3] = dot2(f[k].w, BF_ONES, s[3]);
        }
        SB();
    }
    const float t = xor32_sum((s[0] + s[1]) + (s[2] + s[3]));
    if (c.lane < 32) lds_st<float>(c.L, SC_CS + (csidx + c.lane) * 4, t);
}
__device__ __forceinline__ int tr_lane_off(int lane) {
    const int g4 = lane >> 4, h = g4 >> 1, nh = g4 & 1, li = lane & 15;
    return (8 * h + (li >> 2)) * 64 + nh * 32 + (li & 3) * 8;
}
__device__ __forceinline__ const LAS unsigned char* lds_base_raw(const Ctx& c, int off) {
    u32 a = c.Laddr + off;
    asm volatile("" : "+v"(a));
    return (const LAS unsigned char*)(size_t)a;
}

__device__ __forceinline__ void phase_qkv_att(const Ctx& c0, int layer, const Args& a) {
    const Ctx c = fresh(c0);
    const int lane = c.lane, w = c.w, tb = w & 3, h = lane >> 5, r = lane & 31;
    const u32 lbase = (u32)(WS_L0 + (size_t)layer * WS_LSTRIDE);
    const int tro = tr_lane_off(lane);
    STAMP(0);
    if (w < 4) {
        auto ready = [&]() {
            if (layer > 0 && w == 0) poll_flags<64>(c, layer - 1, 0, 16);
            BAR();
            STAMP(1);
        };
        u32x4 xf[32], wf[8];
        const LAS unsigned char* wK = lds_base_raw(c, slot_of(CQ(0, 0)) * SLOT + tro);
        const LAS unsigned char* wV = lds_base_raw(c, slot_of(CQ(1, 0)) * SLOT + tro);
        const LAS unsigned char* wQ = lds_base_raw(c, slot_of(CQ(2, 0)) * SLOT + tro);
        f32x16 accK = {}, accV = {}, accQ = {};
        f32x2 ms;
        if (layer == 0) ms = c_tile0<true, true, true>(c, accK, wf, wK, wK + SLOT, wV, xf, __builtin_amdgcn_make_buffer_rsrc((void*)a.x, 0, SEQ * DM * 2, 0x00020000),
                                                        (u32)((4 * c.i + tb) * 32 * 1024), ready);
        else ms = c_tile0<true, true, false>(c, accK, wf, wK, wK + SLOT, wV, xf, c.rs, (u32)(WS_L0 + L_X2N) + (u32)((4 * c.i + tb) * 32) * 1024, ready);
        if (lane < 32) lds_st<f32x2>(c.L, SC_ST + (tb * 32 + r) * 8, ms);
        const float msk = ms.x * ms.y;
        BARL(); STAMP(2);
        f32x4 cs[4];
#pragma unroll
        for (int q = 0; q < 4; ++q) cs[q] = lds_ld<f32x4>(c.L, SC_CS + (0 * 32 + 8 * q + 4 * h) * 4);
        f32x4 mr[8];
        float csv;
        {
            u32 o[8]; LS ls;
            c_tile<true, true, true>(accV, wf, wV, wV + SLOT, wQ, xf, [&](int k) {
                if (k >= 4 && k < 20) {
                    const int pi = (k - 4) >> 1, e = 2 * pi;
                    if (((k - 4) & 1) == 0) lnq_a(ls, accK[e], cs[e >> 2][e & 3], cs[e >> 2][(e & 3) + 1], ms.y, msk);
                    else o[pi] = lnq_b(ls, accK[e + 1], ms.y);
                }
                if (k == 20 || k == 21) {
                    const int hh = k - 20;
                    const u32x4 ov = {o[4 * hh], o[4 * hh + 1], o[4 * hh + 2], o[4 * hh + 3]};
                    lds_st<u32x4>(c.L, SC_KS + (tb * 2 + hh) * 1024 + c.voff, ov);
                    st_sc1(c, lbase + (u32)L_KF + (u32)((4 * c.i + tb) * 32 + 2 * c.j + hh) * 1024, ov);
                }
                if (k >= 22 && k < 30) { const int q = (k - 22) >> 1; mr[k - 22] = lds_ld<f32x4>(c.L, SC_ST + (tb * 32 + 8 * q + 4 * h + 2 * ((k - 22) & 1)) * 8); }
                if (k == 30) csv = lds_ld<float>(c.L, SC_CS + (32 + r) * 4);
            });
        }
        BARL();
        {
            u32 o[8]; LS ls;
            c_tile<false, false, true>(accQ, wf, wQ, wQ + SLOT, wQ, xf, [&](int k) {
                if (k >= 2 && k < 18) {
                    const int pi = (k - 2) >> 1;
                    if (((k - 2) & 1) == 0) lnv_a(ls, accV[2 * pi], accV[2 * pi + 1], mr[pi].x, mr[pi].z, mr[pi].y, csv);
                    else o[pi] = lnv_b(ls, mr[pi].w);
                }
                if (k == 18 || k == 20) {
                    const int sv = (k - 18) >> 1;
                    const u32x4 ov = {o[4 * sv], o[4 * sv + 1], o[4 * sv + 2], o[4 * sv + 3]};
                    lds_st<u32x4>(c.L, SC_VS + (tb * 2 + sv) * VREC + c.voff, ov);
                    st_sc1(c, lbase + (u32)L_VF + (u32)(((4 * c.i + tb) * 16 + c.j) * 2 + sv) * 1024, ov);
                }
                if (k >= 24 && k < 28) cs[k - 24] = lds_ld<f32x4>(c.L, SC_CS + (64 + 8 * (k - 24) + 4 * h) * 4);
            });
            if (lane < 2) { u32x4 ones = {BF_ONES, BF_ONES, BF_ONES, BF_ONES}; lds_st<u32x4>(c.L, SC_VS + (tb * 2 + lane) * VREC + 1024, ones); }
        }
        {
            const float sc = ms.y * QSCALE, msc = ms.x * sc;
            asm volatile("s_nop 15" : "+v"(accQ));
            u32 o[8]; LS ls[8];
#pragma unroll
            for (int pi = 0; pi < 8; ++pi) { const int e = 2 * pi; lnq_a(ls[pi], accQ[e], cs[e >> 2][e & 3], cs[e >> 2][(e & 3) + 1], sc, msc); }
#pragma unroll
            for (int pi = 0; pi < 8; ++pi) o[pi] = lnq_b(ls[pi], accQ[2 * pi + 1], sc);
#pragma unroll
            for (int hh = 0; hh < 2; ++hh) lds_st<u32x4>(c.L, SC_QS + (tb * 2 + hh) * 1024 + c.voff, u32x4{o[4 * hh], o[4 * hh + 1], o[4 * hh + 2], o[4 * hh + 3]});
        }
        WAIT_VM(0);
        BAR(); STAMP(5);
        if (w == 0 && lane < 2 && c.i + 1 + lane < 16) flag_store(c, layer, 1, c.i + 1 + lane, c.j, lane);
    } else {
        WAIT_VM(16);
        BAR();
        WAIT_VM(8);
        BARL();
        const int cb = c.j & 7, sidx = c.i * 2 + (c.j >> 3);
        if (layer == 0) {
            const int hw = w & 3;
            LAS unsigned char* t = c.L + SC_VS + hw * 1024;
            WItem it[6]; u32x4 v[6];
#pragma unroll
            for (int q = 0; q < 6; ++q) {
                int f;
                const int fD = NF_WO + NF_WG + cb * 224 + sidx * 7, fO = cb * 64 + sidx * 2;
                if (q < 2 || (q < 4 && hw < 3)) { const int ks = 2 * c.i + (q & 1), cc = hw + 4 * (q >> 1); f = NF_WO + c.j * 224 + (2 * cc + (ks >> 4)) * 16 + (ks & 15); }
                else if (q < 4) f = fD + 4 + (q & 1);
                else if (q == 4) f = fD + (hw < 3 ? hw : 6);
                else f = hw < 2 ? fO + hw : fD + 3;
                it[q] = w_item(a, f, lane);
                v[q] = *(const u32x4*)it[q].src;
            }
            const int k = lane >> 2, n8 = lane & 3;
            const int g4 = lane >> 4, hh = g4 >> 1, nh = g4 & 1, li = lane & 15, qq = li >> 2, p = li & 3;
            float part = 0.f;
#pragma unroll
            for (int q = 0; q < 6; ++q) {
                if (q < 5 || hw < 3) {
                    lds_st<u32x4>(t, k * 64 + n8 * 16, v[q]);
                    asm volatile("s_waitcnt lgkmcnt(0)" ::: "memory");
                    const s16x4 lo = __builtin_amdgcn_ds_read_tr16_b64_v4i16((LAS s16x4*)(t + (4 * hh + qq) * 64 + nh * 32 + p * 8));
                    const s16x4 hi = __builtin_amdgcn_ds_read_tr16_b64_v4i16((LAS s16x4*)(t + (8 + 4 * hh + qq) * 64 + nh * 32 + p * 8));
                    const u32x2 l2 = __builtin_bit_cast(u32x2, lo), h2 = __builtin_bit_cast(u32x2, hi);
                    __builtin_amdgcn_raw_buffer_store_b128(u32x4{l2.x, l2.y, h2.x, h2.y}, c.rs, c.voff, (u32)it[q].dst, 16);
                    if (q < 2 || (q < 4 && hw < 3)) {
                        if ((q & 1) == 0) part = 0.f;
                        part = dot2(l2.x, BF_ONES, part); part = dot2(l2.y, BF_ONES, part); part = dot2(h2.x, BF_ONES, part); part = dot2(h2.y, BF_ONES, part);
                        if (q & 1) {
                            const float tot = xor32_sum(part);
                            const int cc = hw + 4 * (q >> 1);
                            if (lane < 32) __builtin_amdgcn_raw_buffer_store_b32(__builtin_bit_cast(u32, tot), c.rs, (u32)lane * 4u, (u32)WS_CSP + (u32)((c.j * 16 + c.i) * 224 + cc * 32) * 4u, 16);
                        }
                    }
                    asm volatile("s_waitcnt lgkmcnt(0)" ::: "memory");
                }
            }
        }
        WAIT_VM(0);
        if (layer == 0 && w < 7) {
            const int t = w - 4;
            const LAS unsigned char* wb = lds_base_raw(c, (t == 0 ? slot_of(CQ(0, 0)) : t == 1 ? slot_of(CQ(1, 0)) : slot_of(CQ(2, 0))) * SLOT + tro);
            tile_colsum<true>(c, wb, wb + SLOT, 32 * t);
        }
        BAR();
        if (layer == 0 && w == 4) {
            if (lane < 16) flag_store(c, 0, 5, lane, c.j, c.i);
            if (lane < 32) flag_store(c, 0, 6, lane >> 1, cb + 8 * (lane & 1), sidx);
        }
        BAR();
        if (layer == 0) poll_flags<64>(c, 0, 6, 32);
        issue_chunk<CO(0)>(c); issue_chunk<CO(1)>(c); issue_chunk<CO(2)>(c); issue_chunk<CO(3)>(c);
        BAR();
        if (layer == 0) {
            poll_flags<64>(c, 0, 5, 16);
            const int col = (w & 3) * 64 + lane;
            if (col < 224) {
                float t = 0.f;
#pragma unroll
                for (int i2 = 0; i2 < 16; ++i2) t += __builtin_bit_cast(float, __builtin_amdgcn_raw_buffer_load_b32(c.rs, (u32)col * 4u, (u32)WS_CSP + (u32)((c.j * 16 + i2) * 224) * 4u, 16));
                lds_st<float>(c.L, SC_CS + (96 + col) * 4, t);
            }
        }
        issue_chunk<CG(0, 0)>(c); issue_chunk<CG(0, 1)>(c);
    }
    const int qb = w < 4 ? w : 7 - w, hh = w >> 2;
    const u32x4 qf = lds_ld<u32x4>(c.L, SC_QS + (qb * 2 + hh) * 1024 + c.voff);
    const int vlane = (((lane >> 4) & 1) == hh) ? lane * 16 : 1024;
    f32x16 oacc = {};
    float m = 0.f;
    const LAS unsigned char* mk = lds_base(c, SC_MK);
    {
        const LAS unsigned char* kb = c.L + SC_KS + hh * 1024 + c.voff;
        const LAS unsigned char* vb = c.L + SC_VS + vlane;
        if (qb == 0) att_segment<1, 0, 0, 1, true>(oacc, m, kb, vb, mk, qf);
        else if (qb == 1) att_segment<2, 1, 0, 1, true>(oacc, m, kb, vb, mk, qf);
        else if (qb == 2) att_segment<3, 2, 0, 1, true>(oacc, m, kb, vb, mk, qf);
        else att_segment<4, 3, 0, 1, true>(oacc, m, kb, vb, mk, qf);
    }
    STAMP(6);
    if (c.i > 0) {
        if (w == 0) poll_flags<64>(c, layer, 1, c.i >= 2 ? 2 : 1);
        BAR();
        STAMP(7);
        {
            const int idx = w;
            const int g = 4 * c.i - 8 + idx;
            if (g >= 0) {
                const u32x4 s0 = ld_sc1(c, lbase + (u32)L_KF + (u32)(g * 32 + 2 * c.j + 0) * 1024);
                const u32x4 s1 = ld_sc1(c, lbase + (u32)L_KF + (u32)(g * 32 + 2 * c.j + 1) * 1024);
                const u32x4 s2 = ld_sc1(c, lbase + (u32)L_VF + (u32)((g * 16 + c.j) * 2 + 0) * 1024);
                const u32x4 s3 = ld_sc1(c, lbase + (u32)L_VF + (u32)((g * 16 + c.j) * 2 + 1) * 1024);
                lds_st<u32x4>(c.L, SC_RK + (idx * 2 + 0) * 1024 + c.voff, s0);
                lds_st<u32x4>(c.L, SC_RK + (idx * 2 + 1) * 1024 + c.voff, s1);
                lds_st<u32x4>(c.L, SC_RV + (idx * 2 + 0) * VREC + c.voff, s2);
                lds_st<u32x4>(c.L, SC_RV + (idx * 2 + 1) * VREC + c.voff, s3);
                if (lane < 2) { u32x4 ones = {BF_ONES, BF_ONES, BF_ONES, BF_ONES}; lds_st<u32x4>(c.L, SC_RV + (idx * 2 + lane) * VREC + 1024, ones); }
            }
        }
        BAR();
        STAMP(8);
        const LAS unsigned char* kb = c.L + SC_RK + hh * 1024 + c.voff;
        const LAS unsigned char* vb = c.L + SC_RV + vlane;
        if (c.i == 1) att_segment<4, 4, 5, 0, false>(oacc, m, kb, vb, mk, qf);
        else if (qb == 0) att_segment<8, 0, 1, 2, false>(oacc, m, kb, vb, mk, qf);
        else if (qb == 1) att_segment<7, 1, 2, 2, false>(oacc, m, kb, vb, mk, qf);
        else if (qb == 2) att_segment<6, 2, 3, 2, false>(oacc, m, kb, vb, mk, qf);
        else att_segment<5, 3, 4, 2, false>(oacc, m, kb, vb, mk, qf);
    }
    {
        u32x4 o;
        if (hh == 0) {
            float il = __builtin_amdgcn_rcpf(oacc[8]);
            asm volatile("s_nop 1" : "+v"(il));
            o = u32x4{pack2(smul(oacc[0], il), smul(oacc[1], il)), pack2(smul(oacc[2], il), smul(oacc[3], il)), pack2(smul(oacc[4], il), smul(oacc[5], il)), pack2(smul(oacc[6], il), smul(oacc[7], il))};
        } else {
            float il = __builtin_amdgcn_rcpf(oacc[0]);
            asm volatile("s_nop 1" : "+v"(il));
            o = u32x4{pack2(smul(oacc[8], il), smul(oacc[9], il)), pack2(smul(oacc[10], il), smul(oacc[11], il)), pack2(smul(oacc[12], il), smul(oacc[13], il)), pack2(smul(oacc[14], il), smul(oacc[15], il))};
        }
        st_sc1(c, lbase + (u32)L_OF + (u32)((4 * c.i + qb) * 32 + 2 * c.j + hh) * 1024, o);
    }
    STAMP(9);
    WAIT_VM(0);
    BAR();
    if (w == 0) signal_group(c, layer, 2);
    if (w >= 4) { issue_chunk<CG(1, 0)>(c); issue_chunk<CG(1, 1)>(c); }
    STAMP(10);
}

__device__ __forceinline__ void red_write(const Ctx& c, const f32x16& acc0, const f32x16& acc1, int t2, int kq) {
#pragma unroll
    for (int d = 0; d < 4; ++d) {
        const int fin = d & 1;
        if (kq != fin) {
            const int slot = kq < fin ? kq : kq - 1;
            const int base = red_off(t2 * 4 + d) + slot * 2048 + c.voff;
            const f32x16& a = d < 2 ? acc0 : acc1;
            const int o = (d & 1) * 8;
            lds_st<f32x4>(c.L, base, f32x4{a[o], a[o + 1], a[o + 2], a[o + 3]});
            lds_st<f32x4>(c.L, base + 1024, f32x4{a[o + 4], a[o + 5], a[o + 6], a[o + 7]});
        }
    }
}
__device__ __forceinline__ void red_read(const Ctx& c, const f32x16& acc0, const f32x16& acc1, int t2, int kq, float (&v)[2][8]) {
#pragma unroll
    for (int e = 0; e < 8; ++e) { v[0][e] = kq ? acc0[8 + e] : acc0[e]; v[1][e] = kq ? acc1[8 + e] : acc1[e]; }
#pragma unroll
    for (int x = 0; x < 2; ++x)
#pragma unroll
        for (int s = 0; s < 3; ++s) {
            const int base = red_off(t2 * 4 + 2 * x + kq) + s * 2048 + c.voff;
            const f32x4 lo = lds_ld<f32x4>(c.L, base), hi = lds_ld<f32x4>(c.L, base + 1024);
            v[x][0] += lo.x; v[x][1] += lo.y; v[x][2] += lo.z; v[x][3] += lo.w; v[x][4] += hi.x; v[x][5] += hi.y; v[x][6] += hi.z; v[x][7] += hi.w;
        }
}
__device__ __forceinline__ u32x4 add_res(const float (&v)[8], const u32x4& res) {
    return u32x4{pack2(v[0] + bflo(res.x), v[1] + bfhi(res.x)), pack2(v[2] + bflo(res.y), v[3] + bfhi(res.y)),
                 pack2(v[4] + bflo(res.z), v[5] + bfhi(res.z)), pack2(v[6] + bflo(res.w), v[7] + bfhi(res.w))};
}

__device__ __forceinline__ void phase_wo(const Ctx& c0, int layer, const Args& a) {
    const Ctx c = fresh(c0);
    const int w = c.w, t2 = w & 1, kq = w >> 1;
    const int th = c.j >> 3, cb = c.j & 7;
    const int tbk = 4 * c.i + 2 * th + t2;
    const u32 lbase = (u32)(WS_L0 + (size_t)layer * WS_LSTRIDE);
    if (w == 0) poll_flags<64>(c, layer, 2, 16);
    if (w >= 4) WAIT_VM(8);
    BAR();
    STAMP(11);
    u32x4 xf[8];
#pragma unroll
    for (int s = 0; s < 8; ++s) xf[s] = ld_sc1(c, lbase + (u32)L_OF + (u32)(tbk * 32 + 8 * kq + s) * 1024);
    u32x4 res[2];
    if (w < 4) {
#pragma unroll
        for (int x = 0; x < 2; ++x) {
            const int kbo = 4 * cb + 2 * x + kq;
            if (layer == 0) {
                const u16* src = a.x + (size_t)(tbk * 32 + (c.lane & 31)) * DM + kbo * 16 + 4 * (c.lane >> 5);
                const u32x2 lo = *(const u32x2*)src, hi = *(const u32x2*)(src + 8);
                res[x] = u32x4{lo.x, lo.y, hi.x, hi.y};
            } else {
                res[x] = ld_sc1(c, (u32)(WS_L0 + L_X2) + (u32)(tbk * 32 + kbo) * 1024);
            }
        }
    }
    u32x4 wf[16];
#pragma unroll
    for (int ch = 0; ch < 4; ++ch) {
        const LAS unsigned char* wb = lds_base(c, slot_of(CO(ch)) * SLOT + kq * 4096);
#pragma unroll
        for (int q = 0; q < 4; ++q) wf[4 * ch + q] = LDF(wb, q);
    }
    SB();
    f32x16 acc0 = {}, acc1 = {};
#pragma unroll
    for (int s = 0; s < 8; ++s) {
        acc0 = mfma32(wf[2 * s], xf[s], acc0);
        acc1 = mfma32(wf[2 * s + 1], xf[s], acc1);
        SB();
    }
    BAR();
    STAMP(12);
    red_write(c, acc0, acc1, t2, kq);
    BAR();
    if (w < 4) {
        float v[2][8];
        red_read(c, acc0, acc1, t2, kq, v);
#pragma unroll
        for (int x = 0; x < 2; ++x) st_sc1(c, lbase + (u32)L_X1 + (u32)(tbk * 32 + 4 * cb + 2 * x + kq) * 1024, add_res(v[x], res[x]));
        STAMP(13);
        WAIT_VM(0);
    }
    BAR();
    if (w == 0) signal_group(c, layer, 3);
    STAMP(14);
}

__device__ __forceinline__ int gu_slot(int T) { return (int)((0x02046204u >> (4 * (T & 7))) & 15u); }
__device__ __forceinline__ void gu_tile(const Ctx& c, int T, f32x16& acc, const f32x16& prev, u32x4 (&wf)[8], const u32x4 (&xf)[32], int h,
                                        f32x4 (&cs)[4], const GK& gk, u32 hbase) {
    const LAS unsigned char* cur = lds_base(c, gu_slot(T) * SLOT);
    const LAS unsigned char* nxt = lds_base(c, gu_slot(T < 6 ? T + 1 : T) * SLOT);
    const LAS unsigned char* csb = lds_base_raw(c, SC_CS + (96 + T * 32 + 4 * h) * 4);
    GS g;
    u32 o[4];
    acc = f32x16{};
    SB();
    c_tile<false, true, false>(acc, wf, cur, cur + SLOT, nxt, xf, [&](int k) {
        const int p = k >> 3, e = 2 * p, q2 = p >> 1, e2 = e & 3;
        if ((k & 7) < 7) geglu_sub(g, o[p], k & 7, prev[e], prev[e + 1], prev[8 + e], prev[8 + e + 1], cs[q2][e2], cs[q2][e2 + 1], cs[2 + q2][e2], cs[2 + q2][e2 + 1], gk);
        if (k == 15) cs[0] = *(const LAS f32x4*)(csb);
        if (k == 15) cs[2] = *(const LAS f32x4*)(csb + 64);
        if (k == 31) cs[1] = *(const LAS f32x4*)(csb + 32);
        if (k == 31) cs[3] = *(const LAS f32x4*)(csb + 96);
    });
    st_sc1(c, hbase + (u32)(T - 1) * 1024, u32x4{o[0], o[1], o[2], o[3]});
}
static_assert(slot_of(CG(0, 0)) == 4 && slot_of(CG(1, 0)) == 0 && slot_of(CG(2, 0)) == 2 && slot_of(CG(3, 0)) == 6 && slot_of(CG(4, 0)) == 4 && slot_of(CG(5, 0)) == 0 && slot_of(CG(6, 0)) == 2, "gu_slot");

__device__ __forceinline__ void phase_gu(const Ctx& c0, int layer) {
    const Ctx c = fresh(c0);
    const int lane = c.lane, w = c.w, tb = w & 3, h = lane >> 5;
    const u32 lbase = (u32)(WS_L0 + (size_t)layer * WS_LSTRIDE);
    if (w < 4) {
        u32x4 xf[32], wf[8];
        const LAS unsigned char* w0 = lds_base(c, slot_of(CG(0, 0)) * SLOT);
        const LAS unsigned char* w1 = lds_base(c, slot_of(CG(1, 0)) * SLOT);
        f32x16 a0 = {}, a1;
        const f32x2 ms = c_tile0<true, false, false>(c, a0, wf, w0, w0 + SLOT, w1, xf, c.rs, lbase + (u32)L_X1 + (u32)((4 * c.i + tb) * 32) * 1024, [&]() {
            if (w == 0) poll_flags<64>(c, layer, 3, 16);
            BAR();
            STAMP(15);
        });
        constexpr float GC1 = -2.0f * 0.7978845608028654f * 1.4426950408889634f, GC2 = GC1 * 0.044715f;
        GK gk; gk.mean = ms.x; gk.A = ms.y * GC1; gk.B = ms.y * ms.y * ms.y * GC2; gk.ir2 = 1.0f / (ms.y * ms.y);
        BARL(); STAMP(16);
        f32x4 cs[4];
#pragma unroll
        for (int q = 0; q < 4; ++q) cs[q] = lds_ld<f32x4>(c.L, SC_CS + (96 + (q >> 1) * 16 + 8 * (q & 1) + 4 * h) * 4);
        const u32 hbase = lbase + (u32)L_HF + (u32)((4 * c.i + tb) * 112 + 7 * c.j) * 1024;
#pragma unroll 1
        for (int T = 1; T < 7; T += 2) {
            gu_tile(c, T, a1, a0, wf, xf, h, cs, gk, hbase);
            BARL();
            gu_tile(c, T + 1, a0, a1, wf, xf, h, cs, gk, hbase);
            if (T < 5) BARL();
        }
        STAMP(22);
        {
            asm volatile("s_nop 15" : "+v"(a0));
            GS g[4];
            u32 o[4];
#pragma unroll
            for (int q = 0; q < 7; ++q)
#pragma unroll
                for (int p = 0; p < 4; ++p) {
                    const int e = 2 * p, q2 = p >> 1, e2 = e & 3;
                    geglu_sub(g[p], o[p], q, a0[e], a0[e + 1], a0[8 + e], a0[8 + e + 1], cs[q2][e2], cs[q2][e2 + 1], cs[2 + q2][e2], cs[2 + q2][e2 + 1], gk);
                }
            st_sc1(c, hbase + 6u * 1024, u32x4{o[0], o[1], o[2], o[3]});
        }
        STAMP(21);
        WAIT_VM(0);
        BAR();
        STAMP(23);
        if (w == 0) signal_group(c, layer, 4);
    } else {
        WAIT_VM(0);
        BAR();
        __builtin_amdgcn_s_sleep(16);
        issue_chunk<CG(2, 0)>(c); issue_chunk<CG(2, 1)>(c); issue_chunk<CG(3, 0)>(c); issue_chunk<CG(3, 1)>(c);
        if (layer == 1 && w == 7 && c.i == 0 && c.j == 0) {
            for (u32 spins = 0;; ++spins) {
                asm volatile("" ::: "memory");
                const u32x4 f = __builtin_amdgcn_raw_buffer_load_b128(c.rs, c.voff, (u32)WS_STARTED, 16);
                if (__all(f.x == c.tag && f.y == c.tag && f.z == c.tag && f.w == c.tag)) { if (lane == 0) __builtin_amdgcn_raw_buffer_store_b32(c.tag + 31u, c.rs, (u32)WS_EPOCH, 0, 16); break; }
                if (spins > SPIN_CAP) break;
                __builtin_amdgcn_s_sleep(1);
            }
        }
        WAIT_VM(8);
        BAR();
        issue_chunk<CG(4, 0)>(c); issue_chunk<CG(4, 1)>(c);
        WAIT_VM(8);
        BAR();
        issue_chunk<CG(5, 0)>(c); issue_chunk<CG(5, 1)>(c);
        WAIT_VM(8);
        BAR();
        issue_chunk<CG(6, 0)>(c); issue_chunk<CG(6, 1)>(c);
        WAIT_VM(8);
        BAR();
        issue_chunk<CD(0)>(c); issue_chunk<CD(1)>(c);
        WAIT_VM(8);
        BAR();
        issue_chunk<CD(2)>(c); issue_chunk<CD(3)>(c);
        BAR();
        issue_chunk<CD(4)>(c); issue_chunk<CD(5)>(c);
        BAR();
        issue_chunk<CD(6)>(c); issue_chunk<CD(7)>(c);
    }
}

__device__ __forceinline__ void phase_dn(const Ctx& c0, int layer, const Args& a) {
    const Ctx c = fresh(c0);
    const int lane = c.lane, w = c.w, t2 = w & 1, kq = w >> 1, h = lane >> 5, r = lane & 31;
    const int th = c.j >> 3, cb = c.j & 7;
    const int tbk = 4 * c.i + 2 * th + t2;
    const u32 lbase = (u32)(WS_L0 + (size_t)layer * WS_LSTRIDE);
    const u32 xsrc = lbase + (u32)L_HF + (u32)(tbk * 112 + 28 * kq) * 1024;
    if (w >= 4) WAIT_VM(8);
    BAR();
    u32x4 wr[24];
#pragma unroll
    for (int ch = 0; ch < 6; ++ch) {
        const LAS unsigned char* wb = lds_base(c, chunk_lds(CD(ch)) + kq * 4096);
#pragma unroll
        for (int q = 0; q < 4; ++q) wr[4 * ch + q] = LDF(wb, q);
    }
    BAR();
    if (w >= 4) {
        issue_chunk<CD(8)>(c); issue_chunk<CD(9)>(c); issue_chunk<CD(10)>(c);
        issue_chunk<CD(11)>(c); issue_chunk<CD(12)>(c); issue_chunk<CD(13)>(c);
    }
    if (w == 0) poll_flags<64>(c, layer, 4, 16);
    BAR();
    STAMP(25);
    u32x4 res[2];
    if (w < 4) {
        res[0] = ld_sc1(c, lbase + (u32)L_X1 + (u32)(tbk * 32 + 4 * cb + kq) * 1024);
        res[1] = ld_sc1(c, lbase + (u32)L_X1 + (u32)(tbk * 32 + 4 * cb + 2 + kq) * 1024);
    }
    constexpr int XD = 10;
    u32x4 xf[28];
#pragma unroll
    for (int s = 0; s < XD; ++s) xf[s] = ld_sc1(c, xsrc + (u32)s * 1024);
    SB();
    f32x16 acc0 = {}, acc1 = {};
#pragma unroll
    for (int s = 0; s < 12; ++s) {
        xf[s + XD] = ld_sc1(c, xsrc + (u32)(s + XD) * 1024);
        acc0 = mfma32(wr[2 * s], xf[s], acc0);
        acc1 = mfma32(wr[2 * s + 1], xf[s], acc1);
        PIN(acc0); PIN(acc1);
        SB();
    }
    WAIT_VM(22);
    BARL();
    STAMP(24);
    {
        const LAS unsigned char* wb[8];
#pragma unroll
        for (int ch = 0; ch < 8; ++ch) wb[ch] = lds_base(c, chunk_lds(CD(6 + ch)) + kq * 4096);
        u32x4 wf[8];
#pragma unroll
        for (int q = 0; q < 8; ++q) wf[q] = LDF(wb[q >> 2], q & 3);
        SB();
#pragma unroll
        for (int s = 0; s < 16; ++s) {
            const u32x4 w0 = wf[(2 * s) & 7], w1 = wf[(2 * s + 1) & 7];
            if (12 + s + XD < 28) xf[12 + s + XD] = ld_sc1(c, xsrc + (u32)(12 + s + XD) * 1024);
            if (2 * s + 8 < 32) {
                wf[(2 * s) & 7] = LDF(wb[(2 * s + 8) >> 2], (2 * s + 8) & 3);
                wf[(2 * s + 1) & 7] = LDF(wb[(2 * s + 9) >> 2], (2 * s + 9) & 3);
            }
            acc0 = mfma32(w0, xf[12 + s], acc0);
            acc1 = mfma32(w1, xf[12 + s], acc1);
            PIN(acc0); PIN(acc1);
            SB();
        }
    }
    BAR();
    STAMP(26);
    red_write(c, acc0, acc1, t2, kq);
    BAR();
    if (w >= 4) {
        if (layer == 0) issue_qkv_tile(c, a, 0);
    } else {
        float v[2][8];
        red_read(c, acc0, acc1, t2, kq, v);
#pragma unroll
        for (int x = 0; x < 2; ++x) {
            const int kbo = 4 * cb + 2 * x + kq;
            const u32x4 o = add_res(v[x], res[x]);
            const auto r0 = __builtin_amdgcn_permlane32_swap(o.x, o.z, false, false);
            const auto r1 = __builtin_amdgcn_permlane32_swap(o.y, o.w, false, false);
            const u32x4 on = u32x4{r0[0], r1[0], r0[1], r1[1]};
            if (layer == 1) {
                *(u32x4*)(a.out + (size_t)(tbk * 32 + r) * DM + kbo * 16 + 8 * h) = on;
            } else {
                st_sc1(c, lbase + (u32)L_X2N + (u32)(tbk * 32 + kbo) * 1024, on);
                st_sc1(c, lbase + (u32)L_X2 + (u32)(tbk * 32 + kbo) * 1024, o);
            }
        }
        STAMP(27);
        if (layer == 0) WAIT_VM(0);
    }
    if (layer == 0) {
        BAR();
        if (w == 0) signal_group(c, layer, 0);
        if (w >= 4) { issue_qkv_tile(c, a, 1); issue_qkv_tile(c, a, 2); }
        STAMP(28);
    }
}

__global__ void __launch_bounds__(512, 2) mega_kernel(Args a) {
    extern __shared__ __attribute__((aligned(16))) unsigned char lds[];
    Ctx c;
    c.L = (LAS unsigned char*)lds;
    c.Laddr = (u32)(size_t)c.L;
    c.ws = a.ws;
    c.rs = __builtin_amdgcn_make_buffer_rsrc((void*)a.ws, 0, (int)WS_END, 0x00020000);
    const int tid = threadIdx.x;
    c.lane = tid & 63;
    c.w = __builtin_amdgcn_readfirstlane(tid >> 6);
    const int bid = blockIdx.x;
    c.i = 2 * (bid & 7) + (bid >> 7);
    c.j = (bid >> 3) & 15;
    c.voff = c.lane * 16;
    {
        c.tag = 0;
        if (c.w >= 4) { issue_qkv_tile(c, a, 0); issue_qkv_tile(c, a, 1); issue_qkv_tile(c, a, 2); }
        u32 e;
        asm volatile("s_load_dword %0, %1, 0x0\n\ts_waitcnt lgkmcnt(0)" : "=s"(e) : "s"(a.ws + WS_EPOCH) : "memory");
        c.tag = e + 1u;
        if (tid == 0) __builtin_amdgcn_raw_buffer_store_b32(c.tag, c.rs, (u32)bid * 4u, (u32)WS_STARTED, 16);
    }
    if (c.w < 4) {
        const int hh = c.lane >> 5, row = c.lane & 31, half = c.w & 1;
        u32 d[4];
#pragma unroll
        for (int q = 0; q < 4; ++q) {
            const int c0 = 16 * half + 8 * (q >> 1) + 4 * hh + 2 * (q & 1), c1 = c0 + 1;
            const u32 lo = c.w < 2 ? (row > c0 ? 0xff7fu : 0u) : (row == c0 ? 0x3f80u : 0u);
            const u32 hi = c.w < 2 ? (row > c1 ? 0xff7fu : 0u) : (row == c1 ? 0x3f80u : 0u);
            d[q] = lo | (hi << 16);
        }
        lds_st<u32x4>(c.L, SC_MK + c.w * 1024 + c.voff, u32x4{d[0], d[1], d[2], d[3]});
    }
    for (int layer = 0; layer < 2; ++layer) {
        phase_qkv_att(c, layer, a);
        phase_wo(c, layer, a);
        phase_gu(c, layer);
        phase_dn(c, layer, a);
    }
}

}

extern "C" void kernel_launch(void* const* d_in, const int* in_sizes, int n_in, void* d_out, int out_size, void* d_ws, size_t ws_size,
                              hipStream_t stream) {
    Args a;
    a.x = (const u16*)d_in[0]; a.wqkv = (const u16*)d_in[1]; a.wo = (const u16*)d_in[2];
    a.wg = (const u16*)d_in[4]; a.wu = (const u16*)d_in[5]; a.wd = (const u16*)d_in[6];
    a.out = (u16*)d_out; a.ws = (unsigned char*)d_ws;
    (void)hipFuncSetAttribute((const void*)mega_kernel, hipFuncAttributeMaxDynamicSharedMemorySize, LDS_BYTES);
    mega_kernel<<<256, 512, LDS_BYTES, stream>>>(a);
}
```

```cpp
#include <hip/hip_runtime.h>
#include <hip/hip_bf16.h>

#define LAS __attribute__((address_space(3)))
#define GAS __attribute__((address_space(1)))
typedef unsigned u32;
typedef unsigned short u16;
typedef u32 u32x4 __attribute__((ext_vector_type(4)));
typedef u32 u32x2 __attribute__((ext_vector_type(2)));
typedef float f32x16 __attribute__((ext_vector_type(16)));
typedef float f32x4 __attribute__((ext_vector_type(4)));
typedef float f32x2 __attribute__((ext_vector_type(2)));
typedef __bf16 bf16x8_t __attribute__((ext_vector_type(8)));
typedef __bf16 bf16x2_t __attribute__((ext_vector_type(2)));
typedef short s16x4 __attribute__((ext_vector_type(4)));

namespace {

constexpr int SEQ = 2048, DM = 512, FF = 1792;
constexpr size_t KiB = 1024, MiB = 1024 * 1024;
constexpr size_t WS_FLAGS = 1 * MiB;
constexpr size_t WS_EPOCH = 640 * KiB;
constexpr size_t WS_STARTED = WS_EPOCH + 4096;
constexpr size_t WS_TMO = 768 * KiB;
constexpr size_t WS_CSP = 3 * MiB + 256 * KiB;
constexpr size_t WS_WO = 4 * MiB + 512 * KiB;
constexpr size_t WS_WG = 5 * MiB;
constexpr size_t WS_WD = 9 * MiB;
constexpr size_t WS_L0 = 16 * MiB, WS_LSTRIDE = 32 * MiB;
constexpr size_t L_KF = 0, L_VF = 2 * MiB, L_OF = 4 * MiB, L_X1 = 6 * MiB, L_X2 = 8 * MiB, L_HF = 10 * MiB, L_X2N = 18 * MiB;
constexpr size_t WS_END = WS_L0 + 2 * WS_LSTRIDE;

constexpr int NSLOT = 8, SLOT = 16384;
constexpr int LDS_SCR = NSLOT * SLOT;
constexpr int VREC = 1040;
constexpr int SC_RV = LDS_SCR;
constexpr int SC_VS = SC_RV + 16640;
constexpr int SC_ST = SC_VS + 8320;
constexpr int SC_CS = SC_ST + 1024;
constexpr int SC_MK = SC_CS + 1280;
constexpr int LDS_BYTES = SC_MK + 4096;
constexpr int SC_KS = 0 * SLOT;
constexpr int SC_QS = 0 * SLOT + 8192;
constexpr int SC_RK = 1 * SLOT;
static_assert(LDS_BYTES <= 163840, "LDS");
__device__ __forceinline__ int red_off(int q) { return q < 5 ? 2 * SLOT + q * 6144 : 6 * SLOT + (q - 5) * 6144; }

constexpr float LN_EPS = 1e-5f;
constexpr float QSCALE = 0.25f * 1.4426950408889634f;
constexpr u32 SPIN_CAP = 1u << 17;
constexpr u32 BF_ONES = 0x3f803f80u;

struct Args {
    const u16* x; const u16* wqkv; const u16* wo; const u16* wg; const u16* wu; const u16* wd;
    u16* out; unsigned char* ws;
};

__device__ __forceinline__ f32x16 mfma32(u32x4 a, u32x4 b, f32x16 c) {
    return __builtin_amdgcn_mfma_f32_32x32x16_bf16(__builtin_bit_cast(bf16x8_t, a), __builtin_bit_cast(bf16x8_t, b), c, 0, 0, 0);
}
__device__ __forceinline__ u32 pack2(float lo, float hi) {
    f32x2 v = {lo, hi};
    return __builtin_bit_cast(u32, __builtin_convertvector(v, bf16x2_t));
}
__device__ __forceinline__ float bflo(u32 u) { return __builtin_bit_cast(float, u << 16); }
__device__ __forceinline__ float bfhi(u32 u) { return __builtin_bit_cast(float, u & 0xffff0000u); }
__device__ __forceinline__ float dot2(u32 a, u32 b, float c) {
    return __builtin_amdgcn_fdot2_f32_bf16(__builtin_bit_cast(bf16x2_t, a), __builtin_bit_cast(bf16x2_t, b), c, false);
}
__device__ __forceinline__ void swap32(float& a, float& b) {
    asm volatile("s_nop 1\n\tv_permlane32_swap_b32 %0, %1\n\ts_nop 1" : "+v"(a), "+v"(b));
}
__device__ __forceinline__ float fmx(float a, float b) { return __builtin_elementwise_maximum(a, b); }
__device__ __forceinline__ float xor32_max(float v) { float a = v, b = v; swap32(a, b); return fmx(a, b); }
__device__ __forceinline__ float xor32_sum(float v) { float a = v, b = v; swap32(a, b); return a + b; }
#define BAR() do { asm volatile("s_waitcnt lgkmcnt(0)" ::: "memory"); __builtin_amdgcn_s_barrier(); asm volatile("" ::: "memory"); } while (0)
#define BARL() do { asm volatile("" ::: "memory"); __builtin_amdgcn_s_barrier(); asm volatile("" ::: "memory"); } while (0)
#define WAIT_VM(n) asm volatile("s_waitcnt vmcnt(" #n ")" ::: "memory")
#define SB() __builtin_amdgcn_sched_barrier(0)

template <class T> __device__ __forceinline__ T lds_ld(const LAS unsigned char* base, int off) { return *(const LAS T*)(base + off); }
template <class T> __device__ __forceinline__ void lds_st(LAS unsigned char* base, int off, T v) { *(LAS T*)(base + off) = v; }

__device__ __forceinline__ void glds4k(const unsigned char* src, u32 voff, u32 dst) {
    u32 keep;
    asm volatile(
        "s_mov_b32 %0, m0\n\t"
        "s_mov_b32 m0, %3\n\t"
        "s_nop 0\n\t"
        "global_load_lds_dwordx4 %1, %2 sc1\n\t"
        "global_load_lds_dwordx4 %1, %2 offset:1024 sc1\n\t"
        "global_load_lds_dwordx4 %1, %2 offset:2048 sc1\n\t"
        "global_load_lds_dwordx4 %1, %2 offset:3072 sc1\n\t"
        "s_mov_b32 m0, %0"
        : "=&s"(keep) : "v"(voff), "s"(src), "s"(dst) : "memory");
}

__device__ __forceinline__ void glds1k(const unsigned char* src, u32 voff, u32 dst) {
    u32 keep;
    asm volatile(
        "s_mov_b32 %0, m0\n\t"
        "s_mov_b32 m0, %3\n\t"
        "s_nop 0\n\t"
        "global_load_lds_dwordx4 %1, %2\n\t"
        "s_mov_b32 m0, %0"
        : "=&s"(keep) : "v"(voff), "s"(src), "s"(dst) : "memory");
}
#define PIN(x) asm volatile("" : "+v"(x))

constexpr int NF_WO = 8 * 64, NF_WG = 16 * 224, NF_WD = 8 * 224;
__host__ __device__ constexpr int qkv_col(int t) { return t == 0 ? 512 : (t == 1 ? 1024 : 0); }
__host__ __device__ constexpr int dn_kstep(int s) { return s < 16 ? 7 * (s >> 2) + (s & 3) : 7 * ((s - 16) / 3) + 4 + (s - 16) % 3; }
struct WItem { const u16* src; size_t dst; };
__device__ __forceinline__ WItem w_item(const Args& a, int f, int lane) {
    const u16 *pA, *pB; int ld, ks; size_t dst;
    if (f < NF_WO) {
        const int cb = f >> 6, rem = f & 63, c = rem >> 4, r2 = rem & 15, kq = r2 >> 2, ks2 = (r2 >> 1) & 1, nt = r2 & 1;
        ks = 8 * kq + 2 * c + ks2; pA = a.wo + 64 * cb + 32 * nt; pB = pA + 16; ld = 512; dst = WS_WO + (size_t)f * 1024;
    } else if ((f -= NF_WO) < NF_WG) {
        const int j = f / 224, rem = f % 224, q = rem >> 4, ksl = rem & 15, c = q >> 1, hf = q & 1;
        ks = 16 * hf + ksl; pA = a.wg + 112 * j + 16 * c; pB = a.wu + 112 * j + 16 * c; ld = FF; dst = WS_WG + (size_t)f * 1024;
    } else {
        f -= NF_WG;
        const int cb = f / 224, rem = f % 224;
        int kq, sidx, nt;
        if (rem < 112) { const int c = rem >> 4, r2 = rem & 15; kq = r2 >> 3; sidx = 4 * c + ((r2 >> 1) & 3); nt = r2 & 1; }
        else { const int g = rem - 112; kq = 2 + g / 56; sidx = (g % 56) >> 1; nt = g & 1; }
        ks = 28 * kq + dn_kstep(sidx); pA = a.wd + 64 * cb + 32 * nt; pB = pA + 16; ld = 512; dst = WS_WD + (size_t)f * 1024;
    }
    const int k = lane >> 2, n8 = lane & 3;
    WItem it;
    it.src = (n8 < 2 ? pA + n8 * 8 : pB + (n8 - 2) * 8) + (size_t)(16 * ks + k) * ld;
    it.dst = dst;
    return it;
}

#define STAMP(k) do { } while (0)
struct Ctx {
    LAS unsigned char* L;
    u32 Laddr;
    unsigned char* ws;
    __amdgpu_buffer_rsrc_t rs;
    int lane, w, i, j;
    u32 voff;
    u32 tag;
};

__device__ __forceinline__ u32x4 ld_sc1(const Ctx& c, u32 soff) { return __builtin_amdgcn_raw_buffer_load_b128(c.rs, c.voff, soff, 16); }
__device__ __forceinline__ void st_sc1(const Ctx& c, u32 soff, u32x4 v) { __builtin_amdgcn_raw_buffer_store_b128(v, c.rs, c.voff, soff, 16); }
__device__ __forceinline__ Ctx fresh(const Ctx& c0) {
    Ctx c = c0;
    asm volatile("" : "+s"(c.i), "+s"(c.j), "+s"(c.w), "+s"(c.tag));
    asm volatile("" : "+v"(c.lane));
    c.voff = c.lane * 16;
    return c;
}

constexpr int CQ(int t, int hf) { return 2 * t + hf; }
constexpr int CO(int k) { return 6 + k; }
constexpr int CG(int t, int hf) { return 10 + 2 * t + hf; }
constexpr int CD(int k) { return 24 + k; }
constexpr int slot_of(int n) {
    constexpr int tab[38] = {
          0, 1,   2, 3,   4, 5,
          6, 7, 2, 3,
          4, 5,   0, 1,   2, 3,   6, 7,   4, 5,   0, 1,   2, 3,
          6, 7, 4, 5, 0, 1, 2,   0, 0, 0, 0, 0, 0, 0};
    return tab[n];
}
template <int N> __device__ __forceinline__ const unsigned char* chunk_src(const Ctx& c) {
    size_t src;
    const int cb = c.j & 7;
    if (N < 10) src = WS_WO + (size_t)(cb * 4 + (N - 6)) * SLOT;
    else if (N < 24) src = WS_WG + (size_t)(c.j * 14 + (N - 10)) * SLOT;
    else src = WS_WD + (size_t)(cb * 14 + (N - 24)) * SLOT;
    return c.ws + src + (c.w & 3) * 4096;
}
constexpr int chunk_lds(int n) { return slot_of(n) * SLOT; }
template <int N> __device__ __forceinline__ void issue_chunk(const Ctx& c) {
    glds4k(chunk_src<N>(c), c.voff, c.Laddr + chunk_lds(N) + (c.w & 3) * 4096);
}
__device__ __forceinline__ const LAS unsigned char* lds_base(const Ctx& c, int off) {
    u32 a = c.Laddr + off + c.voff;
    asm volatile("" : "+v"(a));
    return (const LAS unsigned char*)(size_t)a;
}

__device__ __forceinline__ u32 flag_off(int layer, int seam, int ci, int cj) {
    return (u32)(WS_FLAGS + (size_t)(((layer * 8 + seam) * 256) + ci * 16 + cj) * 512);
}
__device__ __forceinline__ u32 flag_tag(const Ctx& c, int layer, int seam) { return c.tag + (u32)(layer * 8 + seam); }
__device__ __forceinline__ void flag_store(const Ctx& c, int layer, int seam, int ci, int cj, int word) {
    __builtin_amdgcn_raw_buffer_store_b32(flag_tag(c, layer, seam), c.rs, flag_off(layer, seam, ci, cj) + word * 4, 0, 16);
}
__device__ __forceinline__ void signal_group(const Ctx& c, int layer, int seam) {
    if (c.lane < 16) flag_store(c, layer, seam, c.i, c.lane, c.j);
}
template <int NWORDS> __device__ __forceinline__ void poll_flags(const Ctx& c, int layer, int seam, int nwords = NWORDS) {
    const u32 off = flag_off(layer, seam, c.i, c.j) + c.lane * 4;
    const u32 tag = flag_tag(c, layer, seam);
    for (u32 spins = 0;; ++spins) {
        asm volatile("" ::: "memory");
        const u32 f0 = __builtin_amdgcn_raw_buffer_load_b32(c.rs, off, 0, 16);
        bool ok = c.lane >= nwords || f0 == tag;
        if (NWORDS > 64) { const u32 f1 = __builtin_amdgcn_raw_buffer_load_b32(c.rs, off + 256, 0, 16); ok = ok && f1 == tag; }
        if (__all(ok)) break;
        if (spins > SPIN_CAP) { if (c.lane == 0) __builtin_amdgcn_raw_buffer_store_b32(1u, c.rs, (u32)WS_TMO, 0, 16); break; }
        __builtin_amdgcn_s_sleep(1);
    }
}

__device__ __forceinline__ int crow(int r, int h) { return (r & 3) + 8 * (r >> 2) + 4 * h; }

#define LDF(p, k) (*(const LAS u32x4*)((p) + (k) * 1024))
__device__ __forceinline__ u32x4 ldf_tr(const LAS unsigned char* p, int k) {
    const u32x2 lo = __builtin_bit_cast(u32x2, __builtin_amdgcn_ds_read_tr16_b64_v4i16((LAS s16x4*)(p + k * 1024)));
    const u32x2 hi = __builtin_bit_cast(u32x2, __builtin_amdgcn_ds_read_tr16_b64_v4i16((LAS s16x4*)(p + k * 1024 + 256)));
    return u32x4{lo.x, lo.y, hi.x, hi.y};
}
template <bool TR> __device__ __forceinline__ u32x4 ldf(const LAS unsigned char* p, int k) { if constexpr (TR) return ldf_tr(p, k); else return LDF(p, k); }

template <bool SWAP, bool PRE, bool TR, class F>
__device__ __forceinline__ void c_tile(f32x16& acc, u32x4 (&wf)[8], const LAS unsigned char* lo, const LAS unsigned char* hi, const LAS unsigned char* nxt,
                                       const u32x4 (&xf)[32], F&& filler) {
#pragma unroll
    for (int k = 0; k < 32; ++k) {
        const u32x4 wv = wf[k & 7];
        if (k + 8 < 16) wf[k & 7] = ldf<TR>(lo, k + 8);
        else if (k + 8 < 32) wf[k & 7] = ldf<TR>(hi, k + 8 - 16);
        else if (PRE) wf[k & 7] = ldf<TR>(nxt, k + 8 - 32);
        acc = SWAP ? mfma32(xf[k], wv, acc) : mfma32(wv, xf[k], acc);
        PIN(acc);
        filler(k);
        SB();
    }
}
template <bool PRE, bool TR, bool STAGE, class W>
__device__ __forceinline__ f32x2 c_tile0(const Ctx& c, f32x16& acc, u32x4 (&wf)[8], const LAS unsigned char* lo, const LAS unsigned char* hi,
                                         const LAS unsigned char* nxt, u32x4 (&xf)[32], __amdgpu_buffer_rsrc_t xrs, u32 xsoff, W&& wait_ready) {
    const int lane = c.lane;
    u32x4 raw[STAGE ? 32 : 1];
    const u32 rvoff = (u32)(lane >> 3) * 1024u + (u32)(lane & 7) * 16u;
    LAS unsigned char* st = c.L + 6 * SLOT + (c.w & 3) * 8192;
    const int wr_off = (lane >> 3) * 128 + (((lane & 7) ^ ((lane >> 3) & 7)) * 16);
    const int rd_row = (lane & 31) * 128, rd_sw = lane & 7, rd_h = lane >> 5;
#define XLD(k) __builtin_amdgcn_raw_buffer_load_b128(xrs, c.voff, xsoff + (u32)(k) * 1024u, 16)
#define RLD(q) __builtin_amdgcn_raw_buffer_load_b128(xrs, rvoff, xsoff + (u32)((q) & 3) * 8192u + (u32)((q) >> 2) * 128u, 0)
#define STG(g) do { _Pragma("unroll") for (int q = 0; q < 4; ++q) lds_st<u32x4>(st + ((g) & 1) * 4096, q * 1024 + wr_off, raw[4 * (g) + q]); \
                    _Pragma("unroll") for (int q = 0; q < 4; ++q) xf[4 * (g) + q] = lds_ld<u32x4>(st + ((g) & 1) * 4096, rd_row + (((2 * q + rd_h) ^ rd_sw) * 16)); } while (0)
    if (STAGE) {
#pragma unroll
        for (int q = 0; q < 16; ++q) raw[q] = RLD(q);
        SB();
        wait_ready();
        STG(0);
    } else {
        wait_ready();
#pragma unroll
        for (int k = 0; k < 14; ++k) xf[k] = XLD(k);
    }
#pragma unroll
    for (int k = 0; k < 8; ++k) wf[k] = ldf<TR>(lo, k);
    SB();
    float s1[4] = {0.f, 0.f, 0.f, 0.f}, s2[4] = {0.f, 0.f, 0.f, 0.f};
#pragma unroll
    for (int k = 0; k < 32; ++k) {
        if (STAGE) {
            if ((k & 3) == 0) {
                const int g = k >> 2;
                if (4 * (g + 4) < 32) {
#pragma unroll
                    for (int q = 0; q < 4; ++q) raw[4 * (g + 4) + q] = RLD(4 * (g + 4) + q);
                }
                if (g + 1 < 8) STG(g + 1);
            }
        } else {
            if (k + 14 < 32) xf[k + 14] = XLD(k + 14);
        }
        const u32x4 wv = wf[k & 7];
        if (k + 8 < 16) wf[k & 7] = ldf<TR>(lo, k + 8);
        else if (k + 8 < 32) wf[k & 7] = ldf<TR>(hi, k + 8 - 16);
        else if (PRE) wf[k & 7] = ldf<TR>(nxt, k + 8 - 32);
        acc = mfma32(wv, xf[k], acc);
        PIN(acc);
        s1[0] = dot2(xf[k].x, BF_ONES, s1[0]); s2[0] = dot2(xf[k].x, xf[k].x, s2[0]);
        s1[1] = dot2(xf[k].y, BF_ONES, s1[1]); s2[1] = dot2(xf[k].y, xf[k].y, s2[1]);
        s1[2] = dot2(xf[k].z, BF_ONES, s1[2]); s2[2] = dot2(xf[k].z, xf[k].z, s2[2]);
        s1[3] = dot2(xf[k].w, BF_ONES, s1[3]); s2[3] = dot2(xf[k].w, xf[k].w, s2[3]);
        asm volatile("" : "+v"(s1[0]), "+v"(s1[1]), "+v"(s1[2]), "+v"(s1[3]), "+v"(s2[0]), "+v"(s2[1]), "+v"(s2[2]), "+v"(s2[3]));
        if (TR && k == 3) BARL();
        SB();
    }
    const float a = xor32_sum((s1[0] + s1[1]) + (s1[2] + s1[3])), b = xor32_sum((s2[0] + s2[1]) + (s2[2] + s2[3]));
    const float mean = a * (1.0f / DM);
    float var = b * (1.0f / DM) - mean * mean;
    var = var > 0.f ? var : 0.f;
    f32x2 o; o.x = mean; o.y = __builtin_amdgcn_rsqf(var + LN_EPS);
    return o;
#undef XLD
#undef RLD
#undef STG
}

struct GS { float g0, g1, t0, t1, u0, u1; };
struct GK { float mean, A, B, ir2; };
__device__ __forceinline__ void geglu_sub(GS& s, u32& o, int q, float ag0, float ag1, float au0, float au1, float cg0, float cg1, float cu0, float cu1, const GK& k) {
    if (q == 0) asm volatile("v_fma_f32 %0, -%7, %5, %3\n\tv_fma_f32 %1, -%7, %6, %4\n\tv_mul_f32 %2, %0, %0"
                             : "=&v"(s.g0), "=&v"(s.g1), "=&v"(s.t0) : "v"(ag0), "v"(ag1), "v"(cg0), "v"(cg1), "v"(k.mean));
    if (q == 1) asm volatile("v_mul_f32 %1, %2, %2\n\tv_fma_f32 %0, %0, %4, %3\n\tv_fma_f32 %1, %1, %4, %3"
                             : "+v"(s.t0), "=&v"(s.t1) : "v"(s.g1), "v"(k.A), "v"(k.B));
    if (q == 2) asm volatile("v_mul_f32 %0, %2, %0\n\tv_mul_f32 %1, %3, %1\n\tv_exp_f32 %0, %0"
                             : "+v"(s.t0), "+v"(s.t1) : "v"(s.g0), "v"(s.g1));
    if (q == 3) asm volatile("v_exp_f32 %0, %0\n\tv_fma_f32 %1, -%7, %5, %3\n\tv_fma_f32 %2, -%7, %6, %4"
                             : "+v"(s.t1), "=&v"(s.u0), "=&v"(s.u1) : "v"(au0), "v"(au1), "v"(cu0), "v"(cu1), "v"(k.mean));
    if (q == 4) asm volatile("v_fma_f32 %0, %0, %4, %4\n\tv_mul_f32 %2, %2, %3\n\tv_fma_f32 %1, %1, %4, %4"
                             : "+v"(s.t0), "+v"(s.t1), "+v"(s.g0) : "v"(s.u0), "v"(k.ir2));
    if (q == 5) asm volatile("v_rcp_f32 %0, %0\n\tv_mul_f32 %2, %2, %3\n\tv_rcp_f32 %1, %1"
                             : "+v"(s.t0), "+v"(s.t1), "+v"(s.g1) : "v"(s.u1));
    if (q == 6) asm volatile("v_mul_f32 %1, %1, %3\n\tv_mul_f32 %2, %2, %4\n\tv_cvt_pk_bf16_f32 %0, %1, %2"
                             : "=&v"(o), "+v"(s.g0), "+v"(s.g1) : "v"(s.t0), "v"(s.t1));
}
struct LS { float t0, t1; };
__device__ __forceinline__ void lnq_a(LS& s, float a0, float c0, float c1, float sc, float msc) {
    asm volatile("v_mul_f32 %0, %6, %3\n\tv_mul_f32 %1, %6, %4\n\tv_fma_f32 %0, %2, %5, -%0"
                 : "=&v"(s.t0), "=&v"(s.t1) : "v"(a0), "v"(c0), "v"(c1), "v"(sc), "v"(msc));
}
__device__ __forceinline__ u32 lnq_b(LS& s, float a1, float sc) {
    u32 o;
    asm volatile("v_fma_f32 %1, %3, %4, -%1\n\tv_cvt_pk_bf16_f32 %0, %2, %1" : "=v"(o), "+v"(s.t1) : "v"(s.t0), "v"(a1), "v"(sc));
    return o;
}
__device__ __forceinline__ void lnv_a(LS& s, float a0, float a1, float m0, float m1, float r0, float cs) {
    asm volatile("v_fma_f32 %0, -%4, %7, %2\n\tv_fma_f32 %1, -%5, %7, %3\n\tv_mul_f32 %0, %0, %6"
                 : "=&v"(s.t0), "=&v"(s.t1) : "v"(a0), "v"(a1), "v"(m0), "v"(m1), "v"(r0), "v"(cs));
}
__device__ __forceinline__ u32 lnv_b(LS& s, float r1) {
    u32 o;
    asm volatile("v_mul_f32 %1, %1, %3\n\tv_cvt_pk_bf16_f32 %0, %2, %1" : "=v"(o), "+v"(s.t1) : "v"(s.t0), "v"(r1));
    return o;
}
__device__ __forceinline__ u32x4 pack8(const float* v) { return u32x4{pack2(v[0], v[1]), pack2(v[2], v[3]), pack2(v[4], v[5]), pack2(v[6], v[7])}; }

__device__ __forceinline__ float max16(const f32x16& s, float m) {
    float a = fmx(fmx(s[0], s[1]), m), b = fmx(s[2], s[3]);
    a = fmx(fmx(s[4], s[5]), a); b = fmx(fmx(s[6], s[7]), b);
    a = fmx(fmx(s[8], s[9]), a); b = fmx(fmx(s[10], s[11]), b);
    a = fmx(fmx(s[12], s[13]), a); b = fmx(fmx(s[14], s[15]), b);
    return fmx(a, b);
}
__device__ __forceinline__ float smul(float a, float b) { float r; asm("v_mul_f32 %0, %1, %2" : "=v"(r) : "v"(a), "v"(b)); return r; }
struct MaskF { u32x4 alo, ahi, ilo, ihi; };
__device__ __forceinline__ f32x16 add_mask(const MaskF& mf, f32x16 s) { s = mfma32(mf.alo, mf.ilo, s); return mfma32(mf.ahi, mf.ihi, s); }
template <int NB, int FIRST, int REST0, int MASK, bool FIRSTSEG>
__device__ __forceinline__ void att_segment(f32x16& oacc, float& m, const LAS unsigned char* kb, const LAS unsigned char* vb, const LAS unsigned char* mk, const u32x4& qf) {
    u32x4 kf[NB];
#pragma unroll
    for (int t = 0; t < NB; ++t) { const int b = t == 0 ? FIRST : REST0 + t - 1; kf[t] = *(const LAS u32x4*)(kb + b * 2048); }
    MaskF mf;
    if (MASK) {
        mf.alo = LDF(mk, 0); mf.ahi = LDF(mk, 1); mf.ilo = LDF(mk, 2); mf.ihi = LDF(mk, 3);
        if (MASK == 2) { mf.alo ^= 0xff7fff7fu; mf.ahi ^= 0xff7fff7fu; }
    }
    SB();
    float mx = FIRSTSEG ? -__builtin_inff() : m;
    f32x16 sv[4];
    sv[0] = mfma32(kf[0], qf, f32x16{});
    if (NB > 1) sv[1] = mfma32(kf[1], qf, f32x16{});
    if (MASK) sv[0] = add_mask(mf, sv[0]);
    SB();
#pragma unroll
    for (int t = 0; t < NB; ++t) {
        const int u = NB > 1 ? (t == 0 ? 1 : t == 1 ? 0 : t) : 0;
        if (t + 2 < NB) sv[(t + 2) & 3] = mfma32(kf[t + 2], qf, f32x16{});
        mx = max16(sv[u & 3], mx);
        SB();
    }
    mx = xor32_max(mx);
    f32x16 negm;
#pragma unroll
    for (int e = 0; e < 16; ++e) negm[e] = -mx;
    SB();
    sv[0] = mfma32(kf[0], qf, negm);
    if (MASK) sv[0] = add_mask(mf, sv[0]);
    u32x4 v0 = *(const LAS u32x4*)(vb + (FIRST * 2 + 0) * VREC);
    u32x4 v1 = *(const LAS u32x4*)(vb + (FIRST * 2 + 1) * VREC);
    if (NB > 1) sv[1] = mfma32(kf[1], qf, negm);
    if (!FIRSTSEG) {
        float f = __builtin_amdgcn_exp2f(m - mx);
        asm volatile("s_nop 1" : "+v"(f));
#pragma unroll
        for (int e = 0; e < 16; ++e) oacc[e] = smul(oacc[e], f);
    }
    m = mx;
    SB();
#pragma unroll
    for (int t = 0; t < NB; ++t) {
        u32x4 n0, n1;
        if (t + 1 < NB) {
            if (t > 0) sv[(t + 1) & 1] = mfma32(kf[t + 1], qf, negm);
            n0 = *(const LAS u32x4*)(vb + ((REST0 + t) * 2 + 0) * VREC);
            n1 = *(const LAS u32x4*)(vb + ((REST0 + t) * 2 + 1) * VREC);
        }
        SB();
        const f32x16& s = sv[t & 1];
        u32 p[8];
#pragma unroll
        for (int e = 0; e < 4; ++e) p[e] = pack2(__builtin_amdgcn_exp2f(s[2 * e]), __builtin_amdgcn_exp2f(s[2 * e + 1]));
        SB();
        oacc = mfma32(v0, u32x4{p[0], p[1], p[2], p[3]}, oacc);
#pragma unroll
        for (int e = 4; e < 8; ++e) p[e] = pack2(__builtin_amdgcn_exp2f(s[2 * e]), __builtin_amdgcn_exp2f(s[2 * e + 1]));
        SB();
        oacc = mfma32(v1, u32x4{p[4], p[5], p[6], p[7]}, oacc);
        if (t + 1 < NB) { v0 = n0; v1 = n1; }
        SB();
    }
}

__device__ __forceinline__ void issue_qkv_tile(const Ctx& c, const Args& a, int t) {
    const u32 voff = (u32)(c.lane >> 2) * 3072u + (u32)(c.lane & 3) * 16u;
    const unsigned char* src = (const unsigned char*)(a.wqkv + qkv_col(t) + 32 * c.j) + (size_t)(c.w & 3) * 8 * 16 * 3072;
    const u32 dst = c.Laddr + slot_of(CQ(t, 0)) * SLOT + (c.w & 3) * 8192;
#pragma unroll
    for (int q = 0; q < 8; ++q) glds1k(src + (size_t)q * 16 * 3072, voff, dst + q * 1024);
}
template <bool TR> __device__ __forceinline__ void tile_colsum(const Ctx& c, const LAS unsigned char* lo, const LAS unsigned char* hi, int csidx) {
    float s[4] = {0.f, 0.f, 0.f, 0.f};
#pragma unroll
    for (int hf = 0; hf < 2; ++hf) {
        u32x4 f[16];
#pragma unroll
        for (int k = 0; k < 16; ++k) f[k] = ldf<TR>(hf ? hi : lo, k);
        SB();
#pragma unroll
        for (int k = 0; k < 16; ++k) {
            s[0] = dot2(f[k].x, BF_ONES, s[0]); s[1] = dot2(f[k].y, BF_ONES, s[1]); s[2] = dot2(f[k].z, BF_ONES, s[2]); s[3] = dot2(f[k].w, BF_ONES, s[3]);
        }
        SB();
    }
    const float t = xor32_sum((s[0] + s[1]) + (s[2] + s[3]));
    if (c.lane < 32) lds_st<float>(c.L, SC_CS + (csidx + c.lane) * 4, t);
}
__device__ __forceinline__ int tr_lane_off(int lane) {
    const int g4 = lane >> 4, h = g4 >> 1, nh = g4 & 1, li = lane & 15;
    return (8 * h + (li >> 2)) * 64 + nh * 32 + (li & 3) * 8;
}
__device__ __forceinline__ const LAS unsigned char* lds_base_raw(const Ctx& c, int off) {
    u32 a = c.Laddr + off;
    asm volatile("" : "+v"(a));
    return (const LAS unsigned char*)(size_t)a;
}

__device__ __forceinline__ void phase_qkv_att(const Ctx& c0, int layer, const Args& a) {
    const Ctx c = fresh(c0);
    const int lane = c.lane, w = c.w, tb = w & 3, h = lane >> 5, r = lane & 31;
    const u32 lbase = (u32)(WS_L0 + (size_t)layer * WS_LSTRIDE);
    const int tro = tr_lane_off(lane);
    STAMP(0);
    if (w < 4) {
        auto ready = [&]() {
            if (layer > 0 && w == 0) poll_flags<64>(c, layer - 1, 0, 16);
            BAR();
            STAMP(1);
        };
        u32x4 xf[32], wf[8];
        const LAS unsigned char* wK = lds_base_raw(c, slot_of(CQ(0, 0)) * SLOT + tro);
        const LAS unsigned char* wV = lds_base_raw(c, slot_of(CQ(1, 0)) * SLOT + tro);
        const LAS unsigned char* wQ = lds_base_raw(c, slot_of(CQ(2, 0)) * SLOT + tro);
        f32x16 accK = {}, accV = {}, accQ = {};
        f32x2 ms;
        if (layer == 0) ms = c_tile0<true, true, true>(c, accK, wf, wK, wK + SLOT, wV, xf, __builtin_amdgcn_make_buffer_rsrc((void*)a.x, 0, SEQ * DM * 2, 0x00020000),
                                                        (u32)((4 * c.i + tb) * 32 * 1024), ready);
        else ms = c_tile0<true, true, false>(c, accK, wf, wK, wK + SLOT, wV, xf, c.rs, (u32)(WS_L0 + L_X2N) + (u32)((4 * c.i + tb) * 32) * 1024, ready);
        if (lane < 32) lds_st<f32x2>(c.L, SC_ST + (tb * 32 + r) * 8, ms);
        const float msk = ms.x * ms.y;
        BARL(); STAMP(2);
        f32x4 cs[4];
#pragma unroll
        for (int q = 0; q < 4; ++q) cs[q] = lds_ld<f32x4>(c.L, SC_CS + (0 * 32 + 8 * q + 4 * h) * 4);
        f32x4 mr[8];
        float csv;
        {
            u32 o[8]; LS ls;
            c_tile<true, true, true>(accV, wf, wV, wV + SLOT, wQ, xf, [&](int k) {
                if (k >= 4 && k < 20) {
                    const int pi = (k - 4) >> 1, e = 2 * pi;
                    if (((k - 4) & 1) == 0) lnq_a(ls, accK[e], cs[e >> 2][e & 3], cs[e >> 2][(e & 3) + 1], ms.y, msk);
                    else o[pi] = lnq_b(ls, accK[e + 1], ms.y);
                }
                if (k == 20 || k == 21) {
                    const int hh = k - 20;
                    const u32x4 ov = {o[4 * hh], o[4 * hh + 1], o[4 * hh + 2], o[4 * hh + 3]};
                    lds_st<u32x4>(c.L, SC_KS + (tb * 2 + hh) * 1024 + c.voff, ov);
                    st_sc1(c, lbase + (u32)L_KF + (u32)((4 * c.i + tb) * 32 + 2 * c.j + hh) * 1024, ov);
                }
                if (k >= 22 && k < 30) { const int q = (k - 22) >> 1; mr[k - 22] = lds_ld<f32x4>(c.L, SC_ST + (tb * 32 + 8 * q + 4 * h + 2 * ((k - 22) & 1)) * 8); }
                if (k == 30) csv = lds_ld<float>(c.L, SC_CS + (32 + r) * 4);
            });
        }
        BARL();
        {
            u32 o[8]; LS ls;
            c_tile<false, false, true>(accQ, wf, wQ, wQ + SLOT, wQ, xf, [&](int k) {
                if (k >= 2 && k < 18) {
                    const int pi = (k - 2) >> 1;
                    if (((k - 2) & 1) == 0) lnv_a(ls, accV[2 * pi], accV[2 * pi + 1], mr[pi].x, mr[pi].z, mr[pi].y, csv);
                    else o[pi] = lnv_b(ls, mr[pi].w);
                }
                if (k == 18 || k == 20) {
                    const int sv = (k - 18) >> 1;
                    const u32x4 ov = {o[4 * sv], o[4 * sv + 1], o[4 * sv + 2], o[4 * sv + 3]};
                    lds_st<u32x4>(c.L, SC_VS + (tb * 2 + sv) * VREC + c.voff, ov);
                    st_sc1(c, lbase + (u32)L_VF + (u32)(((4 * c.i + tb) * 16 + c.j) * 2 + sv) * 1024, ov);
                }
                if (k >= 24 && k < 28) cs[k - 24] = lds_ld<f32x4>(c.L, SC_CS + (64 + 8 * (k - 24) + 4 * h) * 4);
            });
            if (lane < 2) { u32x4 ones = {BF_ONES, BF_ONES, BF_ONES, BF_ONES}; lds_st<u32x4>(c.L, SC_VS + (tb * 2 + lane) * VREC + 1024, ones); }
        }
        {
            const float sc = ms.y * QSCALE, msc = ms.x * sc;
            asm volatile("s_nop 15" : "+v"(accQ));
            u32 o[8]; LS ls[8];
#pragma unroll
            for (int pi = 0; pi < 8; ++pi) { const int e = 2 * pi; lnq_a(ls[pi], accQ[e], cs[e >> 2][e & 3], cs[e >> 2][(e & 3) + 1], sc, msc); }
#pragma unroll
            for (int pi = 0; pi < 8; ++pi) o[pi] = lnq_b(ls[pi], accQ[2 * pi + 1], sc);
#pragma unroll
            for (int hh = 0; hh < 2; ++hh) lds_st<u32x4>(c.L, SC_QS + (tb * 2 + hh) * 1024 + c.voff, u32x4{o[4 * hh], o[4 * hh + 1], o[4 * hh + 2], o[4 * hh + 3]});
        }
        WAIT_VM(0);
        BAR(); STAMP(5);
        if (w == 0 && lane < 2 && c.i + 1 + lane < 16) flag_store(c, layer, 1, c.i + 1 + lane, c.j, lane);
    } else {
        WAIT_VM(16);
        BAR();
        WAIT_VM(8);
        BARL();
        const int cb = c.j & 7, sidx = c.i * 2 + (c.j >> 3);
        if (layer == 0) {
            const int hw = w & 3;
            LAS unsigned char* t = c.L + SC_VS + hw * 1024;
            WItem it[6]; u32x4 v[6];
#pragma unroll
            for (int q = 0; q < 6; ++q) {
                int f;
                const int fD = NF_WO + NF_WG + cb * 224 + sidx * 7, fO = cb * 64 + sidx * 2;
                if (q < 2 || (q < 4 && hw < 3)) { const int ks = 2 * c.i + (q & 1), cc = hw + 4 * (q >> 1); f = NF_WO + c.j * 224 + (2 * cc + (ks >> 4)) * 16 + (ks & 15); }
                else if (q < 4) f = fD + 4 + (q & 1);
                else if (q == 4) f = fD + (hw < 3 ? hw : 6);
                else f = hw < 2 ? fO + hw : fD + 3;
                it[q] = w_item(a, f, lane);
                v[q] = *(const u32x4*)it[q].src;
            }
            const int k = lane >> 2, n8 = lane & 3;
            const int g4 = lane >> 4, hh = g4 >> 1, nh = g4 & 1, li = lane & 15, qq = li >> 2, p = li & 3;
            float part = 0.f;
#pragma unroll
            for (int q = 0; q < 6; ++q) {
                if (q < 5 || hw < 3) {
                    lds_st<u32x4>(t, k * 64 + n8 * 16, v[q]);
                    asm volatile("s_waitcnt lgkmcnt(0)" ::: "memory");
                    const s16x4 lo = __builtin_amdgcn_ds_read_tr16_b64_v4i16((LAS s16x4*)(t + (4 * hh + qq) * 64 + nh * 32 + p * 8));
                    const s16x4 hi = __builtin_amdgcn_ds_read_tr16_b64_v4i16((LAS s16x4*)(t + (8 + 4 * hh + qq) * 64 + nh * 32 + p * 8));
                    const u32x2 l2 = __builtin_bit_cast(u32x2, lo), h2 = __builtin_bit_cast(u32x2, hi);
                    __builtin_amdgcn_raw_buffer_store_b128(u32x4{l2.x, l2.y, h2.x, h2.y}, c.rs, c.voff, (u32)it[q].dst, 16);
                    if (q < 2 || (q < 4 && hw < 3)) {
                        if ((q & 1) == 0) part = 0.f;
                        part = dot2(l2.x, BF_ONES, part); part = dot2(l2.y, BF_ONES, part); part = dot2(h2.x, BF_ONES, part); part = dot2(h2.y, BF_ONES, part);
                        if (q & 1) {
                            const float tot = xor32_sum(part);
                            const int cc = hw + 4 * (q >> 1);
                            if (lane < 32) __builtin_amdgcn_raw_buffer_store_b32(__builtin_bit_cast(u32, tot), c.rs, (u32)lane * 4u, (u32)WS_CSP + (u32)((c.j * 16 + c.i) * 224 + cc * 32) * 4u, 16);
                        }
                    }
                    asm volatile("s_waitcnt lgkmcnt(0)" ::: "memory");
                }
            }
        }
        WAIT_VM(0);
        if (layer == 0 && w < 7) {
            const int t = w - 4;
            const LAS unsigned char* wb = lds_base_raw(c, (t == 0 ? slot_of(CQ(0, 0)) : t == 1 ? slot_of(CQ(1, 0)) : slot_of(CQ(2, 0))) * SLOT + tro);
            tile_colsum<true>(c, wb, wb + SLOT, 32 * t);
        }
        BAR();
        if (layer == 0 && w == 4) {
            if (lane < 16) flag_store(c, 0, 5, lane, c.j, c.i);
            if (lane < 32) flag_store(c, 0, 6, lane >> 1, cb + 8 * (lane & 1), sidx);
        }
        BAR();
        if (layer == 0) poll_flags<64>(c, 0, 6, 32);
        issue_chunk<CO(0)>(c); issue_chunk<CO(1)>(c); issue_chunk<CO(2)>(c); issue_chunk<CO(3)>(c);
        BAR();
        if (layer == 0) {
            poll_flags<64>(c, 0, 5, 16);
            const int col = (w & 3) * 64 + lane;
            if (col < 224) {
                float t = 0.f;
#pragma unroll
                for (int i2 = 0; i2 < 16; ++i2) t += __builtin_bit_cast(float, __builtin_amdgcn_raw_buffer_load_b32(c.rs, (u32)col * 4u, (u32)WS_CSP + (u32)((c.j * 16 + i2) * 224) * 4u, 16));
                lds_st<float>(c.L, SC_CS + (96 + col) * 4, t);
            }
        }
        issue_chunk<CG(0, 0)>(c); issue_chunk<CG(0, 1)>(c);
    }
    const int qb = w < 4 ? w : 7 - w, hh = w >> 2;
    const u32x4 qf = lds_ld<u32x4>(c.L, SC_QS + (qb * 2 + hh) * 1024 + c.voff);
    const int vlane = (((lane >> 4) & 1) == hh) ? lane * 16 : 1024;
    f32x16 oacc = {};
    float m = 0.f;
    const LAS unsigned char* mk = lds_base(c, SC_MK);
    {
        const LAS unsigned char* kb = c.L + SC_KS + hh * 1024 + c.voff;
        const LAS unsigned char* vb = c.L + SC_VS + vlane;
        if (qb == 0) att_segment<1, 0, 0, 1, true>(oacc, m, kb, vb, mk, qf);
        else if (qb == 1) att_segment<2, 1, 0, 1, true>(oacc, m, kb, vb, mk, qf);
        else if (qb == 2) att_segment<3, 2, 0, 1, true>(oacc, m, kb, vb, mk, qf);
        else att_segment<4, 3, 0, 1, true>(oacc, m, kb, vb, mk, qf);
    }
    STAMP(6);
    if (c.i > 0) {
        if (w == 0) poll_flags<64>(c, layer, 1, c.i >= 2 ? 2 : 1);
        BAR();
        STAMP(7);
        {
            const int idx = w;
            const int g = 4 * c.i - 8 + idx;
            if (g >= 0) {
                const u32x4 s0 = ld_sc1(c, lbase + (u32)L_KF + (u32)(g * 32 + 2 * c.j + 0) * 1024);
                const u32x4 s1 = ld_sc1(c, lbase + (u32)L_KF + (u32)(g * 32 + 2 * c.j + 1) * 1024);
                const u32x4 s2 = ld_sc1(c, lbase + (u32)L_VF + (u32)((g * 16 + c.j) * 2 + 0) * 1024);
                const u32x4 s3 = ld_sc1(c, lbase + (u32)L_VF + (u32)((g * 16 + c.j) * 2 + 1) * 1024);
                lds_st<u32x4>(c.L, SC_RK + (idx * 2 + 0) * 1024 + c.voff, s0);
                lds_st<u32x4>(c.L, SC_RK + (idx * 2 + 1) * 1024 + c.voff, s1);
                lds_st<u32x4>(c.L, SC_RV + (idx * 2 + 0) * VREC + c.voff, s2);
                lds_st<u32x4>(c.L, SC_RV + (idx * 2 + 1) * VREC + c.voff, s3);
                if (lane < 2) { u32x4 ones = {BF_ONES, BF_ONES, BF_ONES, BF_ONES}; lds_st<u32x4>(c.L, SC_RV + (idx * 2 + lane) * VREC + 1024, ones); }
            }
        }
        BAR();
        STAMP(8);
        const LAS unsigned char* kb = c.L + SC_RK + hh * 1024 + c.voff;
        const LAS unsigned char* vb = c.L + SC_RV + vlane;
        if (c.i == 1) att_segment<4, 4, 5, 0, false>(oacc, m, kb, vb, mk, qf);
        else if (qb == 0) att_segment<8, 0, 1, 2, false>(oacc, m, kb, vb, mk, qf);
        else if (qb == 1) att_segment<7, 1, 2, 2, false>(oacc, m, kb, vb, mk, qf);
        else if (qb == 2) att_segment<6, 2, 3, 2, false>(oacc, m, kb, vb, mk, qf);
        else att_segment<5, 3, 4, 2, false>(oacc, m, kb, vb, mk, qf);
    }
    {
        u32x4 o;
        if (hh == 0) {
            float il = __builtin_amdgcn_rcpf(oacc[8]);
            asm volatile("s_nop 1" : "+v"(il));
            o = u32x4{pack2(smul(oacc[0], il), smul(oacc[1], il)), pack2(smul(oacc[2], il), smul(oacc[3], il)), pack2(smul(oacc[4], il), smul(oacc[5], il)), pack2(smul(oacc[6], il), smul(oacc[7], il))};
        } else {
            float il = __builtin_amdgcn_rcpf(oacc[0]);
            asm volatile("s_nop 1" : "+v"(il));
            o = u32x4{pack2(smul(oacc[8], il), smul(oacc[9], il)), pack2(smul(oacc[10], il), smul(oacc[11], il)), pack2(smul(oacc[12], il), smul(oacc[13], il)), pack2(smul(oacc[14], il), smul(oacc[15], il))};
        }
        st_sc1(c, lbase + (u32)L_OF + (u32)((4 * c.i + qb) * 32 + 2 * c.j + hh) * 1024, o);
    }
    STAMP(9);
    WAIT_VM(0);
    BAR();
    if (w == 0) signal_group(c, layer, 2);
    if (w >= 4) { issue_chunk<CG(1, 0)>(c); issue_chunk<CG(1, 1)>(c); }
    STAMP(10);
}

__device__ __forceinline__ void red_write(const Ctx& c, const f32x16& acc0, const f32x16& acc1, int t2, int kq) {
#pragma unroll
    for (int d = 0; d < 4; ++d) {
        const int fin = d & 1;
        if (kq != fin) {
            const int slot = kq < fin ? kq : kq - 1;
            const int base = red_off(t2 * 4 + d) + slot * 2048 + c.voff;
            const f32x16& a = d < 2 ? acc0 : acc1;
            const int o = (d & 1) * 8;
            lds_st<f32x4>(c.L, base, f32x4{a[o], a[o + 1], a[o + 2], a[o + 3]});
            lds_st<f32x4>(c.L, base + 1024, f32x4{a[o + 4], a[o + 5], a[o + 6], a[o + 7]});
        }
    }
}
__device__ __forceinline__ void red_read(const Ctx& c, const f32x16& acc0, const f32x16& acc1, int t2, int kq, float (&v)[2][8]) {
#pragma unroll
    for (int e = 0; e < 8; ++e) { v[0][e] = kq ? acc0[8 + e] : acc0[e]; v[1][e] = kq ? acc1[8 + e] : acc1[e]; }
#pragma unroll
    for (int x = 0; x < 2; ++x)
#pragma unroll
        for (int s = 0; s < 3; ++s) {
            const int base = red_off(t2 * 4 + 2 * x + kq) + s * 2048 + c.voff;
            const f32x4 lo = lds_ld<f32x4>(c.L, base), hi = lds_ld<f32x4>(c.L, base + 1024);
            v[x][0] += lo.x; v[x][1] += lo.y; v[x][2] += lo.z; v[x][3] += lo.w; v[x][4] += hi.x; v[x][5] += hi.y; v[x][6] += hi.z; v[x][7] += hi.w;
        }
}
__device__ __forceinline__ u32x4 add_res(const float (&v)[8], const u32x4& res) {
    return u32x4{pack2(v[0] + bflo(res.x), v[1] + bfhi(res.x)), pack2(v[2] + bflo(res.y), v[3] + bfhi(res.y)),
                 pack2(v[4] + bflo(res.z), v[5] + bfhi(res.z)), pack2(v[6] + bflo(res.w), v[7] + bfhi(res.w))};
}

__device__ __forceinline__ void phase_wo(const Ctx& c0, int layer, const Args& a) {
    const Ctx c = fresh(c0);
    const int w = c.w, t2 = w & 1, kq = w >> 1;
    const int th = c.j >> 3, cb = c.j & 7;
    const int tbk = 4 * c.i + 2 * th + t2;
    const u32 lbase = (u32)(WS_L0 + (size_t)layer * WS_LSTRIDE);
    if (w == 0) poll_flags<64>(c, layer, 2, 16);
    if (w >= 4) WAIT_VM(8);
    BAR();
    STAMP(11);
    u32x4 xf[8];
#pragma unroll
    for (int s = 0; s < 8; ++s) xf[s] = ld_sc1(c, lbase + (u32)L_OF + (u32)(tbk * 32 + 8 * kq + s) * 1024);
    u32x4 res[2];
    if (w < 4) {
#pragma unroll
        for (int x = 0; x < 2; ++x) {
            const int kbo = 4 * cb + 2 * x + kq;
            if (layer == 0) {
                const u16* src = a.x + (size_t)(tbk * 32 + (c.lane & 31)) * DM + kbo * 16 + 4 * (c.lane >> 5);
                const u32x2 lo = *(const u32x2*)src, hi = *(const u32x2*)(src + 8);
                res[x] = u32x4{lo.x, lo.y, hi.x, hi.y};
            } else {
                res[x] = ld_sc1(c, (u32)(WS_L0 + L_X2) + (u32)(tbk * 32 + kbo) * 1024);
            }
        }
    }
    u32x4 wf[16];
#pragma unroll
    for (int ch = 0; ch < 4; ++ch) {
        const LAS unsigned char* wb = lds_base(c, slot_of(CO(ch)) * SLOT + kq * 4096);
#pragma unroll
        for (int q = 0; q < 4; ++q) wf[4 * ch + q] = LDF(wb, q);
    }
    SB();
    f32x16 acc0 = {}, acc1 = {};
#pragma unroll
    for (int s = 0; s < 8; ++s) {
        acc0 = mfma32(wf[2 * s], xf[s], acc0);
        acc1 = mfma32(wf[2 * s + 1], xf[s], acc1);
        SB();
    }
    BAR();
    STAMP(12);
    red_write(c, acc0, acc1, t2, kq);
    BAR();
    if (w < 4) {
        float v[2][8];
        red_read(c, acc0, acc1, t2, kq, v);
#pragma unroll
        for (int x = 0; x < 2; ++x) st_sc1(c, lbase + (u32)L_X1 + (u32)(tbk * 32 + 4 * cb + 2 * x + kq) * 1024, add_res(v[x], res[x]));
        STAMP(13);
        WAIT_VM(0);
    }
    BAR();
    if (w == 0) signal_group(c, layer, 3);
    STAMP(14);
}

__device__ __forceinline__ int gu_slot(int T) { return (int)((0x02046204u >> (4 * (T & 7))) & 15u); }
__device__ __forceinline__ void gu_tile(const Ctx& c, int T, f32x16& acc, const f32x16& prev, u32x4 (&wf)[8], const u32x4 (&xf)[32], int h,
                                        f32x4 (&cs)[4], const GK& gk, u32 hbase) {
    const LAS unsigned char* cur = lds_base(c, gu_slot(T) * SLOT);
    const LAS unsigned char* nxt = lds_base(c, gu_slot(T < 6 ? T + 1 : T) * SLOT);
    const LAS unsigned char* csb = lds_base_raw(c, SC_CS + (96 + T * 32 + 4 * h) * 4);
    GS g;
    u32 o[4];
    acc = f32x16{};
    SB();
    c_tile<false, true, false>(acc, wf, cur, cur + SLOT, nxt, xf, [&](int k) {
        const int p = k >> 3, e = 2 * p, q2 = p >> 1, e2 = e & 3;
        if ((k & 7) < 7) geglu_sub(g, o[p], k & 7, prev[e], prev[e + 1], prev[8 + e], prev[8 + e + 1], cs[q2][e2], cs[q2][e2 + 1], cs[2 + q2][e2], cs[2 + q2][e2 + 1], gk);
        if (k == 15) cs[0] = *(const LAS f32x4*)(csb);
        if (k == 15) cs[2] = *(const LAS f32x4*)(csb + 64);
        if (k == 31) cs[1] = *(const LAS f32x4*)(csb + 32);
        if (k == 31) cs[3] = *(const LAS f32x4*)(csb + 96);
    });
    st_sc1(c, (u32)__builtin_amdgcn_readfirstlane((int)(hbase + (u32)(T - 1) * 1024)), u32x4{o[0], o[1], o[2], o[3]});
}
static_assert(slot_of(CG(0, 0)) == 4 && slot_of(CG(1, 0)) == 0 && slot_of(CG(2, 0)) == 2 && slot_of(CG(3, 0)) == 6 && slot_of(CG(4, 0)) == 4 && slot_of(CG(5, 0)) == 0 && slot_of(CG(6, 0)) == 2, "gu_slot");

__device__ __forceinline__ u32 gu_compute(const Ctx& c0, int layer) {
    const Ctx c = fresh(c0);
    const int lane = c.lane, w = c.w, tb = w & 3, h = lane >> 5;
    const u32 lbase = (u32)(WS_L0 + (size_t)layer * WS_LSTRIDE);
    u32 pf1 = 0;
    {
        u32x4 xf[32], wf[8];
        const LAS unsigned char* w0 = lds_base(c, slot_of(CG(0, 0)) * SLOT);
        const LAS unsigned char* w1 = lds_base(c, slot_of(CG(1, 0)) * SLOT);
        f32x16 a0 = {}, a1;
        const f32x2 ms = c_tile0<true, false, false>(c, a0, wf, w0, w0 + SLOT, w1, xf, c.rs, lbase + (u32)L_X1 + (u32)((4 * c.i + tb) * 32) * 1024, [&]() {
            if (w == 0) poll_flags<64>(c, layer, 3, 16);
            BAR();
            STAMP(15);
        });
        constexpr float GC1 = -2.0f * 0.7978845608028654f * 1.4426950408889634f, GC2 = GC1 * 0.044715f;
        GK gk; gk.mean = ms.x; gk.A = ms.y * GC1; gk.B = ms.y * ms.y * ms.y * GC2; gk.ir2 = 1.0f / (ms.y * ms.y);
        BARL(); STAMP(16);
        f32x4 cs[4];
#pragma unroll
        for (int q = 0; q < 4; ++q) cs[q] = lds_ld<f32x4>(c.L, SC_CS + (96 + (q >> 1) * 16 + 8 * (q & 1) + 4 * h) * 4);
        const u32 hbase = lbase + (u32)L_HF + (u32)((4 * c.i + tb) * 112 + 7 * c.j) * 1024;
#pragma unroll 1
        for (int T = 1; T < 7; T += 2) {
            gu_tile(c, T, a1, a0, wf, xf, h, cs, gk, hbase);
            if (T == 5) WAIT_VM(1);
            BARL();
            if (T == 5 && w == 0) signal_group(c, layer, 7);
            gu_tile(c, T + 1, a0, a1, wf, xf, h, cs, gk, hbase);
            if (T < 5) BARL();
        }
        STAMP(22);
        if (w == 0) pf1 = __builtin_amdgcn_raw_buffer_load_b32(c.rs, flag_off(layer, 7, c.i, c.j) + (lane & 15) * 4, 0, 16);
        {
            asm volatile("s_nop 15" : "+v"(a0));
            GS g[4];
            u32 o[4];
#pragma unroll
            for (int q = 0; q < 7; ++q)
#pragma unroll
                for (int p = 0; p < 4; ++p) {
                    const int e = 2 * p, q2 = p >> 1, e2 = e & 3;
                    geglu_sub(g[p], o[p], q, a0[e], a0[e + 1], a0[8 + e], a0[8 + e + 1], cs[q2][e2], cs[q2][e2 + 1], cs[2 + q2][e2], cs[2 + q2][e2 + 1], gk);
                }
            st_sc1(c, hbase + 6u * 1024, u32x4{o[0], o[1], o[2], o[3]});
        }
        STAMP(21);
        WAIT_VM(0);
        BAR();
        STAMP(23);
        if (w == 0) signal_group(c, layer, 4);
    }
    return pf1;
}
__device__ __forceinline__ void gu_helper(const Ctx& c0, int layer, u32x4 (&wr)[40]) {
    const Ctx c = fresh(c0);
    const int lane = c.lane, w = c.w;
    {
        WAIT_VM(0);
        BAR();
        __builtin_amdgcn_s_sleep(16);
        issue_chunk<CG(2, 0)>(c); issue_chunk<CG(2, 1)>(c); issue_chunk<CG(3, 0)>(c); issue_chunk<CG(3, 1)>(c);
        if (layer == 1 && w == 7 && c.i == 0 && c.j == 0) {
            for (u32 spins = 0;; ++spins) {
                asm volatile("" ::: "memory");
                const u32x4 f = __builtin_amdgcn_raw_buffer_load_b128(c.rs, c.voff, (u32)WS_STARTED, 16);
                if (__all(f.x == c.tag && f.y == c.tag && f.z == c.tag && f.w == c.tag)) { if (lane == 0) __builtin_amdgcn_raw_buffer_store_b32(c.tag + 31u, c.rs, (u32)WS_EPOCH, 0, 16); break; }
                if (spins > SPIN_CAP) break;
                __builtin_amdgcn_s_sleep(1);
            }
        }
        WAIT_VM(8);
        BAR();
        const u32 wdb = (u32)WS_WD + (u32)((c.j & 7) * 224 + 112 + ((w >> 1) - 2) * 56) * 1024;
#pragma unroll
        for (int q = 0; q < 20; ++q) wr[q] = __builtin_amdgcn_raw_buffer_load_b128(c.rs, c.voff, wdb + q * 1024, 16);
        issue_chunk<CG(4, 0)>(c); issue_chunk<CG(4, 1)>(c);
        WAIT_VM(8);
        BAR();
#pragma unroll
        for (int q = 20; q < 40; ++q) wr[q] = __builtin_amdgcn_raw_buffer_load_b128(c.rs, c.voff, wdb + q * 1024, 16);
        issue_chunk<CG(5, 0)>(c); issue_chunk<CG(5, 1)>(c);
        WAIT_VM(8);
        BAR();
        issue_chunk<CG(6, 0)>(c); issue_chunk<CG(6, 1)>(c);
        WAIT_VM(8);
        BAR();
        issue_chunk<CD(0)>(c); issue_chunk<CD(1)>(c);
        WAIT_VM(8);
        BAR();
        issue_chunk<CD(2)>(c); issue_chunk<CD(3)>(c);
        BAR();
        issue_chunk<CD(4)>(c); issue_chunk<CD(5)>(c);
        WAIT_VM(0);
        BAR();
        issue_chunk<CD(6)>(c);
    }
}

__device__ __forceinline__ void hld(const Ctx& c, u32x4& dst, u32 soff) {
    asm volatile("buffer_load_dwordx4 %0, %1, %2, %3 offen sc1" : "=v"(dst) : "v"(c.voff), "s"(c.rs), "s"(soff) : "memory");
}
template <int N> __device__ __forceinline__ void hwait(u32x4& x) { asm volatile("s_waitcnt vmcnt(%1)" : "+v"(x) : "i"(N) : "memory"); }
template <bool HELPER>
__device__ __forceinline__ void dn_main(const Ctx& c, int layer, int kq, u32 xsrc, u32 wdb, const u32x4* wr, f32x16& acc0, f32x16& acc1) {
    constexpr int XD = 10;
    u32x4 xf[28], wf[8], wl[16];
    const LAS unsigned char* wb[7];
#pragma unroll
    for (int s = 0; s < XD; ++s) hld(c, xf[s], xsrc + (u32)dn_kstep(s) * 1024);
    if (!HELPER) {
#pragma unroll
        for (int ch = 0; ch < 7; ++ch) wb[ch] = lds_base(c, chunk_lds(CD(ch)) + kq * 8192);
#pragma unroll
        for (int q = 0; q < 8; ++q) wf[q] = LDF(wb[0], q);
    }
    SB();
    u32 pf = 0;
#pragma unroll
    for (int s = 0; s < 28; ++s) {
        if (s + XD < 28) hld(c, xf[s + XD], xsrc + (u32)dn_kstep(s + XD) * 1024);
        u32x4 w0, w1;
        if (HELPER) {
            if (s < 20) { w0 = wr[2 * s]; w1 = wr[2 * s + 1]; } else { w0 = wl[2 * (s - 20)]; w1 = wl[2 * (s - 20) + 1]; }
        } else {
            w0 = wf[(2 * s) & 7]; w1 = wf[(2 * s + 1) & 7];
            if (s + 4 < 28) { wf[(2 * s) & 7] = LDF(wb[(s + 4) >> 2], 2 * ((s + 4) & 3)); wf[(2 * s + 1) & 7] = LDF(wb[(s + 4) >> 2], 2 * ((s + 4) & 3) + 1); }
        }
        if (s + XD < 28) { if (HELPER && s >= 8 && s < 12 + XD - 10 + 10 && s <= 21) hwait<10>(xf[s]); else hwait<10>(xf[s]); }
        else if (s == 18) hwait<9>(xf[s]); else if (s == 19) hwait<8>(xf[s]); else if (s == 20) hwait<7>(xf[s]); else if (s == 21) hwait<6>(xf[s]); else if (s == 22) hwait<5>(xf[s]);
        else if (s == 23) hwait<4>(xf[s]); else if (s == 24) hwait<3>(xf[s]); else if (s == 25) hwait<2>(xf[s]); else if (s == 26) hwait<1>(xf[s]); else hwait<0>(xf[s]);
        if (HELPER && s == 20) {
            asm volatile("s_waitcnt vmcnt(6)" : "+v"(wl[0]), "+v"(wl[1]), "+v"(wl[2]), "+v"(wl[3]), "+v"(wl[4]), "+v"(wl[5]), "+v"(wl[6]), "+v"(wl[7]) :: "memory");
            asm volatile("" : "+v"(wl[8]), "+v"(wl[9]), "+v"(wl[10]), "+v"(wl[11]), "+v"(wl[12]), "+v"(wl[13]), "+v"(wl[14]), "+v"(wl[15]));
            w0 = wl[0]; w1 = wl[1];
        }
        acc0 = mfma32(w0, xf[s], acc0);
        acc1 = mfma32(w1, xf[s], acc1);
        PIN(acc0); PIN(acc1);
        if (HELPER && s >= 8 && s < 12) {
#pragma unroll
            for (int q = 0; q < 4; ++q) hld(c, wl[4 * (s - 8) + q], wdb + (u32)(40 + 4 * (s - 8) + q) * 1024);
        }
        if (!HELPER && s == 2 && c.w == 0)
            asm volatile("buffer_load_dword %0, %1, %2, 0 offen sc1" : "=v"(pf) : "v"(flag_off(layer, 4, c.i, c.j) + (c.lane & 15) * 4), "s"(c.rs) : "memory");
        if (s + XD == 15) {
            if (!HELPER && c.w == 0) {
                asm volatile("s_waitcnt vmcnt(3)" : "+v"(pf) :: "memory");
                if (!__all(pf == flag_tag(c, layer, 4))) poll_flags<64>(c, layer, 4, 16);
            }
            BARL();
        }
        SB();
    }
}

__device__ __forceinline__ void phase_ffn(const Ctx& c0, int layer, const Args& a) {
    f32x16 acc0 = {}, acc1 = {};
    u32x4 res[2];
    if (c0.w < 4) {
        const u32 pf1 = gu_compute(c0, layer);
        const Ctx c = fresh(c0);
        const int w = c.w, t2 = w & 1, kq = w >> 1, th = c.j >> 3, cb = c.j & 7;
        const int tbk = 4 * c.i + 2 * th + t2;
        const u32 lbase = (u32)(WS_L0 + (size_t)layer * WS_LSTRIDE);
        res[0] = ld_sc1(c, lbase + (u32)L_X1 + (u32)(tbk * 32 + 4 * cb + kq) * 1024);
        res[1] = ld_sc1(c, lbase + (u32)L_X1 + (u32)(tbk * 32 + 4 * cb + 2 + kq) * 1024);
        if (w == 0 && !__all(pf1 == flag_tag(c, layer, 7))) poll_flags<64>(c, layer, 7, 16);
        BARL();
        STAMP(25);
        dn_main<false>(c, layer, kq, lbase + (u32)L_HF + (u32)(tbk * 112 + 28 * kq) * 1024, 0u, nullptr, acc0, acc1);
    } else {
        u32x4 wr[40];
        gu_helper(c0, layer, wr);
        const Ctx c = fresh(c0);
        const int w = c.w, t2 = w & 1, kq = w >> 1, th = c.j >> 3, cb = c.j & 7;
        const int tbk = 4 * c.i + 2 * th + t2;
        const u32 lbase = (u32)(WS_L0 + (size_t)layer * WS_LSTRIDE);
        BARL();
        dn_main<true>(c, layer, kq, lbase + (u32)L_HF + (u32)(tbk * 112 + 28 * kq) * 1024, (u32)WS_WD + (u32)(cb * 224 + 112 + (kq - 2) * 56) * 1024, wr, acc0, acc1);
    }
    const Ctx c = fresh(c0);
    const int lane = c.lane, w = c.w, t2 = w & 1, kq = w >> 1, h = lane >> 5, r = lane & 31;
    const int th = c.j >> 3, cb = c.j & 7;
    const int tbk = 4 * c.i + 2 * th + t2;
    const u32 lbase = (u32)(WS_L0 + (size_t)layer * WS_LSTRIDE);
    BAR();
    STAMP(26);
    red_write(c, acc0, acc1, t2, kq);
    BAR();
    if (w >= 4) {
        if (layer == 0) issue_qkv_tile(c, a, 0);
    } else {
        float v[2][8];
        red_read(c, acc0, acc1, t2, kq, v);
#pragma unroll
        for (int x = 0; x < 2; ++x) {
            const int kbo = 4 * cb + 2 * x + kq;
            const u32x4 o = add_res(v[x], res[x]);
            const auto r0 = __builtin_amdgcn_permlane32_swap(o.x, o.z, false, false);
            const auto r1 = __builtin_amdgcn_permlane32_swap(o.y, o.w, false, false);
            const u32x4 on = u32x4{r0[0], r1[0], r0[1], r1[1]};
            if (layer == 1) {
                *(u32x4*)(a.out + (size_t)(tbk * 32 + r) * DM + kbo * 16 + 8 * h) = on;
            } else {
                st_sc1(c, lbase + (u32)L_X2N + (u32)(tbk * 32 + kbo) * 1024, on);
                st_sc1(c, lbase + (u32)L_X2 + (u32)(tbk * 32 + kbo) * 1024, o);
            }
        }
        STAMP(27);
        if (layer == 0) WAIT_VM(0);
    }
    if (layer == 0) {
        BAR();
        if (w == 0) signal_group(c, layer, 0);
        if (w >= 4) { issue_qkv_tile(c, a, 1); issue_qkv_tile(c, a, 2); }
        STAMP(28);
    }
}

__global__ void __launch_bounds__(512, 2) mega_kernel(Args a) {
    extern __shared__ __attribute__((aligned(16))) unsigned char lds[];
    Ctx c;
    c.L = (LAS unsigned char*)lds;
    c.Laddr = (u32)(size_t)c.L;
    c.ws = a.ws;
    c.rs = __builtin_amdgcn_make_buffer_rsrc((void*)a.ws, 0, (int)WS_END, 0x00020000);
    const int tid = threadIdx.x;
    c.lane = tid & 63;
    c.w = __builtin_amdgcn_readfirstlane(tid >> 6);
    const int bid = blockIdx.x;
    c.i = 2 * (bid & 7) + (bid >> 7);
    c.j = (bid >> 3) & 15;
    c.voff = c.lane * 16;
    {
        c.tag = 0;
        if (c.w >= 4) { issue_qkv_tile(c, a, 0); issue_qkv_tile(c, a, 1); issue_qkv_tile(c, a, 2); }
        u32 e;
        asm volatile("s_load_dword %0, %1, 0x0\n\ts_waitcnt lgkmcnt(0)" : "=s"(e) : "s"(a.ws + WS_EPOCH) : "memory");
        c.tag = e + 1u;
        if (tid == 0) __builtin_amdgcn_raw_buffer_store_b32(c.tag, c.rs, (u32)bid * 4u, (u32)WS_STARTED, 16);
    }
    if (c.w < 4) {
        const int hh = c.lane >> 5, row = c.lane & 31, half = c.w & 1;
        u32 d[4];
#pragma unroll
        for (int q = 0; q < 4; ++q) {
            const int c0 = 16 * half + 8 * (q >> 1) + 4 * hh + 2 * (q & 1), c1 = c0 + 1;
            const u32 lo = c.w < 2 ? (row > c0 ? 0xff7fu : 0u) : (row == c0 ? 0x3f80u : 0u);
            const u32 hi = c.w < 2 ? (row > c1 ? 0xff7fu : 0u) : (row == c1 ? 0x3f80u : 0u);
            d[q] = lo | (hi << 16);
        }
        lds_st<u32x4>(c.L, SC_MK + c.w * 1024 + c.voff, u32x4{d[0], d[1], d[2], d[3]});
    }
    for (int layer = 0; layer < 2; ++layer) {
        phase_qkv_att(c, layer, a);
        phase_wo(c, layer, a);
        phase_ffn(c, layer, a);
    }
}

}

extern "C" void kernel_launch(void* const* d_in, const int* in_sizes, int n_in, void* d_out, int out_size, void* d_ws, size_t ws_size,
                              hipStream_t stream) {
    Args a;
    a.x = (const u16*)d_in[0]; a.wqkv = (const u16*)d_in[1]; a.wo = (const u16*)d_in[2];
    a.wg = (const u16*)d_in[4]; a.wu = (const u16*)d_in[5]; a.wd = (const u16*)d_in[6];
    a.out = (u16*)d_out; a.ws = (unsigned char*)d_ws;
    (void)hipFuncSetAttribute((const void*)mega_kernel, hipFuncAttributeMaxDynamicSharedMemorySize, LDS_BYTES);
    mega_kernel<<<256, 512, LDS_BYTES, stream>>>(a);
}
```

```cpp
#include <hip/hip_runtime.h>
#include <hip/hip_bf16.h>

#define LAS __attribute__((address_space(3)))
#define GAS __attribute__((address_space(1)))
typedef unsigned u32;
typedef unsigned short u16;
typedef u32 u32x4 __attribute__((ext_vector_type(4)));
typedef u32 u32x2 __attribute__((ext_vector_type(2)));
typedef float f32x16 __attribute__((ext_vector_type(16)));
typedef float f32x4 __attribute__((ext_vector_type(4)));
typedef float f32x2 __attribute__((ext_vector_type(2)));
typedef __bf16 bf16x8_t __attribute__((ext_vector_type(8)));
typedef __bf16 bf16x2_t __attribute__((ext_vector_type(2)));
typedef short s16x4 __attribute__((ext_vector_type(4)));

namespace {

constexpr int SEQ = 2048, DM = 512, FF = 1792;
constexpr size_t KiB = 1024, MiB = 1024 * 1024;
constexpr size_t WS_FLAGS = 1 * MiB;
constexpr size_t WS_EPOCH = 640 * KiB;
constexpr size_t WS_STARTED = WS_EPOCH + 4096;
constexpr size_t WS_TMO = 768 * KiB;
constexpr size_t WS_CSP = 3 * MiB + 256 * KiB;
constexpr size_t WS_WO = 4 * MiB + 512 * KiB;
constexpr size_t WS_WG = 5 * MiB;
constexpr size_t WS_WD = 9 * MiB;
constexpr size_t WS_L0 = 16 * MiB, WS_LSTRIDE = 32 * MiB;
constexpr size_t L_KF = 0, L_VF = 2 * MiB, L_OF = 4 * MiB, L_X1 = 6 * MiB, L_X2 = 8 * MiB, L_HF = 10 * MiB, L_X2N = 18 * MiB;
constexpr size_t WS_END = WS_L0 + 2 * WS_LSTRIDE;

constexpr int NSLOT = 8, SLOT = 16384;
constexpr int LDS_SCR = NSLOT * SLOT;
constexpr int VREC = 1040;
constexpr int SC_RV = LDS_SCR;
constexpr int SC_VS = SC_RV + 16640;
constexpr int SC_ST = SC_VS + 8320;
constexpr int SC_CS = SC_ST + 1024;
constexpr int SC_MK = SC_CS + 1280;
constexpr int LDS_BYTES = SC_MK + 4096;
constexpr int SC_KS = 0 * SLOT;
constexpr int SC_QS = 0 * SLOT + 8192;
constexpr int SC_RK = 1 * SLOT;
static_assert(LDS_BYTES <= 163840, "LDS");
__device__ __forceinline__ int red_off(int q) { return q < 5 ? 2 * SLOT + q * 6144 : 6 * SLOT + (q - 5) * 6144; }

constexpr float LN_EPS = 1e-5f;
constexpr float QSCALE = 0.25f * 1.4426950408889634f;
constexpr u32 SPIN_CAP = 1u << 17;
constexpr u32 BF_ONES = 0x3f803f80u;

struct Args {
    const u16* x; const u16* wqkv; const u16* wo; const u16* wg; const u16* wu; const u16* wd;
    u16* out; unsigned char* ws;
};

__device__ __forceinline__ f32x16 mfma32(u32x4 a, u32x4 b, f32x16 c) {
    return __builtin_amdgcn_mfma_f32_32x32x16_bf16(__builtin_bit_cast(bf16x8_t, a), __builtin_bit_cast(bf16x8_t, b), c, 0, 0, 0);
}
__device__ __forceinline__ u32 pack2(float lo, float hi) {
    f32x2 v = {lo, hi};
    return __builtin_bit_cast(u32, __builtin_convertvector(v, bf16x2_t));
}
__device__ __forceinline__ float bflo(u32 u) { return __builtin_bit_cast(float, u << 16); }
__device__ __forceinline__ float bfhi(u32 u) { return __builtin_bit_cast(float, u & 0xffff0000u); }
__device__ __forceinline__ float dot2(u32 a, u32 b, float c) {
    return __builtin_amdgcn_fdot2_f32_bf16(__builtin_bit_cast(bf16x2_t, a), __builtin_bit_cast(bf16x2_t, b), c, false);
}
__device__ __forceinline__ void swap32(float& a, float& b) {
    asm volatile("s_nop 1\n\tv_permlane32_swap_b32 %0, %1\n\ts_nop 1" : "+v"(a), "+v"(b));
}
__device__ __forceinline__ float fmx(float a, float b) { return __builtin_elementwise_maximum(a, b); }
__device__ __forceinline__ float xor32_max(float v) { float a = v, b = v; swap32(a, b); return fmx(a, b); }
__device__ __forceinline__ float xor32_sum(float v) { float a = v, b = v; swap32(a, b); return a + b; }
#define BAR() do { asm volatile("s_waitcnt lgkmcnt(0)" ::: "memory"); __builtin_amdgcn_s_barrier(); asm volatile("" ::: "memory"); } while (0)
#define BARL() do { asm volatile("" ::: "memory"); __builtin_amdgcn_s_barrier(); asm volatile("" ::: "memory"); } while (0)
#define WAIT_VM(n) asm volatile("s_waitcnt vmcnt(" #n ")" ::: "memory")
#define SB() __builtin_amdgcn_sched_barrier(0)

template <class T> __device__ __forceinline__ T lds_ld(const LAS unsigned char* base, int off) { return *(const LAS T*)(base + off); }
template <class T> __device__ __forceinline__ void lds_st(LAS unsigned char* base, int off, T v) { *(LAS T*)(base + off) = v; }

__device__ __forceinline__ void glds4k(const unsigned char* src, u32 voff, u32 dst) {
    u32 keep;
    asm volatile(
        "s_mov_b32 %0, m0\n\t"
        "s_mov_b32 m0, %3\n\t"
        "s_nop 0\n\t"
        "global_load_lds_dwordx4 %1, %2 sc1\n\t"
        "global_load_lds_dwordx4 %1, %2 offset:1024 sc1\n\t"
        "global_load_lds_dwordx4 %1, %2 offset:2048 sc1\n\t"
        "global_load_lds_dwordx4 %1, %2 offset:3072 sc1\n\t"
        "s_mov_b32 m0, %0"
        : "=&s"(keep) : "v"(voff), "s"(src), "s"(dst) : "memory");
}

__device__ __forceinline__ void glds1k(const unsigned char* src, u32 voff, u32 dst) {
    u32 keep;
    asm volatile(
        "s_mov_b32 %0, m0\n\t"
        "s_mov_b32 m0, %3\n\t"
        "s_nop 0\n\t"
        "global_load_lds_dwordx4 %1, %2\n\t"
        "s_mov_b32 m0, %0"
        : "=&s"(keep) : "v"(voff), "s"(src), "s"(dst) : "memory");
}
#define PIN(x) asm volatile("" : "+v"(x))

constexpr int NF_WO = 8 * 64, NF_WG = 16 * 224, NF_WD = 8 * 224;
__host__ __device__ constexpr int qkv_col(int t) { return t == 0 ? 512 : (t == 1 ? 1024 : 0); }
__host__ __device__ constexpr int dn_kstep(int s) { return s < 16 ? 7 * (s >> 2) + (s & 3) : 7 * ((s - 16) / 3) + 4 + (s - 16) % 3; }
struct WItem { const u16* src; size_t dst; };
__device__ __forceinline__ WItem w_item(const Args& a, int f, int lane) {
    const u16 *pA, *pB; int ld, ks; size_t dst;
    if (f < NF_WO) {
        const int cb = f >> 6, rem = f & 63, c = rem >> 4, r2 = rem & 15, kq = r2 >> 2, ks2 = (r2 >> 1) & 1, nt = r2 & 1;
        ks = 8 * kq + 2 * c + ks2; pA = a.wo + 64 * cb + 32 * nt; pB = pA + 16; ld = 512; dst = WS_WO + (size_t)f * 1024;
    } else if ((f -= NF_WO) < NF_WG) {
        const int j = f / 224, rem = f % 224, q = rem >> 4, ksl = rem & 15, c = q >> 1, hf = q & 1;
        ks = 16 * hf + ksl; pA = a.wg + 112 * j + 16 * c; pB = a.wu + 112 * j + 16 * c; ld = FF; dst = WS_WG + (size_t)f * 1024;
    } else {
        f -= NF_WG;
        const int cb = f / 224, rem = f % 224;
        int kq, sidx, nt;
        if (rem < 112) { const int c = rem >> 4, r2 = rem & 15; kq = r2 >> 3; sidx = 4 * c + ((r2 >> 1) & 3); nt = r2 & 1; }
        else { const int g = rem - 112; kq = 2 + g / 56; sidx = (g % 56) >> 1; nt = g & 1; }
        ks = 28 * kq + dn_kstep(sidx); pA = a.wd + 64 * cb + 32 * nt; pB = pA + 16; ld = 512; dst = WS_WD + (size_t)f * 1024;
    }
    const int k = lane >> 2, n8 = lane & 3;
    WItem it;
    it.src = (n8 < 2 ? pA + n8 * 8 : pB + (n8 - 2) * 8) + (size_t)(16 * ks + k) * ld;
    it.dst = dst;
    return it;
}

#define STAMP(k) do { } while (0)
struct Ctx {
    LAS unsigned char* L;
    u32 Laddr;
    unsigned char* ws;
    __amdgpu_buffer_rsrc_t rs;
    int lane, w, i, j;
    u32 voff;
    u32 tag;
};

__device__ __forceinline__ u32x4 ld_sc1(const Ctx& c, u32 soff) { return __builtin_amdgcn_raw_buffer_load_b128(c.rs, c.voff, soff, 16); }
__device__ __forceinline__ void st_sc1(const Ctx& c, u32 soff, u32x4 v) { __builtin_amdgcn_raw_buffer_store_b128(v, c.rs, c.voff, soff, 16); }
__device__ __forceinline__ Ctx fresh(const Ctx& c0) {
    Ctx c = c0;
    asm volatile("" : "+s"(c.i), "+s"(c.j), "+s"(c.w), "+s"(c.tag));
    asm volatile("" : "+v"(c.lane));
    c.voff = c.lane * 16;
    return c;
}

constexpr int CQ(int t, int hf) { return 2 * t + hf; }
constexpr int CO(int k) { return 6 + k; }
constexpr int CG(int t, int hf) { return 10 + 2 * t + hf; }
constexpr int CD(int k) { return 24 + k; }
constexpr int slot_of(int n) {
    constexpr int tab[38] = {
          0, 1,   2, 3,   4, 5,
          6, 7, 2, 3,
          4, 5,   0, 1,   2, 3,   6, 7,   4, 5,   0, 1,   2, 3,
          6, 7, 4, 5, 0, 1, 2,   0, 0, 0, 0, 0, 0, 0};
    return tab[n];
}
template <int N> __device__ __forceinline__ const unsigned char* chunk_src(const Ctx& c) {
    size_t src;
    const int cb = c.j & 7;
    if (N < 10) src = WS_WO + (size_t)(cb * 4 + (N - 6)) * SLOT;
    else if (N < 24) src = WS_WG + (size_t)(c.j * 14 + (N - 10)) * SLOT;
    else src = WS_WD + (size_t)(cb * 14 + (N - 24)) * SLOT;
    return c.ws + src + (c.w & 3) * 4096;
}
constexpr int chunk_lds(int n) { return slot_of(n) * SLOT; }
template <int N> __device__ __forceinline__ void issue_chunk(const Ctx& c) {
    glds4k(chunk_src<N>(c), c.voff, c.Laddr + chunk_lds(N) + (c.w & 3) * 4096);
}
__device__ __forceinline__ const LAS unsigned char* lds_base(const Ctx& c, int off) {
    u32 a = c.Laddr + off + c.voff;
    asm volatile("" : "+v"(a));
    return (const LAS unsigned char*)(size_t)a;
}

__device__ __forceinline__ u32 flag_off(int layer, int seam, int ci, int cj) {
    return (u32)(WS_FLAGS + (size_t)(((layer * 8 + seam) * 256) + ci * 16 + cj) * 512);
}
__device__ __forceinline__ u32 flag_tag(const Ctx& c, int layer, int seam) { return c.tag + (u32)(layer * 8 + seam); }
__device__ __forceinline__ void flag_store(const Ctx& c, int layer, int seam, int ci, int cj, int word) {
    __builtin_amdgcn_raw_buffer_store_b32(flag_tag(c, layer, seam), c.rs, flag_off(layer, seam, ci, cj) + word * 4, 0, 16);
}
__device__ __forceinline__ void signal_group(const Ctx& c, int layer, int seam) {
    if (c.lane < 16) flag_store(c, layer, seam, c.i, c.lane, c.j);
}
template <int NWORDS> __device__ __forceinline__ void poll_flags(const Ctx& c, int layer, int seam, int nwords = NWORDS) {
    const u32 off = flag_off(layer, seam, c.i, c.j) + c.lane * 4;
    const u32 tag = flag_tag(c, layer, seam);
    for (u32 spins = 0;; ++spins) {
        asm volatile("" ::: "memory");
        const u32 f0 = __builtin_amdgcn_raw_buffer_load_b32(c.rs, off, 0, 16);
        bool ok = c.lane >= nwords || f0 == tag;
        if (NWORDS > 64) { const u32 f1 = __builtin_amdgcn_raw_buffer_load_b32(c.rs, off + 256, 0, 16); ok = ok && f1 == tag; }
        if (__all(ok)) break;
        if (spins > SPIN_CAP) { if (c.lane == 0) __builtin_amdgcn_raw_buffer_store_b32(1u, c.rs, (u32)WS_TMO, 0, 16); break; }
        __builtin_amdgcn_s_sleep(1);
    }
}

__device__ __forceinline__ int crow(int r, int h) { return (r & 3) + 8 * (r >> 2) + 4 * h; }

#define LDF(p, k) (*(const LAS u32x4*)((p) + (k) * 1024))
__device__ __forceinline__ u32x4 ldf_tr(const LAS unsigned char* p, int k) {
    const u32x2 lo = __builtin_bit_cast(u32x2, __builtin_amdgcn_ds_read_tr16_b64_v4i16((LAS s16x4*)(p + k * 1024)));
    const u32x2 hi = __builtin_bit_cast(u32x2, __builtin_amdgcn_ds_read_tr16_b64_v4i16((LAS s16x4*)(p + k * 1024 + 256)));
    return u32x4{lo.x, lo.y, hi.x, hi.y};
}
template <bool TR> __device__ __forceinline__ u32x4 ldf(const LAS unsigned char* p, int k) { if constexpr (TR) return ldf_tr(p, k); else return LDF(p, k); }

template <bool SWAP, bool PRE, bool TR, class F>
__device__ __forceinline__ void c_tile(f32x16& acc, u32x4 (&wf)[8], const LAS unsigned char* lo, const LAS unsigned char* hi, const LAS unsigned char* nxt,
                                       const u32x4 (&xf)[32], F&& filler) {
#pragma unroll
    for (int k = 0; k < 32; ++k) {
        const u32x4 wv = wf[k & 7];
        if (k + 8 < 16) wf[k & 7] = ldf<TR>(lo, k + 8);
        else if (k + 8 < 32) wf[k & 7] = ldf<TR>(hi, k + 8 - 16);
        else if (PRE) wf[k & 7] = ldf<TR>(nxt, k + 8 - 32);
        acc = SWAP ? mfma32(xf[k], wv, acc) : mfma32(wv, xf[k], acc);
        PIN(acc);
        filler(k);
        SB();
    }
}
template <bool PRE, bool TR, bool STAGE, class W>
__device__ __forceinline__ f32x2 c_tile0(const Ctx& c, f32x16& acc, u32x4 (&wf)[8], const LAS unsigned char* lo, const LAS unsigned char* hi,
                                         const LAS unsigned char* nxt, u32x4 (&xf)[32], __amdgpu_buffer_rsrc_t xrs, u32 xsoff, W&& wait_ready) {
    const int lane = c.lane;
    u32x4 raw[STAGE ? 32 : 1];
    const u32 rvoff = (u32)(lane >> 3) * 1024u + (u32)(lane & 7) * 16u;
    LAS unsigned char* st = c.L + 6 * SLOT + (c.w & 3) * 8192;
    const int wr_off = (lane >> 3) * 128 + (((lane & 7) ^ ((lane >> 3) & 7)) * 16);
    const int rd_row = (lane & 31) * 128, rd_sw = lane & 7, rd_h = lane >> 5;
#define XLD(k) __builtin_amdgcn_raw_buffer_load_b128(xrs, c.voff, xsoff + (u32)(k) * 1024u, 16)
#define RLD(q) __builtin_amdgcn_raw_buffer_load_b128(xrs, rvoff, xsoff + (u32)((q) & 3) * 8192u + (u32)((q) >> 2) * 128u, 0)
#define STG(g) do { _Pragma("unroll") for (int q = 0; q < 4; ++q) lds_st<u32x4>(st + ((g) & 1) * 4096, q * 1024 + wr_off, raw[4 * (g) + q]); \
                    _Pragma("unroll") for (int q = 0; q < 4; ++q) xf[4 * (g) + q] = lds_ld<u32x4>(st + ((g) & 1) * 4096, rd_row + (((2 * q + rd_h) ^ rd_sw) * 16)); } while (0)
    if (STAGE) {
#pragma unroll
        for (int q = 0; q < 16; ++q) raw[q] = RLD(q);
        SB();
        wait_ready();
        STG(0);
    } else {
        wait_ready();
#pragma unroll
        for (int k = 0; k < 14; ++k) xf[k] = XLD(k);
    }
#pragma unroll
    for (int k = 0; k < 8; ++k) wf[k] = ldf<TR>(lo, k);
    SB();
    float s1[4] = {0.f, 0.f, 0.f, 0.f}, s2[4] = {0.f, 0.f, 0.f, 0.f};
#pragma unroll
    for (int k = 0; k < 32; ++k) {
        if (STAGE) {
            if ((k & 3) == 0) {
                const int g = k >> 2;
                if (4 * (g + 4) < 32) {
#pragma unroll
                    for (int q = 0; q < 4; ++q) raw[4 * (g + 4) + q] = RLD(4 * (g + 4) + q);
                }
                if (g + 1 < 8) STG(g + 1);
            }
        } else {
            if (k + 14 < 32) xf[k + 14] = XLD(k + 14);
        }
        const u32x4 wv = wf[k & 7];
        if (k + 8 < 16) wf[k & 7] = ldf<TR>(lo, k + 8);
        else if (k + 8 < 32) wf[k & 7] = ldf<TR>(hi, k + 8 - 16);
        else if (PRE) wf[k & 7] = ldf<TR>(nxt, k + 8 - 32);
        acc = mfma32(wv, xf[k], acc);
        PIN(acc);
        s1[0] = dot2(xf[k].x, BF_ONES, s1[0]); s2[0] = dot2(xf[k].x, xf[k].x, s2[0]);
        s1[1] = dot2(xf[k].y, BF_ONES, s1[1]); s2[1] = dot2(xf[k].y, xf[k].y, s2[1]);
        s1[2] = dot2(xf[k].z, BF_ONES, s1[2]); s2[2] = dot2(xf[k].z, xf[k].z, s2[2]);
        s1[3] = dot2(xf[k].w, BF_ONES, s1[3]); s2[3] = dot2(xf[k].w, xf[k].w, s2[3]);
        asm volatile("" : "+v"(s1[0]), "+v"(s1[1]), "+v"(s1[2]), "+v"(s1[3]), "+v"(s2[0]), "+v"(s2[1]), "+v"(s2[2]), "+v"(s2[3]));
        if (TR && k == 3) BARL();
        SB();
    }
    const float a = xor32_sum((s1[0] + s1[1]) + (s1[2] + s1[3])), b = xor32_sum((s2[0] + s2[1]) + (s2[2] + s2[3]));
    const float mean = a * (1.0f / DM);
    float var = b * (1.0f / DM) - mean * mean;
    var = var > 0.f ? var : 0.f;
    f32x2 o; o.x = mean; o.y = __builtin_amdgcn_rsqf(var + LN_EPS);
    return o;
#undef XLD
#undef RLD
#undef STG
}

struct GS { float g0, g1, t0, t1, u0, u1; };
struct GK { float mean, A, B, ir2; };
__device__ __forceinline__ void geglu_sub(GS& s, u32& o, int q, float ag0, float ag1, float au0, float au1, float cg0, float cg1, float cu0, float cu1, const GK& k) {
    if (q == 0) asm volatile("v_fma_f32 %0, -%7, %5, %3\n\tv_fma_f32 %1, -%7, %6, %4\n\tv_mul_f32 %2, %0, %0"
                             : "=&v"(s.g0), "=&v"(s.g1), "=&v"(s.t0) : "v"(ag0), "v"(ag1), "v"(cg0), "v"(cg1), "v"(k.mean));
    if (q == 1) asm volatile("v_mul_f32 %1, %2, %2\n\tv_fma_f32 %0, %0, %4, %3\n\tv_fma_f32 %1, %1, %4, %3"
                             : "+v"(s.t0), "=&v"(s.t1) : "v"(s.g1), "v"(k.A), "v"(k.B));
    if (q == 2) asm volatile("v_mul_f32 %0, %2, %0\n\tv_mul_f32 %1, %3, %1\n\tv_exp_f32 %0, %0"
                             : "+v"(s.t0), "+v"(s.t1) : "v"(s.g0), "v"(s.g1));
    if (q == 3) asm volatile("v_exp_f32 %0, %0\n\tv_fma_f32 %1, -%7, %5, %3\n\tv_fma_f32 %2, -%7, %6, %4"
                             : "+v"(s.t1), "=&v"(s.u0), "=&v"(s.u1) : "v"(au0), "v"(au1), "v"(cu0), "v"(cu1), "v"(k.mean));
    if (q == 4) asm volatile("v_fma_f32 %0, %0, %4, %4\n\tv_mul_f32 %2, %2, %3\n\tv_fma_f32 %1, %1, %4, %4"
                             : "+v"(s.t0), "+v"(s.t1), "+v"(s.g0) : "v"(s.u0), "v"(k.ir2));
    if (q == 5) asm volatile("v_rcp_f32 %0, %0\n\tv_mul_f32 %2, %2, %3\n\tv_rcp_f32 %1, %1"
                             : "+v"(s.t0), "+v"(s.t1), "+v"(s.g1) : "v"(s.u1));
    if (q == 6) asm volatile("v_mul_f32 %1, %1, %3\n\tv_mul_f32 %2, %2, %4\n\tv_cvt_pk_bf16_f32 %0, %1, %2"
                             : "=&v"(o), "+v"(s.g0), "+v"(s.g1) : "v"(s.t0), "v"(s.t1));
}
struct LS { float t0, t1; };
__device__ __forceinline__ void lnq_a(LS& s, float a0, float c0, float c1, float sc, float msc) {
    asm volatile("v_mul_f32 %0, %6, %3\n\tv_mul_f32 %1, %6, %4\n\tv_fma_f32 %0, %2, %5, -%0"
                 : "=&v"(s.t0), "=&v"(s.t1) : "v"(a0), "v"(c0), "v"(c1), "v"(sc), "v"(msc));
}
__device__ __forceinline__ u32 lnq_b(LS& s, float a1, float sc) {
    u32 o;
    asm volatile("v_fma_f32 %1, %3, %4, -%1\n\tv_cvt_pk_bf16_f32 %0, %2, %1" : "=v"(o), "+v"(s.t1) : "v"(s.t0), "v"(a1), "v"(sc));
    return o;
}
__device__ __forceinline__ void lnv_a(LS& s, float a0, float a1, float m0, float m1, float r0, float cs) {
    asm volatile("v_fma_f32 %0, -%4, %7, %2\n\tv_fma_f32 %1, -%5, %7, %3\n\tv_mul_f32 %0, %0, %6"
                 : "=&v"(s.t0), "=&v"(s.t1) : "v"(a0), "v"(a1), "v"(m0), "v"(m1), "v"(r0), "v"(cs));
}
__device__ __forceinline__ u32 lnv_b(LS& s, float r1) {
    u32 o;
    asm volatile("v_mul_f32 %1, %1, %3\n\tv_cvt_pk_bf16_f32 %0, %2, %1" : "=v"(o), "+v"(s.t1) : "v"(s.t0), "v"(r1));
    return o;
}
__device__ __forceinline__ u32x4 pack8(const float* v) { return u32x4{pack2(v[0], v[1]), pack2(v[2], v[3]), pack2(v[4], v[5]), pack2(v[6], v[7])}; }

__device__ __forceinline__ float max16(const f32x16& s, float m) {
    float a = fmx(fmx(s[0], s[1]), m), b = fmx(s[2], s[3]);
    a = fmx(fmx(s[4], s[5]), a); b = fmx(fmx(s[6], s[7]), b);
    a = fmx(fmx(s[8], s[9]), a); b = fmx(fmx(s[10], s[11]), b);
    a = fmx(fmx(s[12], s[13]), a); b = fmx(fmx(s[14], s[15]), b);
    return fmx(a, b);
}
__device__ __forceinline__ float smul(float a, float b) { float r; asm("v_mul_f32 %0, %1, %2" : "=v"(r) : "v"(a), "v"(b)); return r; }
struct MaskF { u32x4 alo, ahi, ilo, ihi; };
__device__ __forceinline__ f32x16 add_mask(const MaskF& mf, f32x16 s) { s = mfma32(mf.alo, mf.ilo, s); return mfma32(mf.ahi, mf.ihi, s); }
template <int NB, int FIRST, int REST0, int MASK, bool FIRSTSEG>
__device__ __forceinline__ void att_segment(f32x16& oacc, float& m, const LAS unsigned char* kb, const LAS unsigned char* vb, const LAS unsigned char* mk, const u32x4& qf) {
    u32x4 kf[NB];
#pragma unroll
    for (int t = 0; t < NB; ++t) { const int b = t == 0 ? FIRST : REST0 + t - 1; kf[t] = *(const LAS u32x4*)(kb + b * 2048); }
    MaskF mf;
    if (MASK) {
        mf.alo = LDF(mk, 0); mf.ahi = LDF(mk, 1); mf.ilo = LDF(mk, 2); mf.ihi = LDF(mk, 3);
        if (MASK == 2) { mf.alo ^= 0xff7fff7fu; mf.ahi ^= 0xff7fff7fu; }
    }
    SB();
    float mx = FIRSTSEG ? -__builtin_inff() : m;
    f32x16 sv[4];
    sv[0] = mfma32(kf[0], qf, f32x16{});
    if (NB > 1) sv[1] = mfma32(kf[1], qf, f32x16{});
    if (MASK) sv[0] = add_mask(mf, sv[0]);
    SB();
#pragma unroll
    for (int t = 0; t < NB; ++t) {
        const int u = NB > 1 ? (t == 0 ? 1 : t == 1 ? 0 : t) : 0;
        if (t + 2 < NB) sv[(t + 2) & 3] = mfma32(kf[t + 2], qf, f32x16{});
        mx = max16(sv[u & 3], mx);
        SB();
    }
    mx = xor32_max(mx);
    f32x16 negm;
#pragma unroll
    for (int e = 0; e < 16; ++e) negm[e] = -mx;
    SB();
    sv[0] = mfma32(kf[0], qf, negm);
    if (MASK) sv[0] = add_mask(mf, sv[0]);
    u32x4 v0 = *(const LAS u32x4*)(vb + (FIRST * 2 + 0) * VREC);
    u32x4 v1 = *(const LAS u32x4*)(vb + (FIRST * 2 + 1) * VREC);
    if (NB > 1) sv[1] = mfma32(kf[1], qf, negm);
    if (!FIRSTSEG) {
        float f = __builtin_amdgcn_exp2f(m - mx);
        asm volatile("s_nop 1" : "+v"(f));
#pragma unroll
        for (int e = 0; e < 16; ++e) oacc[e] = smul(oacc[e], f);
    }
    m = mx;
    SB();
#pragma unroll
    for (int t = 0; t < NB; ++t) {
        u32x4 n0, n1;
        if (t + 1 < NB) {
            if (t > 0) sv[(t + 1) & 1] = mfma32(kf[t + 1], qf, negm);
            n0 = *(const LAS u32x4*)(vb + ((REST0 + t) * 2 + 0) * VREC);
            n1 = *(const LAS u32x4*)(vb + ((REST0 + t) * 2 + 1) * VREC);
        }
        SB();
        const f32x16& s = sv[t & 1];
        u32 p[8];
#pragma unroll
        for (int e = 0; e < 4; ++e) p[e] = pack2(__builtin_amdgcn_exp2f(s[2 * e]), __builtin_amdgcn_exp2f(s[2 * e + 1]));
        SB();
        oacc = mfma32(v0, u32x4{p[0], p[1], p[2], p[3]}, oacc);
#pragma unroll
        for (int e = 4; e < 8; ++e) p[e] = pack2(__builtin_amdgcn_exp2f(s[2 * e]), __builtin_amdgcn_exp2f(s[2 * e + 1]));
        SB();
        oacc = mfma32(v1, u32x4{p[4], p[5], p[6], p[7]}, oacc);
        if (t + 1 < NB) { v0 = n0; v1 = n1; }
        SB();
    }
}

__device__ __forceinline__ void issue_qkv_tile(const Ctx& c, const Args& a, int t) {
    const u32 voff = (u32)(c.lane >> 2) * 3072u + (u32)(c.lane & 3) * 16u;
    const unsigned char* src = (const unsigned char*)(a.wqkv + qkv_col(t) + 32 * c.j) + (size_t)(c.w & 3) * 8 * 16 * 3072;
    const u32 dst = c.Laddr + slot_of(CQ(t, 0)) * SLOT + (c.w & 3) * 8192;
#pragma unroll
    for (int q = 0; q < 8; ++q) glds1k(src + (size_t)q * 16 * 3072, voff, dst + q * 1024);
}
template <bool TR> __device__ __forceinline__ void tile_colsum(const Ctx& c, const LAS unsigned char* lo, const LAS unsigned char* hi, int csidx) {
    float s[4] = {0.f, 0.f, 0.f, 0.f};
#pragma unroll
    for (int hf = 0; hf < 2; ++hf) {
        u32x4 f[16];
#pragma unroll
        for (int k = 0; k < 16; ++k) f[k] = ldf<TR>(hf ? hi : lo, k);
        SB();
#pragma unroll
        for (int k = 0; k < 16; ++k) {
            s[0] = dot2(f[k].x, BF_ONES, s[0]); s[1] = dot2(f[k].y, BF_ONES, s[1]); s[2] = dot2(f[k].z, BF_ONES, s[2]); s[3] = dot2(f[k].w, BF_ONES, s[3]);
        }
        SB();
    }
    const float t = xor32_sum((s[0] + s[1]) + (s[2] + s[3]));
    if (c.lane < 32) lds_st<float>(c.L, SC_CS + (csidx + c.lane) * 4, t);
}
__device__ __forceinline__ int tr_lane_off(int lane) {
    const int g4 = lane >> 4, h = g4 >> 1, nh = g4 & 1, li = lane & 15;
    return (8 * h + (li >> 2)) * 64 + nh * 32 + (li & 3) * 8;
}
__device__ __forceinline__ const LAS unsigned char* lds_base_raw(const Ctx& c, int off) {
    u32 a = c.Laddr + off;
    asm volatile("" : "+v"(a));
    return (const LAS unsigned char*)(size_t)a;
}

__device__ __forceinline__ void phase_qkv_att(const Ctx& c0, int layer, const Args& a) {
    const Ctx c = fresh(c0);
    const int lane = c.lane, w = c.w, tb = w & 3, h = lane >> 5, r = lane & 31;
    const u32 lbase = (u32)(WS_L0 + (size_t)layer * WS_LSTRIDE);
    const int tro = tr_lane_off(lane);
    STAMP(0);
    if (w < 4) {
        auto ready = [&]() {
            if (layer > 0 && w == 0) poll_flags<64>(c, layer - 1, 0, 16);
            BAR();
            STAMP(1);
        };
        u32x4 xf[32], wf[8];
        const LAS unsigned char* wK = lds_base_raw(c, slot_of(CQ(0, 0)) * SLOT + tro);
        const LAS unsigned char* wV = lds_base_raw(c, slot_of(CQ(1, 0)) * SLOT + tro);
        const LAS unsigned char* wQ = lds_base_raw(c, slot_of(CQ(2, 0)) * SLOT + tro);
        f32x16 accK = {}, accV = {}, accQ = {};
        f32x2 ms;
        if (layer == 0) ms = c_tile0<true, true, true>(c, accK, wf, wK, wK + SLOT, wV, xf, __builtin_amdgcn_make_buffer_rsrc((void*)a.x, 0, SEQ * DM * 2, 0x00020000),
                                                        (u32)((4 * c.i + tb) * 32 * 1024), ready);
        else ms = c_tile0<true, true, false>(c, accK, wf, wK, wK + SLOT, wV, xf, c.rs, (u32)(WS_L0 + L_X2N) + (u32)((4 * c.i + tb) * 32) * 1024, ready);
        if (lane < 32) lds_st<f32x2>(c.L, SC_ST + (tb * 32 + r) * 8, ms);
        const float msk = ms.x * ms.y;
        BARL(); STAMP(2);
        f32x4 cs[4];
#pragma unroll
        for (int q = 0; q < 4; ++q) cs[q] = lds_ld<f32x4>(c.L, SC_CS + (0 * 32 + 8 * q + 4 * h) * 4);
        f32x4 mr[8];
        float csv;
        {
            u32 o[8]; LS ls;
            c_tile<true, true, true>(accV, wf, wV, wV + SLOT, wQ, xf, [&](int k) {
                if (k >= 4 && k < 20) {
                    const int pi = (k - 4) >> 1, e = 2 * pi;
                    if (((k - 4) & 1) == 0) lnq_a(ls, accK[e], cs[e >> 2][e & 3], cs[e >> 2][(e & 3) + 1], ms.y, msk);
                    else o[pi] = lnq_b(ls, accK[e + 1], ms.y);
                }
                if (k == 20 || k == 21) {
                    const int hh = k - 20;
                    const u32x4 ov = {o[4 * hh], o[4 * hh + 1], o[4 * hh + 2], o[4 * hh + 3]};
                    lds_st<u32x4>(c.L, SC_KS + (tb * 2 + hh) * 1024 + c.voff, ov);
                    st_sc1(c, lbase + (u32)L_KF + (u32)((4 * c.i + tb) * 32 + 2 * c.j + hh) * 1024, ov);
                }
                if (k >= 22 && k < 30) { const int q = (k - 22) >> 1; mr[k - 22] = lds_ld<f32x4>(c.L, SC_ST + (tb * 32 + 8 * q + 4 * h + 2 * ((k - 22) & 1)) * 8); }
                if (k == 30) csv = lds_ld<float>(c.L, SC_CS + (32 + r) * 4);
            });
        }
        BARL();
        {
            u32 o[8]; LS ls;
            c_tile<false, false, true>(accQ, wf, wQ, wQ + SLOT, wQ, xf, [&](int k) {
                if (k >= 2 && k < 18) {
                    const int pi = (k - 2) >> 1;
                    if (((k - 2) & 1) == 0) lnv_a(ls, accV[2 * pi], accV[2 * pi + 1], mr[pi].x, mr[pi].z, mr[pi].y, csv);
                    else o[pi] = lnv_b(ls, mr[pi].w);
                }
                if (k == 18 || k == 20) {
                    const int sv = (k - 18) >> 1;
                    const u32x4 ov = {o[4 * sv], o[4 * sv + 1], o[4 * sv + 2], o[4 * sv + 3]};
                    lds_st<u32x4>(c.L, SC_VS + (tb * 2 + sv) * VREC + c.voff, ov);
                    st_sc1(c, lbase + (u32)L_VF + (u32)(((4 * c.i + tb) * 16 + c.j) * 2 + sv) * 1024, ov);
                }
                if (k >= 24 && k < 28) cs[k - 24] = lds_ld<f32x4>(c.L, SC_CS + (64 + 8 * (k - 24) + 4 * h) * 4);
            });
            if (lane < 2) { u32x4 ones = {BF_ONES, BF_ONES, BF_ONES, BF_ONES}; lds_st<u32x4>(c.L, SC_VS + (tb * 2 + lane) * VREC + 1024, ones); }
        }
        {
            const float sc = ms.y * QSCALE, msc = ms.x * sc;
            asm volatile("s_nop 15" : "+v"(accQ));
            u32 o[8]; LS ls[8];
#pragma unroll
            for (int pi = 0; pi < 8; ++pi) { const int e = 2 * pi; lnq_a(ls[pi], accQ[e], cs[e >> 2][e & 3], cs[e >> 2][(e & 3) + 1], sc, msc); }
#pragma unroll
            for (int pi = 0; pi < 8; ++pi) o[pi] = lnq_b(ls[pi], accQ[2 * pi + 1], sc);
#pragma unroll
            for (int hh = 0; hh < 2; ++hh) lds_st<u32x4>(c.L, SC_QS + (tb * 2 + hh) * 1024 + c.voff, u32x4{o[4 * hh], o[4 * hh + 1], o[4 * hh + 2], o[4 * hh + 3]});
        }
        WAIT_VM(0);
        BAR(); STAMP(5);
        if (w == 0 && lane < 2 && c.i + 1 + lane < 16) flag_store(c, layer, 1, c.i + 1 + lane, c.j, lane);
    } else {
        WAIT_VM(16);
        BAR();
        WAIT_VM(8);
        BARL();
        const int cb = c.j & 7, sidx = c.i * 2 + (c.j >> 3);
        if (layer == 0) {
            const int hw = w & 3;
            LAS unsigned char* t = c.L + SC_VS + hw * 1024;
            WItem it[6]; u32x4 v[6];
#pragma unroll
            for (int q = 0; q < 6; ++q) {
                int f;
                const int fD = NF_WO + NF_WG + cb * 224 + sidx * 7, fO = cb * 64 + sidx * 2;
                if (q < 2 || (q < 4 && hw < 3)) { const int ks = 2 * c.i + (q & 1), cc = hw + 4 * (q >> 1); f = NF_WO + c.j * 224 + (2 * cc + (ks >> 4)) * 16 + (ks & 15); }
                else if (q < 4) f = fD + 4 + (q & 1);
                else if (q == 4) f = fD + (hw < 3 ? hw : 6);
                else f = hw < 2 ? fO + hw : fD + 3;
                it[q] = w_item(a, f, lane);
                v[q] = *(const u32x4*)it[q].src;
            }
            const int k = lane >> 2, n8 = lane & 3;
            const int g4 = lane >> 4, hh = g4 >> 1, nh = g4 & 1, li = lane & 15, qq = li >> 2, p = li & 3;
            float part = 0.f;
#pragma unroll
            for (int q = 0; q < 6; ++q) {
                if (q < 5 || hw < 3) {
                    lds_st<u32x4>(t, k * 64 + n8 * 16, v[q]);
                    asm volatile("s_waitcnt lgkmcnt(0)" ::: "memory");
                    const s16x4 lo = __builtin_amdgcn_ds_read_tr16_b64_v4i16((LAS s16x4*)(t + (4 * hh + qq) * 64 + nh * 32 + p * 8));
                    const s16x4 hi = __builtin_amdgcn_ds_read_tr16_b64_v4i16((LAS s16x4*)(t + (8 + 4 * hh + qq) * 64 + nh * 32 + p * 8));
                    const u32x2 l2 = __builtin_bit_cast(u32x2, lo), h2 = __builtin_bit_cast(u32x2, hi);
                    __builtin_amdgcn_raw_buffer_store_b128(u32x4{l2.x, l2.y, h2.x, h2.y}, c.rs, c.voff, (u32)it[q].dst, 16);
                    if (q < 2 || (q < 4 && hw < 3)) {
                        if ((q & 1) == 0) part = 0.f;
                        part = dot2(l2.x, BF_ONES, part); part = dot2(l2.y, BF_ONES, part); part = dot2(h2.x, BF_ONES, part); part = dot2(h2.y, BF_ONES, part);
                        if (q & 1) {
                            const float tot = xor32_sum(part);
                            const int cc = hw + 4 * (q >> 1);
                            if (lane < 32) __builtin_amdgcn_raw_buffer_store_b32(__builtin_bit_cast(u32, tot), c.rs, (u32)lane * 4u, (u32)WS_CSP + (u32)((c.j * 16 + c.i) * 224 + cc * 32) * 4u, 16);
                        }
                    }
                    asm volatile("s_waitcnt lgkmcnt(0)" ::: "memory");
                }
            }
        }
        WAIT_VM(0);
        if (layer == 0 && w < 7) {
            const int t = w - 4;
            const LAS unsigned char* wb = lds_base_raw(c, (t == 0 ? slot_of(CQ(0, 0)) : t == 1 ? slot_of(CQ(1, 0)) : slot_of(CQ(2, 0))) * SLOT + tro);
            tile_colsum<true>(c, wb, wb + SLOT, 32 * t);
        }
        BAR();
        if (layer == 0 && w == 4) {
            if (lane < 16) flag_store(c, 0, 5, lane, c.j, c.i);
            if (lane < 32) flag_store(c, 0, 6, lane >> 1, cb + 8 * (lane & 1), sidx);
        }
        BAR();
        if (layer == 0) poll_flags<64>(c, 0, 6, 32);
        issue_chunk<CO(0)>(c); issue_chunk<CO(1)>(c); issue_chunk<CO(2)>(c); issue_chunk<CO(3)>(c);
        BAR();
        if (layer == 0) {
            poll_flags<64>(c, 0, 5, 16);
            const int col = (w & 3) * 64 + lane;
            if (col < 224) {
                float t = 0.f;
#pragma unroll
                for (int i2 = 0; i2 < 16; ++i2) t += __builtin_bit_cast(float, __builtin_amdgcn_raw_buffer_load_b32(c.rs, (u32)col * 4u, (u32)WS_CSP + (u32)((c.j * 16 + i2) * 224) * 4u, 16));
                lds_st<float>(c.L, SC_CS + (96 + col) * 4, t);
            }
        }
        issue_chunk<CG(0, 0)>(c); issue_chunk<CG(0, 1)>(c);
    }
    const int qb = w < 4 ? w : 7 - w, hh = w >> 2;
    const u32x4 qf = lds_ld<u32x4>(c.L, SC_QS + (qb * 2 + hh) * 1024 + c.voff);
    const int vlane = (((lane >> 4) & 1) == hh) ? lane * 16 : 1024;
    f32x16 oacc = {};
    float m = 0.f;
    const LAS unsigned char* mk = lds_base(c, SC_MK);
    {
        const LAS unsigned char* kb = c.L + SC_KS + hh * 1024 + c.voff;
        const LAS unsigned char* vb = c.L + SC_VS + vlane;
        if (qb == 0) att_segment<1, 0, 0, 1, true>(oacc, m, kb, vb, mk, qf);
        else if (qb == 1) att_segment<2, 1, 0, 1, true>(oacc, m, kb, vb, mk, qf);
        else if (qb == 2) att_segment<3, 2, 0, 1, true>(oacc, m, kb, vb, mk, qf);
        else att_segment<4, 3, 0, 1, true>(oacc, m, kb, vb, mk, qf);
    }
    STAMP(6);
    if (c.i > 0) {
        if (w == 0) poll_flags<64>(c, layer, 1, c.i >= 2 ? 2 : 1);
        BAR();
        STAMP(7);
        {
            const int idx = w;
            const int g = 4 * c.i - 8 + idx;
            if (g >= 0) {
                const u32x4 s0 = ld_sc1(c, lbase + (u32)L_KF + (u32)(g * 32 + 2 * c.j + 0) * 1024);
                const u32x4 s1 = ld_sc1(c, lbase + (u32)L_KF + (u32)(g * 32 + 2 * c.j + 1) * 1024);
                const u32x4 s2 = ld_sc1(c, lbase + (u32)L_VF + (u32)((g * 16 + c.j) * 2 + 0) * 1024);
                const u32x4 s3 = ld_sc1(c, lbase + (u32)L_VF + (u32)((g * 16 + c.j) * 2 + 1) * 1024);
                lds_st<u32x4>(c.L, SC_RK + (idx * 2 + 0) * 1024 + c.voff, s0);
                lds_st<u32x4>(c.L, SC_RK + (idx * 2 + 1) * 1024 + c.voff, s1);
                lds_st<u32x4>(c.L, SC_RV + (idx * 2 + 0) * VREC + c.voff, s2);
                lds_st<u32x4>(c.L, SC_RV + (idx * 2 + 1) * VREC + c.voff, s3);
                if (lane < 2) { u32x4 ones = {BF_ONES, BF_ONES, BF_ONES, BF_ONES}; lds_st<u32x4>(c.L, SC_RV + (idx * 2 + lane) * VREC + 1024, ones); }
            }
        }
        BAR();
        STAMP(8);
        const LAS unsigned char* kb = c.L + SC_RK + hh * 1024 + c.voff;
        const LAS unsigned char* vb = c.L + SC_RV + vlane;
        if (c.i == 1) att_segment<4, 4, 5, 0, false>(oacc, m, kb, vb, mk, qf);
        else if (qb == 0) att_segment<8, 0, 1, 2, false>(oacc, m, kb, vb, mk, qf);
        else if (qb == 1) att_segment<7, 1, 2, 2, false>(oacc, m, kb, vb, mk, qf);
        else if (qb == 2) att_segment<6, 2, 3, 2, false>(oacc, m, kb, vb, mk, qf);
        else att_segment<5, 3, 4, 2, false>(oacc, m, kb, vb, mk, qf);
    }
    {
        u32x4 o;
        if (hh == 0) {
            float il = __builtin_amdgcn_rcpf(oacc[8]);
            asm volatile("s_nop 1" : "+v"(il));
            o = u32x4{pack2(smul(oacc[0], il), smul(oacc[1], il)), pack2(smul(oacc[2], il), smul(oacc[3], il)), pack2(smul(oacc[4], il), smul(oacc[5], il)), pack2(smul(oacc[6], il), smul(oacc[7], il))};
        } else {
            float il = __builtin_amdgcn_rcpf(oacc[0]);
            asm volatile("s_nop 1" : "+v"(il));
            o = u32x4{pack2(smul(oacc[8], il), smul(oacc[9], il)), pack2(smul(oacc[10], il), smul(oacc[11], il)), pack2(smul(oacc[12], il), smul(oacc[13], il)), pack2(smul(oacc[14], il), smul(oacc[15], il))};
        }
        st_sc1(c, lbase + (u32)L_OF + (u32)((4 * c.i + qb) * 32 + 2 * c.j + hh) * 1024, o);
    }
    STAMP(9);
    WAIT_VM(0);
    BAR();
    if (w == 0) signal_group(c, layer, 2);
    if (w >= 4) { issue_chunk<CG(1, 0)>(c); issue_chunk<CG(1, 1)>(c); }
    STAMP(10);
}

__device__ __forceinline__ void red_write(const Ctx& c, const f32x16& acc0, const f32x16& acc1, int t2, int kq) {
#pragma unroll
    for (int d = 0; d < 4; ++d) {
        const int fin = d & 1;
        if (kq != fin) {
            const int slot = kq < fin ? kq : kq - 1;
            const int base = red_off(t2 * 4 + d) + slot * 2048 + c.voff;
            const f32x16& a = d < 2 ? acc0 : acc1;
            const int o = (d & 1) * 8;
            lds_st<f32x4>(c.L, base, f32x4{a[o], a[o + 1], a[o + 2], a[o + 3]});
            lds_st<f32x4>(c.L, base + 1024, f32x4{a[o + 4], a[o + 5], a[o + 6], a[o + 7]});
        }
    }
}
__device__ __forceinline__ void red_read(const Ctx& c, const f32x16& acc0, const f32x16& acc1, int t2, int kq, float (&v)[2][8]) {
#pragma unroll
    for (int e = 0; e < 8; ++e) { v[0][e] = kq ? acc0[8 + e] : acc0[e]; v[1][e] = kq ? acc1[8 + e] : acc1[e]; }
#pragma unroll
    for (int x = 0; x < 2; ++x)
#pragma unroll
        for (int s = 0; s < 3; ++s) {
            const int base = red_off(t2 * 4 + 2 * x + kq) + s * 2048 + c.voff;
            const f32x4 lo = lds_ld<f32x4>(c.L, base), hi = lds_ld<f32x4>(c.L, base + 1024);
            v[x][0] += lo.x; v[x][1] += lo.y; v[x][2] += lo.z; v[x][3] += lo.w; v[x][4] += hi.x; v[x][5] += hi.y; v[x][6] += hi.z; v[x][7] += hi.w;
        }
}
__device__ __forceinline__ u32x4 add_res(const float (&v)[8], const u32x4& res) {
    return u32x4{pack2(v[0] + bflo(res.x), v[1] + bfhi(res.x)), pack2(v[2] + bflo(res.y), v[3] + bfhi(res.y)),
                 pack2(v[4] + bflo(res.z), v[5] + bfhi(res.z)), pack2(v[6] + bflo(res.w), v[7] + bfhi(res.w))};
}

__device__ __forceinline__ void phase_wo(const Ctx& c0, int layer, const Args& a) {
    const Ctx c = fresh(c0);
    const int w = c.w, t2 = w & 1, kq = w >> 1;
    const int th = c.j >> 3, cb = c.j & 7;
    const int tbk = 4 * c.i + 2 * th + t2;
    const u32 lbase = (u32)(WS_L0 + (size_t)layer * WS_LSTRIDE);
    if (w == 0) poll_flags<64>(c, layer, 2, 16);
    if (w >= 4) WAIT_VM(8);
    BAR();
    STAMP(11);
    u32x4 xf[8];
#pragma unroll
    for (int s = 0; s < 8; ++s) xf[s] = ld_sc1(c, lbase + (u32)L_OF + (u32)(tbk * 32 + 8 * kq + s) * 1024);
    u32x4 res[2];
    if (w < 4) {
#pragma unroll
        for (int x = 0; x < 2; ++x) {
            const int kbo = 4 * cb + 2 * x + kq;
            if (layer == 0) {
                const u16* src = a.x + (size_t)(tbk * 32 + (c.lane & 31)) * DM + kbo * 16 + 4 * (c.lane >> 5);
                const u32x2 lo = *(const u32x2*)src, hi = *(const u32x2*)(src + 8);
                res[x] = u32x4{lo.x, lo.y, hi.x, hi.y};
            } else {
                res[x] = ld_sc1(c, (u32)(WS_L0 + L_X2) + (u32)(tbk * 32 + kbo) * 1024);
            }
        }
    }
    u32x4 wf[16];
#pragma unroll
    for (int ch = 0; ch < 4; ++ch) {
        const LAS unsigned char* wb = lds_base(c, slot_of(CO(ch)) * SLOT + kq * 4096);
#pragma unroll
        for (int q = 0; q < 4; ++q) wf[4 * ch + q] = LDF(wb, q);
    }
    SB();
    f32x16 acc0 = {}, acc1 = {};
#pragma unroll
    for (int s = 0; s < 8; ++s) {
        acc0 = mfma32(wf[2 * s], xf[s], acc0);
        acc1 = mfma32(wf[2 * s + 1], xf[s], acc1);
        SB();
    }
    BAR();
    STAMP(12);
    red_write(c, acc0, acc1, t2, kq);
    BAR();
    if (w < 4) {
        float v[2][8];
        red_read(c, acc0, acc1, t2, kq, v);
#pragma unroll
        for (int x = 0; x < 2; ++x) st_sc1(c, lbase + (u32)L_X1 + (u32)(tbk * 32 + 4 * cb + 2 * x + kq) * 1024, add_res(v[x], res[x]));
        STAMP(13);
        WAIT_VM(0);
    }
    BAR();
    if (w == 0) signal_group(c, layer, 3);
    STAMP(14);
}

template <int T> __device__ __forceinline__ void gu_tile(const Ctx& c, f32x16& acc, const f32x16& prev, u32x4 (&wf)[8], const u32x4 (&xf)[32], int h,
                                                         f32x4 (&cs)[4], const GK& gk, u32 hbase) {
    const LAS unsigned char* cur = lds_base(c, slot_of(CG(T, 0)) * SLOT);
    const LAS unsigned char* nxt = lds_base(c, slot_of(CG(T < 6 ? T + 1 : T, 0)) * SLOT);
    GS g;
    u32 o[4];
    acc = f32x16{};
    SB();
    c_tile<false, (T < 6), false>(acc, wf, cur, cur + SLOT, nxt, xf, [&](int k) {
        const int p = k >> 3, e = 2 * p, q2 = p >> 1, e2 = e & 3;
        if ((k & 7) < 7) geglu_sub(g, o[p], k & 7, prev[e], prev[e + 1], prev[8 + e], prev[8 + e + 1], cs[q2][e2], cs[q2][e2 + 1], cs[2 + q2][e2], cs[2 + q2][e2 + 1], gk);
        if (k == 15) cs[0] = lds_ld<f32x4>(c.L, SC_CS + (96 + T * 32 + 4 * h) * 4);
        if (k == 15) cs[2] = lds_ld<f32x4>(c.L, SC_CS + (96 + T * 32 + 16 + 4 * h) * 4);
        if (k == 31) cs[1] = lds_ld<f32x4>(c.L, SC_CS + (96 + T * 32 + 8 + 4 * h) * 4);
        if (k == 31) cs[3] = lds_ld<f32x4>(c.L, SC_CS + (96 + T * 32 + 16 + 8 + 4 * h) * 4);
    });
    st_sc1(c, hbase + (u32)(T - 1) * 1024, u32x4{o[0], o[1], o[2], o[3]});
}

__device__ __forceinline__ u32 gu_compute(const Ctx& c0, int layer) {
    const Ctx c = fresh(c0);
    const int lane = c.lane, w = c.w, tb = w & 3, h = lane >> 5;
    const u32 lbase = (u32)(WS_L0 + (size_t)layer * WS_LSTRIDE);
    u32 pf1 = 0;
    {
        u32x4 xf[32], wf[8];
        const LAS unsigned char* w0 = lds_base(c, slot_of(CG(0, 0)) * SLOT);
        const LAS unsigned char* w1 = lds_base(c, slot_of(CG(1, 0)) * SLOT);
        f32x16 a0 = {}, a1;
        const f32x2 ms = c_tile0<true, false, false>(c, a0, wf, w0, w0 + SLOT, w1, xf, c.rs, lbase + (u32)L_X1 + (u32)((4 * c.i + tb) * 32) * 1024, [&]() {
            if (w == 0) poll_flags<64>(c, layer, 3, 16);
            BAR();
            STAMP(15);
        });
        constexpr float GC1 = -2.0f * 0.7978845608028654f * 1.4426950408889634f, GC2 = GC1 * 0.044715f;
        GK gk; gk.mean = ms.x; gk.A = ms.y * GC1; gk.B = ms.y * ms.y * ms.y * GC2; gk.ir2 = 1.0f / (ms.y * ms.y);
        BARL(); STAMP(16);
        f32x4 cs[4];
#pragma unroll
        for (int q = 0; q < 4; ++q) cs[q] = lds_ld<f32x4>(c.L, SC_CS + (96 + (q >> 1) * 16 + 8 * (q & 1) + 4 * h) * 4);
        const u32 hbase = lbase + (u32)L_HF + (u32)((4 * c.i + tb) * 112 + 7 * c.j) * 1024;
        gu_tile<1>(c, a1, a0, wf, xf, h, cs, gk, hbase); BARL();
        gu_tile<2>(c, a0, a1, wf, xf, h, cs, gk, hbase); BARL();
        gu_tile<3>(c, a1, a0, wf, xf, h, cs, gk, hbase); BARL();
        gu_tile<4>(c, a0, a1, wf, xf, h, cs, gk, hbase); BARL();
        gu_tile<5>(c, a1, a0, wf, xf, h, cs, gk, hbase);
        WAIT_VM(1);
        BARL();
        if (w == 0) signal_group(c, layer, 7);
        gu_tile<6>(c, a0, a1, wf, xf, h, cs, gk, hbase);
        STAMP(22);
        if (w == 0) pf1 = __builtin_amdgcn_raw_buffer_load_b32(c.rs, flag_off(layer, 7, c.i, c.j) + (lane & 15) * 4, 0, 16);
        {
            asm volatile("s_nop 15" : "+v"(a0));
            GS g[4];
            u32 o[4];
#pragma unroll
            for (int q = 0; q < 7; ++q)
#pragma unroll
                for (int p = 0; p < 4; ++p) {
                    const int e = 2 * p, q2 = p >> 1, e2 = e & 3;
                    geglu_sub(g[p], o[p], q, a0[e], a0[e + 1], a0[8 + e], a0[8 + e + 1], cs[q2][e2], cs[q2][e2 + 1], cs[2 + q2][e2], cs[2 + q2][e2 + 1], gk);
                }
            st_sc1(c, hbase + 6u * 1024, u32x4{o[0], o[1], o[2], o[3]});
        }
        STAMP(21);
        WAIT_VM(0);
        BAR();
        STAMP(23);
        if (w == 0) signal_group(c, layer, 4);
    }
    return pf1;
}
__device__ __forceinline__ void gu_helper(const Ctx& c0, int layer, u32x4 (&wr)[40]) {
    const Ctx c = fresh(c0);
    const int lane = c.lane, w = c.w;
    {
        WAIT_VM(0);
        BAR();
        __builtin_amdgcn_s_sleep(16);
        issue_chunk<CG(2, 0)>(c); issue_chunk<CG(2, 1)>(c); issue_chunk<CG(3, 0)>(c); issue_chunk<CG(3, 1)>(c);
        if (layer == 1 && w == 7 && c.i == 0 && c.j == 0) {
            for (u32 spins = 0;; ++spins) {
                asm volatile("" ::: "memory");
                const u32x4 f = __builtin_amdgcn_raw_buffer_load_b128(c.rs, c.voff, (u32)WS_STARTED, 16);
                if (__all(f.x == c.tag && f.y == c.tag && f.z == c.tag && f.w == c.tag)) { if (lane == 0) __builtin_amdgcn_raw_buffer_store_b32(c.tag + 31u, c.rs, (u32)WS_EPOCH, 0, 16); break; }
                if (spins > SPIN_CAP) break;
                __builtin_amdgcn_s_sleep(1);
            }
        }
        WAIT_VM(8);
        BAR();
        const u32 wdb = (u32)WS_WD + (u32)((c.j & 7) * 224 + 112 + ((w >> 1) - 2) * 56) * 1024;
#pragma unroll
        for (int q = 0; q < 20; ++q) wr[q] = __builtin_amdgcn_raw_buffer_load_b128(c.rs, c.voff, wdb + q * 1024, 16);
        issue_chunk<CG(4, 0)>(c); issue_chunk<CG(4, 1)>(c);
        WAIT_VM(8);
        BAR();
#pragma unroll
        for (int q = 20; q < 40; ++q) wr[q] = __builtin_amdgcn_raw_buffer_load_b128(c.rs, c.voff, wdb + q * 1024, 16);
        issue_chunk<CG(5, 0)>(c); issue_chunk<CG(5, 1)>(c);
        WAIT_VM(8);
        BAR();
        issue_chunk<CG(6, 0)>(c); issue_chunk<CG(6, 1)>(c);
        WAIT_VM(8);
        BAR();
        issue_chunk<CD(0)>(c); issue_chunk<CD(1)>(c);
        WAIT_VM(8);
        BAR();
        issue_chunk<CD(2)>(c); issue_chunk<CD(3)>(c);
        BAR();
        issue_chunk<CD(4)>(c); issue_chunk<CD(5)>(c);
        WAIT_VM(0);
        BAR();
        issue_chunk<CD(6)>(c);
    }
}

__device__ __forceinline__ void hld(const Ctx& c, u32x4& dst, u32 soff) {
    asm volatile("buffer_load_dwordx4 %0, %1, %2, %3 offen sc1" : "=v"(dst) : "v"(c.voff), "s"(c.rs), "s"(soff) : "memory");
}
template <int N> __device__ __forceinline__ void hwait(u32x4& x) { asm volatile("s_waitcnt vmcnt(%1)" : "+v"(x) : "i"(N) : "memory"); }
template <bool HELPER>
__device__ __forceinline__ void dn_main(const Ctx& c, int layer, int kq, u32 xsrc, u32 wdb, const u32x4* wr, f32x16& acc0, f32x16& acc1) {
    constexpr int XD = 10;
    u32x4 xf[28], wf[8], wl[16];
    const LAS unsigned char* wb[7];
#pragma unroll
    for (int s = 0; s < XD; ++s) hld(c, xf[s], xsrc + (u32)dn_kstep(s) * 1024);
    if (!HELPER) {
#pragma unroll
        for (int ch = 0; ch < 7; ++ch) wb[ch] = lds_base(c, chunk_lds(CD(ch)) + kq * 8192);
#pragma unroll
        for (int q = 0; q < 8; ++q) wf[q] = LDF(wb[0], q);
    }
    SB();
    u32 pf = 0;
#pragma unroll
    for (int s = 0; s < 28; ++s) {
        if (s + XD < 28) hld(c, xf[s + XD], xsrc + (u32)dn_kstep(s + XD) * 1024);
        u32x4 w0, w1;
        if (HELPER) {
            if (s < 20) { w0 = wr[2 * s]; w1 = wr[2 * s + 1]; } else { w0 = wl[2 * (s - 20)]; w1 = wl[2 * (s - 20) + 1]; }
        } else {
            w0 = wf[(2 * s) & 7]; w1 = wf[(2 * s + 1) & 7];
            if (s + 4 < 28) { wf[(2 * s) & 7] = LDF(wb[(s + 4) >> 2], 2 * ((s + 4) & 3)); wf[(2 * s + 1) & 7] = LDF(wb[(s + 4) >> 2], 2 * ((s + 4) & 3) + 1); }
        }
        if (s + XD < 28) { if (HELPER && s >= 8 && s < 12 + XD - 10 + 10 && s <= 21) hwait<10>(xf[s]); else hwait<10>(xf[s]); }
        else if (s == 18) hwait<9>(xf[s]); else if (s == 19) hwait<8>(xf[s]); else if (s == 20) hwait<7>(xf[s]); else if (s == 21) hwait<6>(xf[s]); else if (s == 22) hwait<5>(xf[s]);
        else if (s == 23) hwait<4>(xf[s]); else if (s == 24) hwait<3>(xf[s]); else if (s == 25) hwait<2>(xf[s]); else if (s == 26) hwait<1>(xf[s]); else hwait<0>(xf[s]);
        if (HELPER && s == 20) {
            asm volatile("s_waitcnt vmcnt(6)" : "+v"(wl[0]), "+v"(wl[1]), "+v"(wl[2]), "+v"(wl[3]), "+v"(wl[4]), "+v"(wl[5]), "+v"(wl[6]), "+v"(wl[7]) :: "memory");
            asm volatile("" : "+v"(wl[8]), "+v"(wl[9]), "+v"(wl[10]), "+v"(wl[11]), "+v"(wl[12]), "+v"(wl[13]), "+v"(wl[14]), "+v"(wl[15]));
            w0 = wl[0]; w1 = wl[1];
        }
        acc0 = mfma32(w0, xf[s], acc0);
        acc1 = mfma32(w1, xf[s], acc1);
        PIN(acc0); PIN(acc1);
        if (HELPER && s >= 8 && s < 12) {
#pragma unroll
            for (int q = 0; q < 4; ++q) hld(c, wl[4 * (s - 8) + q], wdb + (u32)(40 + 4 * (s - 8) + q) * 1024);
        }
        if (!HELPER && s == 2 && c.w == 0)
            asm volatile("buffer_load_dword %0, %1, %2, 0 offen sc1" : "=v"(pf) : "v"(flag_off(layer, 4, c.i, c.j) + (c.lane & 15) * 4), "s"(c.rs) : "memory");
        if (s + XD == 15) {
            if (!HELPER && c.w == 0) {
                asm volatile("s_waitcnt vmcnt(3)" : "+v"(pf) :: "memory");
                if (!__all(pf == flag_tag(c, layer, 4))) poll_flags<64>(c, layer, 4, 16);
            }
            BARL();
        }
        SB();
    }
}

__device__ __forceinline__ void phase_ffn(const Ctx& c0, int layer, const Args& a) {
    f32x16 acc0 = {}, acc1 = {};
    u32x4 res[2];
    if (c0.w < 4) {
        const u32 pf1 = gu_compute(c0, layer);
        const Ctx c = fresh(c0);
        const int w = c.w, t2 = w & 1, kq = w >> 1, th = c.j >> 3, cb = c.j & 7;
        const int tbk = 4 * c.i + 2 * th + t2;
        const u32 lbase = (u32)(WS_L0 + (size_t)layer * WS_LSTRIDE);
        res[0] = ld_sc1(c, lbase + (u32)L_X1 + (u32)(tbk * 32 + 4 * cb + kq) * 1024);
        res[1] = ld_sc1(c, lbase + (u32)L_X1 + (u32)(tbk * 32 + 4 * cb + 2 + kq) * 1024);
        if (w == 0 && !__all(pf1 == flag_tag(c, layer, 7))) poll_flags<64>(c, layer, 7, 16);
        BARL();
        STAMP(25);
        dn_main<false>(c, layer, kq, lbase + (u32)L_HF + (u32)(tbk * 112 + 28 * kq) * 1024, 0u, nullptr, acc0, acc1);
    } else {
        u32x4 wr[40];
        gu_helper(c0, layer, wr);
        const Ctx c = fresh(c0);
        const int w = c.w, t2 = w & 1, kq = w >> 1, th = c.j >> 3, cb = c.j & 7;
        const int tbk = 4 * c.i + 2 * th + t2;
        const u32 lbase = (u32)(WS_L0 + (size_t)layer * WS_LSTRIDE);
        BARL();
        dn_main<true>(c, layer, kq, lbase + (u32)L_HF + (u32)(tbk * 112 + 28 * kq) * 1024, (u32)WS_WD + (u32)(cb * 224 + 112 + (kq - 2) * 56) * 1024, wr, acc0, acc1);
    }
    const Ctx c = fresh(c0);
    const int lane = c.lane, w = c.w, t2 = w & 1, kq = w >> 1, h = lane >> 5, r = lane & 31;
    const int th = c.j >> 3, cb = c.j & 7;
    const int tbk = 4 * c.i + 2 * th + t2;
    const u32 lbase = (u32)(WS_L0 + (size_t)layer * WS_LSTRIDE);
    BAR();
    STAMP(26);
    red_write(c, acc0, acc1, t2, kq);
    BAR();
    if (w >= 4) {
        if (layer == 0) issue_qkv_tile(c, a, 0);
    } else {
        float v[2][8];
        red_read(c, acc0, acc1, t2, kq, v);
#pragma unroll
        for (int x = 0; x < 2; ++x) {
            const int kbo = 4 * cb + 2 * x + kq;
            const u32x4 o = add_res(v[x], res[x]);
            const auto r0 = __builtin_amdgcn_permlane32_swap(o.x, o.z, false, false);
            const auto r1 = __builtin_amdgcn_permlane32_swap(o.y, o.w, false, false);
            const u32x4 on = u32x4{r0[0], r1[0], r0[1], r1[1]};
            if (layer == 1) {
                *(u32x4*)(a.out + (size_t)(tbk * 32 + r) * DM + kbo * 16 + 8 * h) = on;
            } else {
                st_sc1(c, lbase + (u32)L_X2N + (u32)(tbk * 32 + kbo) * 1024, on);
                st_sc1(c, lbase + (u32)L_X2 + (u32)(tbk * 32 + kbo) * 1024, o);
            }
        }
        STAMP(27);
        if (layer == 0) WAIT_VM(0);
    }
    if (layer == 0) {
        BAR();
        if (w == 0) signal_group(c, layer, 0);
        if (w >= 4) { issue_qkv_tile(c, a, 1); issue_qkv_tile(c, a, 2); }
        STAMP(28);
    }
}

__global__ void __launch_bounds__(512, 2) mega_kernel(Args a) {
    extern __shared__ __attribute__((aligned(16))) unsigned char lds[];
    Ctx c;
    c.L = (LAS unsigned char*)lds;
    c.Laddr = (u32)(size_t)c.L;
    c.ws = a.ws;
    c.rs = __builtin_amdgcn_make_buffer_rsrc((void*)a.ws, 0, (int)WS_END, 0x00020000);
    const int tid = threadIdx.x;
    c.lane = tid & 63;
    c.w = __builtin_amdgcn_readfirstlane(tid >> 6);
    const int bid = blockIdx.x;
    c.i = 2 * (bid & 7) + (bid >> 7);
    c.j = (bid >> 3) & 15;
    c.voff = c.lane * 16;
    {
        c.tag = 0;
        if (c.w >= 4) { issue_qkv_tile(c, a, 0); issue_qkv_tile(c, a, 1); issue_qkv_tile(c, a, 2); }
        u32 e;
        asm volatile("s_load_dword %0, %1, 0x0\n\ts_waitcnt lgkmcnt(0)" : "=s"(e) : "s"(a.ws + WS_EPOCH) : "memory");
        c.tag = e + 1u;
        if (tid == 0) __builtin_amdgcn_raw_buffer_store_b32(c.tag, c.rs, (u32)bid * 4u, (u32)WS_STARTED, 16);
    }
    if (c.w < 4) {
        const int hh = c.lane >> 5, row = c.lane & 31, half = c.w & 1;
        u32 d[4];
#pragma unroll
        for (int q = 0; q < 4; ++q) {
            const int c0 = 16 * half + 8 * (q >> 1) + 4 * hh + 2 * (q & 1), c1 = c0 + 1;
            const u32 lo = c.w < 2 ? (row > c0 ? 0xff7fu : 0u) : (row == c0 ? 0x3f80u : 0u);
            const u32 hi = c.w < 2 ? (row > c1 ? 0xff7fu : 0u) : (row == c1 ? 0x3f80u : 0u);
            d[q] = lo | (hi << 16);
        }
        lds_st<u32x4>(c.L, SC_MK + c.w * 1024 + c.voff, u32x4{d[0], d[1], d[2], d[3]});
    }
    for (int layer = 0; layer < 2; ++layer) {
        phase_qkv_att(c, layer, a);
        phase_wo(c, layer, a);
        phase_ffn(c, layer, a);
    }
}

}

extern "C" void kernel_launch(void* const* d_in, const int* in_sizes, int n_in, void* d_out, int out_size, void* d_ws, size_t ws_size,
                              hipStream_t stream) {
    Args a;
    a.x = (const u16*)d_in[0]; a.wqkv = (const u16*)d_in[1]; a.wo = (const u16*)d_in[2];
    a.wg = (const u16*)d_in[4]; a.wu = (const u16*)d_in[5]; a.wd = (const u16*)d_in[6];
    a.out = (u16*)d_out; a.ws = (unsigned char*)d_ws;
    (void)hipFuncSetAttribute((const void*)mega_kernel, hipFuncAttributeMaxDynamicSharedMemorySize, LDS_BYTES);
    mega_kernel<<<256, 512, LDS_BYTES, stream>>>(a);
}
```

```cpp
#include <hip/hip_runtime.h>
#include <hip/hip_bf16.h>

#define LAS __attribute__((address_space(3)))
#define GAS __attribute__((address_space(1)))
typedef unsigned u32;
typedef unsigned short u16;
typedef u32 u32x4 __attribute__((ext_vector_type(4)));
typedef u32 u32x2 __attribute__((ext_vector_type(2)));
typedef float f32x16 __attribute__((ext_vector_type(16)));
typedef float f32x4 __attribute__((ext_vector_type(4)));
typedef float f32x2 __attribute__((ext_vector_type(2)));
typedef __bf16 bf16x8_t __attribute__((ext_vector_type(8)));
typedef __bf16 bf16x2_t __attribute__((ext_vector_type(2)));
typedef short s16x4 __attribute__((ext_vector_type(4)));

namespace {

constexpr int SEQ = 2048, DM = 512, FF = 1792;
constexpr size_t KiB = 1024, MiB = 1024 * 1024;
constexpr size_t WS_FLAGS = 1 * MiB;
constexpr size_t WS_EPOCH = 640 * KiB;
constexpr size_t WS_STARTED = WS_EPOCH + 4096;
constexpr size_t WS_TMO = 768 * KiB;
constexpr size_t WS_CS = 512 * KiB;
constexpr size_t WS_XF0 = 12 * MiB;
constexpr size_t WS_WQ = 14 * MiB;
constexpr size_t WS_CSP = 3 * MiB + 256 * KiB;
constexpr size_t WS_WO = 4 * MiB + 512 * KiB;
constexpr size_t WS_WG = 5 * MiB;
constexpr size_t WS_WD = 9 * MiB;
constexpr size_t WS_L0 = 16 * MiB, WS_LSTRIDE = 32 * MiB;
constexpr size_t L_KF = 0, L_VF = 2 * MiB, L_OF = 4 * MiB, L_X1 = 6 * MiB, L_X2 = 8 * MiB, L_HF = 10 * MiB, L_X2N = 18 * MiB;
constexpr size_t WS_END = WS_L0 + 2 * WS_LSTRIDE;

constexpr int NSLOT = 8, SLOT = 16384;
constexpr int LDS_SCR = NSLOT * SLOT;
constexpr int VREC = 1040;
constexpr int SC_RV = LDS_SCR;
constexpr int SC_VS = SC_RV + 16640;
constexpr int SC_ST = SC_VS + 8320;
constexpr int SC_CS = SC_ST + 1024;
constexpr int SC_MK = SC_CS + 1280;
constexpr int LDS_BYTES = SC_MK + 4096;
constexpr int SC_KS = 0 * SLOT;
constexpr int SC_QS = 0 * SLOT + 8192;
constexpr int SC_RK = 1 * SLOT;
static_assert(LDS_BYTES <= 163840, "LDS");
__device__ __forceinline__ int red_off(int q) { return q < 5 ? 2 * SLOT + q * 6144 : 6 * SLOT + (q - 5) * 6144; }

constexpr float LN_EPS = 1e-5f;
constexpr float QSCALE = 0.25f * 1.4426950408889634f;
constexpr u32 SPIN_CAP = 1u << 17;
constexpr u32 BF_ONES = 0x3f803f80u;

struct Args {
    const u16* x; const u16* wqkv; const u16* wo; const u16* wg; const u16* wu; const u16* wd;
    u16* out; unsigned char* ws;
};

__device__ __forceinline__ f32x16 mfma32(u32x4 a, u32x4 b, f32x16 c) {
    return __builtin_amdgcn_mfma_f32_32x32x16_bf16(__builtin_bit_cast(bf16x8_t, a), __builtin_bit_cast(bf16x8_t, b), c, 0, 0, 0);
}
__device__ __forceinline__ u32 pack2(float lo, float hi) {
    f32x2 v = {lo, hi};
    return __builtin_bit_cast(u32, __builtin_convertvector(v, bf16x2_t));
}
__device__ __forceinline__ float bflo(u32 u) { return __builtin_bit_cast(float, u << 16); }
__device__ __forceinline__ float bfhi(u32 u) { return __builtin_bit_cast(float, u & 0xffff0000u); }
__device__ __forceinline__ float dot2(u32 a, u32 b, float c) {
    return __builtin_amdgcn_fdot2_f32_bf16(__builtin_bit_cast(bf16x2_t, a), __builtin_bit_cast(bf16x2_t, b), c, false);
}
__device__ __forceinline__ void swap32(float& a, float& b) {
    asm volatile("s_nop 1\n\tv_permlane32_swap_b32 %0, %1\n\ts_nop 1" : "+v"(a), "+v"(b));
}
__device__ __forceinline__ float fmx(float a, float b) { return __builtin_elementwise_maximum(a, b); }
__device__ __forceinline__ float xor32_max(float v) { float a = v, b = v; swap32(a, b); return fmx(a, b); }
__device__ __forceinline__ float xor32_sum(float v) { float a = v, b = v; swap32(a, b); return a + b; }
#define BAR() do { asm volatile("s_waitcnt lgkmcnt(0)" ::: "memory"); __builtin_amdgcn_s_barrier(); asm volatile("" ::: "memory"); } while (0)
#define BARL() do { asm volatile("" ::: "memory"); __builtin_amdgcn_s_barrier(); asm volatile("" ::: "memory"); } while (0)
#define WAIT_VM(n) asm volatile("s_waitcnt vmcnt(" #n ")" ::: "memory")
#define SB() __builtin_amdgcn_sched_barrier(0)

template <class T> __device__ __forceinline__ T lds_ld(const LAS unsigned char* base, int off) { return *(const LAS T*)(base + off); }
template <class T> __device__ __forceinline__ void lds_st(LAS unsigned char* base, int off, T v) { *(LAS T*)(base + off) = v; }

__device__ __forceinline__ void glds4k(const unsigned char* src, u32 voff, u32 dst) {
    u32 keep;
    asm volatile(
        "s_mov_b32 %0, m0\n\t"
        "s_mov_b32 m0, %3\n\t"
        "s_nop 0\n\t"
        "global_load_lds_dwordx4 %1, %2 sc1\n\t"
        "global_load_lds_dwordx4 %1, %2 offset:1024 sc1\n\t"
        "global_load_lds_dwordx4 %1, %2 offset:2048 sc1\n\t"
        "global_load_lds_dwordx4 %1, %2 offset:3072 sc1\n\t"
        "s_mov_b32 m0, %0"
        : "=&s"(keep) : "v"(voff), "s"(src), "s"(dst) : "memory");
}

__device__ __forceinline__ void glds1k(const unsigned char* src, u32 voff, u32 dst) {
    u32 keep;
    asm volatile(
        "s_mov_b32 %0, m0\n\t"
        "s_mov_b32 m0, %3\n\t"
        "s_nop 0\n\t"
        "global_load_lds_dwordx4 %1, %2\n\t"
        "s_mov_b32 m0, %0"
        : "=&s"(keep) : "v"(voff), "s"(src), "s"(dst) : "memory");
}
#define PIN(x) asm volatile("" : "+v"(x))

constexpr int NF_WO = 8 * 64, NF_WG = 16 * 224, NF_WD = 8 * 224;
__host__ __device__ constexpr int qkv_col(int t) { return t == 0 ? 512 : (t == 1 ? 1024 : 0); }
__host__ __device__ constexpr int dn_kstep(int s) { return s < 16 ? 7 * (s >> 2) + (s & 3) : 7 * ((s - 16) / 3) + 4 + (s - 16) % 3; }
struct WItem { const u16* src; size_t dst; };
__device__ __forceinline__ WItem w_item(const Args& a, int f, int lane) {
    const u16 *pA, *pB; int ld, ks; size_t dst;
    if (f < NF_WO) {
        const int cb = f >> 6, rem = f & 63, c = rem >> 4, r2 = rem & 15, kq = r2 >> 2, ks2 = (r2 >> 1) & 1, nt = r2 & 1;
        ks = 8 * kq + 2 * c + ks2; pA = a.wo + 64 * cb + 32 * nt; pB = pA + 16; ld = 512; dst = WS_WO + (size_t)f * 1024;
    } else if ((f -= NF_WO) < NF_WG) {
        const int j = f / 224, rem = f % 224, q = rem >> 4, ksl = rem & 15, c = q >> 1, hf = q & 1;
        ks = 16 * hf + ksl; pA = a.wg + 112 * j + 16 * c; pB = a.wu + 112 * j + 16 * c; ld = FF; dst = WS_WG + (size_t)f * 1024;
    } else {
        f -= NF_WG;
        const int cb = f / 224, rem = f % 224;
        int kq, sidx, nt;
        if (rem < 112) { const int c = rem >> 4, r2 = rem & 15; kq = r2 >> 3; sidx = 4 * c + ((r2 >> 1) & 3); nt = r2 & 1; }
        else { const int g = rem - 112; kq = 2 + g / 56; sidx = (g % 56) >> 1; nt = g & 1; }
        ks = 28 * kq + dn_kstep(sidx); pA = a.wd + 64 * cb + 32 * nt; pB = pA + 16; ld = 512; dst = WS_WD + (size_t)f * 1024;
    }
    const int k = lane >> 2, n8 = lane & 3;
    WItem it;
    it.src = (n8 < 2 ? pA + n8 * 8 : pB + (n8 - 2) * 8) + (size_t)(16 * ks + k) * ld;
    it.dst = dst;
    return it;
}

constexpr int PREP_XI = 64 * 8, NF_WQ = 16 * 96;
constexpr int PREP_WG_TILE = (PREP_XI + NF_WQ) / 8;
constexpr int PREP_WG_CS = 1536 / 64, PREP_WG_FLAGS = 16;
constexpr int PREP_GRID = PREP_WG_TILE + PREP_WG_CS + PREP_WG_FLAGS;
constexpr int PREP_LDS_WAVE = 4608;
constexpr size_t FLAG_BYTES = 2 * 8 * 256 * 512;
__global__ void __launch_bounds__(512) prep_kernel(Args a) {
    __shared__ __attribute__((aligned(16))) unsigned char sm[8 * PREP_LDS_WAVE];
    const int tid = threadIdx.x, lane = tid & 63, w = tid >> 6;
    const int b0 = blockIdx.x;
    unsigned char* ws = a.ws;
    if (b0 < PREP_WG_CS) {
        const u16* src = a.wqkv + b0 * 64;
        const int rg = lane >> 3, ch = lane & 7;
        u32x4 v[8];
#pragma unroll
        for (int it = 0; it < 8; ++it) v[it] = *(const u32x4*)(src + (size_t)(64 * w + 8 * it + rg) * 1536 + ch * 8);
        float s[8];
#pragma unroll
        for (int e = 0; e < 8; ++e) s[e] = 0.f;
#pragma unroll
        for (int it = 0; it < 8; ++it) {
            s[0] += bflo(v[it].x); s[1] += bfhi(v[it].x); s[2] += bflo(v[it].y); s[3] += bfhi(v[it].y);
            s[4] += bflo(v[it].z); s[5] += bfhi(v[it].z); s[6] += bflo(v[it].w); s[7] += bfhi(v[it].w);
        }
#pragma unroll
        for (int e = 0; e < 8; ++e) { s[e] += __shfl_xor(s[e], 8); s[e] += __shfl_xor(s[e], 16); s[e] += __shfl_xor(s[e], 32); }
        float* red = (float*)sm;
        if (lane < 8) {
#pragma unroll
            for (int e = 0; e < 8; ++e) red[w * 64 + lane * 8 + e] = s[e];
        }
        __syncthreads();
        if (tid < 64) ((float*)(ws + WS_CS))[b0 * 64 + tid] = ((red[tid] + red[64 + tid]) + (red[128 + tid] + red[192 + tid])) + ((red[256 + tid] + red[320 + tid]) + (red[384 + tid] + red[448 + tid]));
    } else if (b0 < PREP_WG_CS + PREP_WG_FLAGS) {
        const int g = b0 - PREP_WG_CS;
        const u32x4 z = {0u, 0u, 0u, 0u};
        for (size_t o = (size_t)(g * 512 + tid) * 16; o < FLAG_BYTES; o += (size_t)PREP_WG_FLAGS * 512 * 16) *(u32x4*)(ws + WS_FLAGS + o) = z;
    } else {
        const int item = (b0 - PREP_WG_CS - PREP_WG_FLAGS) * 8 + w;
        LAS unsigned char* t = (LAS unsigned char*)sm + w * PREP_LDS_WAVE;
        if (item < PREP_XI) {
            const int tb = item >> 3, g = item & 7;
            const u16* src = a.x + (size_t)(tb * 32) * DM + g * 64;
            u32x4 v[4];
#pragma unroll
            for (int q = 0; q < 4; ++q) v[q] = *(const u32x4*)(src + (size_t)(8 * q + (lane >> 3)) * DM + (lane & 7) * 8);
#pragma unroll
            for (int q = 0; q < 4; ++q) lds_st<u32x4>(t, (8 * q + (lane >> 3)) * 144 + (lane & 7) * 16, v[q]);
            asm volatile("s_waitcnt lgkmcnt(0)" ::: "memory");
            const int h = lane >> 5, r = lane & 31;
#pragma unroll
            for (int kbl = 0; kbl < 4; ++kbl) {
                const u32x2 lo = lds_ld<u32x2>(t, r * 144 + kbl * 32 + 8 * h);
                const u32x2 hi = lds_ld<u32x2>(t, r * 144 + kbl * 32 + 16 + 8 * h);
                *(u32x4*)(ws + WS_XF0 + (size_t)(tb * 32 + 4 * g + kbl) * 1024 + lane * 16) = u32x4{lo.x, lo.y, hi.x, hi.y};
            }
        } else {
            const int f = item - PREP_XI;
            const int j = f / 96, rem = f % 96, q = rem >> 4, ksl = rem & 15, ct = q >> 1, hf = q & 1;
            const int k = lane >> 2, n8 = lane & 3;
            const u32x4 v = *(const u32x4*)(a.wqkv + qkv_col(ct) + 32 * j + n8 * 8 + (size_t)(16 * (16 * hf + ksl) + k) * 1536);
            lds_st<u32x4>(t, k * 64 + n8 * 16, v);
            asm volatile("s_waitcnt lgkmcnt(0)" ::: "memory");
            const int g4 = lane >> 4, h = g4 >> 1, nh = g4 & 1, li = lane & 15, qq = li >> 2, p = li & 3;
            const s16x4 lo = __builtin_amdgcn_ds_read_tr16_b64_v4i16((LAS s16x4*)(t + (4 * h + qq) * 64 + nh * 32 + p * 8));
            const s16x4 hi = __builtin_amdgcn_ds_read_tr16_b64_v4i16((LAS s16x4*)(t + (8 + 4 * h + qq) * 64 + nh * 32 + p * 8));
            const u32x2 l2 = __builtin_bit_cast(u32x2, lo), h2 = __builtin_bit_cast(u32x2, hi);
            *(u32x4*)(ws + WS_WQ + (size_t)f * 1024 + lane * 16) = u32x4{l2.x, l2.y, h2.x, h2.y};
        }
    }
}

#define STAMP(k) do { } while (0)
struct Ctx {
    LAS unsigned char* L;
    u32 Laddr;
    unsigned char* ws;
    __amdgpu_buffer_rsrc_t rs;
    int lane, w, i, j;
    u32 voff;
    u32 tag;
};

__device__ __forceinline__ u32x4 ld_sc1(const Ctx& c, u32 soff) { return __builtin_amdgcn_raw_buffer_load_b128(c.rs, c.voff, soff, 16); }
__device__ __forceinline__ void st_sc1(const Ctx& c, u32 soff, u32x4 v) { __builtin_amdgcn_raw_buffer_store_b128(v, c.rs, c.voff, soff, 16); }
__device__ __forceinline__ Ctx fresh(const Ctx& c0) {
    Ctx c = c0;
    asm volatile("" : "+s"(c.i), "+s"(c.j), "+s"(c.w), "+s"(c.tag));
    asm volatile("" : "+v"(c.lane));
    c.voff = c.lane * 16;
    return c;
}

constexpr int CQ(int t, int hf) { return 2 * t + hf; }
constexpr int CO(int k) { return 6 + k; }
constexpr int CG(int t, int hf) { return 10 + 2 * t + hf; }
constexpr int CD(int k) { return 24 + k; }
constexpr int slot_of(int n) {
    constexpr int tab[38] = {
          0, 1,   2, 3,   4, 5,
          6, 7, 2, 3,
          4, 5,   0, 1,   2, 3,   6, 7,   4, 5,   0, 1,   2, 3,
          6, 7, 4, 5, 0, 1, 2,   0, 0, 0, 0, 0, 0, 0};
    return tab[n];
}
template <int N> __device__ __forceinline__ const unsigned char* chunk_src(const Ctx& c) {
    size_t src;
    const int cb = c.j & 7;
    if (N < 6) src = WS_WQ + (size_t)(c.j * 6 + N) * SLOT;
    else if (N < 10) src = WS_WO + (size_t)(cb * 4 + (N - 6)) * SLOT;
    else if (N < 24) src = WS_WG + (size_t)(c.j * 14 + (N - 10)) * SLOT;
    else src = WS_WD + (size_t)(cb * 14 + (N - 24)) * SLOT;
    return c.ws + src + (c.w & 3) * 4096;
}
constexpr int chunk_lds(int n) { return slot_of(n) * SLOT; }
template <int N> __device__ __forceinline__ void issue_chunk(const Ctx& c) {
    glds4k(chunk_src<N>(c), c.voff, c.Laddr + chunk_lds(N) + (c.w & 3) * 4096);
}
__device__ __forceinline__ const LAS unsigned char* lds_base(const Ctx& c, int off) {
    u32 a = c.Laddr + off + c.voff;
    asm volatile("" : "+v"(a));
    return (const LAS unsigned char*)(size_t)a;
}

__device__ __forceinline__ u32 flag_off(int layer, int seam, int ci, int cj) {
    return (u32)(WS_FLAGS + (size_t)(((layer * 8 + seam) * 256) + ci * 16 + cj) * 512);
}
__device__ __forceinline__ u32 flag_tag(const Ctx& c, int layer, int seam) { return c.tag + (u32)(layer * 8 + seam); }
__device__ __forceinline__ void flag_store(const Ctx& c, int layer, int seam, int ci, int cj, int word) {
    __builtin_amdgcn_raw_buffer_store_b32(flag_tag(c, layer, seam), c.rs, flag_off(layer, seam, ci, cj) + word * 4, 0, 16);
}
__device__ __forceinline__ void signal_group(const Ctx& c, int layer, int seam) {
    if (c.lane < 16) flag_store(c, layer, seam, c.i, c.lane, c.j);
}
template <int NWORDS> __device__ __forceinline__ void poll_flags(const Ctx& c, int layer, int seam, int nwords = NWORDS) {
    const u32 off = flag_off(layer, seam, c.i, c.j) + c.lane * 4;
    const u32 tag = flag_tag(c, layer, seam);
    for (u32 spins = 0;; ++spins) {
        asm volatile("" ::: "memory");
        const u32 f0 = __builtin_amdgcn_raw_buffer_load_b32(c.rs, off, 0, 16);
        bool ok = c.lane >= nwords || f0 == tag;
        if (NWORDS > 64) { const u32 f1 = __builtin_amdgcn_raw_buffer_load_b32(c.rs, off + 256, 0, 16); ok = ok && f1 == tag; }
        if (__all(ok)) break;
        if (spins > SPIN_CAP) { if (c.lane == 0) __builtin_amdgcn_raw_buffer_store_b32(1u, c.rs, (u32)WS_TMO, 0, 16); break; }
        __builtin_amdgcn_s_sleep(1);
    }
}

__device__ __forceinline__ int crow(int r, int h) { return (r & 3) + 8 * (r >> 2) + 4 * h; }

#define LDF(p, k) (*(const LAS u32x4*)((p) + (k) * 1024))
__device__ __forceinline__ u32x4 ldf_tr(const LAS unsigned char* p, int k) {
    const u32x2 lo = __builtin_bit_cast(u32x2, __builtin_amdgcn_ds_read_tr16_b64_v4i16((LAS s16x4*)(p + k * 1024)));
    const u32x2 hi = __builtin_bit_cast(u32x2, __builtin_amdgcn_ds_read_tr16_b64_v4i16((LAS s16x4*)(p + k * 1024 + 256)));
    return u32x4{lo.x, lo.y, hi.x, hi.y};
}
template <bool TR> __device__ __forceinline__ u32x4 ldf(const LAS unsigned char* p, int k) { if constexpr (TR) return ldf_tr(p, k); else return LDF(p, k); }

template <bool SWAP, bool PRE, bool TR, class F>
__device__ __forceinline__ void c_tile(f32x16& acc, u32x4 (&wf)[8], const LAS unsigned char* lo, const LAS unsigned char* hi, const LAS unsigned char* nxt,
                                       const u32x4 (&xf)[32], F&& filler) {
#pragma unroll
    for (int k = 0; k < 32; ++k) {
        const u32x4 wv = wf[k & 7];
        if (k + 8 < 16) wf[k & 7] = ldf<TR>(lo, k + 8);
        else if (k + 8 < 32) wf[k & 7] = ldf<TR>(hi, k + 8 - 16);
        else if (PRE) wf[k & 7] = ldf<TR>(nxt, k + 8 - 32);
        acc = SWAP ? mfma32(xf[k], wv, acc) : mfma32(wv, xf[k], acc);
        PIN(acc);
        filler(k);
        SB();
    }
}
template <bool PRE, bool TR, bool STAGE, bool MID, class W>
__device__ __forceinline__ f32x2 c_tile0(const Ctx& c, f32x16& acc, u32x4 (&wf)[8], const LAS unsigned char* lo, const LAS unsigned char* hi,
                                         const LAS unsigned char* nxt, u32x4 (&xf)[32], __amdgpu_buffer_rsrc_t xrs, u32 xsoff, W&& wait_ready) {
    const int lane = c.lane;
    u32x4 raw[STAGE ? 32 : 1];
    const u32 rvoff = (u32)(lane >> 3) * 1024u + (u32)(lane & 7) * 16u;
    LAS unsigned char* st = c.L + 6 * SLOT + (c.w & 3) * 8192;
    const int wr_off = (lane >> 3) * 128 + (((lane & 7) ^ ((lane >> 3) & 7)) * 16);
    const int rd_row = (lane & 31) * 128, rd_sw = lane & 7, rd_h = lane >> 5;
#define XLD(k) __builtin_amdgcn_raw_buffer_load_b128(xrs, c.voff, xsoff + (u32)(k) * 1024u, 16)
#define RLD(q) __builtin_amdgcn_raw_buffer_load_b128(xrs, rvoff, xsoff + (u32)((q) & 3) * 8192u + (u32)((q) >> 2) * 128u, 0)
#define STG(g) do { _Pragma("unroll") for (int q = 0; q < 4; ++q) lds_st<u32x4>(st + ((g) & 1) * 4096, q * 1024 + wr_off, raw[4 * (g) + q]); \
                    _Pragma("unroll") for (int q = 0; q < 4; ++q) xf[4 * (g) + q] = lds_ld<u32x4>(st + ((g) & 1) * 4096, rd_row + (((2 * q + rd_h) ^ rd_sw) * 16)); } while (0)
    if (STAGE) {
#pragma unroll
        for (int q = 0; q < 16; ++q) raw[q] = RLD(q);
        SB();
        wait_ready();
        STG(0);
    } else {
        wait_ready();
#pragma unroll
        for (int k = 0; k < 14; ++k) xf[k] = XLD(k);
    }
#pragma unroll
    for (int k = 0; k < 8; ++k) wf[k] = ldf<TR>(lo, k);
    SB();
    float s1[4] = {0.f, 0.f, 0.f, 0.f}, s2[4] = {0.f, 0.f, 0.f, 0.f};
#pragma unroll
    for (int k = 0; k < 32; ++k) {
        if (STAGE) {
            if ((k & 3) == 0) {
                const int g = k >> 2;
                if (4 * (g + 4) < 32) {
#pragma unroll
                    for (int q = 0; q < 4; ++q) raw[4 * (g + 4) + q] = RLD(4 * (g + 4) + q);
                }
                if (g + 1 < 8) STG(g + 1);
            }
        } else {
            if (k + 14 < 32) xf[k + 14] = XLD(k + 14);
        }
        const u32x4 wv = wf[k & 7];
        if (k + 8 < 16) wf[k & 7] = ldf<TR>(lo, k + 8);
        else if (k + 8 < 32) wf[k & 7] = ldf<TR>(hi, k + 8 - 16);
        else if (PRE) wf[k & 7] = ldf<TR>(nxt, k + 8 - 32);
        acc = mfma32(wv, xf[k], acc);
        PIN(acc);
        s1[0] = dot2(xf[k].x, BF_ONES, s1[0]); s2[0] = dot2(xf[k].x, xf[k].x, s2[0]);
        s1[1] = dot2(xf[k].y, BF_ONES, s1[1]); s2[1] = dot2(xf[k].y, xf[k].y, s2[1]);
        s1[2] = dot2(xf[k].z, BF_ONES, s1[2]); s2[2] = dot2(xf[k].z, xf[k].z, s2[2]);
        s1[3] = dot2(xf[k].w, BF_ONES, s1[3]); s2[3] = dot2(xf[k].w, xf[k].w, s2[3]);
        asm volatile("" : "+v"(s1[0]), "+v"(s1[1]), "+v"(s1[2]), "+v"(s1[3]), "+v"(s2[0]), "+v"(s2[1]), "+v"(s2[2]), "+v"(s2[3]));
        if (MID && k == 3) BARL();
        SB();
    }
    const float a = xor32_sum((s1[0] + s1[1]) + (s1[2] + s1[3])), b = xor32_sum((s2[0] + s2[1]) + (s2[2] + s2[3]));
    const float mean = a * (1.0f / DM);
    float var = b * (1.0f / DM) - mean * mean;
    var = var > 0.f ? var : 0.f;
    f32x2 o; o.x = mean; o.y = __builtin_amdgcn_rsqf(var + LN_EPS);
    return o;
#undef XLD
#undef RLD
#undef STG
}

struct GS { float g0, g1, t0, t1, u0, u1; };
struct GK { float mean, A, B, ir2; };
__device__ __forceinline__ void geglu_sub(GS& s, u32& o, int q, float ag0, float ag1, float au0, float au1, float cg0, float cg1, float cu0, float cu1, const GK& k) {
    if (q == 0) asm volatile("v_fma_f32 %0, -%7, %5, %3\n\tv_fma_f32 %1, -%7, %6, %4\n\tv_mul_f32 %2, %0, %0"
                             : "=&v"(s.g0), "=&v"(s.g1), "=&v"(s.t0) : "v"(ag0), "v"(ag1), "v"(cg0), "v"(cg1), "v"(k.mean));
    if (q == 1) asm volatile("v_mul_f32 %1, %2, %2\n\tv_fma_f32 %0, %0, %4, %3\n\tv_fma_f32 %1, %1, %4, %3"
                             : "+v"(s.t0), "=&v"(s.t1) : "v"(s.g1), "v"(k.A), "v"(k.B));
    if (q == 2) asm volatile("v_mul_f32 %0, %2, %0\n\tv_mul_f32 %1, %3, %1\n\tv_exp_f32 %0, %0"
                             : "+v"(s.t0), "+v"(s.t1) : "v"(s.g0), "v"(s.g1));
    if (q == 3) asm volatile("v_exp_f32 %0, %0\n\tv_fma_f32 %1, -%7, %5, %3\n\tv_fma_f32 %2, -%7, %6, %4"
                             : "+v"(s.t1), "=&v"(s.u0), "=&v"(s.u1) : "v"(au0), "v"(au1), "v"(cu0), "v"(cu1), "v"(k.mean));
    if (q == 4) asm volatile("v_fma_f32 %0, %0, %4, %4\n\tv_mul_f32 %2, %2, %3\n\tv_fma_f32 %1, %1, %4, %4"
                             : "+v"(s.t0), "+v"(s.t1), "+v"(s.g0) : "v"(s.u0), "v"(k.ir2));
    if (q == 5) asm volatile("v_rcp_f32 %0, %0\n\tv_mul_f32 %2, %2, %3\n\tv_rcp_f32 %1, %1"
                             : "+v"(s.t0), "+v"(s.t1), "+v"(s.g1) : "v"(s.u1));
    if (q == 6) asm volatile("v_mul_f32 %1, %1, %3\n\tv_mul_f32 %2, %2, %4\n\tv_cvt_pk_bf16_f32 %0, %1, %2"
                             : "=&v"(o), "+v"(s.g0), "+v"(s.g1) : "v"(s.t0), "v"(s.t1));
}
struct LS { float t0, t1; };
__device__ __forceinline__ void lnq_a(LS& s, float a0, float c0, float c1, float sc, float msc) {
    asm volatile("v_mul_f32 %0, %6, %3\n\tv_mul_f32 %1, %6, %4\n\tv_fma_f32 %0, %2, %5, -%0"
                 : "=&v"(s.t0), "=&v"(s.t1) : "v"(a0), "v"(c0), "v"(c1), "v"(sc), "v"(msc));
}
__device__ __forceinline__ u32 lnq_b(LS& s, float a1, float sc) {
    u32 o;
    asm volatile("v_fma_f32 %1, %3, %4, -%1\n\tv_cvt_pk_bf16_f32 %0, %2, %1" : "=v"(o), "+v"(s.t1) : "v"(s.t0), "v"(a1), "v"(sc));
    return o;
}
__device__ __forceinline__ void lnv_a(LS& s, float a0, float a1, float m0, float m1, float r0, float cs) {
    asm volatile("v_fma_f32 %0, -%4, %7, %2\n\tv_fma_f32 %1, -%5, %7, %3\n\tv_mul_f32 %0, %0, %6"
                 : "=&v"(s.t0), "=&v"(s.t1) : "v"(a0), "v"(a1), "v"(m0), "v"(m1), "v"(r0), "v"(cs));
}
__device__ __forceinline__ u32 lnv_b(LS& s, float r1) {
    u32 o;
    asm volatile("v_mul_f32 %1, %1, %3\n\tv_cvt_pk_bf16_f32 %0, %2, %1" : "=v"(o), "+v"(s.t1) : "v"(s.t0), "v"(r1));
    return o;
}
__device__ __forceinline__ u32x4 pack8(const float* v) { return u32x4{pack2(v[0], v[1]), pack2(v[2], v[3]), pack2(v[4], v[5]), pack2(v[6], v[7])}; }

__device__ __forceinline__ float max16(const f32x16& s, float m) {
    float a = fmx(fmx(s[0], s[1]), m), b = fmx(s[2], s[3]);
    a = fmx(fmx(s[4], s[5]), a); b = fmx(fmx(s[6], s[7]), b);
    a = fmx(fmx(s[8], s[9]), a); b = fmx(fmx(s[10], s[11]), b);
    a = fmx(fmx(s[12], s[13]), a); b = fmx(fmx(s[14], s[15]), b);
    return fmx(a, b);
}
__device__ __forceinline__ float smul(float a, float b) { float r; asm("v_mul_f32 %0, %1, %2" : "=v"(r) : "v"(a), "v"(b)); return r; }
struct MaskF { u32x4 alo, ahi, ilo, ihi; };
__device__ __forceinline__ f32x16 add_mask(const MaskF& mf, f32x16 s) { s = mfma32(mf.alo, mf.ilo, s); return mfma32(mf.ahi, mf.ihi, s); }
template <int NB, int FIRST, int REST0, int MASK, bool FIRSTSEG>
__device__ __forceinline__ void att_segment(f32x16& oacc, float& m, const LAS unsigned char* kb, const LAS unsigned char* vb, const LAS unsigned char* mk, const u32x4& qf) {
    u32x4 kf[NB];
#pragma unroll
    for (int t = 0; t < NB; ++t) { const int b = t == 0 ? FIRST : REST0 + t - 1; kf[t] = *(const LAS u32x4*)(kb + b * 2048); }
    MaskF mf;
    if (MASK) {
        mf.alo = LDF(mk, 0); mf.ahi = LDF(mk, 1); mf.ilo = LDF(mk, 2); mf.ihi = LDF(mk, 3);
        if (MASK == 2) { mf.alo ^= 0xff7fff7fu; mf.ahi ^= 0xff7fff7fu; }
    }
    SB();
    float mx = FIRSTSEG ? -__builtin_inff() : m;
    f32x16 sv[4];
    sv[0] = mfma32(kf[0], qf, f32x16{});
    if (NB > 1) sv[1] = mfma32(kf[1], qf, f32x16{});
    if (MASK) sv[0] = add_mask(mf, sv[0]);
    SB();
#pragma unroll
    for (int t = 0; t < NB; ++t) {
        const int u = NB > 1 ? (t == 0 ? 1 : t == 1 ? 0 : t) : 0;
        if (t + 2 < NB) sv[(t + 2) & 3] = mfma32(kf[t + 2], qf, f32x16{});
        mx = max16(sv[u & 3], mx);
        SB();
    }
    mx = xor32_max(mx);
    f32x16 negm;
#pragma unroll
    for (int e = 0; e < 16; ++e) negm[e] = -mx;
    SB();
    sv[0] = mfma32(kf[0], qf, negm);
    if (MASK) sv[0] = add_mask(mf, sv[0]);
    u32x4 v0 = *(const LAS u32x4*)(vb + (FIRST * 2 + 0) * VREC);
    u32x4 v1 = *(const LAS u32x4*)(vb + (FIRST * 2 + 1) * VREC);
    if (NB > 1) sv[1] = mfma32(kf[1], qf, negm);
    if (!FIRSTSEG) {
        float f = __builtin_amdgcn_exp2f(m - mx);
        asm volatile("s_nop 1" : "+v"(f));
#pragma unroll
        for (int e = 0; e < 16; ++e) oacc[e] = smul(oacc[e], f);
    }
    m = mx;
    SB();
#pragma unroll
    for (int t = 0; t < NB; ++t) {
        u32x4 n0, n1;
        if (t + 1 < NB) {
            if (t > 0) sv[(t + 1) & 1] = mfma32(kf[t + 1], qf, negm);
            n0 = *(const LAS u32x4*)(vb + ((REST0 + t) * 2 + 0) * VREC);
            n1 = *(const LAS u32x4*)(vb + ((REST0 + t) * 2 + 1) * VREC);
        }
        SB();
        const f32x16& s = sv[t & 1];
        u32 p[8];
#pragma unroll
        for (int e = 0; e < 4; ++e) p[e] = pack2(__builtin_amdgcn_exp2f(s[2 * e]), __builtin_amdgcn_exp2f(s[2 * e + 1]));
        SB();
        oacc = mfma32(v0, u32x4{p[0], p[1], p[2], p[3]}, oacc);
#pragma unroll
        for (int e = 4; e < 8; ++e) p[e] = pack2(__builtin_amdgcn_exp2f(s[2 * e]), __builtin_amdgcn_exp2f(s[2 * e + 1]));
        SB();
        oacc = mfma32(v1, u32x4{p[4], p[5], p[6], p[7]}, oacc);
        if (t + 1 < NB) { v0 = n0; v1 = n1; }
        SB();
    }
}

__device__ __forceinline__ void issue_qkv_tile(const Ctx& c, const Args& a, int t) {
    const u32 voff = (u32)(c.lane >> 2) * 3072u + (u32)(c.lane & 3) * 16u;
    const unsigned char* src = (const unsigned char*)(a.wqkv + qkv_col(t) + 32 * c.j) + (size_t)(c.w & 3) * 8 * 16 * 3072;
    const u32 dst = c.Laddr + slot_of(CQ(t, 0)) * SLOT + (c.w & 3) * 8192;
#pragma unroll
    for (int q = 0; q < 8; ++q) glds1k(src + (size_t)q * 16 * 3072, voff, dst + q * 1024);
}
template <bool TR> __device__ __forceinline__ void tile_colsum(const Ctx& c, const LAS unsigned char* lo, const LAS unsigned char* hi, int csidx) {
    float s[4] = {0.f, 0.f, 0.f, 0.f};
#pragma unroll
    for (int hf = 0; hf < 2; ++hf) {
        u32x4 f[16];
#pragma unroll
        for (int k = 0; k < 16; ++k) f[k] = ldf<TR>(hf ? hi : lo, k);
        SB();
#pragma unroll
        for (int k = 0; k < 16; ++k) {
            s[0] = dot2(f[k].x, BF_ONES, s[0]); s[1] = dot2(f[k].y, BF_ONES, s[1]); s[2] = dot2(f[k].z, BF_ONES, s[2]); s[3] = dot2(f[k].w, BF_ONES, s[3]);
        }
        SB();
    }
    const float t = xor32_sum((s[0] + s[1]) + (s[2] + s[3]));
    if (c.lane < 32) lds_st<float>(c.L, SC_CS + (csidx + c.lane) * 4, t);
}
__device__ __forceinline__ int tr_lane_off(int lane) {
    const int g4 = lane >> 4, h = g4 >> 1, nh = g4 & 1, li = lane & 15;
    return (8 * h + (li >> 2)) * 64 + nh * 32 + (li & 3) * 8;
}
__device__ __forceinline__ const LAS unsigned char* lds_base_raw(const Ctx& c, int off) {
    u32 a = c.Laddr + off;
    asm volatile("" : "+v"(a));
    return (const LAS unsigned char*)(size_t)a;
}

__device__ __forceinline__ void phase_qkv_att(const Ctx& c0, int layer, const Args& a) {
    const Ctx c = fresh(c0);
    const int lane = c.lane, w = c.w, tb = w & 3, h = lane >> 5, r = lane & 31;
    const u32 lbase = (u32)(WS_L0 + (size_t)layer * WS_LSTRIDE);
    STAMP(0);
    if (w < 4) {
        auto ready = [&]() {
            if (layer > 0 && w == 0) poll_flags<64>(c, layer - 1, 0, 16);
            BAR();
            STAMP(1);
        };
        u32x4 xf[32], wf[8];
        const LAS unsigned char* wK = lds_base(c, slot_of(CQ(0, 0)) * SLOT);
        const LAS unsigned char* wV = lds_base(c, slot_of(CQ(1, 0)) * SLOT);
        const LAS unsigned char* wQ = lds_base(c, slot_of(CQ(2, 0)) * SLOT);
        f32x16 accK = {}, accV = {}, accQ = {};
        const f32x2 ms = c_tile0<true, false, false, true>(c, accK, wf, wK, wK + SLOT, wV, xf, c.rs,
                                                            (layer == 0 ? (u32)WS_XF0 : (u32)(WS_L0 + L_X2)) + (u32)((4 * c.i + tb) * 32) * 1024, ready);
        if (lane < 32) lds_st<f32x2>(c.L, SC_ST + (tb * 32 + r) * 8, ms);
        const float msk = ms.x * ms.y;
        BARL(); STAMP(2);
        f32x4 cs[4];
#pragma unroll
        for (int q = 0; q < 4; ++q) cs[q] = lds_ld<f32x4>(c.L, SC_CS + (0 * 32 + 8 * q + 4 * h) * 4);
        f32x4 mr[8];
        float csv;
        {
            u32 o[8]; LS ls;
            c_tile<true, true, false>(accV, wf, wV, wV + SLOT, wQ, xf, [&](int k) {
                if (k >= 4 && k < 20) {
                    const int pi = (k - 4) >> 1, e = 2 * pi;
                    if (((k - 4) & 1) == 0) lnq_a(ls, accK[e], cs[e >> 2][e & 3], cs[e >> 2][(e & 3) + 1], ms.y, msk);
                    else o[pi] = lnq_b(ls, accK[e + 1], ms.y);
                }
                if (k == 20 || k == 21) {
                    const int hh = k - 20;
                    const u32x4 ov = {o[4 * hh], o[4 * hh + 1], o[4 * hh + 2], o[4 * hh + 3]};
                    lds_st<u32x4>(c.L, SC_KS + (tb * 2 + hh) * 1024 + c.voff, ov);
                    st_sc1(c, lbase + (u32)L_KF + (u32)((4 * c.i + tb) * 32 + 2 * c.j + hh) * 1024, ov);
                }
                if (k >= 22 && k < 30) { const int q = (k - 22) >> 1; mr[k - 22] = lds_ld<f32x4>(c.L, SC_ST + (tb * 32 + 8 * q + 4 * h + 2 * ((k - 22) & 1)) * 8); }
                if (k == 30) csv = lds_ld<float>(c.L, SC_CS + (32 + r) * 4);
            });
        }
        BARL();
        {
            u32 o[8]; LS ls;
            c_tile<false, false, false>(accQ, wf, wQ, wQ + SLOT, wQ, xf, [&](int k) {
                if (k >= 2 && k < 18) {
                    const int pi = (k - 2) >> 1;
                    if (((k - 2) & 1) == 0) lnv_a(ls, accV[2 * pi], accV[2 * pi + 1], mr[pi].x, mr[pi].z, mr[pi].y, csv);
                    else o[pi] = lnv_b(ls, mr[pi].w);
                }
                if (k == 18 || k == 20) {
                    const int sv = (k - 18) >> 1;
                    const u32x4 ov = {o[4 * sv], o[4 * sv + 1], o[4 * sv + 2], o[4 * sv + 3]};
                    lds_st<u32x4>(c.L, SC_VS + (tb * 2 + sv) * VREC + c.voff, ov);
                    st_sc1(c, lbase + (u32)L_VF + (u32)(((4 * c.i + tb) * 16 + c.j) * 2 + sv) * 1024, ov);
                }
                if (k >= 24 && k < 28) cs[k - 24] = lds_ld<f32x4>(c.L, SC_CS + (64 + 8 * (k - 24) + 4 * h) * 4);
            });
            if (lane < 2) { u32x4 ones = {BF_ONES, BF_ONES, BF_ONES, BF_ONES}; lds_st<u32x4>(c.L, SC_VS + (tb * 2 + lane) * VREC + 1024, ones); }
        }
        {
            const float sc = ms.y * QSCALE, msc = ms.x * sc;
            asm volatile("s_nop 15" : "+v"(accQ));
            u32 o[8]; LS ls[8];
#pragma unroll
            for (int pi = 0; pi < 8; ++pi) { const int e = 2 * pi; lnq_a(ls[pi], accQ[e], cs[e >> 2][e & 3], cs[e >> 2][(e & 3) + 1], sc, msc); }
#pragma unroll
            for (int pi = 0; pi < 8; ++pi) o[pi] = lnq_b(ls[pi], accQ[2 * pi + 1], sc);
#pragma unroll
            for (int hh = 0; hh < 2; ++hh) lds_st<u32x4>(c.L, SC_QS + (tb * 2 + hh) * 1024 + c.voff, u32x4{o[4 * hh], o[4 * hh + 1], o[4 * hh + 2], o[4 * hh + 3]});
        }
        WAIT_VM(0);
        BAR(); STAMP(5);
        if (w == 0 && lane < 2 && c.i + 1 + lane < 16) flag_store(c, layer, 1, c.i + 1 + lane, c.j, lane);
    } else {
        WAIT_VM(16);
        BAR();
        WAIT_VM(8);
        BARL();
        const int cb = c.j & 7, sidx = c.i * 2 + (c.j >> 3);
        if (layer == 0) {
            const int hw = w & 3;
            LAS unsigned char* t = c.L + SC_VS + hw * 1024;
            WItem it[6]; u32x4 v[6];
#pragma unroll
            for (int q = 0; q < 6; ++q) {
                int f;
                const int fD = NF_WO + NF_WG + cb * 224 + sidx * 7, fO = cb * 64 + sidx * 2;
                if (q < 2 || (q < 4 && hw < 3)) { const int ks = 2 * c.i + (q & 1), cc = hw + 4 * (q >> 1); f = NF_WO + c.j * 224 + (2 * cc + (ks >> 4)) * 16 + (ks & 15); }
                else if (q < 4) f = fD + 4 + (q & 1);
                else if (q == 4) f = fD + (hw < 3 ? hw : 6);
                else f = hw < 2 ? fO + hw : fD + 3;
                it[q] = w_item(a, f, lane);
                v[q] = *(const u32x4*)it[q].src;
            }
            const int k = lane >> 2, n8 = lane & 3;
            const int g4 = lane >> 4, hh = g4 >> 1, nh = g4 & 1, li = lane & 15, qq = li >> 2, p = li & 3;
            float part = 0.f;
#pragma unroll
            for (int q = 0; q < 6; ++q) {
                if (q < 5 || hw < 3) {
                    lds_st<u32x4>(t, k * 64 + n8 * 16, v[q]);
                    asm volatile("s_waitcnt lgkmcnt(0)" ::: "memory");
                    const s16x4 lo = __builtin_amdgcn_ds_read_tr16_b64_v4i16((LAS s16x4*)(t + (4 * hh + qq) * 64 + nh * 32 + p * 8));
                    const s16x4 hi = __builtin_amdgcn_ds_read_tr16_b64_v4i16((LAS s16x4*)(t + (8 + 4 * hh + qq) * 64 + nh * 32 + p * 8));
                    const u32x2 l2 = __builtin_bit_cast(u32x2, lo), h2 = __builtin_bit_cast(u32x2, hi);
                    __builtin_amdgcn_raw_buffer_store_b128(u32x4{l2.x, l2.y, h2.x, h2.y}, c.rs, c.voff, (u32)it[q].dst, 16);
                    if (q < 2 || (q < 4 && hw < 3)) {
                        if ((q & 1) == 0) part = 0.f;
                        part = dot2(l2.x, BF_ONES, part); part = dot2(l2.y, BF_ONES, part); part = dot2(h2.x, BF_ONES, part); part = dot2(h2.y, BF_ONES, part);
                        if (q & 1) {
                            const float tot = xor32_sum(part);
                            const int cc = hw + 4 * (q >> 1);
                            if (lane < 32) __builtin_amdgcn_raw_buffer_store_b32(__builtin_bit_cast(u32, tot), c.rs, (u32)lane * 4u, (u32)WS_CSP + (u32)((c.j * 16 + c.i) * 224 + cc * 32) * 4u, 16);
                        }
                    }
                    asm volatile("s_waitcnt lgkmcnt(0)" ::: "memory");
                }
            }
        }
        WAIT_VM(0);
        BAR();
        if (layer == 0 && w == 4) {
            if (lane < 16) flag_store(c, 0, 5, lane, c.j, c.i);
            if (lane < 32) flag_store(c, 0, 6, lane >> 1, cb + 8 * (lane & 1), sidx);
        }
        BAR();
        if (layer == 0) poll_flags<64>(c, 0, 6, 32);
        issue_chunk<CO(0)>(c); issue_chunk<CO(1)>(c); issue_chunk<CO(2)>(c); issue_chunk<CO(3)>(c);
        BAR();
        if (layer == 0) {
            poll_flags<64>(c, 0, 5, 16);
            const int col = (w & 3) * 64 + lane;
            if (col < 224) {
                float t = 0.f;
#pragma unroll
                for (int i2 = 0; i2 < 16; ++i2) t += __builtin_bit_cast(float, __builtin_amdgcn_raw_buffer_load_b32(c.rs, (u32)col * 4u, (u32)WS_CSP + (u32)((c.j * 16 + i2) * 224) * 4u, 16));
                lds_st<float>(c.L, SC_CS + (96 + col) * 4, t);
            }
        }
        issue_chunk<CG(0, 0)>(c); issue_chunk<CG(0, 1)>(c);
    }
    const int qb = w < 4 ? w : 7 - w, hh = w >> 2;
    const u32x4 qf = lds_ld<u32x4>(c.L, SC_QS + (qb * 2 + hh) * 1024 + c.voff);
    const int vlane = (((lane >> 4) & 1) == hh) ? lane * 16 : 1024;
    f32x16 oacc = {};
    float m = 0.f;
    const LAS unsigned char* mk = lds_base(c, SC_MK);
    {
        const LAS unsigned char* kb = c.L + SC_KS + hh * 1024 + c.voff;
        const LAS unsigned char* vb = c.L + SC_VS + vlane;
        if (qb == 0) att_segment<1, 0, 0, 1, true>(oacc, m, kb, vb, mk, qf);
        else if (qb == 1) att_segment<2, 1, 0, 1, true>(oacc, m, kb, vb, mk, qf);
        else if (qb == 2) att_segment<3, 2, 0, 1, true>(oacc, m, kb, vb, mk, qf);
        else att_segment<4, 3, 0, 1, true>(oacc, m, kb, vb, mk, qf);
    }
    STAMP(6);
    if (c.i > 0) {
        if (w == 0) poll_flags<64>(c, layer, 1, c.i >= 2 ? 2 : 1);
        BAR();
        STAMP(7);
        {
            const int idx = w;
            const int g = 4 * c.i - 8 + idx;
            if (g >= 0) {
                const u32x4 s0 = ld_sc1(c, lbase + (u32)L_KF + (u32)(g * 32 + 2 * c.j + 0) * 1024);
                const u32x4 s1 = ld_sc1(c, lbase + (u32)L_KF + (u32)(g * 32 + 2 * c.j + 1) * 1024);
                const u32x4 s2 = ld_sc1(c, lbase + (u32)L_VF + (u32)((g * 16 + c.j) * 2 + 0) * 1024);
                const u32x4 s3 = ld_sc1(c, lbase + (u32)L_VF + (u32)((g * 16 + c.j) * 2 + 1) * 1024);
                lds_st<u32x4>(c.L, SC_RK + (idx * 2 + 0) * 1024 + c.voff, s0);
                lds_st<u32x4>(c.L, SC_RK + (idx * 2 + 1) * 1024 + c.voff, s1);
                lds_st<u32x4>(c.L, SC_RV + (idx * 2 + 0) * VREC + c.voff, s2);
                lds_st<u32x4>(c.L, SC_RV + (idx * 2 + 1) * VREC + c.voff, s3);
                if (lane < 2) { u32x4 ones = {BF_ONES, BF_ONES, BF_ONES, BF_ONES}; lds_st<u32x4>(c.L, SC_RV + (idx * 2 + lane) * VREC + 1024, ones); }
            }
        }
        BAR();
        STAMP(8);
        const LAS unsigned char* kb = c.L + SC_RK + hh * 1024 + c.voff;
        const LAS unsigned char* vb = c.L + SC_RV + vlane;
        if (c.i == 1) att_segment<4, 4, 5, 0, false>(oacc, m, kb, vb, mk, qf);
        else if (qb == 0) att_segment<8, 0, 1, 2, false>(oacc, m, kb, vb, mk, qf);
        else if (qb == 1) att_segment<7, 1, 2, 2, false>(oacc, m, kb, vb, mk, qf);
        else if (qb == 2) att_segment<6, 2, 3, 2, false>(oacc, m, kb, vb, mk, qf);
        else att_segment<5, 3, 4, 2, false>(oacc, m, kb, vb, mk, qf);
    }
    {
        u32x4 o;
        if (hh == 0) {
            float il = __builtin_amdgcn_rcpf(oacc[8]);
            asm volatile("s_nop 1" : "+v"(il));
            o = u32x4{pack2(smul(oacc[0], il), smul(oacc[1], il)), pack2(smul(oacc[2], il), smul(oacc[3], il)), pack2(smul(oacc[4], il), smul(oacc[5], il)), pack2(smul(oacc[6], il), smul(oacc[7], il))};
        } else {
            float il = __builtin_amdgcn_rcpf(oacc[0]);
            asm volatile("s_nop 1" : "+v"(il));
            o = u32x4{pack2(smul(oacc[8], il), smul(oacc[9], il)), pack2(smul(oacc[10], il), smul(oacc[11], il)), pack2(smul(oacc[12], il), smul(oacc[13], il)), pack2(smul(oacc[14], il), smul(oacc[15], il))};
        }
        st_sc1(c, lbase + (u32)L_OF + (u32)((4 * c.i + qb) * 32 + 2 * c.j + hh) * 1024, o);
    }
    STAMP(9);
    WAIT_VM(0);
    BAR();
    if (w == 0) signal_group(c, layer, 2);
    if (w >= 4) { issue_chunk<CG(1, 0)>(c); issue_chunk<CG(1, 1)>(c); }
    STAMP(10);
}

__device__ __forceinline__ void red_write(const Ctx& c, const f32x16& acc0, const f32x16& acc1, int t2, int kq) {
#pragma unroll
    for (int d = 0; d < 4; ++d) {
        const int fin = d & 1;
        if (kq != fin) {
            const int slot = kq < fin ? kq : kq - 1;
            const int base = red_off(t2 * 4 + d) + slot * 2048 + c.voff;
            const f32x16& a = d < 2 ? acc0 : acc1;
            const int o = (d & 1) * 8;
            lds_st<f32x4>(c.L, base, f32x4{a[o], a[o + 1], a[o + 2], a[o + 3]});
            lds_st<f32x4>(c.L, base + 1024, f32x4{a[o + 4], a[o + 5], a[o + 6], a[o + 7]});
        }
    }
}
__device__ __forceinline__ void red_read(const Ctx& c, const f32x16& acc0, const f32x16& acc1, int t2, int kq, float (&v)[2][8]) {
#pragma unroll
    for (int e = 0; e < 8; ++e) { v[0][e] = kq ? acc0[8 + e] : acc0[e]; v[1][e] = kq ? acc1[8 + e] : acc1[e]; }
#pragma unroll
    for (int x = 0; x < 2; ++x)
#pragma unroll
        for (int s = 0; s < 3; ++s) {
            const int base = red_off(t2 * 4 + 2 * x + kq) + s * 2048 + c.voff;
            const f32x4 lo = lds_ld<f32x4>(c.L, base), hi = lds_ld<f32x4>(c.L, base + 1024);
            v[x][0] += lo.x; v[x][1] += lo.y; v[x][2] += lo.z; v[x][3] += lo.w; v[x][4] += hi.x; v[x][5] += hi.y; v[x][6] += hi.z; v[x][7] += hi.w;
        }
}
__device__ __forceinline__ u32x4 add_res(const float (&v)[8], const u32x4& res) {
    return u32x4{pack2(v[0] + bflo(res.x), v[1] + bfhi(res.x)), pack2(v[2] + bflo(res.y), v[3] + bfhi(res.y)),
                 pack2(v[4] + bflo(res.z), v[5] + bfhi(res.z)), pack2(v[6] + bflo(res.w), v[7] + bfhi(res.w))};
}

__device__ __forceinline__ void phase_wo(const Ctx& c0, int layer, const Args& a) {
    const Ctx c = fresh(c0);
    const int w = c.w, t2 = w & 1, kq = w >> 1;
    const int th = c.j >> 3, cb = c.j & 7;
    const int tbk = 4 * c.i + 2 * th + t2;
    const u32 lbase = (u32)(WS_L0 + (size_t)layer * WS_LSTRIDE);
    if (w == 0) poll_flags<64>(c, layer, 2, 16);
    if (w >= 4) WAIT_VM(8);
    BAR();
    STAMP(11);
    u32x4 xf[8];
#pragma unroll
    for (int s = 0; s < 8; ++s) xf[s] = ld_sc1(c, lbase + (u32)L_OF + (u32)(tbk * 32 + 8 * kq + s) * 1024);
    u32x4 res[2];
    if (w < 4) {
#pragma unroll
        for (int x = 0; x < 2; ++x) {
            const int kbo = 4 * cb + 2 * x + kq;
            res[x] = ld_sc1(c, (layer == 0 ? (u32)WS_XF0 : (u32)(WS_L0 + L_X2)) + (u32)(tbk * 32 + kbo) * 1024);
        }
    }
    u32x4 wf[16];
#pragma unroll
    for (int ch = 0; ch < 4; ++ch) {
        const LAS unsigned char* wb = lds_base(c, slot_of(CO(ch)) * SLOT + kq * 4096);
#pragma unroll
        for (int q = 0; q < 4; ++q) wf[4 * ch + q] = LDF(wb, q);
    }
    SB();
    f32x16 acc0 = {}, acc1 = {};
#pragma unroll
    for (int s = 0; s < 8; ++s) {
        acc0 = mfma32(wf[2 * s], xf[s], acc0);
        acc1 = mfma32(wf[2 * s + 1], xf[s], acc1);
        SB();
    }
    BAR();
    STAMP(12);
    red_write(c, acc0, acc1, t2, kq);
    BAR();
    if (w < 4) {
        float v[2][8];
        red_read(c, acc0, acc1, t2, kq, v);
#pragma unroll
        for (int x = 0; x < 2; ++x) st_sc1(c, lbase + (u32)L_X1 + (u32)(tbk * 32 + 4 * cb + 2 * x + kq) * 1024, add_res(v[x], res[x]));
        STAMP(13);
        WAIT_VM(0);
    }
    BAR();
    if (w == 0) signal_group(c, layer, 3);
    STAMP(14);
}

template <int T> __device__ __forceinline__ void gu_tile(const Ctx& c, f32x16& acc, const f32x16& prev, u32x4 (&wf)[8], const u32x4 (&xf)[32], int h,
                                                         f32x4 (&cs)[4], const GK& gk, u32 hbase) {
    const LAS unsigned char* cur = lds_base(c, slot_of(CG(T, 0)) * SLOT);
    const LAS unsigned char* nxt = lds_base(c, slot_of(CG(T < 6 ? T + 1 : T, 0)) * SLOT);
    GS g;
    u32 o[4];
    acc = f32x16{};
    SB();
    c_tile<false, (T < 6), false>(acc, wf, cur, cur + SLOT, nxt, xf, [&](int k) {
        const int p = k >> 3, e = 2 * p, q2 = p >> 1, e2 = e & 3;
        if ((k & 7) < 7) geglu_sub(g, o[p], k & 7, prev[e], prev[e + 1], prev[8 + e], prev[8 + e + 1], cs[q2][e2], cs[q2][e2 + 1], cs[2 + q2][e2], cs[2 + q2][e2 + 1], gk);
        if (k == 15) cs[0] = lds_ld<f32x4>(c.L, SC_CS + (96 + T * 32 + 4 * h) * 4);
        if (k == 15) cs[2] = lds_ld<f32x4>(c.L, SC_CS + (96 + T * 32 + 16 + 4 * h) * 4);
        if (k == 31) cs[1] = lds_ld<f32x4>(c.L, SC_CS + (96 + T * 32 + 8 + 4 * h) * 4);
        if (k == 31) cs[3] = lds_ld<f32x4>(c.L, SC_CS + (96 + T * 32 + 16 + 8 + 4 * h) * 4);
    });
    st_sc1(c, hbase + (u32)(T - 1) * 1024, u32x4{o[0], o[1], o[2], o[3]});
}

__device__ __forceinline__ u32 gu_compute(const Ctx& c0, int layer) {
    const Ctx c = fresh(c0);
    const int lane = c.lane, w = c.w, tb = w & 3, h = lane >> 5;
    const u32 lbase = (u32)(WS_L0 + (size_t)layer * WS_LSTRIDE);
    u32 pf1 = 0;
    {
        u32x4 xf[32], wf[8];
        const LAS unsigned char* w0 = lds_base(c, slot_of(CG(0, 0)) * SLOT);
        const LAS unsigned char* w1 = lds_base(c, slot_of(CG(1, 0)) * SLOT);
        f32x16 a0 = {}, a1;
        const f32x2 ms = c_tile0<true, false, false, false>(c, a0, wf, w0, w0 + SLOT, w1, xf, c.rs, lbase + (u32)L_X1 + (u32)((4 * c.i + tb) * 32) * 1024, [&]() {
            if (w == 0) poll_flags<64>(c, layer, 3, 16);
            BAR();
            STAMP(15);
        });
        constexpr float GC1 = -2.0f * 0.7978845608028654f * 1.4426950408889634f, GC2 = GC1 * 0.044715f;
        GK gk; gk.mean = ms.x; gk.A = ms.y * GC1; gk.B = ms.y * ms.y * ms.y * GC2; gk.ir2 = 1.0f / (ms.y * ms.y);
        BARL(); STAMP(16);
        f32x4 cs[4];
#pragma unroll
        for (int q = 0; q < 4; ++q) cs[q] = lds_ld<f32x4>(c.L, SC_CS + (96 + (q >> 1) * 16 + 8 * (q & 1) + 4 * h) * 4);
        const u32 hbase = lbase + (u32)L_HF + (u32)((4 * c.i + tb) * 112 + 7 * c.j) * 1024;
        gu_tile<1>(c, a1, a0, wf, xf, h, cs, gk, hbase); BARL();
        gu_tile<2>(c, a0, a1, wf, xf, h, cs, gk, hbase); BARL();
        gu_tile<3>(c, a1, a0, wf, xf, h, cs, gk, hbase); BARL();
        gu_tile<4>(c, a0, a1, wf, xf, h, cs, gk, hbase); BARL();
        gu_tile<5>(c, a1, a0, wf, xf, h, cs, gk, hbase);
        WAIT_VM(1);
        BARL();
        if (w == 0) signal_group(c, layer, 7);
        gu_tile<6>(c, a0, a1, wf, xf, h, cs, gk, hbase);
        STAMP(22);
        if (w == 0) pf1 = __builtin_amdgcn_raw_buffer_load_b32(c.rs, flag_off(layer, 7, c.i, c.j) + (lane & 15) * 4, 0, 16);
        {
            asm volatile("s_nop 15" : "+v"(a0));
            GS g[4];
            u32 o[4];
#pragma unroll
            for (int q = 0; q < 7; ++q)
#pragma unroll
                for (int p = 0; p < 4; ++p) {
                    const int e = 2 * p, q2 = p >> 1, e2 = e & 3;
                    geglu_sub(g[p], o[p], q, a0[e], a0[e + 1], a0[8 + e], a0[8 + e + 1], cs[q2][e2], cs[q2][e2 + 1], cs[2 + q2][e2], cs[2 + q2][e2 + 1], gk);
                }
            st_sc1(c, hbase + 6u * 1024, u32x4{o[0], o[1], o[2], o[3]});
        }
        STAMP(21);
        WAIT_VM(0);
        BAR();
        STAMP(23);
        if (w == 0) signal_group(c, layer, 4);
    }
    return pf1;
}
__device__ __forceinline__ void gu_helper(const Ctx& c0, int layer, u32x4 (&wr)[40]) {
    const Ctx c = fresh(c0);
    const int lane = c.lane, w = c.w;
    {
        WAIT_VM(0);
        BAR();
        __builtin_amdgcn_s_sleep(16);
        issue_chunk<CG(2, 0)>(c); issue_chunk<CG(2, 1)>(c); issue_chunk<CG(3, 0)>(c); issue_chunk<CG(3, 1)>(c);
        WAIT_VM(8);
        BAR();
        const u32 wdb = (u32)WS_WD + (u32)((c.j & 7) * 224 + 112 + ((w >> 1) - 2) * 56) * 1024;
#pragma unroll
        for (int q = 0; q < 20; ++q) wr[q] = __builtin_amdgcn_raw_buffer_load_b128(c.rs, c.voff, wdb + q * 1024, 16);
        issue_chunk<CG(4, 0)>(c); issue_chunk<CG(4, 1)>(c);
        WAIT_VM(8);
        BAR();
#pragma unroll
        for (int q = 20; q < 40; ++q) wr[q] = __builtin_amdgcn_raw_buffer_load_b128(c.rs, c.voff, wdb + q * 1024, 16);
        issue_chunk<CG(5, 0)>(c); issue_chunk<CG(5, 1)>(c);
        WAIT_VM(8);
        BAR();
        issue_chunk<CG(6, 0)>(c); issue_chunk<CG(6, 1)>(c);
        WAIT_VM(8);
        BAR();
        issue_chunk<CD(0)>(c); issue_chunk<CD(1)>(c);
        WAIT_VM(8);
        BAR();
        issue_chunk<CD(2)>(c); issue_chunk<CD(3)>(c);
        BAR();
        issue_chunk<CD(4)>(c); issue_chunk<CD(5)>(c);
        WAIT_VM(0);
        BAR();
        issue_chunk<CD(6)>(c);
    }
}

__device__ __forceinline__ void hld(const Ctx& c, u32x4& dst, u32 soff) {
    asm volatile("buffer_load_dwordx4 %0, %1, %2, %3 offen sc1" : "=v"(dst) : "v"(c.voff), "s"(c.rs), "s"(soff) : "memory");
}
template <int N> __device__ __forceinline__ void hwait(u32x4& x) { asm volatile("s_waitcnt vmcnt(%1)" : "+v"(x) : "i"(N) : "memory"); }
template <bool HELPER>
__device__ __forceinline__ void dn_main(const Ctx& c, int layer, int kq, u32 xsrc, u32 wdb, const u32x4* wr, f32x16& acc0, f32x16& acc1) {
    constexpr int XD = 10;
    u32x4 xf[28], wf[8], wl[16];
    const LAS unsigned char* wb[7];
#pragma unroll
    for (int s = 0; s < XD; ++s) hld(c, xf[s], xsrc + (u32)dn_kstep(s) * 1024);
    if (!HELPER) {
#pragma unroll
        for (int ch = 0; ch < 7; ++ch) wb[ch] = lds_base(c, chunk_lds(CD(ch)) + kq * 8192);
#pragma unroll
        for (int q = 0; q < 8; ++q) wf[q] = LDF(wb[0], q);
    }
    SB();
    u32 pf = 0;
#pragma unroll
    for (int s = 0; s < 28; ++s) {
        if (s + XD < 28) hld(c, xf[s + XD], xsrc + (u32)dn_kstep(s + XD) * 1024);
        u32x4 w0, w1;
        if (HELPER) {
            if (s < 20) { w0 = wr[2 * s]; w1 = wr[2 * s + 1]; } else { w0 = wl[2 * (s - 20)]; w1 = wl[2 * (s - 20) + 1]; }
        } else {
            w0 = wf[(2 * s) & 7]; w1 = wf[(2 * s + 1) & 7];
            if (s + 4 < 28) { wf[(2 * s) & 7] = LDF(wb[(s + 4) >> 2], 2 * ((s + 4) & 3)); wf[(2 * s + 1) & 7] = LDF(wb[(s + 4) >> 2], 2 * ((s + 4) & 3) + 1); }
        }
        if (s + XD < 28) { if (HELPER && s >= 8 && s < 12 + XD - 10 + 10 && s <= 21) hwait<10>(xf[s]); else hwait<10>(xf[s]); }
        else if (s == 18) hwait<9>(xf[s]); else if (s == 19) hwait<8>(xf[s]); else if (s == 20) hwait<7>(xf[s]); else if (s == 21) hwait<6>(xf[s]); else if (s == 22) hwait<5>(xf[s]);
        else if (s == 23) hwait<4>(xf[s]); else if (s == 24) hwait<3>(xf[s]); else if (s == 25) hwait<2>(xf[s]); else if (s == 26) hwait<1>(xf[s]); else hwait<0>(xf[s]);
        if (HELPER && s == 20) {
            asm volatile("s_waitcnt vmcnt(6)" : "+v"(wl[0]), "+v"(wl[1]), "+v"(wl[2]), "+v"(wl[3]), "+v"(wl[4]), "+v"(wl[5]), "+v"(wl[6]), "+v"(wl[7]) :: "memory");
            asm volatile("" : "+v"(wl[8]), "+v"(wl[9]), "+v"(wl[10]), "+v"(wl[11]), "+v"(wl[12]), "+v"(wl[13]), "+v"(wl[14]), "+v"(wl[15]));
            w0 = wl[0]; w1 = wl[1];
        }
        acc0 = mfma32(w0, xf[s], acc0);
        acc1 = mfma32(w1, xf[s], acc1);
        PIN(acc0); PIN(acc1);
        if (HELPER && s >= 8 && s < 12) {
#pragma unroll
            for (int q = 0; q < 4; ++q) hld(c, wl[4 * (s - 8) + q], wdb + (u32)(40 + 4 * (s - 8) + q) * 1024);
        }
        if (!HELPER && s == 2 && c.w == 0)
            asm volatile("buffer_load_dword %0, %1, %2, 0 offen sc1" : "=v"(pf) : "v"(flag_off(layer, 4, c.i, c.j) + (c.lane & 15) * 4), "s"(c.rs) : "memory");
        if (s + XD == 15) {
            if (!HELPER && c.w == 0) {
                asm volatile("s_waitcnt vmcnt(3)" : "+v"(pf) :: "memory");
                if (!__all(pf == flag_tag(c, layer, 4))) poll_flags<64>(c, layer, 4, 16);
            }
            BARL();
        }
        SB();
    }
}

__device__ __forceinline__ void phase_ffn(const Ctx& c0, int layer, const Args& a) {
    f32x16 acc0 = {}, acc1 = {};
    u32x4 res[2];
    if (c0.w < 4) {
        const u32 pf1 = gu_compute(c0, layer);
        const Ctx c = fresh(c0);
        const int w = c.w, t2 = w & 1, kq = w >> 1, th = c.j >> 3, cb = c.j & 7;
        const int tbk = 4 * c.i + 2 * th + t2;
        const u32 lbase = (u32)(WS_L0 + (size_t)layer * WS_LSTRIDE);
        res[0] = ld_sc1(c, lbase + (u32)L_X1 + (u32)(tbk * 32 + 4 * cb + kq) * 1024);
        res[1] = ld_sc1(c, lbase + (u32)L_X1 + (u32)(tbk * 32 + 4 * cb + 2 + kq) * 1024);
        if (w == 0 && !__all(pf1 == flag_tag(c, layer, 7))) poll_flags<64>(c, layer, 7, 16);
        BARL();
        STAMP(25);
        dn_main<false>(c, layer, kq, lbase + (u32)L_HF + (u32)(tbk * 112 + 28 * kq) * 1024, 0u, nullptr, acc0, acc1);
    } else {
        u32x4 wr[40];
        gu_helper(c0, layer, wr);
        const Ctx c = fresh(c0);
        const int w = c.w, t2 = w & 1, kq = w >> 1, th = c.j >> 3, cb = c.j & 7;
        const int tbk = 4 * c.i + 2 * th + t2;
        const u32 lbase = (u32)(WS_L0 + (size_t)layer * WS_LSTRIDE);
        BARL();
        dn_main<true>(c, layer, kq, lbase + (u32)L_HF + (u32)(tbk * 112 + 28 * kq) * 1024, (u32)WS_WD + (u32)(cb * 224 + 112 + (kq - 2) * 56) * 1024, wr, acc0, acc1);
    }
    const Ctx c = fresh(c0);
    const int lane = c.lane, w = c.w, t2 = w & 1, kq = w >> 1, h = lane >> 5, r = lane & 31;
    const int th = c.j >> 3, cb = c.j & 7;
    const int tbk = 4 * c.i + 2 * th + t2;
    const u32 lbase = (u32)(WS_L0 + (size_t)layer * WS_LSTRIDE);
    BAR();
    STAMP(26);
    red_write(c, acc0, acc1, t2, kq);
    BAR();
    if (w >= 4) {
        if (layer == 0) { issue_chunk<CQ(0, 0)>(c); issue_chunk<CQ(0, 1)>(c); }
    } else {
        float v[2][8];
        red_read(c, acc0, acc1, t2, kq, v);
#pragma unroll
        for (int x = 0; x < 2; ++x) {
            const int kbo = 4 * cb + 2 * x + kq;
            const u32x4 o = add_res(v[x], res[x]);
            const auto r0 = __builtin_amdgcn_permlane32_swap(o.x, o.z, false, false);
            const auto r1 = __builtin_amdgcn_permlane32_swap(o.y, o.w, false, false);
            const u32x4 on = u32x4{r0[0], r1[0], r0[1], r1[1]};
            if (layer == 1) {
                *(u32x4*)(a.out + (size_t)(tbk * 32 + r) * DM + kbo * 16 + 8 * h) = on;
            } else {
                st_sc1(c, lbase + (u32)L_X2 + (u32)(tbk * 32 + kbo) * 1024, o);
            }
        }
        STAMP(27);
        if (layer == 0) WAIT_VM(0);
    }
    if (layer == 0) {
        BAR();
        if (w == 0) signal_group(c, layer, 0);
        if (w >= 4) { issue_chunk<CQ(1, 0)>(c); issue_chunk<CQ(1, 1)>(c); issue_chunk<CQ(2, 0)>(c); issue_chunk<CQ(2, 1)>(c); }
        STAMP(28);
    }
}

__global__ void __launch_bounds__(512, 2) mega_kernel(Args a) {
    extern __shared__ __attribute__((aligned(16))) unsigned char lds[];
    Ctx c;
    c.L = (LAS unsigned char*)lds;
    c.Laddr = (u32)(size_t)c.L;
    c.ws = a.ws;
    c.rs = __builtin_amdgcn_make_buffer_rsrc((void*)a.ws, 0, (int)(WS_END + 4 * MiB), 0x00020000);
    const int tid = threadIdx.x;
    c.lane = tid & 63;
    c.w = __builtin_amdgcn_readfirstlane(tid >> 6);
    const int bid = blockIdx.x;
    c.i = 2 * (bid & 7) + (bid >> 7);
    c.j = (bid >> 3) & 15;
    c.voff = c.lane * 16;
    c.tag = 1u;
    if (c.w >= 4) {
        issue_chunk<CQ(0, 0)>(c); issue_chunk<CQ(0, 1)>(c); issue_chunk<CQ(1, 0)>(c); issue_chunk<CQ(1, 1)>(c); issue_chunk<CQ(2, 0)>(c); issue_chunk<CQ(2, 1)>(c);
    } else if (tid < 96) {
        lds_st<float>(c.L, SC_CS + tid * 4, ((const float*)(a.ws + WS_CS))[qkv_col(tid >> 5) + 32 * c.j + (tid & 31)]);
    }
    if (c.w < 4) {
        const int hh = c.lane >> 5, row = c.lane & 31, half = c.w & 1;
        u32 d[4];
#pragma unroll
        for (int q = 0; q < 4; ++q) {
            const int c0 = 16 * half + 8 * (q >> 1) + 4 * hh + 2 * (q & 1), c1 = c0 + 1;
            const u32 lo = c.w < 2 ? (row > c0 ? 0xff7fu : 0u) : (row == c0 ? 0x3f80u : 0u);
            const u32 hi = c.w < 2 ? (row > c1 ? 0xff7fu : 0u) : (row == c1 ? 0x3f80u : 0u);
            d[q] = lo | (hi << 16);
        }
        lds_st<u32x4>(c.L, SC_MK + c.w * 1024 + c.voff, u32x4{d[0], d[1], d[2], d[3]});
    }
    for (int layer = 0; layer < 2; ++layer) {
        phase_qkv_att(c, layer, a);
        phase_wo(c, layer, a);
        phase_ffn(c, layer, a);
    }
}

}

extern "C" void kernel_launch(void* const* d_in, const int* in_sizes, int n_in, void* d_out, int out_size, void* d_ws, size_t ws_size,
                              hipStream_t stream) {
    Args a;
    a.x = (const u16*)d_in[0]; a.wqkv = (const u16*)d_in[1]; a.wo = (const u16*)d_in[2];
    a.wg = (const u16*)d_in[4]; a.wu = (const u16*)d_in[5]; a.wd = (const u16*)d_in[6];
    a.out = (u16*)d_out; a.ws = (unsigned char*)d_ws;
    (void)hipFuncSetAttribute((const void*)mega_kernel, hipFuncAttributeMaxDynamicSharedMemorySize, LDS_BYTES);
    prep_kernel<<<PREP_GRID, 512, 0, stream>>>(a);
    mega_kernel<<<256, 512, LDS_BYTES, stream>>>(a);
}
```

```cpp
#include <hip/hip_runtime.h>
#include <hip/hip_bf16.h>

#define LAS __attribute__((address_space(3)))
#define GAS __attribute__((address_space(1)))
typedef unsigned u32;
typedef unsigned short u16;
typedef u32 u32x4 __attribute__((ext_vector_type(4)));
typedef u32 u32x2 __attribute__((ext_vector_type(2)));
typedef float f32x16 __attribute__((ext_vector_type(16)));
typedef float f32x4 __attribute__((ext_vector_type(4)));
typedef float f32x2 __attribute__((ext_vector_type(2)));
typedef __bf16 bf16x8_t __attribute__((ext_vector_type(8)));
typedef __bf16 bf16x2_t __attribute__((ext_vector_type(2)));
typedef short s16x4 __attribute__((ext_vector_type(4)));

namespace {

constexpr int SEQ = 2048, DM = 512, FF = 1792;
constexpr size_t KiB = 1024, MiB = 1024 * 1024;
constexpr size_t WS_FLAGS = 1 * MiB;
constexpr size_t WS_EPOCH = 640 * KiB;
constexpr size_t WS_STARTED = WS_EPOCH + 4096;
constexpr size_t WS_TMO = 768 * KiB;
constexpr size_t WS_CS = 512 * KiB;
constexpr size_t WS_XF0 = 12 * MiB;
constexpr size_t WS_WQ = 14 * MiB;
constexpr size_t WS_CSP = 3 * MiB + 256 * KiB;
constexpr size_t WS_WO = 4 * MiB + 512 * KiB;
constexpr size_t WS_WG = 5 * MiB;
constexpr size_t WS_WD = 9 * MiB;
constexpr size_t WS_L0 = 16 * MiB, WS_LSTRIDE = 32 * MiB;
constexpr size_t L_KF = 0, L_VF = 2 * MiB, L_OF = 4 * MiB, L_X1 = 6 * MiB, L_X2 = 8 * MiB, L_HF = 10 * MiB, L_X2N = 18 * MiB;
constexpr size_t WS_END = WS_L0 + 2 * WS_LSTRIDE;

constexpr int NSLOT = 8, SLOT = 16384;
constexpr int LDS_SCR = NSLOT * SLOT;
constexpr int VREC = 1040;
constexpr int SC_RV = LDS_SCR;
constexpr int SC_VS = SC_RV + 16640;
constexpr int SC_ST = SC_VS + 8320;
constexpr int SC_CS = SC_ST + 1024;
constexpr int SC_MK = SC_CS + 1280;
constexpr int LDS_BYTES = SC_MK + 4096;
constexpr int SC_KS = 0 * SLOT;
constexpr int SC_QS = 0 * SLOT + 8192;
constexpr int SC_RK = 1 * SLOT;
static_assert(LDS_BYTES <= 163840, "LDS");
__device__ __forceinline__ int red_off(int q) { return q < 5 ? 2 * SLOT + q * 6144 : 6 * SLOT + (q - 5) * 6144; }

constexpr float LN_EPS = 1e-5f;
constexpr float QSCALE = 0.25f * 1.4426950408889634f;
constexpr u32 SPIN_CAP = 1u << 17;
constexpr u32 BF_ONES = 0x3f803f80u;

struct Args {
    const u16* x; const u16* wqkv; const u16* wo; const u16* wg; const u16* wu; const u16* wd;
    u16* out; unsigned char* ws;
};

__device__ __forceinline__ f32x16 mfma32(u32x4 a, u32x4 b, f32x16 c) {
    return __builtin_amdgcn_mfma_f32_32x32x16_bf16(__builtin_bit_cast(bf16x8_t, a), __builtin_bit_cast(bf16x8_t, b), c, 0, 0, 0);
}
__device__ __forceinline__ u32 pack2(float lo, float hi) {
    f32x2 v = {lo, hi};
    return __builtin_bit_cast(u32, __builtin_convertvector(v, bf16x2_t));
}
__device__ __forceinline__ float bflo(u32 u) { return __builtin_bit_cast(float, u << 16); }
__device__ __forceinline__ float bfhi(u32 u) { return __builtin_bit_cast(float, u & 0xffff0000u); }
__device__ __forceinline__ float dot2(u32 a, u32 b, float c) {
    return __builtin_amdgcn_fdot2_f32_bf16(__builtin_bit_cast(bf16x2_t, a), __builtin_bit_cast(bf16x2_t, b), c, false);
}
__device__ __forceinline__ void swap32(float& a, float& b) {
    asm volatile("s_nop 1\n\tv_permlane32_swap_b32 %0, %1\n\ts_nop 1" : "+v"(a), "+v"(b));
}
__device__ __forceinline__ float fmx(float a, float b) { return __builtin_elementwise_maximum(a, b); }
__device__ __forceinline__ float xor32_max(float v) { float a = v, b = v; swap32(a, b); return fmx(a, b); }
__device__ __forceinline__ float xor32_sum(float v) { float a = v, b = v; swap32(a, b); return a + b; }
#define BAR() do { asm volatile("s_waitcnt lgkmcnt(0)" ::: "memory"); __builtin_amdgcn_s_barrier(); asm volatile("" ::: "memory"); } while (0)
#define BARL() do { asm volatile("" ::: "memory"); __builtin_amdgcn_s_barrier(); asm volatile("" ::: "memory"); } while (0)
#define WAIT_VM(n) asm volatile("s_waitcnt vmcnt(" #n ")" ::: "memory")
#define SB() __builtin_amdgcn_sched_barrier(0)

template <class T> __device__ __forceinline__ T lds_ld(const LAS unsigned char* base, int off) { return *(const LAS T*)(base + off); }
template <class T> __device__ __forceinline__ void lds_st(LAS unsigned char* base, int off, T v) { *(LAS T*)(base + off) = v; }

__device__ __forceinline__ void glds4k(const unsigned char* src, u32 voff, u32 dst) {
    u32 keep;
    asm volatile(
        "s_mov_b32 %0, m0\n\t"
        "s_mov_b32 m0, %3\n\t"
        "s_nop 0\n\t"
        "global_load_lds_dwordx4 %1, %2 sc1\n\t"
        "global_load_lds_dwordx4 %1, %2 offset:1024 sc1\n\t"
        "global_load_lds_dwordx4 %1, %2 offset:2048 sc1\n\t"
        "global_load_lds_dwordx4 %1, %2 offset:3072 sc1\n\t"
        "s_mov_b32 m0, %0"
        : "=&s"(keep) : "v"(voff), "s"(src), "s"(dst) : "memory");
}

__device__ __forceinline__ void glds1k(const unsigned char* src, u32 voff, u32 dst) {
    u32 keep;
    asm volatile(
        "s_mov_b32 %0, m0\n\t"
        "s_mov_b32 m0, %3\n\t"
        "s_nop 0\n\t"
        "global_load_lds_dwordx4 %1, %2\n\t"
        "s_mov_b32 m0, %0"
        : "=&s"(keep) : "v"(voff), "s"(src), "s"(dst) : "memory");
}
#define PIN(x) asm volatile("" : "+v"(x))

constexpr int NF_WO = 8 * 64, NF_WG = 16 * 224, NF_WD = 8 * 224;
__host__ __device__ constexpr int qkv_col(int t) { return t == 0 ? 512 : (t == 1 ? 1024 : 0); }
__host__ __device__ constexpr int dn_kstep(int s) { return s < 16 ? 7 * (s >> 2) + (s & 3) : 7 * ((s - 16) / 3) + 4 + (s - 16) % 3; }
struct WItem { const u16* src; size_t dst; };
__device__ __forceinline__ WItem w_item(const Args& a, int f, int lane) {
    const u16 *pA, *pB; int ld, ks; size_t dst;
    if (f < NF_WO) {
        const int cb = f >> 6, rem = f & 63, c = rem >> 4, r2 = rem & 15, kq = r2 >> 2, ks2 = (r2 >> 1) & 1, nt = r2 & 1;
        ks = 8 * kq + 2 * c + ks2; pA = a.wo + 64 * cb + 32 * nt; pB = pA + 16; ld = 512; dst = WS_WO + (size_t)f * 1024;
    } else if ((f -= NF_WO) < NF_WG) {
        const int j = f / 224, rem = f % 224, q = rem >> 4, ksl = rem & 15, c = q >> 1, hf = q & 1;
        ks = 16 * hf + ksl; pA = a.wg + 112 * j + 16 * c; pB = a.wu + 112 * j + 16 * c; ld = FF; dst = WS_WG + (size_t)f * 1024;
    } else {
        f -= NF_WG;
        const int cb = f / 224, rem = f % 224;
        int kq, sidx, nt;
        if (rem < 112) { const int c = rem >> 4, r2 = rem & 15; kq = r2 >> 3; sidx = 4 * c + ((r2 >> 1) & 3); nt = r2 & 1; }
        else { const int g = rem - 112; kq = 2 + g / 56; sidx = (g % 56) >> 1; nt = g & 1; }
        ks = 28 * kq + dn_kstep(sidx); pA = a.wd + 64 * cb + 32 * nt; pB = pA + 16; ld = 512; dst = WS_WD + (size_t)f * 1024;
    }
    const int k = lane >> 2, n8 = lane & 3;
    WItem it;
    it.src = (n8 < 2 ? pA + n8 * 8 : pB + (n8 - 2) * 8) + (size_t)(16 * ks + k) * ld;
    it.dst = dst;
    return it;
}

constexpr int PREP_XI = 64 * 8, NF_WQ = 16 * 96;
constexpr int PREP_WG_TILE = (PREP_XI + NF_WQ) / 8;
constexpr int PREP_WG_CS = 1536 / 64, PREP_WG_FLAGS = 16;
constexpr int PREP_GRID = PREP_WG_TILE + PREP_WG_CS + PREP_WG_FLAGS;
constexpr int PREP_LDS_WAVE = 4608;
constexpr size_t FLAG_BYTES = 2 * 8 * 256 * 512;
__global__ void __launch_bounds__(512) prep_kernel(Args a) {
    __shared__ __attribute__((aligned(16))) unsigned char sm[8 * PREP_LDS_WAVE];
    const int tid = threadIdx.x, lane = tid & 63, w = tid >> 6;
    const int b0 = blockIdx.x;
    unsigned char* ws = a.ws;
    if (b0 < PREP_WG_CS) {
        const u16* src = a.wqkv + b0 * 64;
        const int rg = lane >> 3, ch = lane & 7;
        u32x4 v[8];
#pragma unroll
        for (int it = 0; it < 8; ++it) v[it] = *(const u32x4*)(src + (size_t)(64 * w + 8 * it + rg) * 1536 + ch * 8);
        float s[8];
#pragma unroll
        for (int e = 0; e < 8; ++e) s[e] = 0.f;
#pragma unroll
        for (int it = 0; it < 8; ++it) {
            s[0] += bflo(v[it].x); s[1] += bfhi(v[it].x); s[2] += bflo(v[it].y); s[3] += bfhi(v[it].y);
            s[4] += bflo(v[it].z); s[5] += bfhi(v[it].z); s[6] += bflo(v[it].w); s[7] += bfhi(v[it].w);
        }
#pragma unroll
        for (int e = 0; e < 8; ++e) { s[e] += __shfl_xor(s[e], 8); s[e] += __shfl_xor(s[e], 16); s[e] += __shfl_xor(s[e], 32); }
        float* red = (float*)sm;
        if (lane < 8) {
#pragma unroll
            for (int e = 0; e < 8; ++e) red[w * 64 + lane * 8 + e] = s[e];
        }
        __syncthreads();
        if (tid < 64) ((float*)(ws + WS_CS))[b0 * 64 + tid] = ((red[tid] + red[64 + tid]) + (red[128 + tid] + red[192 + tid])) + ((red[256 + tid] + red[320 + tid]) + (red[384 + tid] + red[448 + tid]));
    } else if (b0 < PREP_WG_CS + PREP_WG_FLAGS) {
        const int g = b0 - PREP_WG_CS;
        const u32x4 z = {0u, 0u, 0u, 0u};
        for (size_t o = (size_t)(g * 512 + tid) * 16; o < FLAG_BYTES; o += (size_t)PREP_WG_FLAGS * 512 * 16) *(u32x4*)(ws + WS_FLAGS + o) = z;
    } else {
        const int item = (b0 - PREP_WG_CS - PREP_WG_FLAGS) * 8 + w;
        LAS unsigned char* t = (LAS unsigned char*)sm + w * PREP_LDS_WAVE;
        if (item < PREP_XI) {
            const int tb = item >> 3, g = item & 7;
            const u16* src = a.x + (size_t)(tb * 32) * DM + g * 64;
            u32x4 v[4];
#pragma unroll
            for (int q = 0; q < 4; ++q) v[q] = *(const u32x4*)(src + (size_t)(8 * q + (lane >> 3)) * DM + (lane & 7) * 8);
#pragma unroll
            for (int q = 0; q < 4; ++q) lds_st<u32x4>(t, (8 * q + (lane >> 3)) * 144 + (lane & 7) * 16, v[q]);
            asm volatile("s_waitcnt lgkmcnt(0)" ::: "memory");
            const int h = lane >> 5, r = lane & 31;
#pragma unroll
            for (int kbl = 0; kbl < 4; ++kbl) {
                const u32x2 lo = lds_ld<u32x2>(t, r * 144 + kbl * 32 + 8 * h);
                const u32x2 hi = lds_ld<u32x2>(t, r * 144 + kbl * 32 + 16 + 8 * h);
                *(u32x4*)(ws + WS_XF0 + (size_t)(tb * 32 + 4 * g + kbl) * 1024 + lane * 16) = u32x4{lo.x, lo.y, hi.x, hi.y};
            }
        } else {
            const int f = item - PREP_XI;
            const int j = f / 96, rem = f % 96, q = rem >> 4, ksl = rem & 15, ct = q >> 1, hf = q & 1;
            const int k = lane >> 2, n8 = lane & 3;
            const u32x4 v = *(const u32x4*)(a.wqkv + qkv_col(ct) + 32 * j + n8 * 8 + (size_t)(16 * (16 * hf + ksl) + k) * 1536);
            lds_st<u32x4>(t, k * 64 + n8 * 16, v);
            asm volatile("s_waitcnt lgkmcnt(0)" ::: "memory");
            const int g4 = lane >> 4, h = g4 >> 1, nh = g4 & 1, li = lane & 15, qq = li >> 2, p = li & 3;
            const s16x4 lo = __builtin_amdgcn_ds_read_tr16_b64_v4i16((LAS s16x4*)(t + (4 * h + qq) * 64 + nh * 32 + p * 8));
            const s16x4 hi = __builtin_amdgcn_ds_read_tr16_b64_v4i16((LAS s16x4*)(t + (8 + 4 * h + qq) * 64 + nh * 32 + p * 8));
            const u32x2 l2 = __builtin_bit_cast(u32x2, lo), h2 = __builtin_bit_cast(u32x2, hi);
            *(u32x4*)(ws + WS_WQ + (size_t)f * 1024 + lane * 16) = u32x4{l2.x, l2.y, h2.x, h2.y};
        }
    }
}

#define STAMP(k) do { } while (0)
struct Ctx {
    LAS unsigned char* L;
    u32 Laddr;
    unsigned char* ws;
    __amdgpu_buffer_rsrc_t rs;
    int lane, w, i, j;
    u32 voff;
    u32 tag;
};

__device__ __forceinline__ u32x4 ld_sc1(const Ctx& c, u32 soff) { return __builtin_amdgcn_raw_buffer_load_b128(c.rs, c.voff, soff, 16); }
__device__ __forceinline__ void st_sc1(const Ctx& c, u32 soff, u32x4 v) { __builtin_amdgcn_raw_buffer_store_b128(v, c.rs, c.voff, soff, 16); }
__device__ __forceinline__ Ctx fresh(const Ctx& c0) {
    Ctx c = c0;
    asm volatile("" : "+s"(c.i), "+s"(c.j), "+s"(c.w), "+s"(c.tag));
    asm volatile("" : "+v"(c.lane));
    c.voff = c.lane * 16;
    return c;
}

constexpr int CQ(int t, int hf) { return 2 * t + hf; }
constexpr int CO(int k) { return 6 + k; }
constexpr int CG(int t, int hf) { return 10 + 2 * t + hf; }
constexpr int CD(int k) { return 24 + k; }
constexpr int slot_of(int n) {
    constexpr int tab[38] = {
          0, 1,   2, 3,   4, 5,
          6, 7, 2, 3,
          4, 5,   0, 1,   2, 3,   6, 7,   4, 5,   0, 1,   2, 3,
          6, 7, 4, 5, 0, 1, 2,   0, 0, 0, 0, 0, 0, 0};
    return tab[n];
}
template <int N> __device__ __forceinline__ const unsigned char* chunk_src(const Ctx& c) {
    size_t src;
    const int cb = c.j & 7;
    if (N < 6) src = WS_WQ + (size_t)(c.j * 6 + N) * SLOT;
    else if (N < 10) src = WS_WO + (size_t)(cb * 4 + (N - 6)) * SLOT;
    else if (N < 24) src = WS_WG + (size_t)(c.j * 14 + (N - 10)) * SLOT;
    else src = WS_WD + (size_t)(cb * 14 + (N - 24)) * SLOT;
    return c.ws + src + (c.w & 3) * 4096;
}
constexpr int chunk_lds(int n) { return slot_of(n) * SLOT; }
template <int N> __device__ __forceinline__ void issue_chunk(const Ctx& c) {
    glds4k(chunk_src<N>(c), c.voff, c.Laddr + chunk_lds(N) + (c.w & 3) * 4096);
}
__device__ __forceinline__ const LAS unsigned char* lds_base(const Ctx& c, int off) {
    u32 a = c.Laddr + off + c.voff;
    asm volatile("" : "+v"(a));
    return (const LAS unsigned char*)(size_t)a;
}

__device__ __forceinline__ u32 flag_off(int layer, int seam, int ci, int cj) {
    return (u32)(WS_FLAGS + (size_t)(((layer * 8 + seam) * 256) + ci * 16 + cj) * 512);
}
__device__ __forceinline__ u32 flag_tag(const Ctx& c, int layer, int seam) { return c.tag + (u32)(layer * 8 + seam); }
__device__ __forceinline__ void flag_store(const Ctx& c, int layer, int seam, int ci, int cj, int word) {
    __builtin_amdgcn_raw_buffer_store_b32(flag_tag(c, layer, seam), c.rs, flag_off(layer, seam, ci, cj) + word * 4, 0, 16);
}
__device__ __forceinline__ void signal_group(const Ctx& c, int layer, int seam) {
    if (c.lane < 16) flag_store(c, layer, seam, c.i, c.lane, c.j);
}
template <int NWORDS> __device__ __forceinline__ void poll_flags(const Ctx& c, int layer, int seam, int nwords = NWORDS) {
    const u32 off = flag_off(layer, seam, c.i, c.j) + c.lane * 4;
    const u32 tag = flag_tag(c, layer, seam);
    for (u32 spins = 0;; ++spins) {
        asm volatile("" ::: "memory");
        const u32 f0 = __builtin_amdgcn_raw_buffer_load_b32(c.rs, off, 0, 16);
        bool ok = c.lane >= nwords || f0 == tag;
        if (NWORDS > 64) { const u32 f1 = __builtin_amdgcn_raw_buffer_load_b32(c.rs, off + 256, 0, 16); ok = ok && f1 == tag; }
        if (__all(ok)) break;
        if (spins > SPIN_CAP) { if (c.lane == 0) __builtin_amdgcn_raw_buffer_store_b32(1u, c.rs, (u32)WS_TMO, 0, 16); break; }
        __builtin_amdgcn_s_sleep(1);
    }
}

__device__ __forceinline__ int crow(int r, int h) { return (r & 3) + 8 * (r >> 2) + 4 * h; }

#define LDF(p, k) (*(const LAS u32x4*)((p) + (k) * 1024))
__device__ __forceinline__ u32x4 ldf_tr(const LAS unsigned char* p, int k) {
    const u32x2 lo = __builtin_bit_cast(u32x2, __builtin_amdgcn_ds_read_tr16_b64_v4i16((LAS s16x4*)(p + k * 1024)));
    const u32x2 hi = __builtin_bit_cast(u32x2, __builtin_amdgcn_ds_read_tr16_b64_v4i16((LAS s16x4*)(p + k * 1024 + 256)));
    return u32x4{lo.x, lo.y, hi.x, hi.y};
}
template <bool TR> __device__ __forceinline__ u32x4 ldf(const LAS unsigned char* p, int k) { if constexpr (TR) return ldf_tr(p, k); else return LDF(p, k); }

template <bool SWAP, bool PRE, bool TR, class F>
__device__ __forceinline__ void c_tile(f32x16& acc, u32x4 (&wf)[8], const LAS unsigned char* lo, const LAS unsigned char* hi, const LAS unsigned char* nxt,
                                       const u32x4 (&xf)[32], F&& filler) {
#pragma unroll
    for (int k = 0; k < 32; ++k) {
        const u32x4 wv = wf[k & 7];
        if (k + 8 < 16) wf[k & 7] = ldf<TR>(lo, k + 8);
        else if (k + 8 < 32) wf[k & 7] = ldf<TR>(hi, k + 8 - 16);
        else if (PRE) wf[k & 7] = ldf<TR>(nxt, k + 8 - 32);
        acc = SWAP ? mfma32(xf[k], wv, acc) : mfma32(wv, xf[k], acc);
        PIN(acc);
        filler(k);
        SB();
    }
}
#define XPD 14
template <bool PRE, bool TR, bool STAGE, bool MID, class W>
__device__ __forceinline__ f32x2 c_tile0(const Ctx& c, f32x16& acc, u32x4 (&wf)[8], const LAS unsigned char* lo, const LAS unsigned char* hi,
                                         const LAS unsigned char* nxt, u32x4 (&xf)[32], __amdgpu_buffer_rsrc_t xrs, u32 xsoff, W&& wait_ready) {
    const int lane = c.lane;
    u32x4 raw[STAGE ? 32 : 1];
    const u32 rvoff = (u32)(lane >> 3) * 1024u + (u32)(lane & 7) * 16u;
    LAS unsigned char* st = c.L + 6 * SLOT + (c.w & 3) * 8192;
    const int wr_off = (lane >> 3) * 128 + (((lane & 7) ^ ((lane >> 3) & 7)) * 16);
    const int rd_row = (lane & 31) * 128, rd_sw = lane & 7, rd_h = lane >> 5;
#define XLD(k) __builtin_amdgcn_raw_buffer_load_b128(xrs, c.voff, xsoff + (u32)(k) * 1024u, 16)
#define RLD(q) __builtin_amdgcn_raw_buffer_load_b128(xrs, rvoff, xsoff + (u32)((q) & 3) * 8192u + (u32)((q) >> 2) * 128u, 0)
#define STG(g) do { _Pragma("unroll") for (int q = 0; q < 4; ++q) lds_st<u32x4>(st + ((g) & 1) * 4096, q * 1024 + wr_off, raw[4 * (g) + q]); \
                    _Pragma("unroll") for (int q = 0; q < 4; ++q) xf[4 * (g) + q] = lds_ld<u32x4>(st + ((g) & 1) * 4096, rd_row + (((2 * q + rd_h) ^ rd_sw) * 16)); } while (0)
    if (STAGE) {
#pragma unroll
        for (int q = 0; q < 16; ++q) raw[q] = RLD(q);
        SB();
        wait_ready();
        STG(0);
    } else {
        wait_ready();
#pragma unroll
        for (int k = 0; k < XPD; ++k) xf[k] = XLD(k);
    }
#pragma unroll
    for (int k = 0; k < 8; ++k) wf[k] = ldf<TR>(lo, k);
    SB();
    float s1[4] = {0.f, 0.f, 0.f, 0.f}, s2[4] = {0.f, 0.f, 0.f, 0.f};
#pragma unroll
    for (int k = 0; k < 32; ++k) {
        if (STAGE) {
            if ((k & 3) == 0) {
                const int g = k >> 2;
                if (4 * (g + 4) < 32) {
#pragma unroll
                    for (int q = 0; q < 4; ++q) raw[4 * (g + 4) + q] = RLD(4 * (g + 4) + q);
                }
                if (g + 1 < 8) STG(g + 1);
            }
        } else {
            if (k + XPD < 32) xf[k + XPD] = XLD(k + XPD);
        }
        const u32x4 wv = wf[k & 7];
        if (k + 8 < 16) wf[k & 7] = ldf<TR>(lo, k + 8);
        else if (k + 8 < 32) wf[k & 7] = ldf<TR>(hi, k + 8 - 16);
        else if (PRE) wf[k & 7] = ldf<TR>(nxt, k + 8 - 32);
        acc = mfma32(wv, xf[k], acc);
        PIN(acc);
        {
        s1[0] = dot2(xf[k].x, BF_ONES, s1[0]); s2[0] = dot2(xf[k].x, xf[k].x, s2[0]);
        s1[1] = dot2(xf[k].y, BF_ONES, s1[1]); s2[1] = dot2(xf[k].y, xf[k].y, s2[1]);
        s1[2] = dot2(xf[k].z, BF_ONES, s1[2]); s2[2] = dot2(xf[k].z, xf[k].z, s2[2]);
        s1[3] = dot2(xf[k].w, BF_ONES, s1[3]); s2[3] = dot2(xf[k].w, xf[k].w, s2[3]);
        }
        asm volatile("" : "+v"(s1[0]), "+v"(s1[1]), "+v"(s1[2]), "+v"(s1[3]), "+v"(s2[0]), "+v"(s2[1]), "+v"(s2[2]), "+v"(s2[3]));
        if (MID && k == 3) BARL();
        SB();
    }
    const float a = xor32_sum((s1[0] + s1[1]) + (s1[2] + s1[3])), b = xor32_sum((s2[0] + s2[1]) + (s2[2] + s2[3]));
    const float mean = a * (1.0f / DM);
    float var = b * (1.0f / DM) - mean * mean;
    var = var > 0.f ? var : 0.f;
    f32x2 o; o.x = mean; o.y = __builtin_amdgcn_rsqf(var + LN_EPS);
    return o;
#undef XLD
#undef RLD
#undef STG
}

struct GS { float g0, g1, t0, t1, u0, u1; };
struct GK { float mean, A, B, ir2; };
__device__ __forceinline__ void geglu_sub(GS& s, u32& o, int q, float ag0, float ag1, float au0, float au1, float cg0, float cg1, float cu0, float cu1, const GK& k) {
    if (q == 0) asm volatile("v_fma_f32 %0, -%7, %5, %3\n\tv_fma_f32 %1, -%7, %6, %4\n\tv_mul_f32 %2, %0, %0"
                             : "=&v"(s.g0), "=&v"(s.g1), "=&v"(s.t0) : "v"(ag0), "v"(ag1), "v"(cg0), "v"(cg1), "v"(k.mean));
    if (q == 1) asm volatile("v_mul_f32 %1, %2, %2\n\tv_fma_f32 %0, %0, %4, %3\n\tv_fma_f32 %1, %1, %4, %3"
                             : "+v"(s.t0), "=&v"(s.t1) : "v"(s.g1), "v"(k.A), "v"(k.B));
    if (q == 2) asm volatile("v_mul_f32 %0, %2, %0\n\tv_mul_f32 %1, %3, %1\n\tv_exp_f32 %0, %0"
                             : "+v"(s.t0), "+v"(s.t1) : "v"(s.g0), "v"(s.g1));
    if (q == 3) asm volatile("v_exp_f32 %0, %0\n\tv_fma_f32 %1, -%7, %5, %3\n\tv_fma_f32 %2, -%7, %6, %4"
                             : "+v"(s.t1), "=&v"(s.u0), "=&v"(s.u1) : "v"(au0), "v"(au1), "v"(cu0), "v"(cu1), "v"(k.mean));
    if (q == 4) asm volatile("v_fma_f32 %0, %0, %4, %4\n\tv_mul_f32 %2, %2, %3\n\tv_fma_f32 %1, %1, %4, %4"
                             : "+v"(s.t0), "+v"(s.t1), "+v"(s.g0) : "v"(s.u0), "v"(k.ir2));
    if (q == 5) asm volatile("v_rcp_f32 %0, %0\n\tv_mul_f32 %2, %2, %3\n\tv_rcp_f32 %1, %1"
                             : "+v"(s.t0), "+v"(s.t1), "+v"(s.g1) : "v"(s.u1));
    if (q == 6) asm volatile("v_mul_f32 %1, %1, %3\n\tv_mul_f32 %2, %2, %4\n\tv_cvt_pk_bf16_f32 %0, %1, %2"
                             : "=&v"(o), "+v"(s.g0), "+v"(s.g1) : "v"(s.t0), "v"(s.t1));
}
struct LS { float t0, t1; };
__device__ __forceinline__ void lnq_a(LS& s, float a0, float c0, float c1, float sc, float msc) {
    asm volatile("v_mul_f32 %0, %6, %3\n\tv_mul_f32 %1, %6, %4\n\tv_fma_f32 %0, %2, %5, -%0"
                 : "=&v"(s.t0), "=&v"(s.t1) : "v"(a0), "v"(c0), "v"(c1), "v"(sc), "v"(msc));
}
__device__ __forceinline__ u32 lnq_b(LS& s, float a1, float sc) {
    u32 o;
    asm volatile("v_fma_f32 %1, %3, %4, -%1\n\tv_cvt_pk_bf16_f32 %0, %2, %1" : "=v"(o), "+v"(s.t1) : "v"(s.t0), "v"(a1), "v"(sc));
    return o;
}
__device__ __forceinline__ void lnv_a(LS& s, float a0, float a1, float m0, float m1, float r0, float cs) {
    asm volatile("v_fma_f32 %0, -%4, %7, %2\n\tv_fma_f32 %1, -%5, %7, %3\n\tv_mul_f32 %0, %0, %6"
                 : "=&v"(s.t0), "=&v"(s.t1) : "v"(a0), "v"(a1), "v"(m0), "v"(m1), "v"(r0), "v"(cs));
}
__device__ __forceinline__ u32 lnv_b(LS& s, float r1) {
    u32 o;
    asm volatile("v_mul_f32 %1, %1, %3\n\tv_cvt_pk_bf16_f32 %0, %2, %1" : "=v"(o), "+v"(s.t1) : "v"(s.t0), "v"(r1));
    return o;
}
__device__ __forceinline__ u32x4 pack8(const float* v) { return u32x4{pack2(v[0], v[1]), pack2(v[2], v[3]), pack2(v[4], v[5]), pack2(v[6], v[7])}; }

__device__ __forceinline__ float max16(const f32x16& s, float m) {
    float a = fmx(fmx(s[0], s[1]), m), b = fmx(s[2], s[3]);
    a = fmx(fmx(s[4], s[5]), a); b = fmx(fmx(s[6], s[7]), b);
    a = fmx(fmx(s[8], s[9]), a); b = fmx(fmx(s[10], s[11]), b);
    a = fmx(fmx(s[12], s[13]), a); b = fmx(fmx(s[14], s[15]), b);
    return fmx(a, b);
}
__device__ __forceinline__ float smul(float a, float b) { float r; asm("v_mul_f32 %0, %1, %2" : "=v"(r) : "v"(a), "v"(b)); return r; }
struct MaskF { u32x4 alo, ahi, ilo, ihi; };
__device__ __forceinline__ f32x16 add_mask(const MaskF& mf, f32x16 s) { s = mfma32(mf.alo, mf.ilo, s); return mfma32(mf.ahi, mf.ihi, s); }
template <int NB, int FIRST, int REST0, int MASK, bool FIRSTSEG>
__device__ __forceinline__ void att_segment(f32x16& oacc, float& m, const LAS unsigned char* kb, const LAS unsigned char* vb, const LAS unsigned char* mk, const u32x4& qf) {
    u32x4 kf[NB];
#pragma unroll
    for (int t = 0; t < NB; ++t) { const int b = t == 0 ? FIRST : REST0 + t - 1; kf[t] = *(const LAS u32x4*)(kb + b * 2048); }
    MaskF mf;
    if (MASK) {
        mf.alo = LDF(mk, 0); mf.ahi = LDF(mk, 1); mf.ilo = LDF(mk, 2); mf.ihi = LDF(mk, 3);
        if (MASK == 2) { mf.alo ^= 0xff7fff7fu; mf.ahi ^= 0xff7fff7fu; }
    }
    SB();
    float mx = FIRSTSEG ? -__builtin_inff() : m;
    f32x16 sv[4];
    sv[0] = mfma32(kf[0], qf, f32x16{});
    if (NB > 1) sv[1] = mfma32(kf[1], qf, f32x16{});
    if (MASK) sv[0] = add_mask(mf, sv[0]);
    SB();
#pragma unroll
    for (int t = 0; t < NB; ++t) {
        const int u = NB > 1 ? (t == 0 ? 1 : t == 1 ? 0 : t) : 0;
        if (t + 2 < NB) sv[(t + 2) & 3] = mfma32(kf[t + 2], qf, f32x16{});
        mx = max16(sv[u & 3], mx);
        SB();
    }
    mx = xor32_max(mx);
    f32x16 negm;
#pragma unroll
    for (int e = 0; e < 16; ++e) negm[e] = -mx;
    SB();
    sv[0] = mfma32(kf[0], qf, negm);
    if (MASK) sv[0] = add_mask(mf, sv[0]);
    u32x4 v0 = *(const LAS u32x4*)(vb + (FIRST * 2 + 0) * VREC);
    u32x4 v1 = *(const LAS u32x4*)(vb + (FIRST * 2 + 1) * VREC);
    if (NB > 1) sv[1] = mfma32(kf[1], qf, negm);
    if (!FIRSTSEG) {
        float f = __builtin_amdgcn_exp2f(m - mx);
        asm volatile("s_nop 1" : "+v"(f));
#pragma unroll
        for (int e = 0; e < 16; ++e) oacc[e] = smul(oacc[e], f);
    }
    m = mx;
    SB();
#pragma unroll
    for (int t = 0; t < NB; ++t) {
        u32x4 n0, n1;
        if (t + 1 < NB) {
            if (t > 0) sv[(t + 1) & 1] = mfma32(kf[t + 1], qf, negm);
            n0 = *(const LAS u32x4*)(vb + ((REST0 + t) * 2 + 0) * VREC);
            n1 = *(const LAS u32x4*)(vb + ((REST0 + t) * 2 + 1) * VREC);
        }
        SB();
        const f32x16& s = sv[t & 1];
        u32 p[8];
#pragma unroll
        for (int e = 0; e < 4; ++e) p[e] = pack2(__builtin_amdgcn_exp2f(s[2 * e]), __builtin_amdgcn_exp2f(s[2 * e + 1]));
        SB();
        oacc = mfma32(v0, u32x4{p[0], p[1], p[2], p[3]}, oacc);
#pragma unroll
        for (int e = 4; e < 8; ++e) p[e] = pack2(__builtin_amdgcn_exp2f(s[2 * e]), __builtin_amdgcn_exp2f(s[2 * e + 1]));
        SB();
        oacc = mfma32(v1, u32x4{p[4], p[5], p[6], p[7]}, oacc);
        if (t + 1 < NB) { v0 = n0; v1 = n1; }
        SB();
    }
}

__device__ __forceinline__ void issue_qkv_tile(const Ctx& c, const Args& a, int t) {
    const u32 voff = (u32)(c.lane >> 2) * 3072u + (u32)(c.lane & 3) * 16u;
    const unsigned char* src = (const unsigned char*)(a.wqkv + qkv_col(t) + 32 * c.j) + (size_t)(c.w & 3) * 8 * 16 * 3072;
    const u32 dst = c.Laddr + slot_of(CQ(t, 0)) * SLOT + (c.w & 3) * 8192;
#pragma unroll
    for (int q = 0; q < 8; ++q) glds1k(src + (size_t)q * 16 * 3072, voff, dst + q * 1024);
}
template <bool TR> __device__ __forceinline__ void tile_colsum(const Ctx& c, const LAS unsigned char* lo, const LAS unsigned char* hi, int csidx) {
    float s[4] = {0.f, 0.f, 0.f, 0.f};
#pragma unroll
    for (int hf = 0; hf < 2; ++hf) {
        u32x4 f[16];
#pragma unroll
        for (int k = 0; k < 16; ++k) f[k] = ldf<TR>(hf ? hi : lo, k);
        SB();
#pragma unroll
        for (int k = 0; k < 16; ++k) {
            s[0] = dot2(f[k].x, BF_ONES, s[0]); s[1] = dot2(f[k].y, BF_ONES, s[1]); s[2] = dot2(f[k].z, BF_ONES, s[2]); s[3] = dot2(f[k].w, BF_ONES, s[3]);
        }
        SB();
    }
    const float t = xor32_sum((s[0] + s[1]) + (s[2] + s[3]));
    if (c.lane < 32) lds_st<float>(c.L, SC_CS + (csidx + c.lane) * 4, t);
}
__device__ __forceinline__ int tr_lane_off(int lane) {
    const int g4 = lane >> 4, h = g4 >> 1, nh = g4 & 1, li = lane & 15;
    return (8 * h + (li >> 2)) * 64 + nh * 32 + (li & 3) * 8;
}
__device__ __forceinline__ const LAS unsigned char* lds_base_raw(const Ctx& c, int off) {
    u32 a = c.Laddr + off;
    asm volatile("" : "+v"(a));
    return (const LAS unsigned char*)(size_t)a;
}

__device__ __forceinline__ void phase_qkv_att(const Ctx& c0, int layer, const Args& a) {
    const Ctx c = fresh(c0);
    const int lane = c.lane, w = c.w, tb = w & 3, h = lane >> 5, r = lane & 31;
    const u32 lbase = (u32)(WS_L0 + (size_t)layer * WS_LSTRIDE);
    STAMP(0);
    if (w < 4) {
        auto ready = [&]() {
            if (layer > 0 && w == 0) poll_flags<64>(c, layer - 1, 0, 16);
            BAR();
            STAMP(1);
        };
        u32x4 xf[32], wf[8];
        const LAS unsigned char* wK = lds_base(c, slot_of(CQ(0, 0)) * SLOT);
        const LAS unsigned char* wV = lds_base(c, slot_of(CQ(1, 0)) * SLOT);
        const LAS unsigned char* wQ = lds_base(c, slot_of(CQ(2, 0)) * SLOT);
        f32x16 accK = {}, accV = {}, accQ = {};
        const f32x2 ms = c_tile0<true, false, false, true>(c, accK, wf, wK, wK + SLOT, wV, xf, c.rs,
                                                            (layer == 0 ? (u32)WS_XF0 : (u32)(WS_L0 + L_X2)) + (u32)((4 * c.i + tb) * 32) * 1024, ready);
        if (lane < 32) lds_st<f32x2>(c.L, SC_ST + (tb * 32 + r) * 8, ms);
        const float msk = ms.x * ms.y;
        BARL(); STAMP(2);
        f32x4 cs[4];
#pragma unroll
        for (int q = 0; q < 4; ++q) cs[q] = lds_ld<f32x4>(c.L, SC_CS + (0 * 32 + 8 * q + 4 * h) * 4);
        f32x4 mr[8];
        float csv;
        {
            u32 o[8]; LS ls;
            c_tile<true, true, false>(accV, wf, wV, wV + SLOT, wQ, xf, [&](int k) {
                if (k >= 4 && k < 20) {
                    const int pi = (k - 4) >> 1, e = 2 * pi;
                    if (((k - 4) & 1) == 0) lnq_a(ls, accK[e], cs[e >> 2][e & 3], cs[e >> 2][(e & 3) + 1], ms.y, msk);
                    else o[pi] = lnq_b(ls, accK[e + 1], ms.y);
                }
                if (k == 20 || k == 21) {
                    const int hh = k - 20;
                    const u32x4 ov = {o[4 * hh], o[4 * hh + 1], o[4 * hh + 2], o[4 * hh + 3]};
                    lds_st<u32x4>(c.L, SC_KS + (tb * 2 + hh) * 1024 + c.voff, ov);
                    st_sc1(c, lbase + (u32)L_KF + (u32)((4 * c.i + tb) * 32 + 2 * c.j + hh) * 1024, ov);
                }
                if (k >= 22 && k < 30) { const int q = (k - 22) >> 1; mr[k - 22] = lds_ld<f32x4>(c.L, SC_ST + (tb * 32 + 8 * q + 4 * h + 2 * ((k - 22) & 1)) * 8); }
                if (k == 30) csv = lds_ld<float>(c.L, SC_CS + (32 + r) * 4);
            });
        }
        BARL();
        {
            u32 o[8]; LS ls;
            c_tile<false, false, false>(accQ, wf, wQ, wQ + SLOT, wQ, xf, [&](int k) {
                if (k >= 2 && k < 18) {
                    const int pi = (k - 2) >> 1;
                    if (((k - 2) & 1) == 0) lnv_a(ls, accV[2 * pi], accV[2 * pi + 1], mr[pi].x, mr[pi].z, mr[pi].y, csv);
                    else o[pi] = lnv_b(ls, mr[pi].w);
                }
                if (k == 18 || k == 20) {
                    const int sv = (k - 18) >> 1;
                    const u32x4 ov = {o[4 * sv], o[4 * sv + 1], o[4 * sv + 2], o[4 * sv + 3]};
                    lds_st<u32x4>(c.L, SC_VS + (tb * 2 + sv) * VREC + c.voff, ov);
                    st_sc1(c, lbase + (u32)L_VF + (u32)(((4 * c.i + tb) * 16 + c.j) * 2 + sv) * 1024, ov);
                }
                if (k >= 24 && k < 28) cs[k - 24] = lds_ld<f32x4>(c.L, SC_CS + (64 + 8 * (k - 24) + 4 * h) * 4);
            });
            if (lane < 2) { u32x4 ones = {BF_ONES, BF_ONES, BF_ONES, BF_ONES}; lds_st<u32x4>(c.L, SC_VS + (tb * 2 + lane) * VREC + 1024, ones); }
        }
        {
            const float sc = ms.y * QSCALE, msc = ms.x * sc;
            asm volatile("s_nop 15" : "+v"(accQ));
            u32 o[8]; LS ls[8];
#pragma unroll
            for (int pi = 0; pi < 8; ++pi) { const int e = 2 * pi; lnq_a(ls[pi], accQ[e], cs[e >> 2][e & 3], cs[e >> 2][(e & 3) + 1], sc, msc); }
#pragma unroll
            for (int pi = 0; pi < 8; ++pi) o[pi] = lnq_b(ls[pi], accQ[2 * pi + 1], sc);
#pragma unroll
            for (int hh = 0; hh < 2; ++hh) lds_st<u32x4>(c.L, SC_QS + (tb * 2 + hh) * 1024 + c.voff, u32x4{o[4 * hh], o[4 * hh + 1], o[4 * hh + 2], o[4 * hh + 3]});
        }
        WAIT_VM(0);
        BAR(); STAMP(5);
        if (w == 0 && lane < 2 && c.i + 1 + lane < 16) flag_store(c, layer, 1, c.i + 1 + lane, c.j, lane);
    } else {
        WAIT_VM(16);
        BAR();
        WAIT_VM(8);
        BARL();
        const int cb = c.j & 7, sidx = c.i * 2 + (c.j >> 3);
        if (layer == 0) {
            const int hw = w & 3;
            LAS unsigned char* t = c.L + SC_VS + hw * 1024;
            WItem it[6]; u32x4 v[6];
#pragma unroll
            for (int q = 0; q < 6; ++q) {
                int f;
                const int fD = NF_WO + NF_WG + cb * 224 + sidx * 7, fO = cb * 64 + sidx * 2;
                if (q < 2 || (q < 4 && hw < 3)) { const int ks = 2 * c.i + (q & 1), cc = hw + 4 * (q >> 1); f = NF_WO + c.j * 224 + (2 * cc + (ks >> 4)) * 16 + (ks & 15); }
                else if (q < 4) f = fD + 4 + (q & 1);
                else if (q == 4) f = fD + (hw < 3 ? hw : 6);
                else f = hw < 2 ? fO + hw : fD + 3;
                it[q] = w_item(a, f, lane);
                v[q] = *(const u32x4*)it[q].src;
            }
            const int k = lane >> 2, n8 = lane & 3;
            const int g4 = lane >> 4, hh = g4 >> 1, nh = g4 & 1, li = lane & 15, qq = li >> 2, p = li & 3;
            float part = 0.f;
#pragma unroll
            for (int q = 0; q < 6; ++q) {
                if (q < 5 || hw < 3) {
                    lds_st<u32x4>(t, k * 64 + n8 * 16, v[q]);
                    asm volatile("s_waitcnt lgkmcnt(0)" ::: "memory");
                    const s16x4 lo = __builtin_amdgcn_ds_read_tr16_b64_v4i16((LAS s16x4*)(t + (4 * hh + qq) * 64 + nh * 32 + p * 8));
                    const s16x4 hi = __builtin_amdgcn_ds_read_tr16_b64_v4i16((LAS s16x4*)(t + (8 + 4 * hh + qq) * 64 + nh * 32 + p * 8));
                    const u32x2 l2 = __builtin_bit_cast(u32x2, lo), h2 = __builtin_bit_cast(u32x2, hi);
                    __builtin_amdgcn_raw_buffer_store_b128(u32x4{l2.x, l2.y, h2.x, h2.y}, c.rs, c.voff, (u32)it[q].dst, 16);
                    if (q < 2 || (q < 4 && hw < 3)) {
                        if ((q & 1) == 0) part = 0.f;
                        part = dot2(l2.x, BF_ONES, part); part = dot2(l2.y, BF_ONES, part); part = dot2(h2.x, BF_ONES, part); part = dot2(h2.y, BF_ONES, part);
                        if (q & 1) {
                            const float tot = xor32_sum(part);
                            const int cc = hw + 4 * (q >> 1);
                            if (lane < 32) __builtin_amdgcn_raw_buffer_store_b32(__builtin_bit_cast(u32, tot), c.rs, (u32)lane * 4u, (u32)WS_CSP + (u32)((c.j * 16 + c.i) * 224 + cc * 32) * 4u, 16);
                        }
                    }
                    asm volatile("s_waitcnt lgkmcnt(0)" ::: "memory");
                }
            }
        }
        WAIT_VM(0);
        BAR();
        if (layer == 0 && w == 4) {
            if (lane < 16) flag_store(c, 0, 5, lane, c.j, c.i);
            if (lane < 32) flag_store(c, 0, 6, lane >> 1, cb + 8 * (lane & 1), sidx);
        }
        BAR();
        if (layer == 0) poll_flags<64>(c, 0, 6, 32);
        issue_chunk<CO(0)>(c); issue_chunk<CO(1)>(c); issue_chunk<CO(2)>(c); issue_chunk<CO(3)>(c);
        BAR();
        if (layer == 0) {
            poll_flags<64>(c, 0, 5, 16);
            const int col = (w & 3) * 64 + lane;
            if (col < 224) {
                float t = 0.f;
#pragma unroll
                for (int i2 = 0; i2 < 16; ++i2) t += __builtin_bit_cast(float, __builtin_amdgcn_raw_buffer_load_b32(c.rs, (u32)col * 4u, (u32)WS_CSP + (u32)((c.j * 16 + i2) * 224) * 4u, 16));
                lds_st<float>(c.L, SC_CS + (96 + col) * 4, t);
            }
        }
        issue_chunk<CG(0, 0)>(c); issue_chunk<CG(0, 1)>(c);
    }
    const int qb = w < 4 ? w : 7 - w, hh = w >> 2;
    const u32x4 qf = lds_ld<u32x4>(c.L, SC_QS + (qb * 2 + hh) * 1024 + c.voff);
    const int vlane = (((lane >> 4) & 1) == hh) ? lane * 16 : 1024;
    f32x16 oacc = {};
    float m = 0.f;
    const LAS unsigned char* mk = lds_base(c, SC_MK);
    {
        const LAS unsigned char* kb = c.L + SC_KS + hh * 1024 + c.voff;
        const LAS unsigned char* vb = c.L + SC_VS + vlane;
        if (qb == 0) att_segment<1, 0, 0, 1, true>(oacc, m, kb, vb, mk, qf);
        else if (qb == 1) att_segment<2, 1, 0, 1, true>(oacc, m, kb, vb, mk, qf);
        else if (qb == 2) att_segment<3, 2, 0, 1, true>(oacc, m, kb, vb, mk, qf);
        else att_segment<4, 3, 0, 1, true>(oacc, m, kb, vb, mk, qf);
    }
    STAMP(6);
    if (c.i > 0) {
        if (w == 0) poll_flags<64>(c, layer, 1, c.i >= 2 ? 2 : 1);
        BAR();
        STAMP(7);
        {
            const int idx = w;
            const int g = 4 * c.i - 8 + idx;
            if (g >= 0) {
                const u32x4 s0 = ld_sc1(c, lbase + (u32)L_KF + (u32)(g * 32 + 2 * c.j + 0) * 1024);
                const u32x4 s1 = ld_sc1(c, lbase + (u32)L_KF + (u32)(g * 32 + 2 * c.j + 1) * 1024);
                const u32x4 s2 = ld_sc1(c, lbase + (u32)L_VF + (u32)((g * 16 + c.j) * 2 + 0) * 1024);
                const u32x4 s3 = ld_sc1(c, lbase + (u32)L_VF + (u32)((g * 16 + c.j) * 2 + 1) * 1024);
                lds_st<u32x4>(c.L, SC_RK + (idx * 2 + 0) * 1024 + c.voff, s0);
                lds_st<u32x4>(c.L, SC_RK + (idx * 2 + 1) * 1024 + c.voff, s1);
                lds_st<u32x4>(c.L, SC_RV + (idx * 2 + 0) * VREC + c.voff, s2);
                lds_st<u32x4>(c.L, SC_RV + (idx * 2 + 1) * VREC + c.voff, s3);
                if (lane < 2) { u32x4 ones = {BF_ONES, BF_ONES, BF_ONES, BF_ONES}; lds_st<u32x4>(c.L, SC_RV + (idx * 2 + lane) * VREC + 1024, ones); }
            }
        }
        BAR();
        STAMP(8);
        const LAS unsigned char* kb = c.L + SC_RK + hh * 1024 + c.voff;
        const LAS unsigned char* vb = c.L + SC_RV + vlane;
        if (c.i == 1) att_segment<4, 4, 5, 0, false>(oacc, m, kb, vb, mk, qf);
        else if (qb == 0) att_segment<8, 0, 1, 2, false>(oacc, m, kb, vb, mk, qf);
        else if (qb == 1) att_segment<7, 1, 2, 2, false>(oacc, m, kb, vb, mk, qf);
        else if (qb == 2) att_segment<6, 2, 3, 2, false>(oacc, m, kb, vb, mk, qf);
        else att_segment<5, 3, 4, 2, false>(oacc, m, kb, vb, mk, qf);
    }
    {
        u32x4 o;
        if (hh == 0) {
            float il = __builtin_amdgcn_rcpf(oacc[8]);
            asm volatile("s_nop 1" : "+v"(il));
            o = u32x4{pack2(smul(oacc[0], il), smul(oacc[1], il)), pack2(smul(oacc[2], il), smul(oacc[3], il)), pack2(smul(oacc[4], il), smul(oacc[5], il)), pack2(smul(oacc[6], il), smul(oacc[7], il))};
        } else {
            float il = __builtin_amdgcn_rcpf(oacc[0]);
            asm volatile("s_nop 1" : "+v"(il));
            o = u32x4{pack2(smul(oacc[8], il), smul(oacc[9], il)), pack2(smul(oacc[10], il), smul(oacc[11], il)), pack2(smul(oacc[12], il), smul(oacc[13], il)), pack2(smul(oacc[14], il), smul(oacc[15], il))};
        }
        st_sc1(c, lbase + (u32)L_OF + (u32)((4 * c.i + qb) * 32 + 2 * c.j + hh) * 1024, o);
    }
    STAMP(9);
    WAIT_VM(0);
    BAR();
    if (w == 0) signal_group(c, layer, 2);
    if (w >= 4) { issue_chunk<CG(1, 0)>(c); issue_chunk<CG(1, 1)>(c); }
    STAMP(10);
}

__device__ __forceinline__ void red_write(const Ctx& c, const f32x16& acc0, const f32x16& acc1, int t2, int kq) {
#pragma unroll
    for (int d = 0; d < 4; ++d) {
        const int fin = d & 1;
        if (kq != fin) {
            const int slot = kq < fin ? kq : kq - 1;
            const int base = red_off(t2 * 4 + d) + slot * 2048 + c.voff;
            const f32x16& a = d < 2 ? acc0 : acc1;
            const int o = (d & 1) * 8;
            lds_st<f32x4>(c.L, base, f32x4{a[o], a[o + 1], a[o + 2], a[o + 3]});
            lds_st<f32x4>(c.L, base + 1024, f32x4{a[o + 4], a[o + 5], a[o + 6], a[o + 7]});
        }
    }
}
__device__ __forceinline__ void red_read(const Ctx& c, const f32x16& acc0, const f32x16& acc1, int t2, int kq, float (&v)[2][8]) {
#pragma unroll
    for (int e = 0; e < 8; ++e) { v[0][e] = kq ? acc0[8 + e] : acc0[e]; v[1][e] = kq ? acc1[8 + e] : acc1[e]; }
#pragma unroll
    for (int x = 0; x < 2; ++x)
#pragma unroll
        for (int s = 0; s < 3; ++s) {
            const int base = red_off(t2 * 4 + 2 * x + kq) + s * 2048 + c.voff;
            const f32x4 lo = lds_ld<f32x4>(c.L, base), hi = lds_ld<f32x4>(c.L, base + 1024);
            v[x][0] += lo.x; v[x][1] += lo.y; v[x][2] += lo.z; v[x][3] += lo.w; v[x][4] += hi.x; v[x][5] += hi.y; v[x][6] += hi.z; v[x][7] += hi.w;
        }
}
__device__ __forceinline__ u32x4 add_res(const float (&v)[8], const u32x4& res) {
    return u32x4{pack2(v[0] + bflo(res.x), v[1] + bfhi(res.x)), pack2(v[2] + bflo(res.y), v[3] + bfhi(res.y)),
                 pack2(v[4] + bflo(res.z), v[5] + bfhi(res.z)), pack2(v[6] + bflo(res.w), v[7] + bfhi(res.w))};
}

__device__ __forceinline__ void phase_wo(const Ctx& c0, int layer, const Args& a) {
    const Ctx c = fresh(c0);
    const int w = c.w, t2 = w & 1, kq = w >> 1;
    const int th = c.j >> 3, cb = c.j & 7;
    const int tbk = 4 * c.i + 2 * th + t2;
    const u32 lbase = (u32)(WS_L0 + (size_t)layer * WS_LSTRIDE);
    if (w == 0) poll_flags<64>(c, layer, 2, 16);
    if (w >= 4) WAIT_VM(8);
    BAR();
    STAMP(11);
    u32x4 xf[8];
#pragma unroll
    for (int s = 0; s < 8; ++s) xf[s] = ld_sc1(c, lbase + (u32)L_OF + (u32)(tbk * 32 + 8 * kq + s) * 1024);
    u32x4 res[2];
    if (w < 4) {
#pragma unroll
        for (int x = 0; x < 2; ++x) {
            const int kbo = 4 * cb + 2 * x + kq;
            res[x] = ld_sc1(c, (layer == 0 ? (u32)WS_XF0 : (u32)(WS_L0 + L_X2)) + (u32)(tbk * 32 + kbo) * 1024);
        }
    }
    u32x4 wf[16];
#pragma unroll
    for (int ch = 0; ch < 4; ++ch) {
        const LAS unsigned char* wb = lds_base(c, slot_of(CO(ch)) * SLOT + kq * 4096);
#pragma unroll
        for (int q = 0; q < 4; ++q) wf[4 * ch + q] = LDF(wb, q);
    }
    SB();
    f32x16 acc0 = {}, acc1 = {};
#pragma unroll
    for (int s = 0; s < 8; ++s) {
        acc0 = mfma32(wf[2 * s], xf[s], acc0);
        acc1 = mfma32(wf[2 * s + 1], xf[s], acc1);
        SB();
    }
    BAR();
    STAMP(12);
    red_write(c, acc0, acc1, t2, kq);
    BAR();
    if (w < 4) {
        float v[2][8];
        red_read(c, acc0, acc1, t2, kq, v);
#pragma unroll
        for (int x = 0; x < 2; ++x) st_sc1(c, lbase + (u32)L_X1 + (u32)(tbk * 32 + 4 * cb + 2 * x + kq) * 1024, add_res(v[x], res[x]));
        STAMP(13);
        WAIT_VM(0);
    }
    BAR();
    if (w == 0) signal_group(c, layer, 3);
    STAMP(14);
}

template <int T> __device__ __forceinline__ void gu_tile(const Ctx& c, f32x16& acc, const f32x16& prev, u32x4 (&wf)[8], const u32x4 (&xf)[32], int h,
                                                         f32x4 (&cs)[4], const GK& gk, u32 hbase) {
    const LAS unsigned char* cur = lds_base(c, slot_of(CG(T, 0)) * SLOT);
    const LAS unsigned char* nxt = lds_base(c, slot_of(CG(T < 6 ? T + 1 : T, 0)) * SLOT);
    GS g;
    u32 o[4];
    acc = f32x16{};
    SB();
    c_tile<false, (T < 6), false>(acc, wf, cur, cur + SLOT, nxt, xf, [&](int k) {
        const int p = k >> 3, e = 2 * p, q2 = p >> 1, e2 = e & 3;
        if ((k & 7) < 7) geglu_sub(g, o[p], k & 7, prev[e], prev[e + 1], prev[8 + e], prev[8 + e + 1], cs[q2][e2], cs[q2][e2 + 1], cs[2 + q2][e2], cs[2 + q2][e2 + 1], gk);
        if (k == 15) cs[0] = lds_ld<f32x4>(c.L, SC_CS + (96 + T * 32 + 4 * h) * 4);
        if (k == 15) cs[2] = lds_ld<f32x4>(c.L, SC_CS + (96 + T * 32 + 16 + 4 * h) * 4);
        if (k == 31) cs[1] = lds_ld<f32x4>(c.L, SC_CS + (96 + T * 32 + 8 + 4 * h) * 4);
        if (k == 31) cs[3] = lds_ld<f32x4>(c.L, SC_CS + (96 + T * 32 + 16 + 8 + 4 * h) * 4);
    });
    st_sc1(c, hbase + (u32)(T - 1) * 1024, u32x4{o[0], o[1], o[2], o[3]});
}

__device__ __forceinline__ u32 gu_compute(const Ctx& c0, int layer) {
    const Ctx c = fresh(c0);
    const int lane = c.lane, w = c.w, tb = w & 3, h = lane >> 5;
    const u32 lbase = (u32)(WS_L0 + (size_t)layer * WS_LSTRIDE);
    u32 pf1 = 0;
    {
        u32x4 xf[32], wf[8];
        const LAS unsigned char* w0 = lds_base(c, slot_of(CG(0, 0)) * SLOT);
        const LAS unsigned char* w1 = lds_base(c, slot_of(CG(1, 0)) * SLOT);
        f32x16 a0 = {}, a1;
        const f32x2 ms = c_tile0<true, false, false, false>(c, a0, wf, w0, w0 + SLOT, w1, xf, c.rs, lbase + (u32)L_X1 + (u32)((4 * c.i + tb) * 32) * 1024, [&]() {
            if (w == 0) poll_flags<64>(c, layer, 3, 16);
            BAR();
            STAMP(15);
        });
        constexpr float GC1 = -2.0f * 0.7978845608028654f * 1.4426950408889634f, GC2 = GC1 * 0.044715f;
        GK gk; gk.mean = ms.x; gk.A = ms.y * GC1; gk.B = ms.y * ms.y * ms.y * GC2; gk.ir2 = 1.0f / (ms.y * ms.y);
        BARL(); STAMP(16);
        f32x4 cs[4];
#pragma unroll
        for (int q = 0; q < 4; ++q) cs[q] = lds_ld<f32x4>(c.L, SC_CS + (96 + (q >> 1) * 16 + 8 * (q & 1) + 4 * h) * 4);
        const u32 hbase = lbase + (u32)L_HF + (u32)((4 * c.i + tb) * 112 + 7 * c.j) * 1024;
        gu_tile<1>(c, a1, a0, wf, xf, h, cs, gk, hbase); BARL();
        gu_tile<2>(c, a0, a1, wf, xf, h, cs, gk, hbase); BARL();
        gu_tile<3>(c, a1, a0, wf, xf, h, cs, gk, hbase); BARL();
        gu_tile<4>(c, a0, a1, wf, xf, h, cs, gk, hbase); BARL();
        gu_tile<5>(c, a1, a0, wf, xf, h, cs, gk, hbase);
        WAIT_VM(1);
        BARL();
        if (w == 0) signal_group(c, layer, 7);
        gu_tile<6>(c, a0, a1, wf, xf, h, cs, gk, hbase);
        STAMP(22);
        if (w == 0) pf1 = __builtin_amdgcn_raw_buffer_load_b32(c.rs, flag_off(layer, 7, c.i, c.j) + (lane & 15) * 4, 0, 16);
        {
            asm volatile("s_nop 15" : "+v"(a0));
            GS g[4];
            u32 o[4];
#pragma unroll
            for (int q = 0; q < 7; ++q)
#pragma unroll
                for (int p = 0; p < 4; ++p) {
                    const int e = 2 * p, q2 = p >> 1, e2 = e & 3;
                    geglu_sub(g[p], o[p], q, a0[e], a0[e + 1], a0[8 + e], a0[8 + e + 1], cs[q2][e2], cs[q2][e2 + 1], cs[2 + q2][e2], cs[2 + q2][e2 + 1], gk);
                }
            st_sc1(c, hbase + 6u * 1024, u32x4{o[0], o[1], o[2], o[3]});
        }
        STAMP(21);
        WAIT_VM(0);
        BAR();
        STAMP(23);
        if (w == 0) signal_group(c, layer, 4);
    }
    return pf1;
}
__device__ __forceinline__ void gu_helper(const Ctx& c0, int layer, u32x4 (&wr)[40]) {
    const Ctx c = fresh(c0);
    const int lane = c.lane, w = c.w;
    {
        WAIT_VM(0);
        BAR();
#define EXP_SLEEP 16
        for (int z_ = 0; z_ < EXP_SLEEP; ++z_) __builtin_amdgcn_s_sleep(1);
        issue_chunk<CG(2, 0)>(c); issue_chunk<CG(2, 1)>(c); issue_chunk<CG(3, 0)>(c); issue_chunk<CG(3, 1)>(c);
        WAIT_VM(8);
        BAR();
        const u32 wdb = (u32)WS_WD + (u32)((c.j & 7) * 224 + 112 + ((w >> 1) - 2) * 56) * 1024;
#pragma unroll
        for (int q = 0; q < 20; ++q) wr[q] = __builtin_amdgcn_raw_buffer_load_b128(c.rs, c.voff, wdb + q * 1024, 16);
        issue_chunk<CG(4, 0)>(c); issue_chunk<CG(4, 1)>(c);
        WAIT_VM(8);
        BAR();
#pragma unroll
        for (int q = 20; q < 40; ++q) wr[q] = __builtin_amdgcn_raw_buffer_load_b128(c.rs, c.voff, wdb + q * 1024, 16);
        issue_chunk<CG(5, 0)>(c); issue_chunk<CG(5, 1)>(c);
        WAIT_VM(8);
        BAR();
        issue_chunk<CG(6, 0)>(c); issue_chunk<CG(6, 1)>(c);
        WAIT_VM(8);
        BAR();
        issue_chunk<CD(0)>(c); issue_chunk<CD(1)>(c);
        WAIT_VM(8);
        BAR();
        issue_chunk<CD(2)>(c); issue_chunk<CD(3)>(c);
        BAR();
        issue_chunk<CD(4)>(c); issue_chunk<CD(5)>(c);
        WAIT_VM(0);
        BAR();
        issue_chunk<CD(6)>(c);
    }
}

__device__ __forceinline__ void hld(const Ctx& c, u32x4& dst, u32 soff) {
    asm volatile("buffer_load_dwordx4 %0, %1, %2, %3 offen sc1" : "=v"(dst) : "v"(c.voff), "s"(c.rs), "s"(soff) : "memory");
}
template <int N> __device__ __forceinline__ void hwait(u32x4& x) { asm volatile("s_waitcnt vmcnt(%1)" : "+v"(x) : "i"(N) : "memory"); }
template <bool HELPER>
__device__ __forceinline__ void dn_main(const Ctx& c, int layer, int kq, u32 xsrc, u32 wdb, const u32x4* wr, f32x16& acc0, f32x16& acc1) {
    constexpr int XD = 10;
    u32x4 xf[28], wf[8], wl[16];
    const LAS unsigned char* wb[7];
#pragma unroll
    for (int s = 0; s < XD; ++s) hld(c, xf[s], xsrc + (u32)dn_kstep(s) * 1024);
    if (!HELPER) {
#pragma unroll
        for (int ch = 0; ch < 7; ++ch) wb[ch] = lds_base(c, chunk_lds(CD(ch)) + kq * 8192);
#pragma unroll
        for (int q = 0; q < 8; ++q) wf[q] = LDF(wb[0], q);
    }
    SB();
    u32 pf = 0;
#pragma unroll
    for (int s = 0; s < 28; ++s) {
        if (s + XD < 28) hld(c, xf[s + XD], xsrc + (u32)dn_kstep(s + XD) * 1024);
        u32x4 w0, w1;
        if (HELPER) {
            if (s < 20) { w0 = wr[2 * s]; w1 = wr[2 * s + 1]; } else { w0 = wl[2 * (s - 20)]; w1 = wl[2 * (s - 20) + 1]; }
        } else {
            w0 = wf[(2 * s) & 7]; w1 = wf[(2 * s + 1) & 7];
            if (s + 4 < 28) { wf[(2 * s) & 7] = LDF(wb[(s + 4) >> 2], 2 * ((s + 4) & 3)); wf[(2 * s + 1) & 7] = LDF(wb[(s + 4) >> 2], 2 * ((s + 4) & 3) + 1); }
        }
        if (s + XD < 28) { if (HELPER && s >= 8 && s < 12 + XD - 10 + 10 && s <= 21) hwait<10>(xf[s]); else hwait<10>(xf[s]); }
        else if (s == 18) hwait<9>(xf[s]); else if (s == 19) hwait<8>(xf[s]); else if (s == 20) hwait<7>(xf[s]); else if (s == 21) hwait<6>(xf[s]); else if (s == 22) hwait<5>(xf[s]);
        else if (s == 23) hwait<4>(xf[s]); else if (s == 24) hwait<3>(xf[s]); else if (s == 25) hwait<2>(xf[s]); else if (s == 26) hwait<1>(xf[s]); else hwait<0>(xf[s]);
        if (HELPER && s == 20) {
            asm volatile("s_waitcnt vmcnt(6)" : "+v"(wl[0]), "+v"(wl[1]), "+v"(wl[2]), "+v"(wl[3]), "+v"(wl[4]), "+v"(wl[5]), "+v"(wl[6]), "+v"(wl[7]) :: "memory");
            asm volatile("" : "+v"(wl[8]), "+v"(wl[9]), "+v"(wl[10]), "+v"(wl[11]), "+v"(wl[12]), "+v"(wl[13]), "+v"(wl[14]), "+v"(wl[15]));
            w0 = wl[0]; w1 = wl[1];
        }
        acc0 = mfma32(w0, xf[s], acc0);
        acc1 = mfma32(w1, xf[s], acc1);
        PIN(acc0); PIN(acc1);
        if (HELPER && s >= 8 && s < 12) {
#pragma unroll
            for (int q = 0; q < 4; ++q) hld(c, wl[4 * (s - 8) + q], wdb + (u32)(40 + 4 * (s - 8) + q) * 1024);
        }
        if (!HELPER && s == 2 && c.w == 0)
            asm volatile("buffer_load_dword %0, %1, %2, 0 offen sc1" : "=v"(pf) : "v"(flag_off(layer, 4, c.i, c.j) + (c.lane & 15) * 4), "s"(c.rs) : "memory");
        if (s + XD == 15) {
            if (!HELPER && c.w == 0) {
                asm volatile("s_waitcnt vmcnt(3)" : "+v"(pf) :: "memory");
                if (!__all(pf == flag_tag(c, layer, 4))) poll_flags<64>(c, layer, 4, 16);
            }
            BARL();
        }
        SB();
    }
}

__device__ __forceinline__ void phase_ffn(const Ctx& c0, int layer, const Args& a) {
    f32x16 acc0 = {}, acc1 = {};
    u32x4 res[2];
    if (c0.w < 4) {
        const u32 pf1 = gu_compute(c0, layer);
        const Ctx c = fresh(c0);
        const int w = c.w, t2 = w & 1, kq = w >> 1, th = c.j >> 3, cb = c.j & 7;
        const int tbk = 4 * c.i + 2 * th + t2;
        const u32 lbase = (u32)(WS_L0 + (size_t)layer * WS_LSTRIDE);
        res[0] = ld_sc1(c, lbase + (u32)L_X1 + (u32)(tbk * 32 + 4 * cb + kq) * 1024);
        res[1] = ld_sc1(c, lbase + (u32)L_X1 + (u32)(tbk * 32 + 4 * cb + 2 + kq) * 1024);
        if (w == 0 && !__all(pf1 == flag_tag(c, layer, 7))) poll_flags<64>(c, layer, 7, 16);
        BARL();
        STAMP(25);
        dn_main<false>(c, layer, kq, lbase + (u32)L_HF + (u32)(tbk * 112 + 28 * kq) * 1024, 0u, nullptr, acc0, acc1);
    } else {
        u32x4 wr[40];
        gu_helper(c0, layer, wr);
        const Ctx c = fresh(c0);
        const int w = c.w, t2 = w & 1, kq = w >> 1, th = c.j >> 3, cb = c.j & 7;
        const int tbk = 4 * c.i + 2 * th + t2;
        const u32 lbase = (u32)(WS_L0 + (size_t)layer * WS_LSTRIDE);
        BARL();
        dn_main<true>(c, layer, kq, lbase + (u32)L_HF + (u32)(tbk * 112 + 28 * kq) * 1024, (u32)WS_WD + (u32)(cb * 224 + 112 + (kq - 2) * 56) * 1024, wr, acc0, acc1);
    }
    const Ctx c = fresh(c0);
    const int lane = c.lane, w = c.w, t2 = w & 1, kq = w >> 1, h = lane >> 5, r = lane & 31;
    const int th = c.j >> 3, cb = c.j & 7;
    const int tbk = 4 * c.i + 2 * th + t2;
    const u32 lbase = (u32)(WS_L0 + (size_t)layer * WS_LSTRIDE);
    BAR();
    STAMP(26);
    red_write(c, acc0, acc1, t2, kq);
    BAR();
    if (w >= 4) {
        if (layer == 0) { issue_chunk<CQ(0, 0)>(c); issue_chunk<CQ(0, 1)>(c); }
    } else {
        float v[2][8];
        red_read(c, acc0, acc1, t2, kq, v);
#pragma unroll
        for (int x = 0; x < 2; ++x) {
            const int kbo = 4 * cb + 2 * x + kq;
            const u32x4 o = add_res(v[x], res[x]);
            const auto r0 = __builtin_amdgcn_permlane32_swap(o.x, o.z, false, false);
            const auto r1 = __builtin_amdgcn_permlane32_swap(o.y, o.w, false, false);
            const u32x4 on = u32x4{r0[0], r1[0], r0[1], r1[1]};
            if (layer == 1) {
                *(u32x4*)(a.out + (size_t)(tbk * 32 + r) * DM + kbo * 16 + 8 * h) = on;
            } else {
                st_sc1(c, lbase + (u32)L_X2 + (u32)(tbk * 32 + kbo) * 1024, o);
            }
        }
        STAMP(27);
        if (layer == 0) WAIT_VM(0);
    }
    if (layer == 0) {
        BAR();
        if (w == 0) signal_group(c, layer, 0);
        if (w >= 4) { issue_chunk<CQ(1, 0)>(c); issue_chunk<CQ(1, 1)>(c); issue_chunk<CQ(2, 0)>(c); issue_chunk<CQ(2, 1)>(c); }
        STAMP(28);
    }
}

__global__ void __launch_bounds__(512, 2) mega_kernel(Args a) {
    extern __shared__ __attribute__((aligned(16))) unsigned char lds[];
    Ctx c;
    c.L = (LAS unsigned char*)lds;
    c.Laddr = (u32)(size_t)c.L;
    c.ws = a.ws;
    c.rs = __builtin_amdgcn_make_buffer_rsrc((void*)a.ws, 0, (int)(WS_END + 4 * MiB), 0x00020000);
    const int tid = threadIdx.x;
    c.lane = tid & 63;
    c.w = __builtin_amdgcn_readfirstlane(tid >> 6);
    const int bid = blockIdx.x;
#define MAPV 3
    if (MAPV == 1) { c.i = 2 * (bid & 7) + (bid >> 7); c.j = (bid >> 3) & 15; }
    else if (MAPV == 3) { c.i = 4 * ((bid & 7) >> 1) + (bid >> 6); c.j = 8 * (bid & 1) + ((bid >> 3) & 7); }
    else { c.i = 8 * ((bid & 7) >> 2) + (bid >> 5); c.j = 4 * (bid & 3) + ((bid >> 3) & 3); }
    c.voff = c.lane * 16;
    c.tag = 1u;
    if (c.w >= 4) {
        issue_chunk<CQ(0, 0)>(c); issue_chunk<CQ(0, 1)>(c); issue_chunk<CQ(1, 0)>(c); issue_chunk<CQ(1, 1)>(c); issue_chunk<CQ(2, 0)>(c); issue_chunk<CQ(2, 1)>(c);
    } else if (tid < 96) {
        lds_st<float>(c.L, SC_CS + tid * 4, ((const float*)(a.ws + WS_CS))[qkv_col(tid >> 5) + 32 * c.j + (tid & 31)]);
    }
    if (c.w < 4) {
        const int hh = c.lane >> 5, row = c.lane & 31, half = c.w & 1;
        u32 d[4];
#pragma unroll
        for (int q = 0; q < 4; ++q) {
            const int c0 = 16 * half + 8 * (q >> 1) + 4 * hh + 2 * (q & 1), c1 = c0 + 1;
            const u32 lo = c.w < 2 ? (row > c0 ? 0xff7fu : 0u) : (row == c0 ? 0x3f80u : 0u);
            const u32 hi = c.w < 2 ? (row > c1 ? 0xff7fu : 0u) : (row == c1 ? 0x3f80u : 0u);
            d[q] = lo | (hi << 16);
        }
        lds_st<u32x4>(c.L, SC_MK + c.w * 1024 + c.voff, u32x4{d[0], d[1], d[2], d[3]});
    }
    for (int layer = 0; layer < 2; ++layer) {
        phase_qkv_att(c, layer, a);
        phase_wo(c, layer, a);
        phase_ffn(c, layer, a);
    }
}

}

extern "C" void kernel_launch(void* const* d_in, const int* in_sizes, int n_in, void* d_out, int out_size, void* d_ws, size_t ws_size,
                              hipStream_t stream) {
    Args a;
    a.x = (const u16*)d_in[0]; a.wqkv = (const u16*)d_in[1]; a.wo = (const u16*)d_in[2];
    a.wg = (const u16*)d_in[4]; a.wu = (const u16*)d_in[5]; a.wd = (const u16*)d_in[6];
    a.out = (u16*)d_out; a.ws = (unsigned char*)d_ws;
    (void)hipFuncSetAttribute((const void*)mega_kernel, hipFuncAttributeMaxDynamicSharedMemorySize, LDS_BYTES);
    prep_kernel<<<PREP_GRID, 512, 0, stream>>>(a);
    mega_kernel<<<256, 512, LDS_BYTES, stream>>>(a);
}
```

```cpp
#include <hip/hip_runtime.h>
#include <hip/hip_bf16.h>

#define LAS __attribute__((address_space(3)))
#define GAS __attribute__((address_space(1)))
typedef unsigned u32;
typedef unsigned short u16;
typedef u32 u32x4 __attribute__((ext_vector_type(4)));
typedef u32 u32x2 __attribute__((ext_vector_type(2)));
typedef float f32x16 __attribute__((ext_vector_type(16)));
typedef float f32x4 __attribute__((ext_vector_type(4)));
typedef float f32x2 __attribute__((ext_vector_type(2)));
typedef __bf16 bf16x8_t __attribute__((ext_vector_type(8)));
typedef __bf16 bf16x2_t __attribute__((ext_vector_type(2)));
typedef short s16x4 __attribute__((ext_vector_type(4)));

namespace {

constexpr int SEQ = 2048, DM = 512, FF = 1792;
constexpr size_t KiB = 1024, MiB = 1024 * 1024;
constexpr size_t WS_FLAGS = 1 * MiB;
constexpr size_t WS_EPOCH = 640 * KiB;
constexpr size_t WS_STARTED = WS_EPOCH + 4096;
constexpr size_t WS_TMO = 768 * KiB;
constexpr size_t WS_CS = 512 * KiB;
constexpr size_t WS_XF0 = 12 * MiB;
constexpr size_t WS_WQ = 14 * MiB;
constexpr size_t WS_CSP = 3 * MiB + 256 * KiB;
constexpr size_t WS_WO = 4 * MiB + 512 * KiB;
constexpr size_t WS_WG = 5 * MiB;
constexpr size_t WS_WD = 9 * MiB;
constexpr size_t WS_L0 = 16 * MiB, WS_LSTRIDE = 32 * MiB;
constexpr size_t L_KF = 0, L_VF = 2 * MiB, L_OF = 4 * MiB, L_X1 = 6 * MiB, L_X2 = 8 * MiB, L_HF = 10 * MiB, L_X2N = 18 * MiB;
constexpr size_t WS_END = WS_L0 + 2 * WS_LSTRIDE;

constexpr int NSLOT = 8, SLOT = 16384;
constexpr int LDS_SCR = NSLOT * SLOT;
constexpr int VREC = 1040;
constexpr int SC_RV = LDS_SCR;
constexpr int SC_VS = SC_RV + 16640;
constexpr int SC_ST = SC_VS + 8320;
constexpr int SC_CS = SC_ST + 1024;
constexpr int SC_MK = SC_CS + 1280;
constexpr int LDS_BYTES = SC_MK + 4096;
constexpr int SC_KS = 0 * SLOT;
constexpr int SC_QS = 0 * SLOT + 8192;
constexpr int SC_RK = 1 * SLOT;
static_assert(LDS_BYTES <= 163840, "LDS");
__device__ __forceinline__ int red_off(int q) { return q < 5 ? 2 * SLOT + q * 6144 : 6 * SLOT + (q - 5) * 6144; }

constexpr float LN_EPS = 1e-5f;
constexpr float QSCALE = 0.25f * 1.4426950408889634f;
constexpr u32 SPIN_CAP = 1u << 17;
constexpr u32 BF_ONES = 0x3f803f80u;

struct Args {
    const u16* x; const u16* wqkv; const u16* wo; const u16* wg; const u16* wu; const u16* wd;
    u16* out; unsigned char* ws;
};

__device__ __forceinline__ f32x16 mfma32(u32x4 a, u32x4 b, f32x16 c) {
    return __builtin_amdgcn_mfma_f32_32x32x16_bf16(__builtin_bit_cast(bf16x8_t, a), __builtin_bit_cast(bf16x8_t, b), c, 0, 0, 0);
}
__device__ __forceinline__ u32 pack2(float lo, float hi) {
    f32x2 v = {lo, hi};
    return __builtin_bit_cast(u32, __builtin_convertvector(v, bf16x2_t));
}
__device__ __forceinline__ float bflo(u32 u) { return __builtin_bit_cast(float, u << 16); }
__device__ __forceinline__ float bfhi(u32 u) { return __builtin_bit_cast(float, u & 0xffff0000u); }
__device__ __forceinline__ float dot2(u32 a, u32 b, float c) {
    return __builtin_amdgcn_fdot2_f32_bf16(__builtin_bit_cast(bf16x2_t, a), __builtin_bit_cast(bf16x2_t, b), c, false);
}
__device__ __forceinline__ void swap32(float& a, float& b) {
    asm volatile("s_nop 1\n\tv_permlane32_swap_b32 %0, %1\n\ts_nop 1" : "+v"(a), "+v"(b));
}
__device__ __forceinline__ float fmx(float a, float b) { return __builtin_elementwise_maximum(a, b); }
__device__ __forceinline__ float xor32_max(float v) { float a = v, b = v; swap32(a, b); return fmx(a, b); }
__device__ __forceinline__ float xor32_sum(float v) { float a = v, b = v; swap32(a, b); return a + b; }
#define BAR() do { asm volatile("s_waitcnt lgkmcnt(0)" ::: "memory"); __builtin_amdgcn_s_barrier(); asm volatile("" ::: "memory"); } while (0)
#define BARL() do { asm volatile("" ::: "memory"); __builtin_amdgcn_s_barrier(); asm volatile("" ::: "memory"); } while (0)
#define WAIT_VM(n) asm volatile("s_waitcnt vmcnt(" #n ")" ::: "memory")
#define SB() __builtin_amdgcn_sched_barrier(0)

template <class T> __device__ __forceinline__ T lds_ld(const LAS unsigned char* base, int off) { return *(const LAS T*)(base + off); }
template <class T> __device__ __forceinline__ void lds_st(LAS unsigned char* base, int off, T v) { *(LAS T*)(base + off) = v; }

__device__ __forceinline__ void glds4k(const unsigned char* src, u32 voff, u32 dst) {
    u32 keep;
    asm volatile(
        "s_mov_b32 %0, m0\n\t"
        "s_mov_b32 m0, %3\n\t"
        "s_nop 0\n\t"
        "global_load_lds_dwordx4 %1, %2 sc1\n\t"
        "global_load_lds_dwordx4 %1, %2 offset:1024 sc1\n\t"
        "global_load_lds_dwordx4 %1, %2 offset:2048 sc1\n\t"
        "global_load_lds_dwordx4 %1, %2 offset:3072 sc1\n\t"
        "s_mov_b32 m0, %0"
        : "=&s"(keep) : "v"(voff), "s"(src), "s"(dst) : "memory");
}

__device__ __forceinline__ void glds1k(const unsigned char* src, u32 voff, u32 dst) {
    u32 keep;
    asm volatile(
        "s_mov_b32 %0, m0\n\t"
        "s_mov_b32 m0, %3\n\t"
        "s_nop 0\n\t"
        "global_load_lds_dwordx4 %1, %2\n\t"
        "s_mov_b32 m0, %0"
        : "=&s"(keep) : "v"(voff), "s"(src), "s"(dst) : "memory");
}
#define PIN(x) asm volatile("" : "+v"(x))

constexpr int NF_WO = 8 * 64, NF_WG = 16 * 224, NF_WD = 8 * 224;
__host__ __device__ constexpr int qkv_col(int t) { return t == 0 ? 512 : (t == 1 ? 1024 : 0); }
__host__ __device__ constexpr int dn_kstep(int s) { return s < 16 ? 7 * (s >> 2) + (s & 3) : 7 * ((s - 16) / 3) + 4 + (s - 16) % 3; }
struct WItem { const u16* src; size_t dst; };
__device__ __forceinline__ WItem w_item(const Args& a, int f, int lane) {
    const u16 *pA, *pB; int ld, ks; size_t dst;
    if (f < NF_WO) {
        const int cb = f >> 6, rem = f & 63, c = rem >> 4, r2 = rem & 15, kq = r2 >> 2, ks2 = (r2 >> 1) & 1, nt = r2 & 1;
        ks = 8 * kq + 2 * c + ks2; pA = a.wo + 64 * cb + 32 * nt; pB = pA + 16; ld = 512; dst = WS_WO + (size_t)f * 1024;
    } else if ((f -= NF_WO) < NF_WG) {
        const int j = f / 224, rem = f % 224, q = rem >> 4, ksl = rem & 15, c = q >> 1, hf = q & 1;
        ks = 16 * hf + ksl; pA = a.wg + 112 * j + 16 * c; pB = a.wu + 112 * j + 16 * c; ld = FF; dst = WS_WG + (size_t)f * 1024;
    } else {
        f -= NF_WG;
        const int cb = f / 224, rem = f % 224;
        int kq, sidx, nt;
        if (rem < 112) { const int c = rem >> 4, r2 = rem & 15; kq = r2 >> 3; sidx = 4 * c + ((r2 >> 1) & 3); nt = r2 & 1; }
        else { const int g = rem - 112; kq = 2 + g / 56; sidx = (g % 56) >> 1; nt = g & 1; }
        ks = 28 * kq + dn_kstep(sidx); pA = a.wd + 64 * cb + 32 * nt; pB = pA + 16; ld = 512; dst = WS_WD + (size_t)f * 1024;
    }
    const int k = lane >> 2, n8 = lane & 3;
    WItem it;
    it.src = (n8 < 2 ? pA + n8 * 8 : pB + (n8 - 2) * 8) + (size_t)(16 * ks + k) * ld;
    it.dst = dst;
    return it;
}

constexpr int PREP_XI = 64 * 8, NF_WQ = 16 * 96;
constexpr int PREP_WG_TILE = (PREP_XI + NF_WQ) / 8;
constexpr int PREP_WG_CS = 1536 / 64, PREP_WG_FLAGS = 16;
constexpr int PREP_GRID = PREP_WG_TILE + PREP_WG_CS + PREP_WG_FLAGS;
constexpr int PREP_LDS_WAVE = 4608;
constexpr size_t FLAG_BYTES = 2 * 8 * 256 * 512;
__global__ void __launch_bounds__(512) prep_kernel(Args a) {
    __shared__ __attribute__((aligned(16))) unsigned char sm[8 * PREP_LDS_WAVE];
    const int tid = threadIdx.x, lane = tid & 63, w = tid >> 6;
    const int b0 = blockIdx.x;
    unsigned char* ws = a.ws;
    if (b0 < PREP_WG_CS) {
        const u16* src = a.wqkv + b0 * 64;
        const int rg = lane >> 3, ch = lane & 7;
        u32x4 v[8];
#pragma unroll
        for (int it = 0; it < 8; ++it) v[it] = *(const u32x4*)(src + (size_t)(64 * w + 8 * it + rg) * 1536 + ch * 8);
        float s[8];
#pragma unroll
        for (int e = 0; e < 8; ++e) s[e] = 0.f;
#pragma unroll
        for (int it = 0; it < 8; ++it) {
            s[0] += bflo(v[it].x); s[1] += bfhi(v[it].x); s[2] += bflo(v[it].y); s[3] += bfhi(v[it].y);
            s[4] += bflo(v[it].z); s[5] += bfhi(v[it].z); s[6] += bflo(v[it].w); s[7] += bfhi(v[it].w);
        }
#pragma unroll
        for (int e = 0; e < 8; ++e) { s[e] += __shfl_xor(s[e], 8); s[e] += __shfl_xor(s[e], 16); s[e] += __shfl_xor(s[e], 32); }
        float* red = (float*)sm;
        if (lane < 8) {
#pragma unroll
            for (int e = 0; e < 8; ++e) red[w * 64 + lane * 8 + e] = s[e];
        }
        __syncthreads();
        if (tid < 64) ((float*)(ws + WS_CS))[b0 * 64 + tid] = ((red[tid] + red[64 + tid]) + (red[128 + tid] + red[192 + tid])) + ((red[256 + tid] + red[320 + tid]) + (red[384 + tid] + red[448 + tid]));
    } else if (b0 < PREP_WG_CS + PREP_WG_FLAGS) {
        const int g = b0 - PREP_WG_CS;
        const u32x4 z = {0u, 0u, 0u, 0u};
        for (size_t o = (size_t)(g * 512 + tid) * 16; o < FLAG_BYTES; o += (size_t)PREP_WG_FLAGS * 512 * 16) *(u32x4*)(ws + WS_FLAGS + o) = z;
    } else {
        const int item = (b0 - PREP_WG_CS - PREP_WG_FLAGS) * 8 + w;
        LAS unsigned char* t = (LAS unsigned char*)sm + w * PREP_LDS_WAVE;
        if (item < PREP_XI) {
            const int tb = item >> 3, g = item & 7;
            const u16* src = a.x + (size_t)(tb * 32) * DM + g * 64;
            u32x4 v[4];
#pragma unroll
            for (int q = 0; q < 4; ++q) v[q] = *(const u32x4*)(src + (size_t)(8 * q + (lane >> 3)) * DM + (lane & 7) * 8);
#pragma unroll
            for (int q = 0; q < 4; ++q) lds_st<u32x4>(t, (8 * q + (lane >> 3)) * 144 + (lane & 7) * 16, v[q]);
            asm volatile("s_waitcnt lgkmcnt(0)" ::: "memory");
            const int h = lane >> 5, r = lane & 31;
#pragma unroll
            for (int kbl = 0; kbl < 4; ++kbl) {
                const u32x2 lo = lds_ld<u32x2>(t, r * 144 + kbl * 32 + 8 * h);
                const u32x2 hi = lds_ld<u32x2>(t, r * 144 + kbl * 32 + 16 + 8 * h);
                *(u32x4*)(ws + WS_XF0 + (size_t)(tb * 32 + 4 * g + kbl) * 1024 + lane * 16) = u32x4{lo.x, lo.y, hi.x, hi.y};
            }
        } else {
            const int f = item - PREP_XI;
            const int j = f / 96, rem = f % 96, q = rem >> 4, ksl = rem & 15, ct = q >> 1, hf = q & 1;
            const int k = lane >> 2, n8 = lane & 3;
            const u32x4 v = *(const u32x4*)(a.wqkv + qkv_col(ct) + 32 * j + n8 * 8 + (size_t)(16 * (16 * hf + ksl) + k) * 1536);
            lds_st<u32x4>(t, k * 64 + n8 * 16, v);
            asm volatile("s_waitcnt lgkmcnt(0)" ::: "memory");
            const int g4 = lane >> 4, h = g4 >> 1, nh = g4 & 1, li = lane & 15, qq = li >> 2, p = li & 3;
            const s16x4 lo = __builtin_amdgcn_ds_read_tr16_b64_v4i16((LAS s16x4*)(t + (4 * h + qq) * 64 + nh * 32 + p * 8));
            const s16x4 hi = __builtin_amdgcn_ds_read_tr16_b64_v4i16((LAS s16x4*)(t + (8 + 4 * h + qq) * 64 + nh * 32 + p * 8));
            const u32x2 l2 = __builtin_bit_cast(u32x2, lo), h2 = __builtin_bit_cast(u32x2, hi);
            *(u32x4*)(ws + WS_WQ + (size_t)f * 1024 + lane * 16) = u32x4{l2.x, l2.y, h2.x, h2.y};
        }
    }
}

#define STAMP(k) do { } while (0)
struct Ctx {
    LAS unsigned char* L;
    u32 Laddr;
    unsigned char* ws;
    __amdgpu_buffer_rsrc_t rs;
    int lane, w, i, j;
    u32 voff;
    u32 tag;
};

__device__ __forceinline__ u32x4 ld_sc1(const Ctx& c, u32 soff) { return __builtin_amdgcn_raw_buffer_load_b128(c.rs, c.voff, soff, 16); }
__device__ __forceinline__ void st_sc1(const Ctx& c, u32 soff, u32x4 v) { __builtin_amdgcn_raw_buffer_store_b128(v, c.rs, c.voff, soff, 16); }
__device__ __forceinline__ Ctx fresh(const Ctx& c0) {
    Ctx c = c0;
    asm volatile("" : "+s"(c.i), "+s"(c.j), "+s"(c.w), "+s"(c.tag));
    asm volatile("" : "+v"(c.lane));
    c.voff = c.lane * 16;
    return c;
}

constexpr int CQ(int t, int hf) { return 2 * t + hf; }
constexpr int CO(int k) { return 6 + k; }
constexpr int CG(int t, int hf) { return 10 + 2 * t + hf; }
constexpr int CD(int k) { return 24 + k; }
constexpr int slot_of(int n) {
    constexpr int tab[38] = {
          0, 1,   2, 3,   4, 5,
          6, 7, 2, 3,
          4, 5,   0, 1,   2, 3,   6, 7,   4, 5,   0, 1,   2, 3,
          6, 7, 4, 5, 0, 1, 2,   0, 0, 0, 0, 0, 0, 0};
    return tab[n];
}
template <int N> __device__ __forceinline__ const unsigned char* chunk_src(const Ctx& c) {
    size_t src;
    const int cb = c.j & 7;
    if (N < 6) src = WS_WQ + (size_t)(c.j * 6 + N) * SLOT;
    else if (N < 10) src = WS_WO + (size_t)(cb * 4 + (N - 6)) * SLOT;
    else if (N < 24) src = WS_WG + (size_t)(c.j * 14 + (N - 10)) * SLOT;
    else src = WS_WD + (size_t)(cb * 14 + (N - 24)) * SLOT;
    return c.ws + src + (c.w & 3) * 4096;
}
constexpr int chunk_lds(int n) { return slot_of(n) * SLOT; }
template <int N> __device__ __forceinline__ void issue_chunk(const Ctx& c) {
    glds4k(chunk_src<N>(c), c.voff, c.Laddr + chunk_lds(N) + (c.w & 3) * 4096);
}
__device__ __forceinline__ const LAS unsigned char* lds_base(const Ctx& c, int off) {
    u32 a = c.Laddr + off + c.voff;
    asm volatile("" : "+v"(a));
    return (const LAS unsigned char*)(size_t)a;
}

__device__ __forceinline__ u32 flag_off(int layer, int seam, int ci, int cj) {
    return (u32)(WS_FLAGS + (size_t)(((layer * 8 + seam) * 256) + ci * 16 + cj) * 512);
}
__device__ __forceinline__ u32 flag_tag(const Ctx& c, int layer, int seam) { return c.tag + (u32)(layer * 8 + seam); }
__device__ __forceinline__ void flag_store(const Ctx& c, int layer, int seam, int ci, int cj, int word) {
    __builtin_amdgcn_raw_buffer_store_b32(flag_tag(c, layer, seam), c.rs, flag_off(layer, seam, ci, cj) + word * 4, 0, 16);
}
__device__ __forceinline__ void signal_group(const Ctx& c, int layer, int seam) {
    if (c.lane < 16) flag_store(c, layer, seam, c.i, c.lane, c.j);
}
template <int NWORDS> __device__ __forceinline__ void poll_flags(const Ctx& c, int layer, int seam, int nwords = NWORDS) {
    const u32 off = flag_off(layer, seam, c.i, c.j) + c.lane * 4;
    const u32 tag = flag_tag(c, layer, seam);
    for (u32 spins = 0;; ++spins) {
        asm volatile("" ::: "memory");
        const u32 f0 = __builtin_amdgcn_raw_buffer_load_b32(c.rs, off, 0, 16);
        bool ok = c.lane >= nwords || f0 == tag;
        if (NWORDS > 64) { const u32 f1 = __builtin_amdgcn_raw_buffer_load_b32(c.rs, off + 256, 0, 16); ok = ok && f1 == tag; }
        if (__all(ok)) break;
        if (spins > SPIN_CAP) { if (c.lane == 0) __builtin_amdgcn_raw_buffer_store_b32(1u, c.rs, (u32)WS_TMO, 0, 16); break; }
        __builtin_amdgcn_s_sleep(1);
    }
}

__device__ __forceinline__ int crow(int r, int h) { return (r & 3) + 8 * (r >> 2) + 4 * h; }

#define LDF(p, k) (*(const LAS u32x4*)((p) + (k) * 1024))
__device__ __forceinline__ u32x4 ldf_tr(const LAS unsigned char* p, int k) {
    const u32x2 lo = __builtin_bit_cast(u32x2, __builtin_amdgcn_ds_read_tr16_b64_v4i16((LAS s16x4*)(p + k * 1024)));
    const u32x2 hi = __builtin_bit_cast(u32x2, __builtin_amdgcn_ds_read_tr16_b64_v4i16((LAS s16x4*)(p + k * 1024 + 256)));
    return u32x4{lo.x, lo.y, hi.x, hi.y};
}
template <bool TR> __device__ __forceinline__ u32x4 ldf(const LAS unsigned char* p, int k) { if constexpr (TR) return ldf_tr(p, k); else return LDF(p, k); }

template <bool SWAP, bool PRE, bool TR, class F>
__device__ __forceinline__ void c_tile(f32x16& acc, u32x4 (&wf)[8], const LAS unsigned char* lo, const LAS unsigned char* hi, const LAS unsigned char* nxt,
                                       const u32x4 (&xf)[32], F&& filler) {
#pragma unroll
    for (int k = 0; k < 32; ++k) {
        const u32x4 wv = wf[k & 7];
        if (k + 8 < 16) wf[k & 7] = ldf<TR>(lo, k + 8);
        else if (k + 8 < 32) wf[k & 7] = ldf<TR>(hi, k + 8 - 16);
        else if (PRE) wf[k & 7] = ldf<TR>(nxt, k + 8 - 32);
        acc = SWAP ? mfma32(xf[k], wv, acc) : mfma32(wv, xf[k], acc);
        PIN(acc);
        filler(k);
        SB();
    }
}
#define XPD 14
template <bool PRE, bool TR, bool STAGE, bool MID, class W>
__device__ __forceinline__ f32x2 c_tile0(const Ctx& c, f32x16& acc, u32x4 (&wf)[8], const LAS unsigned char* lo, const LAS unsigned char* hi,
                                         const LAS unsigned char* nxt, u32x4 (&xf)[32], __amdgpu_buffer_rsrc_t xrs, u32 xsoff, W&& wait_ready) {
    const int lane = c.lane;
    u32x4 raw[STAGE ? 32 : 1];
    const u32 rvoff = (u32)(lane >> 3) * 1024u + (u32)(lane & 7) * 16u;
    LAS unsigned char* st = c.L + 6 * SLOT + (c.w & 3) * 8192;
    const int wr_off = (lane >> 3) * 128 + (((lane & 7) ^ ((lane >> 3) & 7)) * 16);
    const int rd_row = (lane & 31) * 128, rd_sw = lane & 7, rd_h = lane >> 5;
#define XLD(k) __builtin_amdgcn_raw_buffer_load_b128(xrs, c.voff, xsoff + (u32)(k) * 1024u, 16)
#define RLD(q) __builtin_amdgcn_raw_buffer_load_b128(xrs, rvoff, xsoff + (u32)((q) & 3) * 8192u + (u32)((q) >> 2) * 128u, 0)
#define STG(g) do { _Pragma("unroll") for (int q = 0; q < 4; ++q) lds_st<u32x4>(st + ((g) & 1) * 4096, q * 1024 + wr_off, raw[4 * (g) + q]); \
                    _Pragma("unroll") for (int q = 0; q < 4; ++q) xf[4 * (g) + q] = lds_ld<u32x4>(st + ((g) & 1) * 4096, rd_row + (((2 * q + rd_h) ^ rd_sw) * 16)); } while (0)
    if (STAGE) {
#pragma unroll
        for (int q = 0; q < 16; ++q) raw[q] = RLD(q);
        SB();
        wait_ready();
        STG(0);
    } else {
        wait_ready();
#pragma unroll
        for (int k = 0; k < XPD; ++k) xf[k] = XLD(k);
    }
#pragma unroll
    for (int k = 0; k < 8; ++k) wf[k] = ldf<TR>(lo, k);
    SB();
    float s1[4] = {0.f, 0.f, 0.f, 0.f}, s2[4] = {0.f, 0.f, 0.f, 0.f};
#pragma unroll
    for (int k = 0; k < 32; ++k) {
        if (STAGE) {
            if ((k & 3) == 0) {
                const int g = k >> 2;
                if (4 * (g + 4) < 32) {
#pragma unroll
                    for (int q = 0; q < 4; ++q) raw[4 * (g + 4) + q] = RLD(4 * (g + 4) + q);
                }
                if (g + 1 < 8) STG(g + 1);
            }
        } else {
            if (k + XPD < 32) xf[k + XPD] = XLD(k + XPD);
        }
        const u32x4 wv = wf[k & 7];
        if (k + 8 < 16) wf[k & 7] = ldf<TR>(lo, k + 8);
        else if (k + 8 < 32) wf[k & 7] = ldf<TR>(hi, k + 8 - 16);
        else if (PRE) wf[k & 7] = ldf<TR>(nxt, k + 8 - 32);
        acc = mfma32(wv, xf[k], acc);
        PIN(acc);
        {
        s1[0] = dot2(xf[k].x, BF_ONES, s1[0]); s2[0] = dot2(xf[k].x, xf[k].x, s2[0]);
        s1[1] = dot2(xf[k].y, BF_ONES, s1[1]); s2[1] = dot2(xf[k].y, xf[k].y, s2[1]);
        s1[2] = dot2(xf[k].z, BF_ONES, s1[2]); s2[2] = dot2(xf[k].z, xf[k].z, s2[2]);
        s1[3] = dot2(xf[k].w, BF_ONES, s1[3]); s2[3] = dot2(xf[k].w, xf[k].w, s2[3]);
        }
        asm volatile("" : "+v"(s1[0]), "+v"(s1[1]), "+v"(s1[2]), "+v"(s1[3]), "+v"(s2[0]), "+v"(s2[1]), "+v"(s2[2]), "+v"(s2[3]));
        if (MID && k == 3) BARL();
        SB();
    }
    const float a = xor32_sum((s1[0] + s1[1]) + (s1[2] + s1[3])), b = xor32_sum((s2[0] + s2[1]) + (s2[2] + s2[3]));
    const float mean = a * (1.0f / DM);
    float var = b * (1.0f / DM) - mean * mean;
    var = var > 0.f ? var : 0.f;
    f32x2 o; o.x = mean; o.y = __builtin_amdgcn_rsqf(var + LN_EPS);
    return o;
#undef XLD
#undef RLD
#undef STG
}

struct GS { float g0, g1, t0, t1, u0, u1; };
struct GK { float mean, A, B, ir2; };
__device__ __forceinline__ void geglu_sub(GS& s, u32& o, int q, float ag0, float ag1, float au0, float au1, float cg0, float cg1, float cu0, float cu1, const GK& k) {
    if (q == 0) asm volatile("v_fma_f32 %0, -%7, %5, %3\n\tv_fma_f32 %1, -%7, %6, %4\n\tv_mul_f32 %2, %0, %0"
                             : "=&v"(s.g0), "=&v"(s.g1), "=&v"(s.t0) : "v"(ag0), "v"(ag1), "v"(cg0), "v"(cg1), "v"(k.mean));
    if (q == 1) asm volatile("v_mul_f32 %1, %2, %2\n\tv_fma_f32 %0, %0, %4, %3\n\tv_fma_f32 %1, %1, %4, %3"
                             : "+v"(s.t0), "=&v"(s.t1) : "v"(s.g1), "v"(k.A), "v"(k.B));
    if (q == 2) asm volatile("v_mul_f32 %0, %2, %0\n\tv_mul_f32 %1, %3, %1\n\tv_exp_f32 %0, %0"
                             : "+v"(s.t0), "+v"(s.t1) : "v"(s.g0), "v"(s.g1));
    if (q == 3) asm volatile("v_exp_f32 %0, %0\n\tv_fma_f32 %1, -%7, %5, %3\n\tv_fma_f32 %2, -%7, %6, %4"
                             : "+v"(s.t1), "=&v"(s.u0), "=&v"(s.u1) : "v"(au0), "v"(au1), "v"(cu0), "v"(cu1), "v"(k.mean));
    if (q == 4) asm volatile("v_fma_f32 %0, %0, %4, %4\n\tv_mul_f32 %2, %2, %3\n\tv_fma_f32 %1, %1, %4, %4"
                             : "+v"(s.t0), "+v"(s.t1), "+v"(s.g0) : "v"(s.u0), "v"(k.ir2));
    if (q == 5) asm volatile("v_rcp_f32 %0, %0\n\tv_mul_f32 %2, %2, %3\n\tv_rcp_f32 %1, %1"
                             : "+v"(s.t0), "+v"(s.t1), "+v"(s.g1) : "v"(s.u1));
    if (q == 6) asm volatile("v_mul_f32 %1, %1, %3\n\tv_mul_f32 %2, %2, %4\n\tv_cvt_pk_bf16_f32 %0, %1, %2"
                             : "=&v"(o), "+v"(s.g0), "+v"(s.g1) : "v"(s.t0), "v"(s.t1));
}
struct LS { float t0, t1; };
__device__ __forceinline__ void lnq_a(LS& s, float a0, float c0, float c1, float sc, float msc) {
    asm volatile("v_mul_f32 %0, %6, %3\n\tv_mul_f32 %1, %6, %4\n\tv_fma_f32 %0, %2, %5, -%0"
                 : "=&v"(s.t0), "=&v"(s.t1) : "v"(a0), "v"(c0), "v"(c1), "v"(sc), "v"(msc));
}
__device__ __forceinline__ u32 lnq_b(LS& s, float a1, float sc) {
    u32 o;
    asm volatile("v_fma_f32 %1, %3, %4, -%1\n\tv_cvt_pk_bf16_f32 %0, %2, %1" : "=v"(o), "+v"(s.t1) : "v"(s.t0), "v"(a1), "v"(sc));
    return o;
}
__device__ __forceinline__ void lnv_a(LS& s, float a0, float a1, float m0, float m1, float r0, float cs) {
    asm volatile("v_fma_f32 %0, -%4, %7, %2\n\tv_fma_f32 %1, -%5, %7, %3\n\tv_mul_f32 %0, %0, %6"
                 : "=&v"(s.t0), "=&v"(s.t1) : "v"(a0), "v"(a1), "v"(m0), "v"(m1), "v"(r0), "v"(cs));
}
__device__ __forceinline__ u32 lnv_b(LS& s, float r1) {
    u32 o;
    asm volatile("v_mul_f32 %1, %1, %3\n\tv_cvt_pk_bf16_f32 %0, %2, %1" : "=v"(o), "+v"(s.t1) : "v"(s.t0), "v"(r1));
    return o;
}
__device__ __forceinline__ u32x4 pack8(const float* v) { return u32x4{pack2(v[0], v[1]), pack2(v[2], v[3]), pack2(v[4], v[5]), pack2(v[6], v[7])}; }

__device__ __forceinline__ float max16(const f32x16& s, float m) {
    float a = fmx(fmx(s[0], s[1]), m), b = fmx(s[2], s[3]);
    a = fmx(fmx(s[4], s[5]), a); b = fmx(fmx(s[6], s[7]), b);
    a = fmx(fmx(s[8], s[9]), a); b = fmx(fmx(s[10], s[11]), b);
    a = fmx(fmx(s[12], s[13]), a); b = fmx(fmx(s[14], s[15]), b);
    return fmx(a, b);
}
__device__ __forceinline__ float smul(float a, float b) { float r; asm("v_mul_f32 %0, %1, %2" : "=v"(r) : "v"(a), "v"(b)); return r; }
struct MaskF { u32x4 alo, ahi, ilo, ihi; };
__device__ __forceinline__ f32x16 add_mask(const MaskF& mf, f32x16 s) { s = mfma32(mf.alo, mf.ilo, s); return mfma32(mf.ahi, mf.ihi, s); }
template <int NB, int FIRST, int REST0, int MASK, bool FIRSTSEG>
__device__ __forceinline__ void att_segment(f32x16& oacc, float& m, const LAS unsigned char* kb, const LAS unsigned char* vb, const LAS unsigned char* mk, const u32x4& qf) {
    u32x4 kf[NB];
#pragma unroll
    for (int t = 0; t < NB; ++t) { const int b = t == 0 ? FIRST : REST0 + t - 1; kf[t] = *(const LAS u32x4*)(kb + b * 2048); }
    MaskF mf;
    if (MASK) {
        mf.alo = LDF(mk, 0); mf.ahi = LDF(mk, 1); mf.ilo = LDF(mk, 2); mf.ihi = LDF(mk, 3);
        if (MASK == 2) { mf.alo ^= 0xff7fff7fu; mf.ahi ^= 0xff7fff7fu; }
    }
    SB();
    float mx = FIRSTSEG ? -__builtin_inff() : m;
    f32x16 sv[4];
    sv[0] = mfma32(kf[0], qf, f32x16{});
    if (NB > 1) sv[1] = mfma32(kf[1], qf, f32x16{});
    if (MASK) sv[0] = add_mask(mf, sv[0]);
    SB();
#pragma unroll
    for (int t = 0; t < NB; ++t) {
        const int u = NB > 1 ? (t == 0 ? 1 : t == 1 ? 0 : t) : 0;
        if (t + 2 < NB) sv[(t + 2) & 3] = mfma32(kf[t + 2], qf, f32x16{});
        mx = max16(sv[u & 3], mx);
        SB();
    }
    mx = xor32_max(mx);
    f32x16 negm;
#pragma unroll
    for (int e = 0; e < 16; ++e) negm[e] = -mx;
    SB();
    sv[0] = mfma32(kf[0], qf, negm);
    if (MASK) sv[0] = add_mask(mf, sv[0]);
    u32x4 v0 = *(const LAS u32x4*)(vb + (FIRST * 2 + 0) * VREC);
    u32x4 v1 = *(const LAS u32x4*)(vb + (FIRST * 2 + 1) * VREC);
    if (NB > 1) sv[1] = mfma32(kf[1], qf, negm);
    if (!FIRSTSEG) {
        float f = __builtin_amdgcn_exp2f(m - mx);
        asm volatile("s_nop 1" : "+v"(f));
#pragma unroll
        for (int e = 0; e < 16; ++e) oacc[e] = smul(oacc[e], f);
    }
    m = mx;
    SB();
#pragma unroll
    for (int t = 0; t < NB; ++t) {
        u32x4 n0, n1;
        if (t + 1 < NB) {
            if (t > 0) sv[(t + 1) & 1] = mfma32(kf[t + 1], qf, negm);
            n0 = *(const LAS u32x4*)(vb + ((REST0 + t) * 2 + 0) * VREC);
            n1 = *(const LAS u32x4*)(vb + ((REST0 + t) * 2 + 1) * VREC);
        }
        SB();
        const f32x16& s = sv[t & 1];
        u32 p[8];
#pragma unroll
        for (int e = 0; e < 4; ++e) p[e] = pack2(__builtin_amdgcn_exp2f(s[2 * e]), __builtin_amdgcn_exp2f(s[2 * e + 1]));
        SB();
        oacc = mfma32(v0, u32x4{p[0], p[1], p[2], p[3]}, oacc);
#pragma unroll
        for (int e = 4; e < 8; ++e) p[e] = pack2(__builtin_amdgcn_exp2f(s[2 * e]), __builtin_amdgcn_exp2f(s[2 * e + 1]));
        SB();
        oacc = mfma32(v1, u32x4{p[4], p[5], p[6], p[7]}, oacc);
        if (t + 1 < NB) { v0 = n0; v1 = n1; }
        SB();
    }
}

__device__ __forceinline__ void issue_qkv_tile(const Ctx& c, const Args& a, int t) {
    const u32 voff = (u32)(c.lane >> 2) * 3072u + (u32)(c.lane & 3) * 16u;
    const unsigned char* src = (const unsigned char*)(a.wqkv + qkv_col(t) + 32 * c.j) + (size_t)(c.w & 3) * 8 * 16 * 3072;
    const u32 dst = c.Laddr + slot_of(CQ(t, 0)) * SLOT + (c.w & 3) * 8192;
#pragma unroll
    for (int q = 0; q < 8; ++q) glds1k(src + (size_t)q * 16 * 3072, voff, dst + q * 1024);
}
template <bool TR> __device__ __forceinline__ void tile_colsum(const Ctx& c, const LAS unsigned char* lo, const LAS unsigned char* hi, int csidx) {
    float s[4] = {0.f, 0.f, 0.f, 0.f};
#pragma unroll
    for (int hf = 0; hf < 2; ++hf) {
        u32x4 f[16];
#pragma unroll
        for (int k = 0; k < 16; ++k) f[k] = ldf<TR>(hf ? hi : lo, k);
        SB();
#pragma unroll
        for (int k = 0; k < 16; ++k) {
            s[0] = dot2(f[k].x, BF_ONES, s[0]); s[1] = dot2(f[k].y, BF_ONES, s[1]); s[2] = dot2(f[k].z, BF_ONES, s[2]); s[3] = dot2(f[k].w, BF_ONES, s[3]);
        }
        SB();
    }
    const float t = xor32_sum((s[0] + s[1]) + (s[2] + s[3]));
    if (c.lane < 32) lds_st<float>(c.L, SC_CS + (csidx + c.lane) * 4, t);
}
__device__ __forceinline__ int tr_lane_off(int lane) {
    const int g4 = lane >> 4, h = g4 >> 1, nh = g4 & 1, li = lane & 15;
    return (8 * h + (li >> 2)) * 64 + nh * 32 + (li & 3) * 8;
}
__device__ __forceinline__ const LAS unsigned char* lds_base_raw(const Ctx& c, int off) {
    u32 a = c.Laddr + off;
    asm volatile("" : "+v"(a));
    return (const LAS unsigned char*)(size_t)a;
}

__device__ __forceinline__ void phase_qkv_att(const Ctx& c0, int layer, const Args& a) {
    const Ctx c = fresh(c0);
    const u32 kvbase = (u32)(WS_L0 + (size_t)layer * 24 * MiB);
    const int lane = c.lane, w = c.w, tb = w & 3, h = lane >> 5, r = lane & 31;
    const u32 lbase = (u32)WS_L0;
    STAMP(0);
    if (w < 4) {
        auto ready = [&]() {
            if (layer > 0 && w == 0) poll_flags<64>(c, layer - 1, 0, 16);
            BAR();
            STAMP(1);
        };
        u32x4 xf[32], wf[8];
        const LAS unsigned char* wK = lds_base(c, slot_of(CQ(0, 0)) * SLOT);
        const LAS unsigned char* wV = lds_base(c, slot_of(CQ(1, 0)) * SLOT);
        const LAS unsigned char* wQ = lds_base(c, slot_of(CQ(2, 0)) * SLOT);
        f32x16 accK = {}, accV = {}, accQ = {};
        const f32x2 ms = c_tile0<true, false, false, true>(c, accK, wf, wK, wK + SLOT, wV, xf, c.rs,
                                                            (layer == 0 ? (u32)WS_XF0 : (u32)(WS_L0 + L_X2)) + (u32)((4 * c.i + tb) * 32) * 1024, ready);
        if (lane < 32) lds_st<f32x2>(c.L, SC_ST + (tb * 32 + r) * 8, ms);
        const float msk = ms.x * ms.y;
        BARL(); STAMP(2);
        f32x4 cs[4];
#pragma unroll
        for (int q = 0; q < 4; ++q) cs[q] = lds_ld<f32x4>(c.L, SC_CS + (0 * 32 + 8 * q + 4 * h) * 4);
        f32x4 mr[8];
        float csv;
        {
            u32 o[8]; LS ls;
            c_tile<true, true, false>(accV, wf, wV, wV + SLOT, wQ, xf, [&](int k) {
                if (k >= 4 && k < 20) {
                    const int pi = (k - 4) >> 1, e = 2 * pi;
                    if (((k - 4) & 1) == 0) lnq_a(ls, accK[e], cs[e >> 2][e & 3], cs[e >> 2][(e & 3) + 1], ms.y, msk);
                    else o[pi] = lnq_b(ls, accK[e + 1], ms.y);
                }
                if (k == 20 || k == 21) {
                    const int hh = k - 20;
                    const u32x4 ov = {o[4 * hh], o[4 * hh + 1], o[4 * hh + 2], o[4 * hh + 3]};
                    lds_st<u32x4>(c.L, SC_KS + (tb * 2 + hh) * 1024 + c.voff, ov);
                    st_sc1(c, kvbase + (u32)L_KF + (u32)((4 * c.i + tb) * 32 + 2 * c.j + hh) * 1024, ov);
                }
                if (k >= 22 && k < 30) { const int q = (k - 22) >> 1; mr[k - 22] = lds_ld<f32x4>(c.L, SC_ST + (tb * 32 + 8 * q + 4 * h + 2 * ((k - 22) & 1)) * 8); }
                if (k == 30) csv = lds_ld<float>(c.L, SC_CS + (32 + r) * 4);
            });
        }
        BARL();
        {
            u32 o[8]; LS ls;
            c_tile<false, false, false>(accQ, wf, wQ, wQ + SLOT, wQ, xf, [&](int k) {
                if (k >= 2 && k < 18) {
                    const int pi = (k - 2) >> 1;
                    if (((k - 2) & 1) == 0) lnv_a(ls, accV[2 * pi], accV[2 * pi + 1], mr[pi].x, mr[pi].z, mr[pi].y, csv);
                    else o[pi] = lnv_b(ls, mr[pi].w);
                }
                if (k == 18 || k == 20) {
                    const int sv = (k - 18) >> 1;
                    const u32x4 ov = {o[4 * sv], o[4 * sv + 1], o[4 * sv + 2], o[4 * sv + 3]};
                    lds_st<u32x4>(c.L, SC_VS + (tb * 2 + sv) * VREC + c.voff, ov);
                    st_sc1(c, kvbase + (u32)L_VF + (u32)(((4 * c.i + tb) * 16 + c.j) * 2 + sv) * 1024, ov);
                }
                if (k >= 24 && k < 28) cs[k - 24] = lds_ld<f32x4>(c.L, SC_CS + (64 + 8 * (k - 24) + 4 * h) * 4);
            });
            if (lane < 2) { u32x4 ones = {BF_ONES, BF_ONES, BF_ONES, BF_ONES}; lds_st<u32x4>(c.L, SC_VS + (tb * 2 + lane) * VREC + 1024, ones); }
        }
        {
            const float sc = ms.y * QSCALE, msc = ms.x * sc;
            asm volatile("s_nop 15" : "+v"(accQ));
            u32 o[8]; LS ls[8];
#pragma unroll
            for (int pi = 0; pi < 8; ++pi) { const int e = 2 * pi; lnq_a(ls[pi], accQ[e], cs[e >> 2][e & 3], cs[e >> 2][(e & 3) + 1], sc, msc); }
#pragma unroll
            for (int pi = 0; pi < 8; ++pi) o[pi] = lnq_b(ls[pi], accQ[2 * pi + 1], sc);
#pragma unroll
            for (int hh = 0; hh < 2; ++hh) lds_st<u32x4>(c.L, SC_QS + (tb * 2 + hh) * 1024 + c.voff, u32x4{o[4 * hh], o[4 * hh + 1], o[4 * hh + 2], o[4 * hh + 3]});
        }
        WAIT_VM(0);
        BAR(); STAMP(5);
        if (w == 0 && lane < 2 && c.i + 1 + lane < 16) flag_store(c, layer, 1, c.i + 1 + lane, c.j, lane);
    } else {
        WAIT_VM(16);
        BAR();
        WAIT_VM(8);
        BARL();
        const int cb = c.j & 7, sidx = c.i * 2 + (c.j >> 3);
        if (layer == 0) {
            const int hw = w & 3;
            LAS unsigned char* t = c.L + SC_VS + hw * 1024;
            WItem it[6]; u32x4 v[6];
#pragma unroll
            for (int q = 0; q < 6; ++q) {
                int f;
                const int fD = NF_WO + NF_WG + cb * 224 + sidx * 7, fO = cb * 64 + sidx * 2;
                if (q < 2 || (q < 4 && hw < 3)) { const int ks = 2 * c.i + (q & 1), cc = hw + 4 * (q >> 1); f = NF_WO + c.j * 224 + (2 * cc + (ks >> 4)) * 16 + (ks & 15); }
                else if (q < 4) f = fD + 4 + (q & 1);
                else if (q == 4) f = fD + (hw < 3 ? hw : 6);
                else f = hw < 2 ? fO + hw : fD + 3;
                it[q] = w_item(a, f, lane);
                v[q] = *(const u32x4*)it[q].src;
            }
            const int k = lane >> 2, n8 = lane & 3;
            const int g4 = lane >> 4, hh = g4 >> 1, nh = g4 & 1, li = lane & 15, qq = li >> 2, p = li & 3;
            float part = 0.f;
#pragma unroll
            for (int q = 0; q < 6; ++q) {
                if (q < 5 || hw < 3) {
                    lds_st<u32x4>(t, k * 64 + n8 * 16, v[q]);
                    asm volatile("s_waitcnt lgkmcnt(0)" ::: "memory");
                    const s16x4 lo = __builtin_amdgcn_ds_read_tr16_b64_v4i16((LAS s16x4*)(t + (4 * hh + qq) * 64 + nh * 32 + p * 8));
                    const s16x4 hi = __builtin_amdgcn_ds_read_tr16_b64_v4i16((LAS s16x4*)(t + (8 + 4 * hh + qq) * 64 + nh * 32 + p * 8));
                    const u32x2 l2 = __builtin_bit_cast(u32x2, lo), h2 = __builtin_bit_cast(u32x2, hi);
                    __builtin_amdgcn_raw_buffer_store_b128(u32x4{l2.x, l2.y, h2.x, h2.y}, c.rs, c.voff, (u32)it[q].dst, 16);
                    if (q < 2 || (q < 4 && hw < 3)) {
                        if ((q & 1) == 0) part = 0.f;
                        part = dot2(l2.x, BF_ONES, part); part = dot2(l2.y, BF_ONES, part); part = dot2(h2.x, BF_ONES, part); part = dot2(h2.y, BF_ONES, part);
                        if (q & 1) {
                            const float tot = xor32_sum(part);
                            const int cc = hw + 4 * (q >> 1);
                            if (lane < 32) __builtin_amdgcn_raw_buffer_store_b32(__builtin_bit_cast(u32, tot), c.rs, (u32)lane * 4u, (u32)WS_CSP + (u32)((c.j * 16 + c.i) * 224 + cc * 32) * 4u, 16);
                        }
                    }
                    asm volatile("s_waitcnt lgkmcnt(0)" ::: "memory");
                }
            }
        }
        WAIT_VM(0);
        BAR();
        if (layer == 0 && w == 4) {
            if (lane < 16) flag_store(c, 0, 5, lane, c.j, c.i);
            if (lane < 32) flag_store(c, 0, 6, lane >> 1, cb + 8 * (lane & 1), sidx);
        }
        BAR();
        if (layer == 0) poll_flags<64>(c, 0, 6, 32);
        issue_chunk<CO(0)>(c); issue_chunk<CO(1)>(c); issue_chunk<CO(2)>(c); issue_chunk<CO(3)>(c);
        BAR();
        if (layer == 0) {
            poll_flags<64>(c, 0, 5, 16);
            const int col = (w & 3) * 64 + lane;
            if (col < 224) {
                float t = 0.f;
#pragma unroll
                for (int i2 = 0; i2 < 16; ++i2) t += __builtin_bit_cast(float, __builtin_amdgcn_raw_buffer_load_b32(c.rs, (u32)col * 4u, (u32)WS_CSP + (u32)((c.j * 16 + i2) * 224) * 4u, 16));
                lds_st<float>(c.L, SC_CS + (96 + col) * 4, t);
            }
        }
        issue_chunk<CG(0, 0)>(c); issue_chunk<CG(0, 1)>(c);
    }
    const int qb = w < 4 ? w : 7 - w, hh = w >> 2;
    const u32x4 qf = lds_ld<u32x4>(c.L, SC_QS + (qb * 2 + hh) * 1024 + c.voff);
    const int vlane = (((lane >> 4) & 1) == hh) ? lane * 16 : 1024;
    f32x16 oacc = {};
    float m = 0.f;
    const LAS unsigned char* mk = lds_base(c, SC_MK);
    {
        const LAS unsigned char* kb = c.L + SC_KS + hh * 1024 + c.voff;
        const LAS unsigned char* vb = c.L + SC_VS + vlane;
        if (qb == 0) att_segment<1, 0, 0, 1, true>(oacc, m, kb, vb, mk, qf);
        else if (qb == 1) att_segment<2, 1, 0, 1, true>(oacc, m, kb, vb, mk, qf);
        else if (qb == 2) att_segment<3, 2, 0, 1, true>(oacc, m, kb, vb, mk, qf);
        else att_segment<4, 3, 0, 1, true>(oacc, m, kb, vb, mk, qf);
    }
    STAMP(6);
    if (c.i > 0) {
        if (w == 0) poll_flags<64>(c, layer, 1, c.i >= 2 ? 2 : 1);
        BAR();
        STAMP(7);
        {
            const int idx = w;
            const int g = 4 * c.i - 8 + idx;
            if (g >= 0) {
                const u32x4 s0 = ld_sc1(c, kvbase + (u32)L_KF + (u32)(g * 32 + 2 * c.j + 0) * 1024);
                const u32x4 s1 = ld_sc1(c, kvbase + (u32)L_KF + (u32)(g * 32 + 2 * c.j + 1) * 1024);
                const u32x4 s2 = ld_sc1(c, kvbase + (u32)L_VF + (u32)((g * 16 + c.j) * 2 + 0) * 1024);
                const u32x4 s3 = ld_sc1(c, kvbase + (u32)L_VF + (u32)((g * 16 + c.j) * 2 + 1) * 1024);
                lds_st<u32x4>(c.L, SC_RK + (idx * 2 + 0) * 1024 + c.voff, s0);
                lds_st<u32x4>(c.L, SC_RK + (idx * 2 + 1) * 1024 + c.voff, s1);
                lds_st<u32x4>(c.L, SC_RV + (idx * 2 + 0) * VREC + c.voff, s2);
                lds_st<u32x4>(c.L, SC_RV + (idx * 2 + 1) * VREC + c.voff, s3);
                if (lane < 2) { u32x4 ones = {BF_ONES, BF_ONES, BF_ONES, BF_ONES}; lds_st<u32x4>(c.L, SC_RV + (idx * 2 + lane) * VREC + 1024, ones); }
            }
        }
        BAR();
        STAMP(8);
        const LAS unsigned char* kb = c.L + SC_RK + hh * 1024 + c.voff;
        const LAS unsigned char* vb = c.L + SC_RV + vlane;
        if (c.i == 1) att_segment<4, 4, 5, 0, false>(oacc, m, kb, vb, mk, qf);
        else if (qb == 0) att_segment<8, 0, 1, 2, false>(oacc, m, kb, vb, mk, qf);
        else if (qb == 1) att_segment<7, 1, 2, 2, false>(oacc, m, kb, vb, mk, qf);
        else if (qb == 2) att_segment<6, 2, 3, 2, false>(oacc, m, kb, vb, mk, qf);
        else att_segment<5, 3, 4, 2, false>(oacc, m, kb, vb, mk, qf);
    }
    {
        u32x4 o;
        if (hh == 0) {
            float il = __builtin_amdgcn_rcpf(oacc[8]);
            asm volatile("s_nop 1" : "+v"(il));
            o = u32x4{pack2(smul(oacc[0], il), smul(oacc[1], il)), pack2(smul(oacc[2], il), smul(oacc[3], il)), pack2(smul(oacc[4], il), smul(oacc[5], il)), pack2(smul(oacc[6], il), smul(oacc[7], il))};
        } else {
            float il = __builtin_amdgcn_rcpf(oacc[0]);
            asm volatile("s_nop 1" : "+v"(il));
            o = u32x4{pack2(smul(oacc[8], il), smul(oacc[9], il)), pack2(smul(oacc[10], il), smul(oacc[11], il)), pack2(smul(oacc[12], il), smul(oacc[13], il)), pack2(smul(oacc[14], il), smul(oacc[15], il))};
        }
        st_sc1(c, lbase + (u32)L_OF + (u32)((4 * c.i + qb) * 32 + 2 * c.j + hh) * 1024, o);
    }
    STAMP(9);
    WAIT_VM(0);
    BAR();
    if (w == 0) signal_group(c, layer, 2);
    if (w >= 4) { issue_chunk<CG(1, 0)>(c); issue_chunk<CG(1, 1)>(c); }
    STAMP(10);
}

__device__ __forceinline__ void red_write(const Ctx& c, const f32x16& acc0, const f32x16& acc1, int t2, int kq) {
#pragma unroll
    for (int d = 0; d < 4; ++d) {
        const int fin = d & 1;
        if (kq != fin) {
            const int slot = kq < fin ? kq : kq - 1;
            const int base = red_off(t2 * 4 + d) + slot * 2048 + c.voff;
            const f32x16& a = d < 2 ? acc0 : acc1;
            const int o = (d & 1) * 8;
            lds_st<f32x4>(c.L, base, f32x4{a[o], a[o + 1], a[o + 2], a[o + 3]});
            lds_st<f32x4>(c.L, base + 1024, f32x4{a[o + 4], a[o + 5], a[o + 6], a[o + 7]});
        }
    }
}
__device__ __forceinline__ void red_read(const Ctx& c, const f32x16& acc0, const f32x16& acc1, int t2, int kq, float (&v)[2][8]) {
#pragma unroll
    for (int e = 0; e < 8; ++e) { v[0][e] = kq ? acc0[8 + e] : acc0[e]; v[1][e] = kq ? acc1[8 + e] : acc1[e]; }
#pragma unroll
    for (int x = 0; x < 2; ++x)
#pragma unroll
        for (int s = 0; s < 3; ++s) {
            const int base = red_off(t2 * 4 + 2 * x + kq) + s * 2048 + c.voff;
            const f32x4 lo = lds_ld<f32x4>(c.L, base), hi = lds_ld<f32x4>(c.L, base + 1024);
            v[x][0] += lo.x; v[x][1] += lo.y; v[x][2] += lo.z; v[x][3] += lo.w; v[x][4] += hi.x; v[x][5] += hi.y; v[x][6] += hi.z; v[x][7] += hi.w;
        }
}
__device__ __forceinline__ u32x4 add_res(const float (&v)[8], const u32x4& res) {
    return u32x4{pack2(v[0] + bflo(res.x), v[1] + bfhi(res.x)), pack2(v[2] + bflo(res.y), v[3] + bfhi(res.y)),
                 pack2(v[4] + bflo(res.z), v[5] + bfhi(res.z)), pack2(v[6] + bflo(res.w), v[7] + bfhi(res.w))};
}

__device__ __forceinline__ void phase_wo(const Ctx& c0, int layer, const Args& a) {
    const Ctx c = fresh(c0);
    const int w = c.w, t2 = w & 1, kq = w >> 1;
    const int th = c.j >> 3, cb = c.j & 7;
    const int tbk = 4 * c.i + 2 * th + t2;
    const u32 lbase = (u32)WS_L0;
    if (w == 0) poll_flags<64>(c, layer, 2, 16);
    if (w >= 4) WAIT_VM(8);
    BAR();
    STAMP(11);
    u32x4 xf[8];
#pragma unroll
    for (int s = 0; s < 8; ++s) xf[s] = ld_sc1(c, lbase + (u32)L_OF + (u32)(tbk * 32 + 8 * kq + s) * 1024);
    u32x4 res[2];
    if (w < 4) {
#pragma unroll
        for (int x = 0; x < 2; ++x) {
            const int kbo = 4 * cb + 2 * x + kq;
            res[x] = ld_sc1(c, (layer == 0 ? (u32)WS_XF0 : (u32)(WS_L0 + L_X2)) + (u32)(tbk * 32 + kbo) * 1024);
        }
    }
    u32x4 wf[16];
#pragma unroll
    for (int ch = 0; ch < 4; ++ch) {
        const LAS unsigned char* wb = lds_base(c, slot_of(CO(ch)) * SLOT + kq * 4096);
#pragma unroll
        for (int q = 0; q < 4; ++q) wf[4 * ch + q] = LDF(wb, q);
    }
    SB();
    f32x16 acc0 = {}, acc1 = {};
#pragma unroll
    for (int s = 0; s < 8; ++s) {
        acc0 = mfma32(wf[2 * s], xf[s], acc0);
        acc1 = mfma32(wf[2 * s + 1], xf[s], acc1);
        SB();
    }
    BAR();
    STAMP(12);
    red_write(c, acc0, acc1, t2, kq);
    BAR();
    if (w < 4) {
        float v[2][8];
        red_read(c, acc0, acc1, t2, kq, v);
#pragma unroll
        for (int x = 0; x < 2; ++x) st_sc1(c, lbase + (u32)L_X1 + (u32)(tbk * 32 + 4 * cb + 2 * x + kq) * 1024, add_res(v[x], res[x]));
        STAMP(13);
        WAIT_VM(0);
    }
    BAR();
    if (w == 0) signal_group(c, layer, 3);
    STAMP(14);
}

template <int T> __device__ __forceinline__ void gu_tile(const Ctx& c, f32x16& acc, const f32x16& prev, u32x4 (&wf)[8], const u32x4 (&xf)[32], int h,
                                                         f32x4 (&cs)[4], const GK& gk, u32 hbase) {
    const LAS unsigned char* cur = lds_base(c, slot_of(CG(T, 0)) * SLOT);
    const LAS unsigned char* nxt = lds_base(c, slot_of(CG(T < 6 ? T + 1 : T, 0)) * SLOT);
    GS g;
    u32 o[4];
    acc = f32x16{};
    SB();
    c_tile<false, (T < 6), false>(acc, wf, cur, cur + SLOT, nxt, xf, [&](int k) {
        const int p = k >> 3, e = 2 * p, q2 = p >> 1, e2 = e & 3;
        if ((k & 7) < 7) geglu_sub(g, o[p], k & 7, prev[e], prev[e + 1], prev[8 + e], prev[8 + e + 1], cs[q2][e2], cs[q2][e2 + 1], cs[2 + q2][e2], cs[2 + q2][e2 + 1], gk);
        if (k == 15) cs[0] = lds_ld<f32x4>(c.L, SC_CS + (96 + T * 32 + 4 * h) * 4);
        if (k == 15) cs[2] = lds_ld<f32x4>(c.L, SC_CS + (96 + T * 32 + 16 + 4 * h) * 4);
        if (k == 31) cs[1] = lds_ld<f32x4>(c.L, SC_CS + (96 + T * 32 + 8 + 4 * h) * 4);
        if (k == 31) cs[3] = lds_ld<f32x4>(c.L, SC_CS + (96 + T * 32 + 16 + 8 + 4 * h) * 4);
    });
    st_sc1(c, hbase + (u32)(T - 1) * 1024, u32x4{o[0], o[1], o[2], o[3]});
}

__device__ __forceinline__ u32 gu_compute(const Ctx& c0, int layer) {
    const Ctx c = fresh(c0);
    const int lane = c.lane, w = c.w, tb = w & 3, h = lane >> 5;
    const u32 lbase = (u32)WS_L0;
    u32 pf1 = 0;
    {
        u32x4 xf[32], wf[8];
        const LAS unsigned char* w0 = lds_base(c, slot_of(CG(0, 0)) * SLOT);
        const LAS unsigned char* w1 = lds_base(c, slot_of(CG(1, 0)) * SLOT);
        f32x16 a0 = {}, a1;
        const f32x2 ms = c_tile0<true, false, false, false>(c, a0, wf, w0, w0 + SLOT, w1, xf, c.rs, lbase + (u32)L_X1 + (u32)((4 * c.i + tb) * 32) * 1024, [&]() {
            if (w == 0) poll_flags<64>(c, layer, 3, 16);
            BAR();
            STAMP(15);
        });
        constexpr float GC1 = -2.0f * 0.7978845608028654f * 1.4426950408889634f, GC2 = GC1 * 0.044715f;
        GK gk; gk.mean = ms.x; gk.A = ms.y * GC1; gk.B = ms.y * ms.y * ms.y * GC2; gk.ir2 = 1.0f / (ms.y * ms.y);
        BARL(); STAMP(16);
        f32x4 cs[4];
#pragma unroll
        for (int q = 0; q < 4; ++q) cs[q] = lds_ld<f32x4>(c.L, SC_CS + (96 + (q >> 1) * 16 + 8 * (q & 1) + 4 * h) * 4);
        const u32 hbase = lbase + (u32)L_HF + (u32)((4 * c.i + tb) * 112 + 7 * c.j) * 1024;
        gu_tile<1>(c, a1, a0, wf, xf, h, cs, gk, hbase); BARL();
        gu_tile<2>(c, a0, a1, wf, xf, h, cs, gk, hbase); BARL();
        gu_tile<3>(c, a1, a0, wf, xf, h, cs, gk, hbase); BARL();
        gu_tile<4>(c, a0, a1, wf, xf, h, cs, gk, hbase); BARL();
        gu_tile<5>(c, a1, a0, wf, xf, h, cs, gk, hbase);
        WAIT_VM(1);
        BARL();
        if (w == 0) signal_group(c, layer, 7);
        gu_tile<6>(c, a0, a1, wf, xf, h, cs, gk, hbase);
        STAMP(22);
        if (w == 0) pf1 = __builtin_amdgcn_raw_buffer_load_b32(c.rs, flag_off(layer, 7, c.i, c.j) + (lane & 15) * 4, 0, 16);
        {
            asm volatile("s_nop 15" : "+v"(a0));
            GS g[4];
            u32 o[4];
#pragma unroll
            for (int q = 0; q < 7; ++q)
#pragma unroll
                for (int p = 0; p < 4; ++p) {
                    const int e = 2 * p, q2 = p >> 1, e2 = e & 3;
                    geglu_sub(g[p], o[p], q, a0[e], a0[e + 1], a0[8 + e], a0[8 + e + 1], cs[q2][e2], cs[q2][e2 + 1], cs[2 + q2][e2], cs[2 + q2][e2 + 1], gk);
                }
            st_sc1(c, hbase + 6u * 1024, u32x4{o[0], o[1], o[2], o[3]});
        }
        STAMP(21);
        WAIT_VM(0);
        BAR();
        STAMP(23);
        if (w == 0) signal_group(c, layer, 4);
    }
    return pf1;
}
__device__ __forceinline__ void gu_helper(const Ctx& c0, int layer, u32x4 (&wr)[40]) {
    const Ctx c = fresh(c0);
    const int lane = c.lane, w = c.w;
    {
        WAIT_VM(0);
        BAR();
#define EXP_SLEEP 16
        for (int z_ = 0; z_ < EXP_SLEEP; ++z_) __builtin_amdgcn_s_sleep(1);
        issue_chunk<CG(2, 0)>(c); issue_chunk<CG(2, 1)>(c); issue_chunk<CG(3, 0)>(c); issue_chunk<CG(3, 1)>(c);
        WAIT_VM(8);
        BAR();
        const u32 wdb = (u32)WS_WD + (u32)((c.j & 7) * 224 + 112 + ((w >> 1) - 2) * 56) * 1024;
#pragma unroll
        for (int q = 0; q < 20; ++q) wr[q] = __builtin_amdgcn_raw_buffer_load_b128(c.rs, c.voff, wdb + q * 1024, 16);
        issue_chunk<CG(4, 0)>(c); issue_chunk<CG(4, 1)>(c);
        WAIT_VM(8);
        BAR();
#pragma unroll
        for (int q = 20; q < 40; ++q) wr[q] = __builtin_amdgcn_raw_buffer_load_b128(c.rs, c.voff, wdb + q * 1024, 16);
        issue_chunk<CG(5, 0)>(c); issue_chunk<CG(5, 1)>(c);
        WAIT_VM(8);
        BAR();
        issue_chunk<CG(6, 0)>(c); issue_chunk<CG(6, 1)>(c);
        WAIT_VM(8);
        BAR();
        issue_chunk<CD(0)>(c); issue_chunk<CD(1)>(c);
        WAIT_VM(8);
        BAR();
        issue_chunk<CD(2)>(c); issue_chunk<CD(3)>(c);
        BAR();
        issue_chunk<CD(4)>(c); issue_chunk<CD(5)>(c);
        WAIT_VM(0);
        BAR();
        issue_chunk<CD(6)>(c);
    }
}

__device__ __forceinline__ void hld(const Ctx& c, u32x4& dst, u32 soff) {
    asm volatile("buffer_load_dwordx4 %0, %1, %2, %3 offen sc1" : "=v"(dst) : "v"(c.voff), "s"(c.rs), "s"(soff) : "memory");
}
template <int N> __device__ __forceinline__ void hwait(u32x4& x) { asm volatile("s_waitcnt vmcnt(%1)" : "+v"(x) : "i"(N) : "memory"); }
template <bool HELPER>
__device__ __forceinline__ void dn_main(const Ctx& c, int layer, int kq, u32 xsrc, u32 wdb, const u32x4* wr, f32x16& acc0, f32x16& acc1) {
    constexpr int XD = 10;
    u32x4 xf[28], wf[8], wl[16];
    const LAS unsigned char* wb[7];
#pragma unroll
    for (int s = 0; s < XD; ++s) hld(c, xf[s], xsrc + (u32)dn_kstep(s) * 1024);
    if (!HELPER) {
#pragma unroll
        for (int ch = 0; ch < 7; ++ch) wb[ch] = lds_base(c, chunk_lds(CD(ch)) + kq * 8192);
#pragma unroll
        for (int q = 0; q < 8; ++q) wf[q] = LDF(wb[0], q);
    }
    SB();
    u32 pf = 0;
#pragma unroll
    for (int s = 0; s < 28; ++s) {
        if (s + XD < 28) hld(c, xf[s + XD], xsrc + (u32)dn_kstep(s + XD) * 1024);
        u32x4 w0, w1;
        if (HELPER) {
            if (s < 20) { w0 = wr[2 * s]; w1 = wr[2 * s + 1]; } else { w0 = wl[2 * (s - 20)]; w1 = wl[2 * (s - 20) + 1]; }
        } else {
            w0 = wf[(2 * s) & 7]; w1 = wf[(2 * s + 1) & 7];
            if (s + 4 < 28) { wf[(2 * s) & 7] = LDF(wb[(s + 4) >> 2], 2 * ((s + 4) & 3)); wf[(2 * s + 1) & 7] = LDF(wb[(s + 4) >> 2], 2 * ((s + 4) & 3) + 1); }
        }
        if (s + XD < 28) { if (HELPER && s >= 8 && s < 12 + XD - 10 + 10 && s <= 21) hwait<10>(xf[s]); else hwait<10>(xf[s]); }
        else if (s == 18) hwait<9>(xf[s]); else if (s == 19) hwait<8>(xf[s]); else if (s == 20) hwait<7>(xf[s]); else if (s == 21) hwait<6>(xf[s]); else if (s == 22) hwait<5>(xf[s]);
        else if (s == 23) hwait<4>(xf[s]); else if (s == 24) hwait<3>(xf[s]); else if (s == 25) hwait<2>(xf[s]); else if (s == 26) hwait<1>(xf[s]); else hwait<0>(xf[s]);
        if (HELPER && s == 20) {
            asm volatile("s_waitcnt vmcnt(6)" : "+v"(wl[0]), "+v"(wl[1]), "+v"(wl[2]), "+v"(wl[3]), "+v"(wl[4]), "+v"(wl[5]), "+v"(wl[6]), "+v"(wl[7]) :: "memory");
            asm volatile("" : "+v"(wl[8]), "+v"(wl[9]), "+v"(wl[10]), "+v"(wl[11]), "+v"(wl[12]), "+v"(wl[13]), "+v"(wl[14]), "+v"(wl[15]));
            w0 = wl[0]; w1 = wl[1];
        }
        acc0 = mfma32(w0, xf[s], acc0);
        acc1 = mfma32(w1, xf[s], acc1);
        PIN(acc0); PIN(acc1);
        if (HELPER && s >= 8 && s < 12) {
#pragma unroll
            for (int q = 0; q < 4; ++q) hld(c, wl[4 * (s - 8) + q], wdb + (u32)(40 + 4 * (s - 8) + q) * 1024);
        }
        if (!HELPER && s == 2 && c.w == 0)
            asm volatile("buffer_load_dword %0, %1, %2, 0 offen sc1" : "=v"(pf) : "v"(flag_off(layer, 4, c.i, c.j) + (c.lane & 15) * 4), "s"(c.rs) : "memory");
        if (s + XD == 15) {
            if (!HELPER && c.w == 0) {
                asm volatile("s_waitcnt vmcnt(3)" : "+v"(pf) :: "memory");
                if (!__all(pf == flag_tag(c, layer, 4))) poll_flags<64>(c, layer, 4, 16);
            }
            BARL();
        }
        SB();
    }
}

__device__ __forceinline__ void phase_ffn(const Ctx& c0, int layer, const Args& a) {
    f32x16 acc0 = {}, acc1 = {};
    u32x4 res[2];
    if (c0.w < 4) {
        const u32 pf1 = gu_compute(c0, layer);
        const Ctx c = fresh(c0);
        const int w = c.w, t2 = w & 1, kq = w >> 1, th = c.j >> 3, cb = c.j & 7;
        const int tbk = 4 * c.i + 2 * th + t2;
        const u32 lbase = (u32)WS_L0;
        res[0] = ld_sc1(c, lbase + (u32)L_X1 + (u32)(tbk * 32 + 4 * cb + kq) * 1024);
        res[1] = ld_sc1(c, lbase + (u32)L_X1 + (u32)(tbk * 32 + 4 * cb + 2 + kq) * 1024);
        if (w == 0 && !__all(pf1 == flag_tag(c, layer, 7))) poll_flags<64>(c, layer, 7, 16);
        BARL();
        STAMP(25);
        dn_main<false>(c, layer, kq, lbase + (u32)L_HF + (u32)(tbk * 112 + 28 * kq) * 1024, 0u, nullptr, acc0, acc1);
    } else {
        u32x4 wr[40];
        gu_helper(c0, layer, wr);
        const Ctx c = fresh(c0);
        const int w = c.w, t2 = w & 1, kq = w >> 1, th = c.j >> 3, cb = c.j & 7;
        const int tbk = 4 * c.i + 2 * th + t2;
        const u32 lbase = (u32)WS_L0;
        BARL();
        dn_main<true>(c, layer, kq, lbase + (u32)L_HF + (u32)(tbk * 112 + 28 * kq) * 1024, (u32)WS_WD + (u32)(cb * 224 + 112 + (kq - 2) * 56) * 1024, wr, acc0, acc1);
    }
    const Ctx c = fresh(c0);
    const int lane = c.lane, w = c.w, t2 = w & 1, kq = w >> 1, h = lane >> 5, r = lane & 31;
    const int th = c.j >> 3, cb = c.j & 7;
    const int tbk = 4 * c.i + 2 * th + t2;
    const u32 lbase = (u32)WS_L0;
    BAR();
    STAMP(26);
    red_write(c, acc0, acc1, t2, kq);
    BAR();
    if (w >= 4) {
        if (layer == 0) { issue_chunk<CQ(0, 0)>(c); issue_chunk<CQ(0, 1)>(c); }
    } else {
        float v[2][8];
        red_read(c, acc0, acc1, t2, kq, v);
#pragma unroll
        for (int x = 0; x < 2; ++x) {
            const int kbo = 4 * cb + 2 * x + kq;
            const u32x4 o = add_res(v[x], res[x]);
            const auto r0 = __builtin_amdgcn_permlane32_swap(o.x, o.z, false, false);
            const auto r1 = __builtin_amdgcn_permlane32_swap(o.y, o.w, false, false);
            const u32x4 on = u32x4{r0[0], r1[0], r0[1], r1[1]};
            if (layer == 1) {
                *(u32x4*)(a.out + (size_t)(tbk * 32 + r) * DM + kbo * 16 + 8 * h) = on;
            } else {
                st_sc1(c, lbase + (u32)L_X2 + (u32)(tbk * 32 + kbo) * 1024, o);
            }
        }
        STAMP(27);
        if (layer == 0) WAIT_VM(0);
    }
    if (layer == 0) {
        BAR();
        if (w == 0) signal_group(c, layer, 0);
        if (w >= 4) { issue_chunk<CQ(1, 0)>(c); issue_chunk<CQ(1, 1)>(c); issue_chunk<CQ(2, 0)>(c); issue_chunk<CQ(2, 1)>(c); }
        STAMP(28);
    }
}

__global__ void __launch_bounds__(512, 2) mega_kernel(Args a) {
    extern __shared__ __attribute__((aligned(16))) unsigned char lds[];
    Ctx c;
    c.L = (LAS unsigned char*)lds;
    c.Laddr = (u32)(size_t)c.L;
    c.ws = a.ws;
    c.rs = __builtin_amdgcn_make_buffer_rsrc((void*)a.ws, 0, (int)(WS_END + 4 * MiB), 0x00020000);
    const int tid = threadIdx.x;
    c.lane = tid & 63;
    c.w = __builtin_amdgcn_readfirstlane(tid >> 6);
    const int bid = blockIdx.x;
#define MAPV 3
    if (MAPV == 1) { c.i = 2 * (bid & 7) + (bid >> 7); c.j = (bid >> 3) & 15; }
    else if (MAPV == 3) { c.i = 4 * ((bid & 7) >> 1) + (bid >> 6); c.j = 8 * (bid & 1) + ((bid >> 3) & 7); }
    else { c.i = 8 * ((bid & 7) >> 2) + (bid >> 5); c.j = 4 * (bid & 3) + ((bid >> 3) & 3); }
    c.voff = c.lane * 16;
    c.tag = 1u;
    if (c.w >= 4) {
        issue_chunk<CQ(0, 0)>(c); issue_chunk<CQ(0, 1)>(c); issue_chunk<CQ(1, 0)>(c); issue_chunk<CQ(1, 1)>(c); issue_chunk<CQ(2, 0)>(c); issue_chunk<CQ(2, 1)>(c);
    } else if (tid < 96) {
        lds_st<float>(c.L, SC_CS + tid * 4, ((const float*)(a.ws + WS_CS))[qkv_col(tid >> 5) + 32 * c.j + (tid & 31)]);
    }
    if (c.w < 4) {
        const int hh = c.lane >> 5, row = c.lane & 31, half = c.w & 1;
        u32 d[4];
#pragma unroll
        for (int q = 0; q < 4; ++q) {
            const int c0 = 16 * half + 8 * (q >> 1) + 4 * hh + 2 * (q & 1), c1 = c0 + 1;
            const u32 lo = c.w < 2 ? (row > c0 ? 0xff7fu : 0u) : (row == c0 ? 0x3f80u : 0u);
            const u32 hi = c.w < 2 ? (row > c1 ? 0xff7fu : 0u) : (row == c1 ? 0x3f80u : 0u);
            d[q] = lo | (hi << 16);
        }
        lds_st<u32x4>(c.L, SC_MK + c.w * 1024 + c.voff, u32x4{d[0], d[1], d[2], d[3]});
    }
    for (int layer = 0; layer < 2; ++layer) {
        phase_qkv_att(c, layer, a);
        phase_wo(c, layer, a);
        phase_ffn(c, layer, a);
    }
}

}

extern "C" void kernel_launch(void* const* d_in, const int* in_sizes, int n_in, void* d_out, int out_size, void* d_ws, size_t ws_size,
                              hipStream_t stream) {
    Args a;
    a.x = (const u16*)d_in[0]; a.wqkv = (const u16*)d_in[1]; a.wo = (const u16*)d_in[2];
    a.wg = (const u16*)d_in[4]; a.wu = (const u16*)d_in[5]; a.wd = (const u16*)d_in[6];
    a.out = (u16*)d_out; a.ws = (unsigned char*)d_ws;
    (void)hipFuncSetAttribute((const void*)mega_kernel, hipFuncAttributeMaxDynamicSharedMemorySize, LDS_BYTES);
    prep_kernel<<<PREP_GRID, 512, 0, stream>>>(a);
    mega_kernel<<<256, 512, LDS_BYTES, stream>>>(a);
}
```

```cpp
#include <hip/hip_runtime.h>
#include <hip/hip_bf16.h>

#define LAS __attribute__((address_space(3)))
typedef unsigned u32;
typedef unsigned short u16;
typedef u32 u32x4 __attribute__((ext_vector_type(4)));
typedef u32 u32x2 __attribute__((ext_vector_type(2)));
typedef float f32x16 __attribute__((ext_vector_type(16)));
typedef float f32x4 __attribute__((ext_vector_type(4)));
typedef float f32x2 __attribute__((ext_vector_type(2)));
typedef __bf16 bf16x8_t __attribute__((ext_vector_type(8)));
typedef __bf16 bf16x2_t __attribute__((ext_vector_type(2)));
typedef short s16x4 __attribute__((ext_vector_type(4)));

namespace {

constexpr int SEQ = 2048, DM = 512, FF = 1792;
constexpr size_t KiB = 1024, MiB = 1024 * 1024;
constexpr size_t WS_FLAGS = 1 * MiB;
constexpr size_t WS_TMO = 768 * KiB;
constexpr size_t WS_CS = 512 * KiB;
constexpr size_t WS_XF0 = 12 * MiB;
constexpr size_t WS_WQ = 14 * MiB;
constexpr size_t WS_CSP = 3 * MiB + 256 * KiB;
constexpr size_t WS_WO = 4 * MiB + 512 * KiB;
constexpr size_t WS_WG = 5 * MiB;
constexpr size_t WS_WD = 9 * MiB;
constexpr size_t WS_L0 = 16 * MiB, WS_LSTRIDE = 32 * MiB;
constexpr size_t L_KF = 0, L_VF = 2 * MiB, L_OF = 4 * MiB, L_X1 = 6 * MiB, L_X2 = 8 * MiB, L_HF = 10 * MiB;
constexpr size_t WS_END = WS_L0 + 2 * WS_LSTRIDE;

constexpr int NSLOT = 8, SLOT = 16384;
constexpr int LDS_SCR = NSLOT * SLOT;
constexpr int VREC = 1040;
constexpr int SC_RV = LDS_SCR;
constexpr int SC_VS = SC_RV + 16640;
constexpr int SC_ST = SC_VS + 8320;
constexpr int SC_CS = SC_ST + 1024;
constexpr int SC_MK = SC_CS + 1280;
constexpr int LDS_BYTES = SC_MK + 4096;
constexpr int SC_KS = 0 * SLOT;
constexpr int SC_QS = 0 * SLOT + 8192;
constexpr int SC_RK = 1 * SLOT;
static_assert(LDS_BYTES <= 163840, "LDS");
__device__ __forceinline__ int red_off(int q) { return q < 5 ? 2 * SLOT + q * 6144 : 6 * SLOT + (q - 5) * 6144; }

constexpr float LN_EPS = 1e-5f;
constexpr float QSCALE = 0.25f * 1.4426950408889634f;
constexpr u32 SPIN_CAP = 1u << 17;
constexpr u32 BF_ONES = 0x3f803f80u;

struct Args {
    const u16* x; const u16* wqkv; const u16* wo; const u16* wg; const u16* wu; const u16* wd;
    u16* out; unsigned char* ws;
};

__device__ __forceinline__ f32x16 mfma32(u32x4 a, u32x4 b, f32x16 c) {
    return __builtin_amdgcn_mfma_f32_32x32x16_bf16(__builtin_bit_cast(bf16x8_t, a), __builtin_bit_cast(bf16x8_t, b), c, 0, 0, 0);
}
__device__ __forceinline__ u32 pack2(float lo, float hi) {
    f32x2 v = {lo, hi};
    return __builtin_bit_cast(u32, __builtin_convertvector(v, bf16x2_t));
}
__device__ __forceinline__ float bflo(u32 u) { return __builtin_bit_cast(float, u << 16); }
__device__ __forceinline__ float bfhi(u32 u) { return __builtin_bit_cast(float, u & 0xffff0000u); }
__device__ __forceinline__ float dot2(u32 a, u32 b, float c) {
    return __builtin_amdgcn_fdot2_f32_bf16(__builtin_bit_cast(bf16x2_t, a), __builtin_bit_cast(bf16x2_t, b), c, false);
}
__device__ __forceinline__ void swap32(float& a, float& b) {
    asm volatile("s_nop 1\n\tv_permlane32_swap_b32 %0, %1\n\ts_nop 1" : "+v"(a), "+v"(b));
}
__device__ __forceinline__ float fmx(float a, float b) { return __builtin_elementwise_maximum(a, b); }
__device__ __forceinline__ float xor32_max(float v) { float a = v, b = v; swap32(a, b); return fmx(a, b); }
__device__ __forceinline__ float xor32_sum(float v) { float a = v, b = v; swap32(a, b); return a + b; }
#define BAR() do { asm volatile("s_waitcnt lgkmcnt(0)" ::: "memory"); __builtin_amdgcn_s_barrier(); asm volatile("" ::: "memory"); } while (0)
#define BARL() do { asm volatile("" ::: "memory"); __builtin_amdgcn_s_barrier(); asm volatile("" ::: "memory"); } while (0)
#define WAIT_VM(n) asm volatile("s_waitcnt vmcnt(" #n ")" ::: "memory")
#define SB() __builtin_amdgcn_sched_barrier(0)

template <class T> __device__ __forceinline__ T lds_ld(const LAS unsigned char* base, int off) { return *(const LAS T*)(base + off); }
template <class T> __device__ __forceinline__ void lds_st(LAS unsigned char* base, int off, T v) { *(LAS T*)(base + off) = v; }

__device__ __forceinline__ void glds4k(const unsigned char* src, u32 voff, u32 dst) {
    u32 keep;
    asm volatile(
        "s_mov_b32 %0, m0\n\t"
        "s_mov_b32 m0, %3\n\t"
        "s_nop 0\n\t"
        "global_load_lds_dwordx4 %1, %2 sc1\n\t"
        "global_load_lds_dwordx4 %1, %2 offset:1024 sc1\n\t"
        "global_load_lds_dwordx4 %1, %2 offset:2048 sc1\n\t"
        "global_load_lds_dwordx4 %1, %2 offset:3072 sc1\n\t"
        "s_mov_b32 m0, %0"
        : "=&s"(keep) : "v"(voff), "s"(src), "s"(dst) : "memory");
}

#define PIN(x) asm volatile("" : "+v"(x))

constexpr int NF_WO = 8 * 64, NF_WG = 16 * 224, NF_WD = 8 * 224;
__host__ __device__ constexpr int qkv_col(int t) { return t == 0 ? 512 : (t == 1 ? 1024 : 0); }
__host__ __device__ constexpr int dn_kstep(int s) { return s < 16 ? 7 * (s >> 2) + (s & 3) : 7 * ((s - 16) / 3) + 4 + (s - 16) % 3; }
struct WItem { const u16* src; size_t dst; };
__device__ __forceinline__ WItem w_item(const Args& a, int f, int lane) {
    const u16 *pA, *pB; int ld, ks; size_t dst;
    if (f < NF_WO) {
        const int cb = f >> 6, rem = f & 63, c = rem >> 4, r2 = rem & 15, kq = r2 >> 2, ks2 = (r2 >> 1) & 1, nt = r2 & 1;
        ks = 8 * kq + 2 * c + ks2; pA = a.wo + 64 * cb + 32 * nt; pB = pA + 16; ld = 512; dst = WS_WO + (size_t)f * 1024;
    } else if ((f -= NF_WO) < NF_WG) {
        const int j = f / 224, rem = f % 224, q = rem >> 4, ksl = rem & 15, c = q >> 1, hf = q & 1;
        ks = 16 * hf + ksl; pA = a.wg + 112 * j + 16 * c; pB = a.wu + 112 * j + 16 * c; ld = FF; dst = WS_WG + (size_t)f * 1024;
    } else {
        f -= NF_WG;
        const int cb = f / 224, rem = f % 224;
        int kq, sidx, nt;
        if (rem < 112) { const int c = rem >> 4, r2 = rem & 15; kq = r2 >> 3; sidx = 4 * c + ((r2 >> 1) & 3); nt = r2 & 1; }
        else { const int g = rem - 112; kq = 2 + g / 56; sidx = (g % 56) >> 1; nt = g & 1; }
        ks = 28 * kq + dn_kstep(sidx); pA = a.wd + 64 * cb + 32 * nt; pB = pA + 16; ld = 512; dst = WS_WD + (size_t)f * 1024;
    }
    const int k = lane >> 2, n8 = lane & 3;
    WItem it;
    it.src = (n8 < 2 ? pA + n8 * 8 : pB + (n8 - 2) * 8) + (size_t)(16 * ks + k) * ld;
    it.dst = dst;
    return it;
}

constexpr int PREP_XI = 64 * 8, NF_WQ = 16 * 96;
constexpr int PREP_WG_TILE = (PREP_XI + NF_WQ) / 8;
constexpr int PREP_WG_CS = 1536 / 64, PREP_WG_FLAGS = 16;
constexpr int PREP_GRID = PREP_WG_TILE + PREP_WG_CS + PREP_WG_FLAGS;
constexpr int PREP_LDS_WAVE = 4608;
constexpr size_t FLAG_BYTES = 2 * 8 * 256 * 512;
__global__ void __launch_bounds__(512) prep_kernel(Args a) {
    __shared__ __attribute__((aligned(16))) unsigned char sm[8 * PREP_LDS_WAVE];
    const int tid = threadIdx.x, lane = tid & 63, w = tid >> 6;
    const int b0 = blockIdx.x;
    unsigned char* ws = a.ws;
    if (b0 < PREP_WG_CS) {
        const u16* src = a.wqkv + b0 * 64;
        const int rg = lane >> 3, ch = lane & 7;
        u32x4 v[8];
#pragma unroll
        for (int it = 0; it < 8; ++it) v[it] = *(const u32x4*)(src + (size_t)(64 * w + 8 * it + rg) * 1536 + ch * 8);
        float s[8];
#pragma unroll
        for (int e = 0; e < 8; ++e) s[e] = 0.f;
#pragma unroll
        for (int it = 0; it < 8; ++it) {
            s[0] += bflo(v[it].x); s[1] += bfhi(v[it].x); s[2] += bflo(v[it].y); s[3] += bfhi(v[it].y);
            s[4] += bflo(v[it].z); s[5] += bfhi(v[it].z); s[6] += bflo(v[it].w); s[7] += bfhi(v[it].w);
        }
#pragma unroll
        for (int e = 0; e < 8; ++e) { s[e] += __shfl_xor(s[e], 8); s[e] += __shfl_xor(s[e], 16); s[e] += __shfl_xor(s[e], 32); }
        float* red = (float*)sm;
        if (lane < 8) {
#pragma unroll
            for (int e = 0; e < 8; ++e) red[w * 64 + lane * 8 + e] = s[e];
        }
        __syncthreads();
        if (tid < 64) ((float*)(ws + WS_CS))[b0 * 64 + tid] = ((red[tid] + red[64 + tid]) + (red[128 + tid] + red[192 + tid])) + ((red[256 + tid] + red[320 + tid]) + (red[384 + tid] + red[448 + tid]));
    } else if (b0 < PREP_WG_CS + PREP_WG_FLAGS) {
        const int g = b0 - PREP_WG_CS;
        const u32x4 z = {0u, 0u, 0u, 0u};
        for (size_t o = (size_t)(g * 512 + tid) * 16; o < FLAG_BYTES; o += (size_t)PREP_WG_FLAGS * 512 * 16) *(u32x4*)(ws + WS_FLAGS + o) = z;
    } else {
        const int item = (b0 - PREP_WG_CS - PREP_WG_FLAGS) * 8 + w;
        LAS unsigned char* t = (LAS unsigned char*)sm + w * PREP_LDS_WAVE;
        if (item < PREP_XI) {
            const int tb = item >> 3, g = item & 7;
            const u16* src = a.x + (size_t)(tb * 32) * DM + g * 64;
            u32x4 v[4];
#pragma unroll
            for (int q = 0; q < 4; ++q) v[q] = *(const u32x4*)(src + (size_t)(8 * q + (lane >> 3)) * DM + (lane & 7) * 8);
#pragma unroll
            for (int q = 0; q < 4; ++q) lds_st<u32x4>(t, (8 * q + (lane >> 3)) * 144 + (lane & 7) * 16, v[q]);
            asm volatile("s_waitcnt lgkmcnt(0)" ::: "memory");
            const int h = lane >> 5, r = lane & 31;
#pragma unroll
            for (int kbl = 0; kbl < 4; ++kbl) {
                const u32x2 lo = lds_ld<u32x2>(t, r * 144 + kbl * 32 + 8 * h);
                const u32x2 hi = lds_ld<u32x2>(t, r * 144 + kbl * 32 + 16 + 8 * h);
                *(u32x4*)(ws + WS_XF0 + (size_t)(tb * 32 + 4 * g + kbl) * 1024 + lane * 16) = u32x4{lo.x, lo.y, hi.x, hi.y};
            }
        } else {
            const int f = item - PREP_XI;
            const int j = f / 96, rem = f % 96, q = rem >> 4, ksl = rem & 15, ct = q >> 1, hf = q & 1;
            const int k = lane >> 2, n8 = lane & 3;
            const u32x4 v = *(const u32x4*)(a.wqkv + qkv_col(ct) + 32 * j + n8 * 8 + (size_t)(16 * (16 * hf + ksl) + k) * 1536);
            lds_st<u32x4>(t, k * 64 + n8 * 16, v);
            asm volatile("s_waitcnt lgkmcnt(0)" ::: "memory");
            const int g4 = lane >> 4, h = g4 >> 1, nh = g4 & 1, li = lane & 15, qq = li >> 2, p = li & 3;
            const s16x4 lo = __builtin_amdgcn_ds_read_tr16_b64_v4i16((LAS s16x4*)(t + (4 * h + qq) * 64 + nh * 32 + p * 8));
            const s16x4 hi = __builtin_amdgcn_ds_read_tr16_b64_v4i16((LAS s16x4*)(t + (8 + 4 * h + qq) * 64 + nh * 32 + p * 8));
            const u32x2 l2 = __builtin_bit_cast(u32x2, lo), h2 = __builtin_bit_cast(u32x2, hi);
            *(u32x4*)(ws + WS_WQ + (size_t)f * 1024 + lane * 16) = u32x4{l2.x, l2.y, h2.x, h2.y};
        }
    }
}

#define STAMP(k) do { } while (0)
struct Ctx {
    LAS unsigned char* L;
    u32 Laddr;
    unsigned char* ws;
    __amdgpu_buffer_rsrc_t rs;
    int lane, w, i, j;
    u32 voff;
    u32 tag;
};

__device__ __forceinline__ u32x4 ld_sc1(const Ctx& c, u32 soff) { return __builtin_amdgcn_raw_buffer_load_b128(c.rs, c.voff, soff, 16); }
__device__ __forceinline__ void st_sc1(const Ctx& c, u32 soff, u32x4 v) { __builtin_amdgcn_raw_buffer_store_b128(v, c.rs, c.voff, soff, 16); }
__device__ __forceinline__ Ctx fresh(const Ctx& c0) {
    Ctx c = c0;
    asm volatile("" : "+s"(c.i), "+s"(c.j), "+s"(c.w), "+s"(c.tag));
    asm volatile("" : "+v"(c.lane));
    c.voff = c.lane * 16;
    return c;
}

constexpr int CQ(int t, int hf) { return 2 * t + hf; }
constexpr int CO(int k) { return 6 + k; }
constexpr int CG(int t, int hf) { return 10 + 2 * t + hf; }
constexpr int CD(int k) { return 24 + k; }
constexpr int slot_of(int n) {
    constexpr int tab[38] = {
          0, 1,   2, 3,   4, 5,
          6, 7, 2, 3,
          4, 5,   0, 1,   2, 3,   6, 7,   4, 5,   0, 1,   2, 3,
          6, 7, 4, 5, 0, 1, 2,   0, 0, 0, 0, 0, 0, 0};
    return tab[n];
}
template <int N> __device__ __forceinline__ const unsigned char* chunk_src(const Ctx& c) {
    size_t src;
    const int cb = c.j & 7;
    if (N < 6) src = WS_WQ + (size_t)(c.j * 6 + N) * SLOT;
    else if (N < 10) src = WS_WO + (size_t)(cb * 4 + (N - 6)) * SLOT;
    else if (N < 24) src = WS_WG + (size_t)(c.j * 14 + (N - 10)) * SLOT;
    else src = WS_WD + (size_t)(cb * 14 + (N - 24)) * SLOT;
    return c.ws + src + (c.w & 3) * 4096;
}
constexpr int chunk_lds(int n) { return slot_of(n) * SLOT; }
template <int N> __device__ __forceinline__ void issue_chunk(const Ctx& c) {
    glds4k(chunk_src<N>(c), c.voff, c.Laddr + chunk_lds(N) + (c.w & 3) * 4096);
}
__device__ __forceinline__ const LAS unsigned char* lds_base(const Ctx& c, int off) {
    u32 a = c.Laddr + off + c.voff;
    asm volatile("" : "+v"(a));
    return (const LAS unsigned char*)(size_t)a;
}

__device__ __forceinline__ u32 flag_off(int layer, int seam, int ci, int cj) {
    return (u32)(WS_FLAGS + (size_t)(((layer * 8 + seam) * 256) + ci * 16 + cj) * 512);
}
__device__ __forceinline__ u32 flag_tag(const Ctx& c, int layer, int seam) { return c.tag + (u32)(layer * 8 + seam); }
__device__ __forceinline__ void flag_store(const Ctx& c, int layer, int seam, int ci, int cj, int word) {
    __builtin_amdgcn_raw_buffer_store_b32(flag_tag(c, layer, seam), c.rs, flag_off(layer, seam, ci, cj) + word * 4, 0, 16);
}
__device__ __forceinline__ void signal_group(const Ctx& c, int layer, int seam) {
    if (c.lane < 16) flag_store(c, layer, seam, c.i, c.lane, c.j);
}
template <int NWORDS> __device__ __forceinline__ void poll_flags(const Ctx& c, int layer, int seam, int nwords = NWORDS) {
    const u32 off = flag_off(layer, seam, c.i, c.j) + c.lane * 4;
    const u32 tag = flag_tag(c, layer, seam);
    for (u32 spins = 0;; ++spins) {
        asm volatile("" ::: "memory");
        const u32 f0 = __builtin_amdgcn_raw_buffer_load_b32(c.rs, off, 0, 16);
        bool ok = c.lane >= nwords || f0 == tag;
        if (NWORDS > 64) { const u32 f1 = __builtin_amdgcn_raw_buffer_load_b32(c.rs, off + 256, 0, 16); ok = ok && f1 == tag; }
        if (__all(ok)) break;
        if (spins > SPIN_CAP) { if (c.lane == 0) __builtin_amdgcn_raw_buffer_store_b32(1u, c.rs, (u32)WS_TMO, 0, 16); break; }
        __builtin_amdgcn_s_sleep(1);
    }
}

__device__ __forceinline__ int crow(int r, int h) { return (r & 3) + 8 * (r >> 2) + 4 * h; }

#define LDF(p, k) (*(const LAS u32x4*)((p) + (k) * 1024))

template <bool SWAP, bool PRE, class F>
__device__ __forceinline__ void c_tile(f32x16& acc, u32x4 (&wf)[8], const LAS unsigned char* lo, const LAS unsigned char* hi, const LAS unsigned char* nxt,
                                       const u32x4 (&xf)[32], F&& filler) {
#pragma unroll
    for (int k = 0; k < 32; ++k) {
        const u32x4 wv = wf[k & 7];
        if (k + 8 < 16) wf[k & 7] = LDF(lo, k + 8);
        else if (k + 8 < 32) wf[k & 7] = LDF(hi, k + 8 - 16);
        else if (PRE) wf[k & 7] = LDF(nxt, k + 8 - 32);
        acc = SWAP ? mfma32(xf[k], wv, acc) : mfma32(wv, xf[k], acc);
        PIN(acc);
        filler(k);
        SB();
    }
}
template <bool MID, class W>
__device__ __forceinline__ f32x2 c_tile0(const Ctx& c, f32x16& acc, u32x4 (&wf)[8], const LAS unsigned char* lo, const LAS unsigned char* hi,
                                         const LAS unsigned char* nxt, u32x4 (&xf)[32], u32 xsoff, W&& wait_ready) {
    constexpr int XPD = 14;
    wait_ready();
#pragma unroll
    for (int k = 0; k < XPD; ++k) xf[k] = ld_sc1(c, xsoff + (u32)k * 1024u);
#pragma unroll
    for (int k = 0; k < 8; ++k) wf[k] = LDF(lo, k);
    SB();
    float s1[4] = {0.f, 0.f, 0.f, 0.f}, s2[4] = {0.f, 0.f, 0.f, 0.f};
#pragma unroll
    for (int k = 0; k < 32; ++k) {
        if (k + XPD < 32) xf[k + XPD] = ld_sc1(c, xsoff + (u32)(k + XPD) * 1024u);
        const u32x4 wv = wf[k & 7];
        if (k + 8 < 16) wf[k & 7] = LDF(lo, k + 8);
        else if (k + 8 < 32) wf[k & 7] = LDF(hi, k + 8 - 16);
        else wf[k & 7] = LDF(nxt, k + 8 - 32);
        acc = mfma32(wv, xf[k], acc);
        PIN(acc);
        s1[0] = dot2(xf[k].x, BF_ONES, s1[0]); s2[0] = dot2(xf[k].x, xf[k].x, s2[0]);
        s1[1] = dot2(xf[k].y, BF_ONES, s1[1]); s2[1] = dot2(xf[k].y, xf[k].y, s2[1]);
        s1[2] = dot2(xf[k].z, BF_ONES, s1[2]); s2[2] = dot2(xf[k].z, xf[k].z, s2[2]);
        s1[3] = dot2(xf[k].w, BF_ONES, s1[3]); s2[3] = dot2(xf[k].w, xf[k].w, s2[3]);
        asm volatile("" : "+v"(s1[0]), "+v"(s1[1]), "+v"(s1[2]), "+v"(s1[3]), "+v"(s2[0]), "+v"(s2[1]), "+v"(s2[2]), "+v"(s2[3]));
        if (MID && k == 3) BARL();
        SB();
    }
    const float a = xor32_sum((s1[0] + s1[1]) + (s1[2] + s1[3])), b = xor32_sum((s2[0] + s2[1]) + (s2[2] + s2[3]));
    const float mean = a * (1.0f / DM);
    float var = b * (1.0f / DM) - mean * mean;
    var = var > 0.f ? var : 0.f;
    f32x2 o; o.x = mean; o.y = __builtin_amdgcn_rsqf(var + LN_EPS);
    return o;
}

struct GS { float g0, g1, t0, t1, u0, u1; };
struct GK { float mean, A, B, ir2; };
__device__ __forceinline__ void geglu_sub(GS& s, u32& o, int q, float ag0, float ag1, float au0, float au1, float cg0, float cg1, float cu0, float cu1, const GK& k) {
    if (q == 0) asm volatile("v_fma_f32 %0, -%7, %5, %3\n\tv_fma_f32 %1, -%7, %6, %4\n\tv_mul_f32 %2, %0, %0"
                             : "=&v"(s.g0), "=&v"(s.g1), "=&v"(s.t0) : "v"(ag0), "v"(ag1), "v"(cg0), "v"(cg1), "v"(k.mean));
    if (q == 1) asm volatile("v_mul_f32 %1, %2, %2\n\tv_fma_f32 %0, %0, %4, %3\n\tv_fma_f32 %1, %1, %4, %3"
                             : "+v"(s.t0), "=&v"(s.t1) : "v"(s.g1), "v"(k.A), "v"(k.B));
    if (q == 2) asm volatile("v_mul_f32 %0, %2, %0\n\tv_mul_f32 %1, %3, %1\n\tv_exp_f32 %0, %0"
                             : "+v"(s.t0), "+v"(s.t1) : "v"(s.g0), "v"(s.g1));
    if (q == 3) asm volatile("v_exp_f32 %0, %0\n\tv_fma_f32 %1, -%7, %5, %3\n\tv_fma_f32 %2, -%7, %6, %4"
                             : "+v"(s.t1), "=&v"(s.u0), "=&v"(s.u1) : "v"(au0), "v"(au1), "v"(cu0), "v"(cu1), "v"(k.mean));
    if (q == 4) asm volatile("v_fma_f32 %0, %0, %4, %4\n\tv_mul_f32 %2, %2, %3\n\tv_fma_f32 %1, %1, %4, %4"
                             : "+v"(s.t0), "+v"(s.t1), "+v"(s.g0) : "v"(s.u0), "v"(k.ir2));
    if (q == 5) asm volatile("v_rcp_f32 %0, %0\n\tv_mul_f32 %2, %2, %3\n\tv_rcp_f32 %1, %1"
                             : "+v"(s.t0), "+v"(s.t1), "+v"(s.g1) : "v"(s.u1));
    if (q == 6) asm volatile("v_mul_f32 %1, %1, %3\n\tv_mul_f32 %2, %2, %4\n\tv_cvt_pk_bf16_f32 %0, %1, %2"
                             : "=&v"(o), "+v"(s.g0), "+v"(s.g1) : "v"(s.t0), "v"(s.t1));
}
struct LS { float t0, t1; };
__device__ __forceinline__ void lnq_a(LS& s, float a0, float c0, float c1, float sc, float msc) {
    asm volatile("v_mul_f32 %0, %6, %3\n\tv_mul_f32 %1, %6, %4\n\tv_fma_f32 %0, %2, %5, -%0"
                 : "=&v"(s.t0), "=&v"(s.t1) : "v"(a0), "v"(c0), "v"(c1), "v"(sc), "v"(msc));
}
__device__ __forceinline__ u32 lnq_b(LS& s, float a1, float sc) {
    u32 o;
    asm volatile("v_fma_f32 %1, %3, %4, -%1\n\tv_cvt_pk_bf16_f32 %0, %2, %1" : "=v"(o), "+v"(s.t1) : "v"(s.t0), "v"(a1), "v"(sc));
    return o;
}
__device__ __forceinline__ void lnv_a(LS& s, float a0, float a1, float m0, float m1, float r0, float cs) {
    asm volatile("v_fma_f32 %0, -%4, %7, %2\n\tv_fma_f32 %1, -%5, %7, %3\n\tv_mul_f32 %0, %0, %6"
                 : "=&v"(s.t0), "=&v"(s.t1) : "v"(a0), "v"(a1), "v"(m0), "v"(m1), "v"(r0), "v"(cs));
}
__device__ __forceinline__ u32 lnv_b(LS& s, float r1) {
    u32 o;
    asm volatile("v_mul_f32 %1, %1, %3\n\tv_cvt_pk_bf16_f32 %0, %2, %1" : "=v"(o), "+v"(s.t1) : "v"(s.t0), "v"(r1));
    return o;
}

__device__ __forceinline__ float max16(const f32x16& s, float m) {
    float a = fmx(fmx(s[0], s[1]), m), b = fmx(s[2], s[3]);
    a = fmx(fmx(s[4], s[5]), a); b = fmx(fmx(s[6], s[7]), b);
    a = fmx(fmx(s[8], s[9]), a); b = fmx(fmx(s[10], s[11]), b);
    a = fmx(fmx(s[12], s[13]), a); b = fmx(fmx(s[14], s[15]), b);
    return fmx(a, b);
}
__device__ __forceinline__ float smul(float a, float b) { float r; asm("v_mul_f32 %0, %1, %2" : "=v"(r) : "v"(a), "v"(b)); return r; }
struct MaskF { u32x4 alo, ahi, ilo, ihi; };
__device__ __forceinline__ f32x16 add_mask(const MaskF& mf, f32x16 s) { s = mfma32(mf.alo, mf.ilo, s); return mfma32(mf.ahi, mf.ihi, s); }
template <int NB, int FIRST, int REST0, int MASK, bool FIRSTSEG>
__device__ __forceinline__ void att_segment(f32x16& oacc, float& m, const LAS unsigned char* kb, const LAS unsigned char* vb, const LAS unsigned char* mk, const u32x4& qf) {
    u32x4 kf[NB];
#pragma unroll
    for (int t = 0; t < NB; ++t) { const int b = t == 0 ? FIRST : REST0 + t - 1; kf[t] = *(const LAS u32x4*)(kb + b * 2048); }
    MaskF mf;
    if (MASK) {
        mf.alo = LDF(mk, 0); mf.ahi = LDF(mk, 1); mf.ilo = LDF(mk, 2); mf.ihi = LDF(mk, 3);
        if (MASK == 2) { mf.alo ^= 0xff7fff7fu; mf.ahi ^= 0xff7fff7fu; }
    }
    SB();
    float mx = FIRSTSEG ? -__builtin_inff() : m;
    f32x16 sv[4];
    sv[0] = mfma32(kf[0], qf, f32x16{});
    if (NB > 1) sv[1] = mfma32(kf[1], qf, f32x16{});
    if (MASK) sv[0] = add_mask(mf, sv[0]);
    SB();
#pragma unroll
    for (int t = 0; t < NB; ++t) {
        const int u = NB > 1 ? (t == 0 ? 1 : t == 1 ? 0 : t) : 0;
        if (t + 2 < NB) sv[(t + 2) & 3] = mfma32(kf[t + 2], qf, f32x16{});
        mx = max16(sv[u & 3], mx);
        SB();
    }
    mx = xor32_max(mx);
    f32x16 negm;
#pragma unroll
    for (int e = 0; e < 16; ++e) negm[e] = -mx;
    SB();
    sv[0] = mfma32(kf[0], qf, negm);
    if (MASK) sv[0] = add_mask(mf, sv[0]);
    u32x4 v0 = *(const LAS u32x4*)(vb + (FIRST * 2 + 0) * VREC);
    u32x4 v1 = *(const LAS u32x4*)(vb + (FIRST * 2 + 1) * VREC);
    if (NB > 1) sv[1] = mfma32(kf[1], qf, negm);
    if (!FIRSTSEG) {
        float f = __builtin_amdgcn_exp2f(m - mx);
        asm volatile("s_nop 1" : "+v"(f));
#pragma unroll
        for (int e = 0; e < 16; ++e) oacc[e] = smul(oacc[e], f);
    }
    m = mx;
    SB();
#pragma unroll
    for (int t = 0; t < NB; ++t) {
        u32x4 n0, n1;
        if (t + 1 < NB) {
            if (t > 0) sv[(t + 1) & 1] = mfma32(kf[t + 1], qf, negm);
            n0 = *(const LAS u32x4*)(vb + ((REST0 + t) * 2 + 0) * VREC);
            n1 = *(const LAS u32x4*)(vb + ((REST0 + t) * 2 + 1) * VREC);
        }
        SB();
        const f32x16& s = sv[t & 1];
        u32 p[8];
#pragma unroll
        for (int e = 0; e < 4; ++e) p[e] = pack2(__builtin_amdgcn_exp2f(s[2 * e]), __builtin_amdgcn_exp2f(s[2 * e + 1]));
        SB();
        oacc = mfma32(v0, u32x4{p[0], p[1], p[2], p[3]}, oacc);
#pragma unroll
        for (int e = 4; e < 8; ++e) p[e] = pack2(__builtin_amdgcn_exp2f(s[2 * e]), __builtin_amdgcn_exp2f(s[2 * e + 1]));
        SB();
        oacc = mfma32(v1, u32x4{p[4], p[5], p[6], p[7]}, oacc);
        if (t + 1 < NB) { v0 = n0; v1 = n1; }
        SB();
    }
}

__device__ __forceinline__ void phase_qkv_att(const Ctx& c0, int layer, const Args& a) {
    const Ctx c = fresh(c0);
    const int lane = c.lane, w = c.w, tb = w & 3, h = lane >> 5, r = lane & 31;
    const u32 lbase = (u32)(WS_L0 + (size_t)layer * WS_LSTRIDE);
    STAMP(0);
    if (w < 4) {
        auto ready = [&]() {
            if (layer > 0 && w == 0) poll_flags<64>(c, layer - 1, 0, 16);
            BAR();
            STAMP(1);
        };
        u32x4 xf[32], wf[8];
        const LAS unsigned char* wK = lds_base(c, slot_of(CQ(0, 0)) * SLOT);
        const LAS unsigned char* wV = lds_base(c, slot_of(CQ(1, 0)) * SLOT);
        const LAS unsigned char* wQ = lds_base(c, slot_of(CQ(2, 0)) * SLOT);
        f32x16 accK = {}, accV = {}, accQ = {};
        const f32x2 ms = c_tile0<true>(c, accK, wf, wK, wK + SLOT, wV, xf,
                                           (layer == 0 ? (u32)WS_XF0 : (u32)(WS_L0 + L_X2)) + (u32)((4 * c.i + tb) * 32) * 1024, ready);
        if (lane < 32) lds_st<f32x2>(c.L, SC_ST + (tb * 32 + r) * 8, ms);
        const float msk = ms.x * ms.y;
        BARL(); STAMP(2);
        f32x4 cs[4];
#pragma unroll
        for (int q = 0; q < 4; ++q) cs[q] = lds_ld<f32x4>(c.L, SC_CS + (0 * 32 + 8 * q + 4 * h) * 4);
        f32x4 mr[8];
        float csv;
        {
            u32 o[8]; LS ls;
            c_tile<true, true>(accV, wf, wV, wV + SLOT, wQ, xf, [&](int k) {
                if (k >= 4 && k < 20) {
                    const int pi = (k - 4) >> 1, e = 2 * pi;
                    if (((k - 4) & 1) == 0) lnq_a(ls, accK[e], cs[e >> 2][e & 3], cs[e >> 2][(e & 3) + 1], ms.y, msk);
                    else o[pi] = lnq_b(ls, accK[e + 1], ms.y);
                }
                if (k == 20 || k == 21) {
                    const int hh = k - 20;
                    const u32x4 ov = {o[4 * hh], o[4 * hh + 1], o[4 * hh + 2], o[4 * hh + 3]};
                    lds_st<u32x4>(c.L, SC_KS + (tb * 2 + hh) * 1024 + c.voff, ov);
                    st_sc1(c, lbase + (u32)L_KF + (u32)((4 * c.i + tb) * 32 + 2 * c.j + hh) * 1024, ov);
                }
                if (k >= 22 && k < 30) { const int q = (k - 22) >> 1; mr[k - 22] = lds_ld<f32x4>(c.L, SC_ST + (tb * 32 + 8 * q + 4 * h + 2 * ((k - 22) & 1)) * 8); }
                if (k == 30) csv = lds_ld<float>(c.L, SC_CS + (32 + r) * 4);
            });
        }
        BARL();
        {
            u32 o[8]; LS ls;
            c_tile<false, false>(accQ, wf, wQ, wQ + SLOT, wQ, xf, [&](int k) {
                if (k >= 2 && k < 18) {
                    const int pi = (k - 2) >> 1;
                    if (((k - 2) & 1) == 0) lnv_a(ls, accV[2 * pi], accV[2 * pi + 1], mr[pi].x, mr[pi].z, mr[pi].y, csv);
                    else o[pi] = lnv_b(ls, mr[pi].w);
                }
                if (k == 18 || k == 20) {
                    const int sv = (k - 18) >> 1;
                    const u32x4 ov = {o[4 * sv], o[4 * sv + 1], o[4 * sv + 2], o[4 * sv + 3]};
                    lds_st<u32x4>(c.L, SC_VS + (tb * 2 + sv) * VREC + c.voff, ov);
                    st_sc1(c, lbase + (u32)L_VF + (u32)(((4 * c.i + tb) * 16 + c.j) * 2 + sv) * 1024, ov);
                }
                if (k >= 24 && k < 28) cs[k - 24] = lds_ld<f32x4>(c.L, SC_CS + (64 + 8 * (k - 24) + 4 * h) * 4);
            });
            if (lane < 2) { u32x4 ones = {BF_ONES, BF_ONES, BF_ONES, BF_ONES}; lds_st<u32x4>(c.L, SC_VS + (tb * 2 + lane) * VREC + 1024, ones); }
        }
        {
            const float sc = ms.y * QSCALE, msc = ms.x * sc;
            asm volatile("s_nop 15" : "+v"(accQ));
            u32 o[8]; LS ls[8];
#pragma unroll
            for (int pi = 0; pi < 8; ++pi) { const int e = 2 * pi; lnq_a(ls[pi], accQ[e], cs[e >> 2][e & 3], cs[e >> 2][(e & 3) + 1], sc, msc); }
#pragma unroll
            for (int pi = 0; pi < 8; ++pi) o[pi] = lnq_b(ls[pi], accQ[2 * pi + 1], sc);
#pragma unroll
            for (int hh = 0; hh < 2; ++hh) lds_st<u32x4>(c.L, SC_QS + (tb * 2 + hh) * 1024 + c.voff, u32x4{o[4 * hh], o[4 * hh + 1], o[4 * hh + 2], o[4 * hh + 3]});
        }
        WAIT_VM(0);
        BAR(); STAMP(5);
        if (w == 0 && lane < 2 && c.i + 1 + lane < 16) flag_store(c, layer, 1, c.i + 1 + lane, c.j, lane);
    } else {
        WAIT_VM(16);
        BAR();
        WAIT_VM(8);
        BARL();
        const int cb = c.j & 7, sidx = c.i * 2 + (c.j >> 3);
        if (layer == 0) {
            const int hw = w & 3;
            LAS unsigned char* t = c.L + SC_VS + hw * 1024;
            WItem it[6]; u32x4 v[6];
#pragma unroll
            for (int q = 0; q < 6; ++q) {
                int f;
                const int fD = NF_WO + NF_WG + cb * 224 + sidx * 7, fO = cb * 64 + sidx * 2;
                if (q < 2 || (q < 4 && hw < 3)) { const int ks = 2 * c.i + (q & 1), cc = hw + 4 * (q >> 1); f = NF_WO + c.j * 224 + (2 * cc + (ks >> 4)) * 16 + (ks & 15); }
                else if (q < 4) f = fD + 4 + (q & 1);
                else if (q == 4) f = fD + (hw < 3 ? hw : 6);
                else f = hw < 2 ? fO + hw : fD + 3;
                it[q] = w_item(a, f, lane);
                v[q] = *(const u32x4*)it[q].src;
            }
            const int k = lane >> 2, n8 = lane & 3;
            const int g4 = lane >> 4, hh = g4 >> 1, nh = g4 & 1, li = lane & 15, qq = li >> 2, p = li & 3;
            float part = 0.f;
#pragma unroll
            for (int q = 0; q < 6; ++q) {
                if (q < 5 || hw < 3) {
                    lds_st<u32x4>(t, k * 64 + n8 * 16, v[q]);
                    asm volatile("s_waitcnt lgkmcnt(0)" ::: "memory");
                    const s16x4 lo = __builtin_amdgcn_ds_read_tr16_b64_v4i16((LAS s16x4*)(t + (4 * hh + qq) * 64 + nh * 32 + p * 8));
                    const s16x4 hi = __builtin_amdgcn_ds_read_tr16_b64_v4i16((LAS s16x4*)(t + (8 + 4 * hh + qq) * 64 + nh * 32 + p * 8));
                    const u32x2 l2 = __builtin_bit_cast(u32x2, lo), h2 = __builtin_bit_cast(u32x2, hi);
                    __builtin_amdgcn_raw_buffer_store_b128(u32x4{l2.x, l2.y, h2.x, h2.y}, c.rs, c.voff, (u32)it[q].dst, 16);
                    if (q < 2 || (q < 4 && hw < 3)) {
                        if ((q & 1) == 0) part = 0.f;
                        part = dot2(l2.x, BF_ONES, part); part = dot2(l2.y, BF_ONES, part); part = dot2(h2.x, BF_ONES, part); part = dot2(h2.y, BF_ONES, part);
                        if (q & 1) {
                            const float tot = xor32_sum(part);
                            const int cc = hw + 4 * (q >> 1);
                            if (lane < 32) __builtin_amdgcn_raw_buffer_store_b32(__builtin_bit_cast(u32, tot), c.rs, (u32)lane * 4u, (u32)WS_CSP + (u32)((c.j * 16 + c.i) * 224 + cc * 32) * 4u, 16);
                        }
                    }
                    asm volatile("s_waitcnt lgkmcnt(0)" ::: "memory");
                }
            }
        }
        WAIT_VM(0);
        BAR();
        if (layer == 0 && w == 4) {
            if (lane < 16) flag_store(c, 0, 5, lane, c.j, c.i);
            if (lane < 32) flag_store(c, 0, 6, lane >> 1, cb + 8 * (lane & 1), sidx);
        }
        BAR();
        if (layer == 0) poll_flags<64>(c, 0, 6, 32);
        issue_chunk<CO(0)>(c); issue_chunk<CO(1)>(c); issue_chunk<CO(2)>(c); issue_chunk<CO(3)>(c);
        BAR();
        if (layer == 0) {
            poll_flags<64>(c, 0, 5, 16);
            const int col = (w & 3) * 64 + lane;
            if (col < 224) {
                float t = 0.f;
#pragma unroll
                for (int i2 = 0; i2 < 16; ++i2) t += __builtin_bit_cast(float, __builtin_amdgcn_raw_buffer_load_b32(c.rs, (u32)col * 4u, (u32)WS_CSP + (u32)((c.j * 16 + i2) * 224) * 4u, 16));
                lds_st<float>(c.L, SC_CS + (96 + col) * 4, t);
            }
        }
        issue_chunk<CG(0, 0)>(c); issue_chunk<CG(0, 1)>(c);
    }
    const int qb = w < 4 ? w : 7 - w, hh = w >> 2;
    const u32x4 qf = lds_ld<u32x4>(c.L, SC_QS + (qb * 2 + hh) * 1024 + c.voff);
    const int vlane = (((lane >> 4) & 1) == hh) ? lane * 16 : 1024;
    f32x16 oacc = {};
    float m = 0.f;
    const LAS unsigned char* mk = lds_base(c, SC_MK);
    {
        const LAS unsigned char* kb = c.L + SC_KS + hh * 1024 + c.voff;
        const LAS unsigned char* vb = c.L + SC_VS + vlane;
        if (qb == 0) att_segment<1, 0, 0, 1, true>(oacc, m, kb, vb, mk, qf);
        else if (qb == 1) att_segment<2, 1, 0, 1, true>(oacc, m, kb, vb, mk, qf);
        else if (qb == 2) att_segment<3, 2, 0, 1, true>(oacc, m, kb, vb, mk, qf);
        else att_segment<4, 3, 0, 1, true>(oacc, m, kb, vb, mk, qf);
    }
    STAMP(6);
    if (c.i > 0) {
        if (w == 0) poll_flags<64>(c, layer, 1, c.i >= 2 ? 2 : 1);
        BAR();
        STAMP(7);
        {
            const int idx = w;
            const int g = 4 * c.i - 8 + idx;
            if (g >= 0) {
                const u32x4 s0 = ld_sc1(c, lbase + (u32)L_KF + (u32)(g * 32 + 2 * c.j + 0) * 1024);
                const u32x4 s1 = ld_sc1(c, lbase + (u32)L_KF + (u32)(g * 32 + 2 * c.j + 1) * 1024);
                const u32x4 s2 = ld_sc1(c, lbase + (u32)L_VF + (u32)((g * 16 + c.j) * 2 + 0) * 1024);
                const u32x4 s3 = ld_sc1(c, lbase + (u32)L_VF + (u32)((g * 16 + c.j) * 2 + 1) * 1024);
                lds_st<u32x4>(c.L, SC_RK + (idx * 2 + 0) * 1024 + c.voff, s0);
                lds_st<u32x4>(c.L, SC_RK + (idx * 2 + 1) * 1024 + c.voff, s1);
                lds_st<u32x4>(c.L, SC_RV + (idx * 2 + 0) * VREC + c.voff, s2);
                lds_st<u32x4>(c.L, SC_RV + (idx * 2 + 1) * VREC + c.voff, s3);
                if (lane < 2) { u32x4 ones = {BF_ONES, BF_ONES, BF_ONES, BF_ONES}; lds_st<u32x4>(c.L, SC_RV + (idx * 2 + lane) * VREC + 1024, ones); }
            }
        }
        BAR();
        STAMP(8);
        const LAS unsigned char* kb = c.L + SC_RK + hh * 1024 + c.voff;
        const LAS unsigned char* vb = c.L + SC_RV + vlane;
        if (c.i == 1) att_segment<4, 4, 5, 0, false>(oacc, m, kb, vb, mk, qf);
        else if (qb == 0) att_segment<8, 0, 1, 2, false>(oacc, m, kb, vb, mk, qf);
        else if (qb == 1) att_segment<7, 1, 2, 2, false>(oacc, m, kb, vb, mk, qf);
        else if (qb == 2) att_segment<6, 2, 3, 2, false>(oacc, m, kb, vb, mk, qf);
        else att_segment<5, 3, 4, 2, false>(oacc, m, kb, vb, mk, qf);
    }
    {
        u32x4 o;
        if (hh == 0) {
            float il = __builtin_amdgcn_rcpf(oacc[8]);
            asm volatile("s_nop 1" : "+v"(il));
            o = u32x4{pack2(smul(oacc[0], il), smul(oacc[1], il)), pack2(smul(oacc[2], il), smul(oacc[3], il)), pack2(smul(oacc[4], il), smul(oacc[5], il)), pack2(smul(oacc[6], il), smul(oacc[7], il))};
        } else {
            float il = __builtin_amdgcn_rcpf(oacc[0]);
            asm volatile("s_nop 1" : "+v"(il));
            o = u32x4{pack2(smul(oacc[8], il), smul(oacc[9], il)), pack2(smul(oacc[10], il), smul(oacc[11], il)), pack2(smul(oacc[12], il), smul(oacc[13], il)), pack2(smul(oacc[14], il), smul(oacc[15], il))};
        }
        st_sc1(c, lbase + (u32)L_OF + (u32)((4 * c.i + qb) * 32 + 2 * c.j + hh) * 1024, o);
    }
    STAMP(9);
    WAIT_VM(0);
    BAR();
    if (w == 0) signal_group(c, layer, 2);
    if (w >= 4) { issue_chunk<CG(1, 0)>(c); issue_chunk<CG(1, 1)>(c); }
    STAMP(10);
}

__device__ __forceinline__ void red_write(const Ctx& c, const f32x16& acc0, const f32x16& acc1, int t2, int kq) {
#pragma unroll
    for (int d = 0; d < 4; ++d) {
        const int fin = d & 1;
        if (kq != fin) {
            const int slot = kq < fin ? kq : kq - 1;
            const int base = red_off(t2 * 4 + d) + slot * 2048 + c.voff;
            const f32x16& a = d < 2 ? acc0 : acc1;
            const int o = (d & 1) * 8;
            lds_st<f32x4>(c.L, base, f32x4{a[o], a[o + 1], a[o + 2], a[o + 3]});
            lds_st<f32x4>(c.L, base + 1024, f32x4{a[o + 4], a[o + 5], a[o + 6], a[o + 7]});
        }
    }
}
__device__ __forceinline__ void red_read(const Ctx& c, const f32x16& acc0, const f32x16& acc1, int t2, int kq, float (&v)[2][8]) {
#pragma unroll
    for (int e = 0; e < 8; ++e) { v[0][e] = kq ? acc0[8 + e] : acc0[e]; v[1][e] = kq ? acc1[8 + e] : acc1[e]; }
#pragma unroll
    for (int x = 0; x < 2; ++x)
#pragma unroll
        for (int s = 0; s < 3; ++s) {
            const int base = red_off(t2 * 4 + 2 * x + kq) + s * 2048 + c.voff;
            const f32x4 lo = lds_ld<f32x4>(c.L, base), hi = lds_ld<f32x4>(c.L, base + 1024);
            v[x][0] += lo.x; v[x][1] += lo.y; v[x][2] += lo.z; v[x][3] += lo.w; v[x][4] += hi.x; v[x][5] += hi.y; v[x][6] += hi.z; v[x][7] += hi.w;
        }
}
__device__ __forceinline__ u32x4 add_res(const float (&v)[8], const u32x4& res) {
    return u32x4{pack2(v[0] + bflo(res.x), v[1] + bfhi(res.x)), pack2(v[2] + bflo(res.y), v[3] + bfhi(res.y)),
                 pack2(v[4] + bflo(res.z), v[5] + bfhi(res.z)), pack2(v[6] + bflo(res.w), v[7] + bfhi(res.w))};
}

__device__ __forceinline__ void phase_wo(const Ctx& c0, int layer, const Args& a) {
    const Ctx c = fresh(c0);
    const int w = c.w, t2 = w & 1, kq = w >> 1;
    const int th = c.j >> 3, cb = c.j & 7;
    const int tbk = 4 * c.i + 2 * th + t2;
    const u32 lbase = (u32)(WS_L0 + (size_t)layer * WS_LSTRIDE);
    if (w == 0) poll_flags<64>(c, layer, 2, 16);
    if (w >= 4) WAIT_VM(8);
    BAR();
    STAMP(11);
    u32x4 xf[8];
#pragma unroll
    for (int s = 0; s < 8; ++s) xf[s] = ld_sc1(c, lbase + (u32)L_OF + (u32)(tbk * 32 + 8 * kq + s) * 1024);
    u32x4 res[2];
    if (w < 4) {
#pragma unroll
        for (int x = 0; x < 2; ++x) {
            const int kbo = 4 * cb + 2 * x + kq;
            res[x] = ld_sc1(c, (layer == 0 ? (u32)WS_XF0 : (u32)(WS_L0 + L_X2)) + (u32)(tbk * 32 + kbo) * 1024);
        }
    }
    u32x4 wf[16];
#pragma unroll
    for (int ch = 0; ch < 4; ++ch) {
        const LAS unsigned char* wb = lds_base(c, slot_of(CO(ch)) * SLOT + kq * 4096);
#pragma unroll
        for (int q = 0; q < 4; ++q) wf[4 * ch + q] = LDF(wb, q);
    }
    SB();
    f32x16 acc0 = {}, acc1 = {};
#pragma unroll
    for (int s = 0; s < 8; ++s) {
        acc0 = mfma32(wf[2 * s], xf[s], acc0);
        acc1 = mfma32(wf[2 * s + 1], xf[s], acc1);
        SB();
    }
    BAR();
    STAMP(12);
    red_write(c, acc0, acc1, t2, kq);
    BAR();
    if (w < 4) {
        float v[2][8];
        red_read(c, acc0, acc1, t2, kq, v);
#pragma unroll
        for (int x = 0; x < 2; ++x) st_sc1(c, lbase + (u32)L_X1 + (u32)(tbk * 32 + 4 * cb + 2 * x + kq) * 1024, add_res(v[x], res[x]));
        STAMP(13);
        WAIT_VM(0);
    }
    BAR();
    if (w == 0) signal_group(c, layer, 3);
    STAMP(14);
}

template <int T> __device__ __forceinline__ void gu_tile(const Ctx& c, f32x16& acc, const f32x16& prev, u32x4 (&wf)[8], const u32x4 (&xf)[32], int h,
                                                         f32x4 (&cs)[4], const GK& gk, u32 hbase) {
    const LAS unsigned char* cur = lds_base(c, slot_of(CG(T, 0)) * SLOT);
    const LAS unsigned char* nxt = lds_base(c, slot_of(CG(T < 6 ? T + 1 : T, 0)) * SLOT);
    GS g;
    u32 o[4];
    acc = f32x16{};
    SB();
    c_tile<false, (T < 6)>(acc, wf, cur, cur + SLOT, nxt, xf, [&](int k) {
        const int p = k >> 3, e = 2 * p, q2 = p >> 1, e2 = e & 3;
        if ((k & 7) < 7) geglu_sub(g, o[p], k & 7, prev[e], prev[e + 1], prev[8 + e], prev[8 + e + 1], cs[q2][e2], cs[q2][e2 + 1], cs[2 + q2][e2], cs[2 + q2][e2 + 1], gk);
        if (k == 15) cs[0] = lds_ld<f32x4>(c.L, SC_CS + (96 + T * 32 + 4 * h) * 4);
        if (k == 15) cs[2] = lds_ld<f32x4>(c.L, SC_CS + (96 + T * 32 + 16 + 4 * h) * 4);
        if (k == 31) cs[1] = lds_ld<f32x4>(c.L, SC_CS + (96 + T * 32 + 8 + 4 * h) * 4);
        if (k == 31) cs[3] = lds_ld<f32x4>(c.L, SC_CS + (96 + T * 32 + 16 + 8 + 4 * h) * 4);
    });
    st_sc1(c, hbase + (u32)(T - 1) * 1024, u32x4{o[0], o[1], o[2], o[3]});
}

__device__ __forceinline__ u32 gu_compute(const Ctx& c0, int layer) {
    const Ctx c = fresh(c0);
    const int lane = c.lane, w = c.w, tb = w & 3, h = lane >> 5;
    const u32 lbase = (u32)(WS_L0 + (size_t)layer * WS_LSTRIDE);
    u32 pf1 = 0;
    {
        u32x4 xf[32], wf[8];
        const LAS unsigned char* w0 = lds_base(c, slot_of(CG(0, 0)) * SLOT);
        const LAS unsigned char* w1 = lds_base(c, slot_of(CG(1, 0)) * SLOT);
        f32x16 a0 = {}, a1;
        const f32x2 ms = c_tile0<false>(c, a0, wf, w0, w0 + SLOT, w1, xf, lbase + (u32)L_X1 + (u32)((4 * c.i + tb) * 32) * 1024, [&]() {
            if (w == 0) poll_flags<64>(c, layer, 3, 16);
            BAR();
            STAMP(15);
        });
        constexpr float GC1 = -2.0f * 0.7978845608028654f * 1.4426950408889634f, GC2 = GC1 * 0.044715f;
        GK gk; gk.mean = ms.x; gk.A = ms.y * GC1; gk.B = ms.y * ms.y * ms.y * GC2; gk.ir2 = 1.0f / (ms.y * ms.y);
        BARL(); STAMP(16);
        f32x4 cs[4];
#pragma unroll
        for (int q = 0; q < 4; ++q) cs[q] = lds_ld<f32x4>(c.L, SC_CS + (96 + (q >> 1) * 16 + 8 * (q & 1) + 4 * h) * 4);
        const u32 hbase = lbase + (u32)L_HF + (u32)((4 * c.i + tb) * 112 + 7 * c.j) * 1024;
        gu_tile<1>(c, a1, a0, wf, xf, h, cs, gk, hbase); BARL();
        gu_tile<2>(c, a0, a1, wf, xf, h, cs, gk, hbase); BARL();
        gu_tile<3>(c, a1, a0, wf, xf, h, cs, gk, hbase); BARL();
        gu_tile<4>(c, a0, a1, wf, xf, h, cs, gk, hbase); BARL();
        gu_tile<5>(c, a1, a0, wf, xf, h, cs, gk, hbase);
        WAIT_VM(1);
        BARL();
        if (w == 0) signal_group(c, layer, 7);
        gu_tile<6>(c, a0, a1, wf, xf, h, cs, gk, hbase);
        STAMP(22);
        if (w == 0) pf1 = __builtin_amdgcn_raw_buffer_load_b32(c.rs, flag_off(layer, 7, c.i, c.j) + (lane & 15) * 4, 0, 16);
        {
            asm volatile("s_nop 15" : "+v"(a0));
            GS g[4];
            u32 o[4];
#pragma unroll
            for (int q = 0; q < 7; ++q)
#pragma unroll
                for (int p = 0; p < 4; ++p) {
                    const int e = 2 * p, q2 = p >> 1, e2 = e & 3;
                    geglu_sub(g[p], o[p], q, a0[e], a0[e + 1], a0[8 + e], a0[8 + e + 1], cs[q2][e2], cs[q2][e2 + 1], cs[2 + q2][e2], cs[2 + q2][e2 + 1], gk);
                }
            st_sc1(c, hbase + 6u * 1024, u32x4{o[0], o[1], o[2], o[3]});
        }
        STAMP(21);
        WAIT_VM(0);
        BAR();
        STAMP(23);
        if (w == 0) signal_group(c, layer, 4);
    }
    return pf1;
}
__device__ __forceinline__ void gu_helper(const Ctx& c0, int layer, u32x4 (&wr)[40]) {
    const Ctx c = fresh(c0);
    const int lane = c.lane, w = c.w;
    {
        WAIT_VM(0);
        BAR();
        __builtin_amdgcn_s_sleep(16);
        issue_chunk<CG(2, 0)>(c); issue_chunk<CG(2, 1)>(c); issue_chunk<CG(3, 0)>(c); issue_chunk<CG(3, 1)>(c);
        WAIT_VM(8);
        BAR();
        const u32 wdb = (u32)WS_WD + (u32)((c.j & 7) * 224 + 112 + ((w >> 1) - 2) * 56) * 1024;
#pragma unroll
        for (int q = 0; q < 20; ++q) wr[q] = __builtin_amdgcn_raw_buffer_load_b128(c.rs, c.voff, wdb + q * 1024, 16);
        issue_chunk<CG(4, 0)>(c); issue_chunk<CG(4, 1)>(c);
        WAIT_VM(8);
        BAR();
#pragma unroll
        for (int q = 20; q < 40; ++q) wr[q] = __builtin_amdgcn_raw_buffer_load_b128(c.rs, c.voff, wdb + q * 1024, 16);
        issue_chunk<CG(5, 0)>(c); issue_chunk<CG(5, 1)>(c);
        WAIT_VM(8);
        BAR();
        issue_chunk<CG(6, 0)>(c); issue_chunk<CG(6, 1)>(c);
        WAIT_VM(8);
        BAR();
        issue_chunk<CD(0)>(c); issue_chunk<CD(1)>(c);
        WAIT_VM(8);
        BAR();
        issue_chunk<CD(2)>(c); issue_chunk<CD(3)>(c);
        BAR();
        issue_chunk<CD(4)>(c); issue_chunk<CD(5)>(c);
        WAIT_VM(0);
        BAR();
        issue_chunk<CD(6)>(c);
    }
}

__device__ __forceinline__ void hld(const Ctx& c, u32x4& dst, u32 soff) {
    asm volatile("buffer_load_dwordx4 %0, %1, %2, %3 offen sc1" : "=v"(dst) : "v"(c.voff), "s"(c.rs), "s"(soff) : "memory");
}
template <int N> __device__ __forceinline__ void hwait(u32x4& x) { asm volatile("s_waitcnt vmcnt(%1)" : "+v"(x) : "i"(N) : "memory"); }
template <bool HELPER>
__device__ __forceinline__ void dn_main(const Ctx& c, int layer, int kq, u32 xsrc, u32 wdb, const u32x4* wr, f32x16& acc0, f32x16& acc1) {
    constexpr int XD = 10;
    u32x4 xf[28], wf[8], wl[16];
    const LAS unsigned char* wb[7];
#pragma unroll
    for (int s = 0; s < XD; ++s) hld(c, xf[s], xsrc + (u32)dn_kstep(s) * 1024);
    if (!HELPER) {
#pragma unroll
        for (int ch = 0; ch < 7; ++ch) wb[ch] = lds_base(c, chunk_lds(CD(ch)) + kq * 8192);
#pragma unroll
        for (int q = 0; q < 8; ++q) wf[q] = LDF(wb[0], q);
    }
    SB();
    u32 pf = 0;
#pragma unroll
    for (int s = 0; s < 28; ++s) {
        if (s + XD < 28) hld(c, xf[s + XD], xsrc + (u32)dn_kstep(s + XD) * 1024);
        u32x4 w0, w1;
        if (HELPER) {
            if (s < 20) { w0 = wr[2 * s]; w1 = wr[2 * s + 1]; } else { w0 = wl[2 * (s - 20)]; w1 = wl[2 * (s - 20) + 1]; }
        } else {
            w0 = wf[(2 * s) & 7]; w1 = wf[(2 * s + 1) & 7];
            if (s + 4 < 28) { wf[(2 * s) & 7] = LDF(wb[(s + 4) >> 2], 2 * ((s + 4) & 3)); wf[(2 * s + 1) & 7] = LDF(wb[(s + 4) >> 2], 2 * ((s + 4) & 3) + 1); }
        }
        if (s + XD < 28) { if (HELPER && s >= 8 && s < 12 + XD - 10 + 10 && s <= 21) hwait<10>(xf[s]); else hwait<10>(xf[s]); }
        else if (s == 18) hwait<9>(xf[s]); else if (s == 19) hwait<8>(xf[s]); else if (s == 20) hwait<7>(xf[s]); else if (s == 21) hwait<6>(xf[s]); else if (s == 22) hwait<5>(xf[s]);
        else if (s == 23) hwait<4>(xf[s]); else if (s == 24) hwait<3>(xf[s]); else if (s == 25) hwait<2>(xf[s]); else if (s == 26) hwait<1>(xf[s]); else hwait<0>(xf[s]);
        if (HELPER && s == 20) {
            asm volatile("s_waitcnt vmcnt(6)" : "+v"(wl[0]), "+v"(wl[1]), "+v"(wl[2]), "+v"(wl[3]), "+v"(wl[4]), "+v"(wl[5]), "+v"(wl[6]), "+v"(wl[7]) :: "memory");
            asm volatile("" : "+v"(wl[8]), "+v"(wl[9]), "+v"(wl[10]), "+v"(wl[11]), "+v"(wl[12]), "+v"(wl[13]), "+v"(wl[14]), "+v"(wl[15]));
            w0 = wl[0]; w1 = wl[1];
        }
        acc0 = mfma32(w0, xf[s], acc0);
        acc1 = mfma32(w1, xf[s], acc1);
        PIN(acc0); PIN(acc1);
        if (HELPER && s >= 8 && s < 12) {
#pragma unroll
            for (int q = 0; q < 4; ++q) hld(c, wl[4 * (s - 8) + q], wdb + (u32)(40 + 4 * (s - 8) + q) * 1024);
        }
        if (!HELPER && s == 2 && c.w == 0)
            asm volatile("buffer_load_dword %0, %1, %2, 0 offen sc1" : "=v"(pf) : "v"(flag_off(layer, 4, c.i, c.j) + (c.lane & 15) * 4), "s"(c.rs) : "memory");
        if (s + XD == 15) {
            if (!HELPER && c.w == 0) {
                asm volatile("s_waitcnt vmcnt(3)" : "+v"(pf) :: "memory");
                if (!__all(pf == flag_tag(c, layer, 4))) poll_flags<64>(c, layer, 4, 16);
            }
            BARL();
        }
        SB();
    }
}

__device__ __forceinline__ void phase_ffn(const Ctx& c0, int layer, const Args& a) {
    f32x16 acc0 = {}, acc1 = {};
    u32x4 res[2];
    if (c0.w < 4) {
        const u32 pf1 = gu_compute(c0, layer);
        const Ctx c = fresh(c0);
        const int w = c.w, t2 = w & 1, kq = w >> 1, th = c.j >> 3, cb = c.j & 7;
        const int tbk = 4 * c.i + 2 * th + t2;
        const u32 lbase = (u32)(WS_L0 + (size_t)layer * WS_LSTRIDE);
        res[0] = ld_sc1(c, lbase + (u32)L_X1 + (u32)(tbk * 32 + 4 * cb + kq) * 1024);
        res[1] = ld_sc1(c, lbase + (u32)L_X1 + (u32)(tbk * 32 + 4 * cb + 2 + kq) * 1024);
        if (w == 0 && !__all(pf1 == flag_tag(c, layer, 7))) poll_flags<64>(c, layer, 7, 16);
        BARL();
        STAMP(25);
        dn_main<false>(c, layer, kq, lbase + (u32)L_HF + (u32)(tbk * 112 + 28 * kq) * 1024, 0u, nullptr, acc0, acc1);
    } else {
        u32x4 wr[40];
        gu_helper(c0, layer, wr);
        const Ctx c = fresh(c0);
        const int w = c.w, t2 = w & 1, kq = w >> 1, th = c.j >> 3, cb = c.j & 7;
        const int tbk = 4 * c.i + 2 * th + t2;
        const u32 lbase = (u32)(WS_L0 + (size_t)layer * WS_LSTRIDE);
        BARL();
        dn_main<true>(c, layer, kq, lbase + (u32)L_HF + (u32)(tbk * 112 + 28 * kq) * 1024, (u32)WS_WD + (u32)(cb * 224 + 112 + (kq - 2) * 56) * 1024, wr, acc0, acc1);
    }
    const Ctx c = fresh(c0);
    const int lane = c.lane, w = c.w, t2 = w & 1, kq = w >> 1, h = lane >> 5, r = lane & 31;
    const int th = c.j >> 3, cb = c.j & 7;
    const int tbk = 4 * c.i + 2 * th + t2;
    const u32 lbase = (u32)(WS_L0 + (size_t)layer * WS_LSTRIDE);
    BAR();
    STAMP(26);
    red_write(c, acc0, acc1, t2, kq);
    BAR();
    if (w >= 4) {
        if (layer == 0) { issue_chunk<CQ(0, 0)>(c); issue_chunk<CQ(0, 1)>(c); }
    } else {
        float v[2][8];
        red_read(c, acc0, acc1, t2, kq, v);
#pragma unroll
        for (int x = 0; x < 2; ++x) {
            const int kbo = 4 * cb + 2 * x + kq;
            const u32x4 o = add_res(v[x], res[x]);
            const auto r0 = __builtin_amdgcn_permlane32_swap(o.x, o.z, false, false);
            const auto r1 = __builtin_amdgcn_permlane32_swap(o.y, o.w, false, false);
            const u32x4 on = u32x4{r0[0], r1[0], r0[1], r1[1]};
            if (layer == 1) {
                *(u32x4*)(a.out + (size_t)(tbk * 32 + r) * DM + kbo * 16 + 8 * h) = on;
            } else {
                st_sc1(c, lbase + (u32)L_X2 + (u32)(tbk * 32 + kbo) * 1024, o);
            }
        }
        STAMP(27);
        if (layer == 0) WAIT_VM(0);
    }
    if (layer == 0) {
        BAR();
        if (w == 0) signal_group(c, layer, 0);
        if (w >= 4) { issue_chunk<CQ(1, 0)>(c); issue_chunk<CQ(1, 1)>(c); issue_chunk<CQ(2, 0)>(c); issue_chunk<CQ(2, 1)>(c); }
        STAMP(28);
    }
}

__global__ void __launch_bounds__(512, 2) mega_kernel(Args a) {
    extern __shared__ __attribute__((aligned(16))) unsigned char lds[];
    Ctx c;
    c.L = (LAS unsigned char*)lds;
    c.Laddr = (u32)(size_t)c.L;
    c.ws = a.ws;
    c.rs = __builtin_amdgcn_make_buffer_rsrc((void*)a.ws, 0, (int)(WS_END + 4 * MiB), 0x00020000);
    const int tid = threadIdx.x;
    c.lane = tid & 63;
    c.w = __builtin_amdgcn_readfirstlane(tid >> 6);
    const int bid = blockIdx.x;
    c.i = 4 * ((bid & 7) >> 1) + (bid >> 6);
    c.j = 8 * (bid & 1) + ((bid >> 3) & 7);
    c.voff = c.lane * 16;
    c.tag = 1u;
    if (c.w >= 4) {
        issue_chunk<CQ(0, 0)>(c); issue_chunk<CQ(0, 1)>(c); issue_chunk<CQ(1, 0)>(c); issue_chunk<CQ(1, 1)>(c); issue_chunk<CQ(2, 0)>(c); issue_chunk<CQ(2, 1)>(c);
    } else if (tid < 96) {
        lds_st<float>(c.L, SC_CS + tid * 4, ((const float*)(a.ws + WS_CS))[qkv_col(tid >> 5) + 32 * c.j + (tid & 31)]);
    }
    if (c.w < 4) {
        const int hh = c.lane >> 5, row = c.lane & 31, half = c.w & 1;
        u32 d[4];
#pragma unroll
        for (int q = 0; q < 4; ++q) {
            const int c0 = 16 * half + 8 * (q >> 1) + 4 * hh + 2 * (q & 1), c1 = c0 + 1;
            const u32 lo = c.w < 2 ? (row > c0 ? 0xff7fu : 0u) : (row == c0 ? 0x3f80u : 0u);
            const u32 hi = c.w < 2 ? (row > c1 ? 0xff7fu : 0u) : (row == c1 ? 0x3f80u : 0u);
            d[q] = lo | (hi << 16);
        }
        lds_st<u32x4>(c.L, SC_MK + c.w * 1024 + c.voff, u32x4{d[0], d[1], d[2], d[3]});
    }
    for (int layer = 0; layer < 2; ++layer) {
        phase_qkv_att(c, layer, a);
        phase_wo(c, layer, a);
        phase_ffn(c, layer, a);
    }
}

}

extern "C" void kernel_launch(void* const* d_in, const int* in_sizes, int n_in, void* d_out, int out_size, void* d_ws, size_t ws_size,
                              hipStream_t stream) {
    Args a;
    a.x = (const u16*)d_in[0]; a.wqkv = (const u16*)d_in[1]; a.wo = (const u16*)d_in[2];
    a.wg = (const u16*)d_in[4]; a.wu = (const u16*)d_in[5]; a.wd = (const u16*)d_in[6];
    a.out = (u16*)d_out; a.ws = (unsigned char*)d_ws;
    (void)hipFuncSetAttribute((const void*)mega_kernel, hipFuncAttributeMaxDynamicSharedMemorySize, LDS_BYTES);
    prep_kernel<<<PREP_GRID, 512, 0, stream>>>(a);
    mega_kernel<<<256, 512, LDS_BYTES, stream>>>(a);
}
```

```cpp
#include <hip/hip_runtime.h>
#include <hip/hip_bf16.h>

#define LAS __attribute__((address_space(3)))
typedef unsigned u32;
typedef unsigned short u16;
typedef u32 u32x4 __attribute__((ext_vector_type(4)));
typedef u32 u32x2 __attribute__((ext_vector_type(2)));
typedef float f32x16 __attribute__((ext_vector_type(16)));
typedef float f32x4 __attribute__((ext_vector_type(4)));
typedef float f32x2 __attribute__((ext_vector_type(2)));
typedef __bf16 bf16x8_t __attribute__((ext_vector_type(8)));
typedef __bf16 bf16x2_t __attribute__((ext_vector_type(2)));
typedef short s16x4 __attribute__((ext_vector_type(4)));

namespace {

constexpr int SEQ = 2048, DM = 512, FF = 1792;
constexpr size_t KiB = 1024, MiB = 1024 * 1024;
constexpr size_t WS_FLAGS = 1 * MiB;
constexpr size_t WS_TMO = 768 * KiB;
constexpr size_t WS_CS = 512 * KiB;
constexpr size_t WS_XF0 = 12 * MiB;
constexpr size_t WS_WQ = 14 * MiB;
constexpr size_t WS_CSP = 3 * MiB + 256 * KiB;
constexpr size_t WS_WO = 4 * MiB + 512 * KiB;
constexpr size_t WS_WG = 5 * MiB;
constexpr size_t WS_WD = 9 * MiB;
constexpr size_t WS_L0 = 16 * MiB, WS_LSTRIDE = 32 * MiB;
constexpr size_t L_KF = 0, L_VF = 2 * MiB, L_OF = 4 * MiB, L_X1 = 6 * MiB, L_X2 = 8 * MiB, L_HF = 10 * MiB;
constexpr size_t WS_END = WS_L0 + 2 * WS_LSTRIDE;

constexpr int NSLOT = 8, SLOT = 16384;
constexpr int LDS_SCR = NSLOT * SLOT;
constexpr int VREC = 1040;
constexpr int SC_RV = LDS_SCR;
constexpr int SC_VS = SC_RV + 16640;
constexpr int SC_ST = SC_VS + 8320;
constexpr int SC_CS = SC_ST + 1024;
constexpr int SC_MK = SC_CS + 1280;
constexpr int LDS_BYTES = SC_MK + 4096;
constexpr int SC_KS = 0 * SLOT;
constexpr int SC_QS = 0 * SLOT + 8192;
constexpr int SC_RK = 1 * SLOT;
static_assert(LDS_BYTES <= 163840, "LDS");
__device__ __forceinline__ int red_off(int q) { return q < 5 ? 2 * SLOT + q * 6144 : 6 * SLOT + (q - 5) * 6144; }

constexpr float LN_EPS = 1e-5f;
constexpr float QSCALE = 0.25f * 1.4426950408889634f;
constexpr u32 SPIN_CAP = 1u << 17;
constexpr u32 BF_ONES = 0x3f803f80u;

struct Args {
    const u16* x; const u16* wqkv; const u16* wo; const u16* wg; const u16* wu; const u16* wd;
    u16* out; unsigned char* ws;
};

__device__ __forceinline__ f32x16 mfma32(u32x4 a, u32x4 b, f32x16 c) {
    return __builtin_amdgcn_mfma_f32_32x32x16_bf16(__builtin_bit_cast(bf16x8_t, a), __builtin_bit_cast(bf16x8_t, b), c, 0, 0, 0);
}
__device__ __forceinline__ u32 pack2(float lo, float hi) {
    f32x2 v = {lo, hi};
    return __builtin_bit_cast(u32, __builtin_convertvector(v, bf16x2_t));
}
__device__ __forceinline__ float bflo(u32 u) { return __builtin_bit_cast(float, u << 16); }
__device__ __forceinline__ float bfhi(u32 u) { return __builtin_bit_cast(float, u & 0xffff0000u); }
__device__ __forceinline__ float dot2(u32 a, u32 b, float c) {
    return __builtin_amdgcn_fdot2_f32_bf16(__builtin_bit_cast(bf16x2_t, a), __builtin_bit_cast(bf16x2_t, b), c, false);
}
__device__ __forceinline__ void swap32(float& a, float& b) {
    asm volatile("s_nop 1\n\tv_permlane32_swap_b32 %0, %1\n\ts_nop 1" : "+v"(a), "+v"(b));
}
__device__ __forceinline__ float fmx(float a, float b) { return __builtin_elementwise_maximum(a, b); }
__device__ __forceinline__ float xor32_max(float v) { float a = v, b = v; swap32(a, b); return fmx(a, b); }
__device__ __forceinline__ float xor32_sum(float v) { float a = v, b = v; swap32(a, b); return a + b; }
#define BAR() do { asm volatile("s_waitcnt lgkmcnt(0)" ::: "memory"); __builtin_amdgcn_s_barrier(); asm volatile("" ::: "memory"); } while (0)
#define BARL() do { asm volatile("" ::: "memory"); __builtin_amdgcn_s_barrier(); asm volatile("" ::: "memory"); } while (0)
#define WAIT_VM(n) asm volatile("s_waitcnt vmcnt(" #n ")" ::: "memory")
#define SB() __builtin_amdgcn_sched_barrier(0)

template <class T> __device__ __forceinline__ T lds_ld(const LAS unsigned char* base, int off) { return *(const LAS T*)(base + off); }
template <class T> __device__ __forceinline__ void lds_st(LAS unsigned char* base, int off, T v) { *(LAS T*)(base + off) = v; }

__device__ __forceinline__ void glds4k(const unsigned char* src, u32 voff, u32 dst) {
    u32 keep;
    asm volatile(
        "s_mov_b32 %0, m0\n\t"
        "s_mov_b32 m0, %3\n\t"
        "s_nop 0\n\t"
        "global_load_lds_dwordx4 %1, %2 sc1\n\t"
        "global_load_lds_dwordx4 %1, %2 offset:1024 sc1\n\t"
        "global_load_lds_dwordx4 %1, %2 offset:2048 sc1\n\t"
        "global_load_lds_dwordx4 %1, %2 offset:3072 sc1\n\t"
        "s_mov_b32 m0, %0"
        : "=&s"(keep) : "v"(voff), "s"(src), "s"(dst) : "memory");
}

#define PIN(x) asm volatile("" : "+v"(x))

constexpr int NF_WO = 8 * 64, NF_WG = 16 * 224, NF_WD = 8 * 224;
__host__ __device__ constexpr int qkv_col(int t) { return t == 0 ? 512 : (t == 1 ? 1024 : 0); }
__host__ __device__ constexpr int dn_kstep(int s) { return s < 16 ? 7 * (s >> 2) + (s & 3) : 7 * ((s - 16) / 3) + 4 + (s - 16) % 3; }
struct WItem { const u16* src; size_t dst; };
__device__ __forceinline__ WItem w_item(const Args& a, int f, int lane) {
    const u16 *pA, *pB; int ld, ks; size_t dst;
    if (f < NF_WO) {
        const int cb = f >> 6, rem = f & 63, c = rem >> 4, r2 = rem & 15, kq = r2 >> 2, ks2 = (r2 >> 1) & 1, nt = r2 & 1;
        ks = 8 * kq + 2 * c + ks2; pA = a.wo + 64 * cb + 32 * nt; pB = pA + 16; ld = 512; dst = WS_WO + (size_t)f * 1024;
    } else if ((f -= NF_WO) < NF_WG) {
        const int j = f / 224, rem = f % 224, q = rem >> 4, ksl = rem & 15, c = q >> 1, hf = q & 1;
        ks = 16 * hf + ksl; pA = a.wg + 112 * j + 16 * c; pB = a.wu + 112 * j + 16 * c; ld = FF; dst = WS_WG + (size_t)f * 1024;
    } else {
        f -= NF_WG;
        const int cb = f / 224, rem = f % 224;
        int kq, sidx, nt;
        if (rem < 112) { const int c = rem >> 4, r2 = rem & 15; kq = r2 >> 3; sidx = 4 * c + ((r2 >> 1) & 3); nt = r2 & 1; }
        else { const int g = rem - 112; kq = 2 + g / 56; sidx = (g % 56) >> 1; nt = g & 1; }
        ks = 28 * kq + dn_kstep(sidx); pA = a.wd + 64 * cb + 32 * nt; pB = pA + 16; ld = 512; dst = WS_WD + (size_t)f * 1024;
    }
    const int k = lane >> 2, n8 = lane & 3;
    WItem it;
    it.src = (n8 < 2 ? pA + n8 * 8 : pB + (n8 - 2) * 8) + (size_t)(16 * ks + k) * ld;
    it.dst = dst;
    return it;
}

constexpr int PREP_XI = 64 * 8, NF_WQ = 16 * 96;
constexpr int PREP_WG_TILE = (PREP_XI + NF_WQ) / 8;
constexpr int PREP_WG_CS = 1536 / 64, PREP_WG_FLAGS = 16;
constexpr int PREP_GRID = PREP_WG_TILE + PREP_WG_CS + PREP_WG_FLAGS;
constexpr int PREP_LDS_WAVE = 4608;
#define PREP_AUX 0
constexpr size_t FLAG_BYTES = 2 * 8 * 256 * 128;
__global__ void __launch_bounds__(512) prep_kernel(Args a) {
    __shared__ __attribute__((aligned(16))) unsigned char sm[8 * PREP_LDS_WAVE];
    const int tid = threadIdx.x, lane = tid & 63, w = tid >> 6;
    const int b0 = blockIdx.x;
    unsigned char* ws = a.ws;
    const __amdgpu_buffer_rsrc_t rs = __builtin_amdgcn_make_buffer_rsrc((void*)a.ws, 0, (int)WS_L0, 0x00020000);
#define PST(off, v) __builtin_amdgcn_raw_buffer_store_b128(v, rs, (u32)(off), 0, PREP_AUX)
    if (b0 < PREP_WG_CS) {
        const u16* src = a.wqkv + b0 * 64;
        const int rg = lane >> 3, ch = lane & 7;
        u32x4 v[8];
#pragma unroll
        for (int it = 0; it < 8; ++it) v[it] = *(const u32x4*)(src + (size_t)(64 * w + 8 * it + rg) * 1536 + ch * 8);
        float s[8];
#pragma unroll
        for (int e = 0; e < 8; ++e) s[e] = 0.f;
#pragma unroll
        for (int it = 0; it < 8; ++it) {
            s[0] += bflo(v[it].x); s[1] += bfhi(v[it].x); s[2] += bflo(v[it].y); s[3] += bfhi(v[it].y);
            s[4] += bflo(v[it].z); s[5] += bfhi(v[it].z); s[6] += bflo(v[it].w); s[7] += bfhi(v[it].w);
        }
#pragma unroll
        for (int e = 0; e < 8; ++e) { s[e] += __shfl_xor(s[e], 8); s[e] += __shfl_xor(s[e], 16); s[e] += __shfl_xor(s[e], 32); }
        float* red = (float*)sm;
        if (lane < 8) {
#pragma unroll
            for (int e = 0; e < 8; ++e) red[w * 64 + lane * 8 + e] = s[e];
        }
        __syncthreads();
        if (tid < 64) ((float*)(ws + WS_CS))[b0 * 64 + tid] = ((red[tid] + red[64 + tid]) + (red[128 + tid] + red[192 + tid])) + ((red[256 + tid] + red[320 + tid]) + (red[384 + tid] + red[448 + tid]));
    } else if (b0 < PREP_WG_CS + PREP_WG_FLAGS) {
        const int g = b0 - PREP_WG_CS;
        const u32x4 z = {0u, 0u, 0u, 0u};
        for (size_t o = (size_t)(g * 512 + tid) * 16; o < FLAG_BYTES; o += (size_t)PREP_WG_FLAGS * 512 * 16) PST(WS_FLAGS + o, z);
    } else {
        const int item = (b0 - PREP_WG_CS - PREP_WG_FLAGS) * 8 + w;
        LAS unsigned char* t = (LAS unsigned char*)sm + w * PREP_LDS_WAVE;
        if (item < PREP_XI) {
            const int tb = item >> 3, g = item & 7;
            const u16* src = a.x + (size_t)(tb * 32) * DM + g * 64;
            u32x4 v[4];
#pragma unroll
            for (int q = 0; q < 4; ++q) v[q] = *(const u32x4*)(src + (size_t)(8 * q + (lane >> 3)) * DM + (lane & 7) * 8);
#pragma unroll
            for (int q = 0; q < 4; ++q) lds_st<u32x4>(t, (8 * q + (lane >> 3)) * 144 + (lane & 7) * 16, v[q]);
            asm volatile("s_waitcnt lgkmcnt(0)" ::: "memory");
            const int h = lane >> 5, r = lane & 31;
#pragma unroll
            for (int kbl = 0; kbl < 4; ++kbl) {
                const u32x2 lo = lds_ld<u32x2>(t, r * 144 + kbl * 32 + 8 * h);
                const u32x2 hi = lds_ld<u32x2>(t, r * 144 + kbl * 32 + 16 + 8 * h);
                PST(WS_XF0 + (size_t)(tb * 32 + 4 * g + kbl) * 1024 + lane * 16, (u32x4{lo.x, lo.y, hi.x, hi.y}));
            }
        } else {
            const int f = item - PREP_XI;
            const int j = f / 96, rem = f % 96, q = rem >> 4, ksl = rem & 15, ct = q >> 1, hf = q & 1;
            const int k = lane >> 2, n8 = lane & 3;
            const u32x4 v = *(const u32x4*)(a.wqkv + qkv_col(ct) + 32 * j + n8 * 8 + (size_t)(16 * (16 * hf + ksl) + k) * 1536);
            lds_st<u32x4>(t, k * 64 + n8 * 16, v);
            asm volatile("s_waitcnt lgkmcnt(0)" ::: "memory");
            const int g4 = lane >> 4, h = g4 >> 1, nh = g4 & 1, li = lane & 15, qq = li >> 2, p = li & 3;
            const s16x4 lo = __builtin_amdgcn_ds_read_tr16_b64_v4i16((LAS s16x4*)(t + (4 * h + qq) * 64 + nh * 32 + p * 8));
            const s16x4 hi = __builtin_amdgcn_ds_read_tr16_b64_v4i16((LAS s16x4*)(t + (8 + 4 * h + qq) * 64 + nh * 32 + p * 8));
            const u32x2 l2 = __builtin_bit_cast(u32x2, lo), h2 = __builtin_bit_cast(u32x2, hi);
            PST(WS_WQ + (size_t)f * 1024 + lane * 16, (u32x4{l2.x, l2.y, h2.x, h2.y}));
        }
    }
}

#define STAMP(k) do { } while (0)
struct Ctx {
    LAS unsigned char* L;
    u32 Laddr;
    unsigned char* ws;
    __amdgpu_buffer_rsrc_t rs;
    int lane, w, i, j;
    u32 voff;
    u32 tag;
};

__device__ __forceinline__ u32x4 ld_sc1(const Ctx& c, u32 soff) { return __builtin_amdgcn_raw_buffer_load_b128(c.rs, c.voff, soff, 16); }
__device__ __forceinline__ void st_sc1(const Ctx& c, u32 soff, u32x4 v) { __builtin_amdgcn_raw_buffer_store_b128(v, c.rs, c.voff, soff, 16); }
__device__ __forceinline__ Ctx fresh(const Ctx& c0) {
    Ctx c = c0;
    asm volatile("" : "+s"(c.i), "+s"(c.j), "+s"(c.w), "+s"(c.tag));
    asm volatile("" : "+v"(c.lane));
    c.voff = c.lane * 16;
    return c;
}

constexpr int CQ(int t, int hf) { return 2 * t + hf; }
constexpr int CO(int k) { return 6 + k; }
constexpr int CG(int t, int hf) { return 10 + 2 * t + hf; }
constexpr int CD(int k) { return 24 + k; }
constexpr int slot_of(int n) {
    constexpr int tab[38] = {
          0, 1,   2, 3,   4, 5,
          6, 7, 2, 3,
          4, 5,   0, 1,   2, 3,   6, 7,   4, 5,   0, 1,   2, 3,
          6, 7, 4, 5, 0, 1, 2,   0, 0, 0, 0, 0, 0, 0};
    return tab[n];
}
template <int N> __device__ __forceinline__ const unsigned char* chunk_src(const Ctx& c) {
    size_t src;
    const int cb = c.j & 7;
    if (N < 6) src = WS_WQ + (size_t)(c.j * 6 + N) * SLOT;
    else if (N < 10) src = WS_WO + (size_t)(cb * 4 + (N - 6)) * SLOT;
    else if (N < 24) src = WS_WG + (size_t)(c.j * 14 + (N - 10)) * SLOT;
    else src = WS_WD + (size_t)(cb * 14 + (N - 24)) * SLOT;
    return c.ws + src + (c.w & 3) * 4096;
}
constexpr int chunk_lds(int n) { return slot_of(n) * SLOT; }
template <int N> __device__ __forceinline__ void issue_chunk(const Ctx& c) {
    glds4k(chunk_src<N>(c), c.voff, c.Laddr + chunk_lds(N) + (c.w & 3) * 4096);
}
__device__ __forceinline__ const LAS unsigned char* lds_base(const Ctx& c, int off) {
    u32 a = c.Laddr + off + c.voff;
    asm volatile("" : "+v"(a));
    return (const LAS unsigned char*)(size_t)a;
}

__device__ __forceinline__ u32 flag_off(int layer, int seam, int ci, int cj) {
    return (u32)(WS_FLAGS + (size_t)(((layer * 8 + seam) * 256) + ci * 16 + cj) * 128);
}
__device__ __forceinline__ u32 flag_tag(const Ctx& c, int layer, int seam) { return c.tag + (u32)(layer * 8 + seam); }
__device__ __forceinline__ void flag_store(const Ctx& c, int layer, int seam, int ci, int cj, int word) {
    __builtin_amdgcn_raw_buffer_store_b32(flag_tag(c, layer, seam), c.rs, flag_off(layer, seam, ci, cj) + word * 4, 0, 16);
}
__device__ __forceinline__ void signal_group(const Ctx& c, int layer, int seam) {
    if (c.lane < 16) flag_store(c, layer, seam, c.i, c.lane, c.j);
}
template <int NWORDS> __device__ __forceinline__ void poll_flags(const Ctx& c, int layer, int seam, int nwords = NWORDS) {
    const u32 off = flag_off(layer, seam, c.i, c.j) + (c.lane & 31) * 4;
    const u32 tag = flag_tag(c, layer, seam);
    for (u32 spins = 0;; ++spins) {
        asm volatile("" ::: "memory");
        const u32 f0 = __builtin_amdgcn_raw_buffer_load_b32(c.rs, off, 0, 16);
        const bool ok = (c.lane & 31) >= nwords || f0 == tag;
        if (__all(ok)) break;
        if (spins > SPIN_CAP) { if (c.lane == 0) __builtin_amdgcn_raw_buffer_store_b32(1u, c.rs, (u32)WS_TMO, 0, 16); break; }
        __builtin_amdgcn_s_sleep(1);
    }
}

__device__ __forceinline__ int crow(int r, int h) { return (r & 3) + 8 * (r >> 2) + 4 * h; }

#define LDF(p, k) (*(const LAS u32x4*)((p) + (k) * 1024))

template <bool SWAP, bool PRE, class F>
__device__ __forceinline__ void c_tile(f32x16& acc, u32x4 (&wf)[8], const LAS unsigned char* lo, const LAS unsigned char* hi, const LAS unsigned char* nxt,
                                       const u32x4 (&xf)[32], F&& filler) {
#pragma unroll
    for (int k = 0; k < 32; ++k) {
        const u32x4 wv = wf[k & 7];
        if (k + 8 < 16) wf[k & 7] = LDF(lo, k + 8);
        else if (k + 8 < 32) wf[k & 7] = LDF(hi, k + 8 - 16);
        else if (PRE) wf[k & 7] = LDF(nxt, k + 8 - 32);
        acc = SWAP ? mfma32(xf[k], wv, acc) : mfma32(wv, xf[k], acc);
        PIN(acc);
        filler(k);
        SB();
    }
}
template <bool MID, class W>
__device__ __forceinline__ f32x2 c_tile0(const Ctx& c, f32x16& acc, u32x4 (&wf)[8], const LAS unsigned char* lo, const LAS unsigned char* hi,
                                         const LAS unsigned char* nxt, u32x4 (&xf)[32], u32 xsoff, W&& wait_ready) {
    constexpr int XPD = 14;
    wait_ready();
#pragma unroll
    for (int k = 0; k < XPD; ++k) xf[k] = ld_sc1(c, xsoff + (u32)k * 1024u);
#pragma unroll
    for (int k = 0; k < 8; ++k) wf[k] = LDF(lo, k);
    SB();
    float s1[4] = {0.f, 0.f, 0.f, 0.f}, s2[4] = {0.f, 0.f, 0.f, 0.f};
#pragma unroll
    for (int k = 0; k < 32; ++k) {
        if (k + XPD < 32) xf[k + XPD] = ld_sc1(c, xsoff + (u32)(k + XPD) * 1024u);
        const u32x4 wv = wf[k & 7];
        if (k + 8 < 16) wf[k & 7] = LDF(lo, k + 8);
        else if (k + 8 < 32) wf[k & 7] = LDF(hi, k + 8 - 16);
        else wf[k & 7] = LDF(nxt, k + 8 - 32);
        acc = mfma32(wv, xf[k], acc);
        PIN(acc);
        s1[0] = dot2(xf[k].x, BF_ONES, s1[0]); s2[0] = dot2(xf[k].x, xf[k].x, s2[0]);
        s1[1] = dot2(xf[k].y, BF_ONES, s1[1]); s2[1] = dot2(xf[k].y, xf[k].y, s2[1]);
        s1[2] = dot2(xf[k].z, BF_ONES, s1[2]); s2[2] = dot2(xf[k].z, xf[k].z, s2[2]);
        s1[3] = dot2(xf[k].w, BF_ONES, s1[3]); s2[3] = dot2(xf[k].w, xf[k].w, s2[3]);
        asm volatile("" : "+v"(s1[0]), "+v"(s1[1]), "+v"(s1[2]), "+v"(s1[3]), "+v"(s2[0]), "+v"(s2[1]), "+v"(s2[2]), "+v"(s2[3]));
        if (MID && k == 3) BARL();
        SB();
    }
    const float a = xor32_sum((s1[0] + s1[1]) + (s1[2] + s1[3])), b = xor32_sum((s2[0] + s2[1]) + (s2[2] + s2[3]));
    const float mean = a * (1.0f / DM);
    float var = b * (1.0f / DM) - mean * mean;
    var = var > 0.f ? var : 0.f;
    f32x2 o; o.x = mean; o.y = __builtin_amdgcn_rsqf(var + LN_EPS);
    return o;
}

struct GS { float g0, g1, t0, t1, u0, u1; };
struct GK { float mean, A, B, ir2; };
__device__ __forceinline__ void geglu_sub(GS& s, u32& o, int q, float ag0, float ag1, float au0, float au1, float cg0, float cg1, float cu0, float cu1, const GK& k) {
    if (q == 0) asm volatile("v_fma_f32 %0, -%7, %5, %3\n\tv_fma_f32 %1, -%7, %6, %4\n\tv_mul_f32 %2, %0, %0"
                             : "=&v"(s.g0), "=&v"(s.g1), "=&v"(s.t0) : "v"(ag0), "v"(ag1), "v"(cg0), "v"(cg1), "v"(k.mean));
    if (q == 1) asm volatile("v_mul_f32 %1, %2, %2\n\tv_fma_f32 %0, %0, %4, %3\n\tv_fma_f32 %1, %1, %4, %3"
                             : "+v"(s.t0), "=&v"(s.t1) : "v"(s.g1), "v"(k.A), "v"(k.B));
    if (q == 2) asm volatile("v_mul_f32 %0, %2, %0\n\tv_mul_f32 %1, %3, %1\n\tv_exp_f32 %0, %0"
                             : "+v"(s.t0), "+v"(s.t1) : "v"(s.g0), "v"(s.g1));
    if (q == 3) asm volatile("v_exp_f32 %0, %0\n\tv_fma_f32 %1, -%7, %5, %3\n\tv_fma_f32 %2, -%7, %6, %4"
                             : "+v"(s.t1), "=&v"(s.u0), "=&v"(s.u1) : "v"(au0), "v"(au1), "v"(cu0), "v"(cu1), "v"(k.mean));
    if (q == 4) asm volatile("v_fma_f32 %0, %0, %4, %4\n\tv_mul_f32 %2, %2, %3\n\tv_fma_f32 %1, %1, %4, %4"
                             : "+v"(s.t0), "+v"(s.t1), "+v"(s.g0) : "v"(s.u0), "v"(k.ir2));
    if (q == 5) asm volatile("v_rcp_f32 %0, %0\n\tv_mul_f32 %2, %2, %3\n\tv_rcp_f32 %1, %1"
                             : "+v"(s.t0), "+v"(s.t1), "+v"(s.g1) : "v"(s.u1));
    if (q == 6) asm volatile("v_mul_f32 %1, %1, %3\n\tv_mul_f32 %2, %2, %4\n\tv_cvt_pk_bf16_f32 %0, %1, %2"
                             : "=&v"(o), "+v"(s.g0), "+v"(s.g1) : "v"(s.t0), "v"(s.t1));
}
struct LS { float t0, t1; };
__device__ __forceinline__ void lnq_a(LS& s, float a0, float c0, float c1, float sc, float msc) {
    asm volatile("v_mul_f32 %0, %6, %3\n\tv_mul_f32 %1, %6, %4\n\tv_fma_f32 %0, %2, %5, -%0"
                 : "=&v"(s.t0), "=&v"(s.t1) : "v"(a0), "v"(c0), "v"(c1), "v"(sc), "v"(msc));
}
__device__ __forceinline__ u32 lnq_b(LS& s, float a1, float sc) {
    u32 o;
    asm volatile("v_fma_f32 %1, %3, %4, -%1\n\tv_cvt_pk_bf16_f32 %0, %2, %1" : "=v"(o), "+v"(s.t1) : "v"(s.t0), "v"(a1), "v"(sc));
    return o;
}
__device__ __forceinline__ void lnv_a(LS& s, float a0, float a1, float m0, float m1, float r0, float cs) {
    asm volatile("v_fma_f32 %0, -%4, %7, %2\n\tv_fma_f32 %1, -%5, %7, %3\n\tv_mul_f32 %0, %0, %6"
                 : "=&v"(s.t0), "=&v"(s.t1) : "v"(a0), "v"(a1), "v"(m0), "v"(m1), "v"(r0), "v"(cs));
}
__device__ __forceinline__ u32 lnv_b(LS& s, float r1) {
    u32 o;
    asm volatile("v_mul_f32 %1, %1, %3\n\tv_cvt_pk_bf16_f32 %0, %2, %1" : "=v"(o), "+v"(s.t1) : "v"(s.t0), "v"(r1));
    return o;
}

__device__ __forceinline__ float max16(const f32x16& s, float m) {
    float a = fmx(fmx(s[0], s[1]), m), b = fmx(s[2], s[3]);
    a = fmx(fmx(s[4], s[5]), a); b = fmx(fmx(s[6], s[7]), b);
    a = fmx(fmx(s[8], s[9]), a); b = fmx(fmx(s[10], s[11]), b);
    a = fmx(fmx(s[12], s[13]), a); b = fmx(fmx(s[14], s[15]), b);
    return fmx(a, b);
}
__device__ __forceinline__ float smul(float a, float b) { float r; asm("v_mul_f32 %0, %1, %2" : "=v"(r) : "v"(a), "v"(b)); return r; }
struct MaskF { u32x4 alo, ahi, ilo, ihi; };
__device__ __forceinline__ f32x16 add_mask(const MaskF& mf, f32x16 s) { s = mfma32(mf.alo, mf.ilo, s); return mfma32(mf.ahi, mf.ihi, s); }
template <int NB, int FIRST, int REST0, int MASK, bool FIRSTSEG>
__device__ __forceinline__ void att_segment(f32x16& oacc, float& m, const LAS unsigned char* kb, const LAS unsigned char* vb, const LAS unsigned char* mk, const u32x4& qf) {
    u32x4 kf[NB];
#pragma unroll
    for (int t = 0; t < NB; ++t) { const int b = t == 0 ? FIRST : REST0 + t - 1; kf[t] = *(const LAS u32x4*)(kb + b * 2048); }
    MaskF mf;
    if (MASK) {
        mf.alo = LDF(mk, 0); mf.ahi = LDF(mk, 1); mf.ilo = LDF(mk, 2); mf.ihi = LDF(mk, 3);
        if (MASK == 2) { mf.alo ^= 0xff7fff7fu; mf.ahi ^= 0xff7fff7fu; }
    }
    SB();
    float mx = FIRSTSEG ? -__builtin_inff() : m;
    f32x16 sv[4];
    sv[0] = mfma32(kf[0], qf, f32x16{});
    if (NB > 1) sv[1] = mfma32(kf[1], qf, f32x16{});
    if (MASK) sv[0] = add_mask(mf, sv[0]);
    SB();
#pragma unroll
    for (int t = 0; t < NB; ++t) {
        const int u = NB > 1 ? (t == 0 ? 1 : t == 1 ? 0 : t) : 0;
        if (t + 2 < NB) sv[(t + 2) & 3] = mfma32(kf[t + 2], qf, f32x16{});
        mx = max16(sv[u & 3], mx);
        SB();
    }
    mx = xor32_max(mx);
    f32x16 negm;
#pragma unroll
    for (int e = 0; e < 16; ++e) negm[e] = -mx;
    SB();
    sv[0] = mfma32(kf[0], qf, negm);
    if (MASK) sv[0] = add_mask(mf, sv[0]);
    u32x4 v0 = *(const LAS u32x4*)(vb + (FIRST * 2 + 0) * VREC);
    u32x4 v1 = *(const LAS u32x4*)(vb + (FIRST * 2 + 1) * VREC);
    if (NB > 1) sv[1] = mfma32(kf[1], qf, negm);
    if (!FIRSTSEG) {
        float f = __builtin_amdgcn_exp2f(m - mx);
        asm volatile("s_nop 1" : "+v"(f));
#pragma unroll
        for (int e = 0; e < 16; ++e) oacc[e] = smul(oacc[e], f);
    }
    m = mx;
    SB();
#pragma unroll
    for (int t = 0; t < NB; ++t) {
        u32x4 n0, n1;
        if (t + 1 < NB) {
            if (t > 0) sv[(t + 1) & 1] = mfma32(kf[t + 1], qf, negm);
            n0 = *(const LAS u32x4*)(vb + ((REST0 + t) * 2 + 0) * VREC);
            n1 = *(const LAS u32x4*)(vb + ((REST0 + t) * 2 + 1) * VREC);
        }
        SB();
        const f32x16& s = sv[t & 1];
        u32 p[8];
#pragma unroll
        for (int e = 0; e < 4; ++e) p[e] = pack2(__builtin_amdgcn_exp2f(s[2 * e]), __builtin_amdgcn_exp2f(s[2 * e + 1]));
        SB();
        oacc = mfma32(v0, u32x4{p[0], p[1], p[2], p[3]}, oacc);
#pragma unroll
        for (int e = 4; e < 8; ++e) p[e] = pack2(__builtin_amdgcn_exp2f(s[2 * e]), __builtin_amdgcn_exp2f(s[2 * e + 1]));
        SB();
        oacc = mfma32(v1, u32x4{p[4], p[5], p[6], p[7]}, oacc);
        if (t + 1 < NB) { v0 = n0; v1 = n1; }
        SB();
    }
}

__device__ __forceinline__ void phase_qkv_att(const Ctx& c0, int layer, const Args& a) {
    const Ctx c = fresh(c0);
    const int lane = c.lane, w = c.w, tb = w & 3, h = lane >> 5, r = lane & 31;
    const u32 lbase = (u32)(WS_L0 + (size_t)layer * WS_LSTRIDE);
    STAMP(0);
    if (w < 4) {
        auto ready = [&]() {
            if (layer > 0 && w == 0) poll_flags<64>(c, layer - 1, 0, 16);
            BAR();
            STAMP(1);
        };
        u32x4 xf[32], wf[8];
        const LAS unsigned char* wK = lds_base(c, slot_of(CQ(0, 0)) * SLOT);
        const LAS unsigned char* wV = lds_base(c, slot_of(CQ(1, 0)) * SLOT);
        const LAS unsigned char* wQ = lds_base(c, slot_of(CQ(2, 0)) * SLOT);
        f32x16 accK = {}, accV = {}, accQ = {};
        const f32x2 ms = c_tile0<true>(c, accK, wf, wK, wK + SLOT, wV, xf,
                                           (layer == 0 ? (u32)WS_XF0 : (u32)(WS_L0 + L_X2)) + (u32)((4 * c.i + tb) * 32) * 1024, ready);
        if (lane < 32) lds_st<f32x2>(c.L, SC_ST + (tb * 32 + r) * 8, ms);
        const float msk = ms.x * ms.y;
        BARL(); STAMP(2);
        f32x4 cs[4];
#pragma unroll
        for (int q = 0; q < 4; ++q) cs[q] = lds_ld<f32x4>(c.L, SC_CS + (0 * 32 + 8 * q + 4 * h) * 4);
        f32x4 mr[8];
        float csv;
        {
            u32 o[8]; LS ls;
            c_tile<true, true>(accV, wf, wV, wV + SLOT, wQ, xf, [&](int k) {
                if (k >= 4 && k < 20) {
                    const int pi = (k - 4) >> 1, e = 2 * pi;
                    if (((k - 4) & 1) == 0) lnq_a(ls, accK[e], cs[e >> 2][e & 3], cs[e >> 2][(e & 3) + 1], ms.y, msk);
                    else o[pi] = lnq_b(ls, accK[e + 1], ms.y);
                }
                if (k == 20 || k == 21) {
                    const int hh = k - 20;
                    const u32x4 ov = {o[4 * hh], o[4 * hh + 1], o[4 * hh + 2], o[4 * hh + 3]};
                    lds_st<u32x4>(c.L, SC_KS + (tb * 2 + hh) * 1024 + c.voff, ov);
                    st_sc1(c, lbase + (u32)L_KF + (u32)((4 * c.i + tb) * 32 + 2 * c.j + hh) * 1024, ov);
                }
                if (k >= 22 && k < 30) { const int q = (k - 22) >> 1; mr[k - 22] = lds_ld<f32x4>(c.L, SC_ST + (tb * 32 + 8 * q + 4 * h + 2 * ((k - 22) & 1)) * 8); }
                if (k == 30) csv = lds_ld<float>(c.L, SC_CS + (32 + r) * 4);
            });
        }
        BARL();
        {
            u32 o[8]; LS ls;
            c_tile<false, false>(accQ, wf, wQ, wQ + SLOT, wQ, xf, [&](int k) {
                if (k >= 2 && k < 18) {
                    const int pi = (k - 2) >> 1;
                    if (((k - 2) & 1) == 0) lnv_a(ls, accV[2 * pi], accV[2 * pi + 1], mr[pi].x, mr[pi].z, mr[pi].y, csv);
                    else o[pi] = lnv_b(ls, mr[pi].w);
                }
                if (k == 18 || k == 20) {
                    const int sv = (k - 18) >> 1;
                    const u32x4 ov = {o[4 * sv], o[4 * sv + 1], o[4 * sv + 2], o[4 * sv + 3]};
                    lds_st<u32x4>(c.L, SC_VS + (tb * 2 + sv) * VREC + c.voff, ov);
                    st_sc1(c, lbase + (u32)L_VF + (u32)(((4 * c.i + tb) * 16 + c.j) * 2 + sv) * 1024, ov);
                }
                if (k >= 24 && k < 28) cs[k - 24] = lds_ld<f32x4>(c.L, SC_CS + (64 + 8 * (k - 24) + 4 * h) * 4);
            });
            if (lane < 2) { u32x4 ones = {BF_ONES, BF_ONES, BF_ONES, BF_ONES}; lds_st<u32x4>(c.L, SC_VS + (tb * 2 + lane) * VREC + 1024, ones); }
        }
        {
            const float sc = ms.y * QSCALE, msc = ms.x * sc;
            asm volatile("s_nop 15" : "+v"(accQ));
            u32 o[8]; LS ls[8];
#pragma unroll
            for (int pi = 0; pi < 8; ++pi) { const int e = 2 * pi; lnq_a(ls[pi], accQ[e], cs[e >> 2][e & 3], cs[e >> 2][(e & 3) + 1], sc, msc); }
#pragma unroll
            for (int pi = 0; pi < 8; ++pi) o[pi] = lnq_b(ls[pi], accQ[2 * pi + 1], sc);
#pragma unroll
            for (int hh = 0; hh < 2; ++hh) lds_st<u32x4>(c.L, SC_QS + (tb * 2 + hh) * 1024 + c.voff, u32x4{o[4 * hh], o[4 * hh + 1], o[4 * hh + 2], o[4 * hh + 3]});
        }
        WAIT_VM(0);
        BAR(); STAMP(5);
        if (w == 0 && lane < 2 && c.i + 1 + lane < 16) flag_store(c, layer, 1, c.i + 1 + lane, c.j, lane);
    } else {
        WAIT_VM(16);
        BAR();
        WAIT_VM(8);
        BARL();
        const int cb = c.j & 7, sidx = c.i * 2 + (c.j >> 3);
        if (layer == 0) {
            const int hw = w & 3;
            LAS unsigned char* t = c.L + SC_VS + hw * 1024;
            WItem it[6]; u32x4 v[6];
#pragma unroll
            for (int q = 0; q < 6; ++q) {
                int f;
                const int fD = NF_WO + NF_WG + cb * 224 + sidx * 7, fO = cb * 64 + sidx * 2;
                if (q < 2 || (q < 4 && hw < 3)) { const int ks = 2 * c.i + (q & 1), cc = hw + 4 * (q >> 1); f = NF_WO + c.j * 224 + (2 * cc + (ks >> 4)) * 16 + (ks & 15); }
                else if (q < 4) f = fD + 4 + (q & 1);
                else if (q == 4) f = fD + (hw < 3 ? hw : 6);
                else f = hw < 2 ? fO + hw : fD + 3;
                it[q] = w_item(a, f, lane);
                v[q] = *(const u32x4*)it[q].src;
            }
            const int k = lane >> 2, n8 = lane & 3;
            const int g4 = lane >> 4, hh = g4 >> 1, nh = g4 & 1, li = lane & 15, qq = li >> 2, p = li & 3;
            float part = 0.f;
#pragma unroll
            for (int q = 0; q < 6; ++q) {
                if (q < 5 || hw < 3) {
                    lds_st<u32x4>(t, k * 64 + n8 * 16, v[q]);
                    asm volatile("s_waitcnt lgkmcnt(0)" ::: "memory");
                    const s16x4 lo = __builtin_amdgcn_ds_read_tr16_b64_v4i16((LAS s16x4*)(t + (4 * hh + qq) * 64 + nh * 32 + p * 8));
                    const s16x4 hi = __builtin_amdgcn_ds_read_tr16_b64_v4i16((LAS s16x4*)(t + (8 + 4 * hh + qq) * 64 + nh * 32 + p * 8));
                    const u32x2 l2 = __builtin_bit_cast(u32x2, lo), h2 = __builtin_bit_cast(u32x2, hi);
                    __builtin_amdgcn_raw_buffer_store_b128(u32x4{l2.x, l2.y, h2.x, h2.y}, c.rs, c.voff, (u32)it[q].dst, 16);
                    if (q < 2 || (q < 4 && hw < 3)) {
                        if ((q & 1) == 0) part = 0.f;
                        part = dot2(l2.x, BF_ONES, part); part = dot2(l2.y, BF_ONES, part); part = dot2(h2.x, BF_ONES, part); part = dot2(h2.y, BF_ONES, part);
                        if (q & 1) {
                            const float tot = xor32_sum(part);
                            const int cc = hw + 4 * (q >> 1);
                            if (lane < 32) __builtin_amdgcn_raw_buffer_store_b32(__builtin_bit_cast(u32, tot), c.rs, (u32)lane * 4u, (u32)WS_CSP + (u32)((c.j * 16 + c.i) * 224 + cc * 32) * 4u, 16);
                        }
                    }
                    asm volatile("s_waitcnt lgkmcnt(0)" ::: "memory");
                }
            }
        }
        WAIT_VM(0);
        BAR();
        if (layer == 0 && w == 4) {
            if (lane < 16) flag_store(c, 0, 5, lane, c.j, c.i);
            if (lane < 32) flag_store(c, 0, 6, lane >> 1, cb + 8 * (lane & 1), sidx);
        }
        BAR();
        if (layer == 0) poll_flags<64>(c, 0, 6, 32);
        issue_chunk<CO(0)>(c); issue_chunk<CO(1)>(c); issue_chunk<CO(2)>(c); issue_chunk<CO(3)>(c);
        BAR();
        if (layer == 0) {
            poll_flags<64>(c, 0, 5, 16);
            const int col = (w & 3) * 64 + lane;
            if (col < 224) {
                float t = 0.f;
#pragma unroll
                for (int i2 = 0; i2 < 16; ++i2) t += __builtin_bit_cast(float, __builtin_amdgcn_raw_buffer_load_b32(c.rs, (u32)col * 4u, (u32)WS_CSP + (u32)((c.j * 16 + i2) * 224) * 4u, 16));
                lds_st<float>(c.L, SC_CS + (96 + col) * 4, t);
            }
        }
        issue_chunk<CG(0, 0)>(c); issue_chunk<CG(0, 1)>(c);
    }
    const int qb = w < 4 ? w : 7 - w, hh = w >> 2;
    const u32x4 qf = lds_ld<u32x4>(c.L, SC_QS + (qb * 2 + hh) * 1024 + c.voff);
    const int vlane = (((lane >> 4) & 1) == hh) ? lane * 16 : 1024;
    f32x16 oacc = {};
    float m = 0.f;
    const LAS unsigned char* mk = lds_base(c, SC_MK);
    {
        const LAS unsigned char* kb = c.L + SC_KS + hh * 1024 + c.voff;
        const LAS unsigned char* vb = c.L + SC_VS + vlane;
        if (qb == 0) att_segment<1, 0, 0, 1, true>(oacc, m, kb, vb, mk, qf);
        else if (qb == 1) att_segment<2, 1, 0, 1, true>(oacc, m, kb, vb, mk, qf);
        else if (qb == 2) att_segment<3, 2, 0, 1, true>(oacc, m, kb, vb, mk, qf);
        else att_segment<4, 3, 0, 1, true>(oacc, m, kb, vb, mk, qf);
    }
    STAMP(6);
    if (c.i > 0) {
        if (w == 0) poll_flags<64>(c, layer, 1, c.i >= 2 ? 2 : 1);
        BAR();
        STAMP(7);
        {
            const int idx = w;
            const int g = 4 * c.i - 8 + idx;
            if (g >= 0) {
                const u32x4 s0 = ld_sc1(c, lbase + (u32)L_KF + (u32)(g * 32 + 2 * c.j + 0) * 1024);
                const u32x4 s1 = ld_sc1(c, lbase + (u32)L_KF + (u32)(g * 32 + 2 * c.j + 1) * 1024);
                const u32x4 s2 = ld_sc1(c, lbase + (u32)L_VF + (u32)((g * 16 + c.j) * 2 + 0) * 1024);
                const u32x4 s3 = ld_sc1(c, lbase + (u32)L_VF + (u32)((g * 16 + c.j) * 2 + 1) * 1024);
                lds_st<u32x4>(c.L, SC_RK + (idx * 2 + 0) * 1024 + c.voff, s0);
                lds_st<u32x4>(c.L, SC_RK + (idx * 2 + 1) * 1024 + c.voff, s1);
                lds_st<u32x4>(c.L, SC_RV + (idx * 2 + 0) * VREC + c.voff, s2);
                lds_st<u32x4>(c.L, SC_RV + (idx * 2 + 1) * VREC + c.voff, s3);
                if (lane < 2) { u32x4 ones = {BF_ONES, BF_ONES, BF_ONES, BF_ONES}; lds_st<u32x4>(c.L, SC_RV + (idx * 2 + lane) * VREC + 1024, ones); }
            }
        }
        BAR();
        STAMP(8);
        const LAS unsigned char* kb = c.L + SC_RK + hh * 1024 + c.voff;
        const LAS unsigned char* vb = c.L + SC_RV + vlane;
        if (c.i == 1) att_segment<4, 4, 5, 0, false>(oacc, m, kb, vb, mk, qf);
        else if (qb == 0) att_segment<8, 0, 1, 2, false>(oacc, m, kb, vb, mk, qf);
        else if (qb == 1) att_segment<7, 1, 2, 2, false>(oacc, m, kb, vb, mk, qf);
        else if (qb == 2) att_segment<6, 2, 3, 2, false>(oacc, m, kb, vb, mk, qf);
        else att_segment<5, 3, 4, 2, false>(oacc, m, kb, vb, mk, qf);
    }
    {
        u32x4 o;
        if (hh == 0) {
            float il = __builtin_amdgcn_rcpf(oacc[8]);
            asm volatile("s_nop 1" : "+v"(il));
            o = u32x4{pack2(smul(oacc[0], il), smul(oacc[1], il)), pack2(smul(oacc[2], il), smul(oacc[3], il)), pack2(smul(oacc[4], il), smul(oacc[5], il)), pack2(smul(oacc[6], il), smul(oacc[7], il))};
        } else {
            float il = __builtin_amdgcn_rcpf(oacc[0]);
            asm volatile("s_nop 1" : "+v"(il));
            o = u32x4{pack2(smul(oacc[8], il), smul(oacc[9], il)), pack2(smul(oacc[10], il), smul(oacc[11], il)), pack2(smul(oacc[12], il), smul(oacc[13], il)), pack2(smul(oacc[14], il), smul(oacc[15], il))};
        }
        st_sc1(c, lbase + (u32)L_OF + (u32)((4 * c.i + qb) * 32 + 2 * c.j + hh) * 1024, o);
    }
    STAMP(9);
    WAIT_VM(0);
    BAR();
    if (w == 0) signal_group(c, layer, 2);
    if (w >= 4) { issue_chunk<CG(1, 0)>(c); issue_chunk<CG(1, 1)>(c); }
    STAMP(10);
}

__device__ __forceinline__ void red_write(const Ctx& c, const f32x16& acc0, const f32x16& acc1, int t2, int kq) {
#pragma unroll
    for (int d = 0; d < 4; ++d) {
        const int fin = d & 1;
        if (kq != fin) {
            const int slot = kq < fin ? kq : kq - 1;
            const int base = red_off(t2 * 4 + d) + slot * 2048 + c.voff;
            const f32x16& a = d < 2 ? acc0 : acc1;
            const int o = (d & 1) * 8;
            lds_st<f32x4>(c.L, base, f32x4{a[o], a[o + 1], a[o + 2], a[o + 3]});
            lds_st<f32x4>(c.L, base + 1024, f32x4{a[o + 4], a[o + 5], a[o + 6], a[o + 7]});
        }
    }
}
__device__ __forceinline__ void red_read(const Ctx& c, const f32x16& acc0, const f32x16& acc1, int t2, int kq, float (&v)[2][8]) {
#pragma unroll
    for (int e = 0; e < 8; ++e) { v[0][e] = kq ? acc0[8 + e] : acc0[e]; v[1][e] = kq ? acc1[8 + e] : acc1[e]; }
#pragma unroll
    for (int x = 0; x < 2; ++x)
#pragma unroll
        for (int s = 0; s < 3; ++s) {
            const int base = red_off(t2 * 4 + 2 * x + kq) + s * 2048 + c.voff;
            const f32x4 lo = lds_ld<f32x4>(c.L, base), hi = lds_ld<f32x4>(c.L, base + 1024);
            v[x][0] += lo.x; v[x][1] += lo.y; v[x][2] += lo.z; v[x][3] += lo.w; v[x][4] += hi.x; v[x][5] += hi.y; v[x][6] += hi.z; v[x][7] += hi.w;
        }
}
__device__ __forceinline__ u32x4 add_res(const float (&v)[8], const u32x4& res) {
    return u32x4{pack2(v[0] + bflo(res.x), v[1] + bfhi(res.x)), pack2(v[2] + bflo(res.y), v[3] + bfhi(res.y)),
                 pack2(v[4] + bflo(res.z), v[5] + bfhi(res.z)), pack2(v[6] + bflo(res.w), v[7] + bfhi(res.w))};
}

__device__ __forceinline__ void phase_wo(const Ctx& c0, int layer, const Args& a) {
    const Ctx c = fresh(c0);
    const int w = c.w, t2 = w & 1, kq = w >> 1;
    const int th = c.j >> 3, cb = c.j & 7;
    const int tbk = 4 * c.i + 2 * th + t2;
    const u32 lbase = (u32)(WS_L0 + (size_t)layer * WS_LSTRIDE);
    if (w == 0) poll_flags<64>(c, layer, 2, 16);
    if (w >= 4) WAIT_VM(8);
    BAR();
    STAMP(11);
    u32x4 xf[8];
#pragma unroll
    for (int s = 0; s < 8; ++s) xf[s] = ld_sc1(c, lbase + (u32)L_OF + (u32)(tbk * 32 + 8 * kq + s) * 1024);
    u32x4 res[2];
    if (w < 4) {
#pragma unroll
        for (int x = 0; x < 2; ++x) {
            const int kbo = 4 * cb + 2 * x + kq;
            res[x] = ld_sc1(c, (layer == 0 ? (u32)WS_XF0 : (u32)(WS_L0 + L_X2)) + (u32)(tbk * 32 + kbo) * 1024);
        }
    }
    u32x4 wf[16];
#pragma unroll
    for (int ch = 0; ch < 4; ++ch) {
        const LAS unsigned char* wb = lds_base(c, slot_of(CO(ch)) * SLOT + kq * 4096);
#pragma unroll
        for (int q = 0; q < 4; ++q) wf[4 * ch + q] = LDF(wb, q);
    }
    SB();
    f32x16 acc0 = {}, acc1 = {};
#pragma unroll
    for (int s = 0; s < 8; ++s) {
        acc0 = mfma32(wf[2 * s], xf[s], acc0);
        acc1 = mfma32(wf[2 * s + 1], xf[s], acc1);
        SB();
    }
    BAR();
    STAMP(12);
    red_write(c, acc0, acc1, t2, kq);
    BAR();
    if (w < 4) {
        float v[2][8];
        red_read(c, acc0, acc1, t2, kq, v);
#pragma unroll
        for (int x = 0; x < 2; ++x) st_sc1(c, lbase + (u32)L_X1 + (u32)(tbk * 32 + 4 * cb + 2 * x + kq) * 1024, add_res(v[x], res[x]));
        STAMP(13);
        WAIT_VM(0);
    }
    BAR();
    if (w == 0) signal_group(c, layer, 3);
    STAMP(14);
}

template <int T> __device__ __forceinline__ void gu_tile(const Ctx& c, f32x16& acc, const f32x16& prev, u32x4 (&wf)[8], const u32x4 (&xf)[32], int h,
                                                         f32x4 (&cs)[4], const GK& gk, u32 hbase) {
    const LAS unsigned char* cur = lds_base(c, slot_of(CG(T, 0)) * SLOT);
    const LAS unsigned char* nxt = lds_base(c, slot_of(CG(T < 6 ? T + 1 : T, 0)) * SLOT);
    GS g;
    u32 o[4];
    acc = f32x16{};
    SB();
    c_tile<false, (T < 6)>(acc, wf, cur, cur + SLOT, nxt, xf, [&](int k) {
        const int p = k >> 3, e = 2 * p, q2 = p >> 1, e2 = e & 3;
        if ((k & 7) < 7) geglu_sub(g, o[p], k & 7, prev[e], prev[e + 1], prev[8 + e], prev[8 + e + 1], cs[q2][e2], cs[q2][e2 + 1], cs[2 + q2][e2], cs[2 + q2][e2 + 1], gk);
        if (k == 15) cs[0] = lds_ld<f32x4>(c.L, SC_CS + (96 + T * 32 + 4 * h) * 4);
        if (k == 15) cs[2] = lds_ld<f32x4>(c.L, SC_CS + (96 + T * 32 + 16 + 4 * h) * 4);
        if (k == 31) cs[1] = lds_ld<f32x4>(c.L, SC_CS + (96 + T * 32 + 8 + 4 * h) * 4);
        if (k == 31) cs[3] = lds_ld<f32x4>(c.L, SC_CS + (96 + T * 32 + 16 + 8 + 4 * h) * 4);
    });
    st_sc1(c, hbase + (u32)(T - 1) * 1024, u32x4{o[0], o[1], o[2], o[3]});
}

__device__ __forceinline__ u32 gu_compute(const Ctx& c0, int layer) {
    const Ctx c = fresh(c0);
    const int lane = c.lane, w = c.w, tb = w & 3, h = lane >> 5;
    const u32 lbase = (u32)(WS_L0 + (size_t)layer * WS_LSTRIDE);
    u32 pf1 = 0;
    {
        u32x4 xf[32], wf[8];
        const LAS unsigned char* w0 = lds_base(c, slot_of(CG(0, 0)) * SLOT);
        const LAS unsigned char* w1 = lds_base(c, slot_of(CG(1, 0)) * SLOT);
        f32x16 a0 = {}, a1;
        const f32x2 ms = c_tile0<false>(c, a0, wf, w0, w0 + SLOT, w1, xf, lbase + (u32)L_X1 + (u32)((4 * c.i + tb) * 32) * 1024, [&]() {
            if (w == 0) poll_flags<64>(c, layer, 3, 16);
            BAR();
            STAMP(15);
        });
        constexpr float GC1 = -2.0f * 0.7978845608028654f * 1.4426950408889634f, GC2 = GC1 * 0.044715f;
        GK gk; gk.mean = ms.x; gk.A = ms.y * GC1; gk.B = ms.y * ms.y * ms.y * GC2; gk.ir2 = 1.0f / (ms.y * ms.y);
        BARL(); STAMP(16);
        f32x4 cs[4];
#pragma unroll
        for (int q = 0; q < 4; ++q) cs[q] = lds_ld<f32x4>(c.L, SC_CS + (96 + (q >> 1) * 16 + 8 * (q & 1) + 4 * h) * 4);
        const u32 hbase = lbase + (u32)L_HF + (u32)((4 * c.i + tb) * 112 + 7 * c.j) * 1024;
        gu_tile<1>(c, a1, a0, wf, xf, h, cs, gk, hbase); BARL();
        gu_tile<2>(c, a0, a1, wf, xf, h, cs, gk, hbase); BARL();
        gu_tile<3>(c, a1, a0, wf, xf, h, cs, gk, hbase); BARL();
        gu_tile<4>(c, a0, a1, wf, xf, h, cs, gk, hbase); BARL();
        gu_tile<5>(c, a1, a0, wf, xf, h, cs, gk, hbase);
        WAIT_VM(1);
        BARL();
        if (w == 0) signal_group(c, layer, 7);
        gu_tile<6>(c, a0, a1, wf, xf, h, cs, gk, hbase);
        STAMP(22);
        if (w == 0) pf1 = __builtin_amdgcn_raw_buffer_load_b32(c.rs, flag_off(layer, 7, c.i, c.j) + (lane & 15) * 4, 0, 16);
        {
            asm volatile("s_nop 15" : "+v"(a0));
            GS g[4];
            u32 o[4];
#pragma unroll
            for (int q = 0; q < 7; ++q)
#pragma unroll
                for (int p = 0; p < 4; ++p) {
                    const int e = 2 * p, q2 = p >> 1, e2 = e & 3;
                    geglu_sub(g[p], o[p], q, a0[e], a0[e + 1], a0[8 + e], a0[8 + e + 1], cs[q2][e2], cs[q2][e2 + 1], cs[2 + q2][e2], cs[2 + q2][e2 + 1], gk);
                }
            st_sc1(c, hbase + 6u * 1024, u32x4{o[0], o[1], o[2], o[3]});
        }
        STAMP(21);
        WAIT_VM(0);
        BAR();
        STAMP(23);
        if (w == 0) signal_group(c, layer, 4);
    }
    return pf1;
}
__device__ __forceinline__ void gu_helper(const Ctx& c0, int layer, u32x4 (&wr)[40]) {
    const Ctx c = fresh(c0);
    const int lane = c.lane, w = c.w;
    {
        WAIT_VM(0);
        BAR();
        __builtin_amdgcn_s_sleep(16);
        issue_chunk<CG(2, 0)>(c); issue_chunk<CG(2, 1)>(c); issue_chunk<CG(3, 0)>(c); issue_chunk<CG(3, 1)>(c);
        WAIT_VM(8);
        BAR();
        const u32 wdb = (u32)WS_WD + (u32)((c.j & 7) * 224 + 112 + ((w >> 1) - 2) * 56) * 1024;
        issue_chunk<CG(4, 0)>(c); issue_chunk<CG(4, 1)>(c);
#pragma unroll
        for (int q = 0; q < 20; ++q) wr[q] = __builtin_amdgcn_raw_buffer_load_b128(c.rs, c.voff, wdb + q * 1024, 16);
        WAIT_VM(28);
        BAR();
        issue_chunk<CG(5, 0)>(c); issue_chunk<CG(5, 1)>(c);
#pragma unroll
        for (int q = 20; q < 40; ++q) wr[q] = __builtin_amdgcn_raw_buffer_load_b128(c.rs, c.voff, wdb + q * 1024, 16);
        WAIT_VM(48);
        BAR();
        issue_chunk<CG(6, 0)>(c); issue_chunk<CG(6, 1)>(c);
        WAIT_VM(28);
        BAR();
        issue_chunk<CD(0)>(c); issue_chunk<CD(1)>(c);
        WAIT_VM(8);
        BAR();
        issue_chunk<CD(2)>(c); issue_chunk<CD(3)>(c);
        BAR();
        issue_chunk<CD(4)>(c); issue_chunk<CD(5)>(c);
        WAIT_VM(0);
        BAR();
        issue_chunk<CD(6)>(c);
    }
}

__device__ __forceinline__ void hld(const Ctx& c, u32x4& dst, u32 soff) {
    asm volatile("buffer_load_dwordx4 %0, %1, %2, %3 offen sc1" : "=v"(dst) : "v"(c.voff), "s"(c.rs), "s"(soff) : "memory");
}
template <int N> __device__ __forceinline__ void hwait(u32x4& x) { asm volatile("s_waitcnt vmcnt(%1)" : "+v"(x) : "i"(N) : "memory"); }
template <bool HELPER>
__device__ __forceinline__ void dn_main(const Ctx& c, int layer, int kq, u32 xsrc, u32 wdb, const u32x4* wr, f32x16& acc0, f32x16& acc1) {
    constexpr int XD = 10;
    u32x4 xf[28], wf[8], wl[16];
    const LAS unsigned char* wb[7];
#pragma unroll
    for (int s = 0; s < XD; ++s) hld(c, xf[s], xsrc + (u32)dn_kstep(s) * 1024);
    if (!HELPER) {
#pragma unroll
        for (int ch = 0; ch < 7; ++ch) wb[ch] = lds_base(c, chunk_lds(CD(ch)) + kq * 8192);
#pragma unroll
        for (int q = 0; q < 8; ++q) wf[q] = LDF(wb[0], q);
    }
    SB();
    u32 pf = 0;
#pragma unroll
    for (int s = 0; s < 28; ++s) {
        if (s + XD < 28) hld(c, xf[s + XD], xsrc + (u32)dn_kstep(s + XD) * 1024);
        u32x4 w0, w1;
        if (HELPER) {
            if (s < 20) { w0 = wr[2 * s]; w1 = wr[2 * s + 1]; } else { w0 = wl[2 * (s - 20)]; w1 = wl[2 * (s - 20) + 1]; }
        } else {
            w0 = wf[(2 * s) & 7]; w1 = wf[(2 * s + 1) & 7];
            if (s + 4 < 28) { wf[(2 * s) & 7] = LDF(wb[(s + 4) >> 2], 2 * ((s + 4) & 3)); wf[(2 * s + 1) & 7] = LDF(wb[(s + 4) >> 2], 2 * ((s + 4) & 3) + 1); }
        }
        if (s + XD < 28) { if (HELPER && s >= 8 && s < 12 + XD - 10 + 10 && s <= 21) hwait<10>(xf[s]); else hwait<10>(xf[s]); }
        else if (s == 18) hwait<9>(xf[s]); else if (s == 19) hwait<8>(xf[s]); else if (s == 20) hwait<7>(xf[s]); else if (s == 21) hwait<6>(xf[s]); else if (s == 22) hwait<5>(xf[s]);
        else if (s == 23) hwait<4>(xf[s]); else if (s == 24) hwait<3>(xf[s]); else if (s == 25) hwait<2>(xf[s]); else if (s == 26) hwait<1>(xf[s]); else hwait<0>(xf[s]);
        if (HELPER && s == 20) {
            asm volatile("s_waitcnt vmcnt(6)" : "+v"(wl[0]), "+v"(wl[1]), "+v"(wl[2]), "+v"(wl[3]), "+v"(wl[4]), "+v"(wl[5]), "+v"(wl[6]), "+v"(wl[7]) :: "memory");
            asm volatile("" : "+v"(wl[8]), "+v"(wl[9]), "+v"(wl[10]), "+v"(wl[11]), "+v"(wl[12]), "+v"(wl[13]), "+v"(wl[14]), "+v"(wl[15]));
            w0 = wl[0]; w1 = wl[1];
        }
        acc0 = mfma32(w0, xf[s], acc0);
        acc1 = mfma32(w1, xf[s], acc1);
        PIN(acc0); PIN(acc1);
        if (HELPER && s >= 8 && s < 12) {
#pragma unroll
            for (int q = 0; q < 4; ++q) hld(c, wl[4 * (s - 8) + q], wdb + (u32)(40 + 4 * (s - 8) + q) * 1024);
        }
        if (!HELPER && s == 2 && c.w == 0)
            asm volatile("buffer_load_dword %0, %1, %2, 0 offen sc1" : "=v"(pf) : "v"(flag_off(layer, 4, c.i, c.j) + (c.lane & 15) * 4), "s"(c.rs) : "memory");
        if (s + XD == 15) {
            if (!HELPER && c.w == 0) {
                asm volatile("s_waitcnt vmcnt(3)" : "+v"(pf) :: "memory");
                if (!__all(pf == flag_tag(c, layer, 4))) poll_flags<64>(c, layer, 4, 16);
            }
            BARL();
        }
        SB();
    }
}

__device__ __forceinline__ void phase_ffn(const Ctx& c0, int layer, const Args& a) {
    f32x16 acc0 = {}, acc1 = {};
    u32x4 res[2];
    if (c0.w < 4) {
        const u32 pf1 = gu_compute(c0, layer);
        const Ctx c = fresh(c0);
        const int w = c.w, t2 = w & 1, kq = w >> 1, th = c.j >> 3, cb = c.j & 7;
        const int tbk = 4 * c.i + 2 * th + t2;
        const u32 lbase = (u32)(WS_L0 + (size_t)layer * WS_LSTRIDE);
        res[0] = ld_sc1(c, lbase + (u32)L_X1 + (u32)(tbk * 32 + 4 * cb + kq) * 1024);
        res[1] = ld_sc1(c, lbase + (u32)L_X1 + (u32)(tbk * 32 + 4 * cb + 2 + kq) * 1024);
        if (w == 0 && !__all(pf1 == flag_tag(c, layer, 7))) poll_flags<64>(c, layer, 7, 16);
        BARL();
        STAMP(25);
        dn_main<false>(c, layer, kq, lbase + (u32)L_HF + (u32)(tbk * 112 + 28 * kq) * 1024, 0u, nullptr, acc0, acc1);
    } else {
        u32x4 wr[40];
        gu_helper(c0, layer, wr);
        const Ctx c = fresh(c0);
        const int w = c.w, t2 = w & 1, kq = w >> 1, th = c.j >> 3, cb = c.j & 7;
        const int tbk = 4 * c.i + 2 * th + t2;
        const u32 lbase = (u32)(WS_L0 + (size_t)layer * WS_LSTRIDE);
        BARL();
        dn_main<true>(c, layer, kq, lbase + (u32)L_HF + (u32)(tbk * 112 + 28 * kq) * 1024, (u32)WS_WD + (u32)(cb * 224 + 112 + (kq - 2) * 56) * 1024, wr, acc0, acc1);
    }
    const Ctx c = fresh(c0);
    const int lane = c.lane, w = c.w, t2 = w & 1, kq = w >> 1, h = lane >> 5, r = lane & 31;
    const int th = c.j >> 3, cb = c.j & 7;
    const int tbk = 4 * c.i + 2 * th + t2;
    const u32 lbase = (u32)(WS_L0 + (size_t)layer * WS_LSTRIDE);
    BAR();
    STAMP(26);
    red_write(c, acc0, acc1, t2, kq);
    BAR();
    if (w >= 4) {
        if (layer == 0) { issue_chunk<CQ(0, 0)>(c); issue_chunk<CQ(0, 1)>(c); }
    } else {
        float v[2][8];
        red_read(c, acc0, acc1, t2, kq, v);
#pragma unroll
        for (int x = 0; x < 2; ++x) {
            const int kbo = 4 * cb + 2 * x + kq;
            const u32x4 o = add_res(v[x], res[x]);
            const auto r0 = __builtin_amdgcn_permlane32_swap(o.x, o.z, false, false);
            const auto r1 = __builtin_amdgcn_permlane32_swap(o.y, o.w, false, false);
            const u32x4 on = u32x4{r0[0], r1[0], r0[1], r1[1]};
            if (layer == 1) {
                *(u32x4*)(a.out + (size_t)(tbk * 32 + r) * DM + kbo * 16 + 8 * h) = on;
            } else {
                st_sc1(c, lbase + (u32)L_X2 + (u32)(tbk * 32 + kbo) * 1024, o);
            }
        }
        STAMP(27);
        if (layer == 0) WAIT_VM(0);
    }
    if (layer == 0) {
        BAR();
        if (w == 0) signal_group(c, layer, 0);
        if (w >= 4) { issue_chunk<CQ(1, 0)>(c); issue_chunk<CQ(1, 1)>(c); issue_chunk<CQ(2, 0)>(c); issue_chunk<CQ(2, 1)>(c); }
        STAMP(28);
    }
}

__global__ void __launch_bounds__(512, 2) mega_kernel(Args a) {
    extern __shared__ __attribute__((aligned(16))) unsigned char lds[];
    Ctx c;
    c.L = (LAS unsigned char*)lds;
    c.Laddr = (u32)(size_t)c.L;
    c.ws = a.ws;
    c.rs = __builtin_amdgcn_make_buffer_rsrc((void*)a.ws, 0, (int)(WS_END + 4 * MiB), 0x00020000);
    const int tid = threadIdx.x;
    c.lane = tid & 63;
    c.w = __builtin_amdgcn_readfirstlane(tid >> 6);
    const int bid = blockIdx.x;
    c.i = 4 * ((bid & 7) >> 1) + (bid >> 6);
    c.j = 8 * (bid & 1) + ((bid >> 3) & 7);
    c.voff = c.lane * 16;
    c.tag = 1u;
    if (c.w >= 4) {
        issue_chunk<CQ(0, 0)>(c); issue_chunk<CQ(0, 1)>(c); issue_chunk<CQ(1, 0)>(c); issue_chunk<CQ(1, 1)>(c); issue_chunk<CQ(2, 0)>(c); issue_chunk<CQ(2, 1)>(c);
    } else if (tid < 96) {
        lds_st<float>(c.L, SC_CS + tid * 4, ((const float*)(a.ws + WS_CS))[qkv_col(tid >> 5) + 32 * c.j + (tid & 31)]);
    }
    if (c.w < 4) {
        const int hh = c.lane >> 5, row = c.lane & 31, half = c.w & 1;
        u32 d[4];
#pragma unroll
        for (int q = 0; q < 4; ++q) {
            const int c0 = 16 * half + 8 * (q >> 1) + 4 * hh + 2 * (q & 1), c1 = c0 + 1;
            const u32 lo = c.w < 2 ? (row > c0 ? 0xff7fu : 0u) : (row == c0 ? 0x3f80u : 0u);
            const u32 hi = c.w < 2 ? (row > c1 ? 0xff7fu : 0u) : (row == c1 ? 0x3f80u : 0u);
            d[q] = lo | (hi << 16);
        }
        lds_st<u32x4>(c.L, SC_MK + c.w * 1024 + c.voff, u32x4{d[0], d[1], d[2], d[3]});
    }
    for (int layer = 0; layer < 2; ++layer) {
        phase_qkv_att(c, layer, a);
        phase_wo(c, layer, a);
        phase_ffn(c, layer, a);
    }
}

}

extern "C" void kernel_launch(void* const* d_in, const int* in_sizes, int n_in, void* d_out, int out_size, void* d_ws, size_t ws_size,
                              hipStream_t stream) {
    Args a;
    a.x = (const u16*)d_in[0]; a.wqkv = (const u16*)d_in[1]; a.wo = (const u16*)d_in[2];
    a.wg = (const u16*)d_in[4]; a.wu = (const u16*)d_in[5]; a.wd = (const u16*)d_in[6];
    a.out = (u16*)d_out; a.ws = (unsigned char*)d_ws;
    (void)hipFuncSetAttribute((const void*)mega_kernel, hipFuncAttributeMaxDynamicSharedMemorySize, LDS_BYTES);
    prep_kernel<<<PREP_GRID, 512, 0, stream>>>(a);
    mega_kernel<<<256, 512, LDS_BYTES, stream>>>(a);
}
```
